```python
import torch

def _softmax(x, axis=-1):
    return torch.softmax(x, dim=axis)
def _gelu(x):
    return torch.nn.functional.gelu(x, approximate="tanh")
def _silu(x):
    return torch.nn.functional.silu(x)
def _sigmoid(x):
    return torch.sigmoid(x)
def _layernorm(x, axis=-1, eps=1e-5):
    m = x.mean(dim=axis, keepdim=True)
    v = x.var(dim=axis, keepdim=True, unbiased=False)
    return (x - m) / torch.sqrt(v + eps)
def _scatter_add(tgt, idx, src, axis=0):
    return tgt.clone().index_add_(axis, idx, src)
def _segment_sum(src, seg, n):
    out = torch.zeros((n,) + src.shape[1:], dtype=src.dtype)
    return out.index_add_(0, seg, src)
def _pad(x, pw):
    flat = tuple(v for pair in reversed(pw) for v in pair)
    return torch.nn.functional.pad(x, flat)

kernel_name = 'parallel_sum__gqa_block_cached__swiglu_ffn__decode'

def setup_inputs():

    _w = (lambda a: (lambda t: t.to(torch.bfloat16) if t.is_floating_point() else t)(torch.as_tensor(a)))
    def _A_setup():
        B, Sq, D = 1, 1, 448
        Hq, Hkv, Sk, Dh = 16, 8, 32768, 256
        assert Hq % Hkv == 0, (Hq, Hkv)
        return {
            "x": _w((lambda *s: torch.randn(*s, dtype=torch.bfloat16) * 0.05)(B, Sq, D)),
            "k_cache": _w((lambda *s: torch.randn(*s, dtype=torch.bfloat16) * 3.2)(B, Hkv, Sk, Dh)),
            "v_cache": _w((lambda *s: torch.randn(*s, dtype=torch.bfloat16) * 1.6)(B, Hkv, Sk, Dh)),
            "wq": _w((lambda *s: torch.randn(*s, dtype=torch.bfloat16) * 3.2)(D, Hq * Dh) * (D ** -0.5)),
            "wo": _w((lambda *s: torch.randn(*s, dtype=torch.bfloat16) * 1.6)(Hq * Dh, D) * ((Hq * Dh) ** -0.5)),
        }
    _A_in = _A_setup()

    def _B_setup():
        B, S, D, F = 1, 1, 448, 1792
        return {
            "x": _w((lambda *s: torch.randn(*s, dtype=torch.bfloat16) * 0.05)(B, S, D)),
            "w_gate": _w((lambda *s: torch.randn(*s, dtype=torch.bfloat16) * 0.05)(D, F)),
            "w_up": _w((lambda *s: torch.randn(*s, dtype=torch.bfloat16) * 0.05)(D, F)),
            "w_down": _w((lambda *s: torch.randn(*s, dtype=torch.bfloat16) * 0.05)(F, D)),
        }
    _B_in = _B_setup()

    return {
        "A_x": _A_in["x"],
        "A_k_cache": _A_in["k_cache"],
        "A_v_cache": _A_in["v_cache"],
        "A_wq": _A_in["wq"],
        "A_wo": _A_in["wo"],
        "B_x": _B_in["x"],
        "B_w_gate": _B_in["w_gate"],
        "B_w_up": _B_in["w_up"],
        "B_w_down": _B_in["w_down"],
    }

def reference(A_x, A_k_cache, A_v_cache, A_wq, A_wo, B_x, B_w_gate, B_w_up, B_w_down):
    def _A_ref(x, k_cache, v_cache, wq, wo):
        B, Sq, D = x.shape
        Hq, Dh = 16, 256
        Hkv = k_cache.shape[1]
        rep = Hq // Hkv
        q = (lambda x, *d: x.permute(*d))((x @ wq).reshape(B, Sq, Hq, Dh), 0, 2, 1, 3)
        k = (lambda x, n, axis: torch.repeat_interleave(x, n, dim=axis))(k_cache, rep, axis=1)
        v = (lambda x, n, axis: torch.repeat_interleave(x, n, dim=axis))(v_cache, rep, axis=1)
        scores = torch.einsum("bhqd,bhkd->bhqk", q, k) * (Dh ** -0.5)
        p = _softmax(scores, axis=-1)
        o = torch.einsum("bhqk,bhkd->bhqd", p, v)
        return (lambda x, *d: x.permute(*d))(o, 0, 2, 1, 3).reshape(B, Sq, Hq * Dh) @ wo

    def _B_ref(x, w_gate, w_up, w_down):
        g = _silu(x @ w_gate)
        u = x @ w_up
        return (g * u) @ w_down

    x = A_x
    xn = _layernorm(x, axis=-1)
    return x + _A_ref(xn, A_k_cache, A_v_cache, A_wq, A_wo) + _B_ref(xn, B_w_gate, B_w_up, B_w_down)
```

```cpp
#include <hip/hip_runtime.h>
#include <hip/hip_bf16.h>
#include <stdint.h>

#define ROUND_SCORES 1

typedef unsigned v4u __attribute__((ext_vector_type(4)));
typedef float f32x4 __attribute__((ext_vector_type(4)));
typedef __bf16 bf16x2 __attribute__((ext_vector_type(2)));
typedef unsigned short u16;

constexpr int D = 448, HQ = 16, HKV = 8, SK = 32768, DH = 256, F = 1792, NQ = HQ * DH;
constexpr int NSPLIT = 32, ROWS_WG = SK / NSPLIT;

__device__ __forceinline__ float bf2f(u16 b) { return __builtin_bit_cast(float, (unsigned)b << 16); }
__device__ __forceinline__ float rbf(float f) {
  __bf16 b = (__bf16)f; return (float)b;
}
__device__ __forceinline__ u16 f2bf(float f) { __bf16 b = (__bf16)f; return __builtin_bit_cast(u16, b); }
__device__ __forceinline__ float dot2(unsigned k, unsigned q, float acc) {
  return __builtin_amdgcn_fdot2_f32_bf16(__builtin_bit_cast(bf16x2, k), __builtin_bit_cast(bf16x2, q), acc, false);
}
__device__ __forceinline__ float lo16(unsigned u) { return __builtin_bit_cast(float, u << 16); }
__device__ __forceinline__ float hi16(unsigned u) { return __builtin_bit_cast(float, u & 0xffff0000u); }

__device__ __forceinline__ float wave_sum(float v) {
#pragma unroll
  for (int o = 32; o >= 1; o >>= 1) v += __shfl_xor(v, o, 64);
  return v;
}
__device__ __forceinline__ float wave_max(float v) {
#pragma unroll
  for (int o = 32; o >= 1; o >>= 1) v = fmaxf(v, __shfl_xor(v, o, 64));
  return v;
}

__device__ __forceinline__ void layernorm_lds(const u16* x, float* xn, int tid) {
  const int lane = tid & 63;
  float v[7];
  float s = 0.f;
#pragma unroll
  for (int i = 0; i < 7; ++i) { v[i] = bf2f(x[lane + 64 * i]); s += v[i]; }
  s = wave_sum(s);
  const float mean = s * (1.0f / D);
  float q = 0.f;
#pragma unroll
  for (int i = 0; i < 7; ++i) { float d = v[i] - mean; q += d * d; }
  q = wave_sum(q);
  const float var = rbf(q * (1.0f / D));
  const float mb = rbf(mean);
  const float sd = rbf(sqrtf(rbf(var + 1e-5f)));
  if (tid < 64) {
#pragma unroll
    for (int i = 0; i < 7; ++i) xn[lane + 64 * i] = rbf(rbf(v[i] - mb) / sd);
  }
  __syncthreads();
}

__device__ __forceinline__ void gemv32(const u16* W, int ldw, int col0, const float* xn, float* red, int tid, float* out32) {
  const int rg = tid >> 2, cc = tid & 3;
  float acc[8];
#pragma unroll
  for (int j = 0; j < 8; ++j) acc[j] = 0.f;
#pragma unroll
  for (int i = 0; i < 7; ++i) {
    const int r = rg + 64 * i;
    const v4u w = *reinterpret_cast<const v4u*>(W + (size_t)r * ldw + col0 + cc * 8);
    const float xv = xn[r];
#pragma unroll
    for (int j = 0; j < 4; ++j) { acc[2 * j] = fmaf(lo16(w[j]), xv, acc[2 * j]); acc[2 * j + 1] = fmaf(hi16(w[j]), xv, acc[2 * j + 1]); }
  }
#pragma unroll
  for (int j = 0; j < 8; ++j) {
    float a = acc[j];
    a += __shfl_xor(a, 4, 64); a += __shfl_xor(a, 8, 64); a += __shfl_xor(a, 16, 64); a += __shfl_xor(a, 32, 64);
    acc[j] = a;
  }
  const int lane = tid & 63, wave = tid >> 6;
  __syncthreads();
  if (lane < 4) {
#pragma unroll
    for (int j = 0; j < 8; ++j) red[wave * 32 + lane * 8 + j] = acc[j];
  }
  __syncthreads();
  if (tid < 32) out32[tid] = red[tid] + red[32 + tid] + red[64 + tid] + red[96 + tid];
  __syncthreads();
}

__global__ void __launch_bounds__(256) k_pre(const u16* x, const u16* wq, const u16* wg, const u16* wu, u16* qout, float* hout) {
  __shared__ float xn[D];
  __shared__ float red[128];
  __shared__ float o1[32], o2[32];
  const int tid = threadIdx.x;
  layernorm_lds(x, xn, tid);
  const int b = blockIdx.x;
  if (b < NQ / 32) {
    gemv32(wq, NQ, b * 32, xn, red, tid, o1);
    if (tid < 32) qout[b * 32 + tid] = f2bf(o1[tid]);
  } else {
    const int c0 = (b - NQ / 32) * 32;
    gemv32(wg, F, c0, xn, red, tid, o1);
    gemv32(wu, F, c0, xn, red, tid, o2);
    if (tid < 32) {
      const float g = rbf(o1[tid]), u = rbf(o2[tid]);
      const float sg = rbf(g / (1.0f + __expf(-g)));
      hout[c0 + tid] = rbf(sg * u);
    }
  }
}

template <int CTRL> __device__ __forceinline__ float dpp(float x) {
  return __builtin_bit_cast(float, __builtin_amdgcn_mov_dpp(__builtin_bit_cast(int, x), CTRL, 0xf, 0xf, true));
}
constexpr int XOR1 = 0xB1, XOR2 = 0x4E, XOR7 = 0x141, XOR8 = 0x128;

__global__ void __launch_bounds__(512, 2) k_attn(const u16* __restrict__ kc, const u16* __restrict__ vc, const u16* __restrict__ q, float* __restrict__ pml, float* __restrict__ po) {
  __shared__ __attribute__((aligned(16))) float lds_p[8][256];
  __shared__ __attribute__((aligned(16))) float lds_o[8][512];
  __shared__ float lds_ml[8][4];
  const int tid = threadIdx.x, lane = tid & 63, wave = __builtin_amdgcn_readfirstlane(tid >> 6);
  const int h = blockIdx.x & 7, s = blockIdx.x >> 3;
  const int r = lane >> 5, c = lane & 31;
  const size_t row0 = (size_t)h * SK + (size_t)s * ROWS_WG + wave * 128;
  const v4u* kp = reinterpret_cast<const v4u*>(kc + row0 * DH) + lane;
  const v4u* vp = reinterpret_cast<const v4u*>(vc + row0 * DH) + lane;
  const v4u q0 = *reinterpret_cast<const v4u*>(q + (2 * h) * DH + 8 * c);
  const v4u q1 = *reinterpret_cast<const v4u*>(q + (2 * h + 1) * DH + 8 * c);
  float sc[4];
#pragma unroll
  for (int t = 0; t < 4; ++t) {
    v4u kk[16];
#pragma unroll
    for (int i = 0; i < 16; ++i) kk[i] = __builtin_nontemporal_load(kp + (t * 16 + i) * 64);
    float x[32];
#pragma unroll
    for (int i = 0; i < 16; ++i) {
      float a = 0.f, b = 0.f;
#pragma unroll
      for (int j = 0; j < 4; ++j) { a = dot2(kk[i][j], q0[j], a); b = dot2(kk[i][j], q1[j], b); }
      x[2 * i] = a; x[2 * i + 1] = b;
    }
#pragma unroll
    for (int v = 0; v < 16; ++v) {
      const float lo = x[v], hi = x[v + 16];
      const float t1 = lo + __shfl_xor(lo, 16, 64), t2 = hi + __shfl_xor(hi, 16, 64);
      x[v] = (c & 16) ? t2 : t1;
    }
#define TR_STEP(HALF, CTRL, BIT) _Pragma("unroll") for (int v = 0; v < (HALF); ++v) { \
      const float lo_ = x[v], hi_ = x[v + (HALF)]; const float t1_ = lo_ + dpp<CTRL>(lo_), t2_ = hi_ + dpp<CTRL>(hi_); \
      x[v] = (c & (BIT)) ? t2_ : t1_; }
    TR_STEP(8, XOR8, 8) TR_STEP(4, XOR7, 4) TR_STEP(2, XOR2, 2) TR_STEP(1, XOR1, 1)
#undef TR_STEP
    sc[t] = rbf(x[0]) * 0.0625f;
  }
  float m = fmaxf(fmaxf(sc[0], sc[1]), fmaxf(sc[2], sc[3]));
#pragma unroll
  for (int o = 32; o >= 2; o >>= 1) m = fmaxf(m, __shfl_xor(m, o, 64));
  float l = 0.f;
#pragma unroll
  for (int t = 0; t < 4; ++t) {
    const float p = __expf(sc[t] - m);
    l += p;
    const int i = c >> 1, hd = c & 1;
    lds_p[wave][r * 128 + (t * 16 + i) * 2 + hd] = p;
  }
#pragma unroll
  for (int o = 32; o >= 2; o >>= 1) l += __shfl_xor(l, o, 64);
  if (lane < 2) { lds_ml[wave][lane * 2] = m; lds_ml[wave][lane * 2 + 1] = l; }
  __builtin_amdgcn_s_waitcnt(0xC07F);
  float acc0[8], acc1[8];
#pragma unroll
  for (int j = 0; j < 8; ++j) { acc0[j] = 0.f; acc1[j] = 0.f; }
#pragma unroll
  for (int t = 0; t < 4; ++t) {
    v4u vv[16];
#pragma unroll
    for (int i = 0; i < 16; ++i) vv[i] = __builtin_nontemporal_load(vp + (t * 16 + i) * 64);
#pragma unroll
    for (int i2 = 0; i2 < 8; ++i2) {
      const f32x4 pp = *reinterpret_cast<const f32x4*>(&lds_p[wave][r * 128 + (t * 16 + i2 * 2) * 2]);
#pragma unroll
      for (int u = 0; u < 2; ++u) {
        const v4u w = vv[i2 * 2 + u];
        const float p0 = pp[2 * u], p1 = pp[2 * u + 1];
#pragma unroll
        for (int j = 0; j < 4; ++j) {
          const float a = lo16(w[j]), b = hi16(w[j]);
          acc0[2 * j] = fmaf(p0, a, acc0[2 * j]); acc0[2 * j + 1] = fmaf(p0, b, acc0[2 * j + 1]);
          acc1[2 * j] = fmaf(p1, a, acc1[2 * j]); acc1[2 * j + 1] = fmaf(p1, b, acc1[2 * j + 1]);
        }
      }
    }
  }
#pragma unroll
  for (int j = 0; j < 8; ++j) { acc0[j] += __shfl_xor(acc0[j], 32, 64); acc1[j] += __shfl_xor(acc1[j], 32, 64); }
#pragma unroll
  for (int j = 0; j < 8; ++j) lds_o[wave][r * 256 + 8 * c + j] = r ? acc1[j] : acc0[j];
  __syncthreads();
  {
    const int hd = tid >> 8;
    float M = -INFINITY;
#pragma unroll
    for (int w = 0; w < 8; ++w) M = fmaxf(M, lds_ml[w][hd * 2]);
    float L = 0.f, o = 0.f;
#pragma unroll
    for (int w = 0; w < 8; ++w) { const float e = __expf(lds_ml[w][hd * 2] - M); L += e * lds_ml[w][hd * 2 + 1]; o += e * lds_o[w][tid]; }
    const int qh = 2 * h + hd;
    po[((size_t)qh * NSPLIT + s) * DH + (tid & 255)] = o;
    if ((tid & 255) == 0) { pml[(qh * NSPLIT + s) * 2] = M; pml[(qh * NSPLIT + s) * 2 + 1] = L; }
  }
}

__global__ void __launch_bounds__(256) k_comb(const float* pml, const float* po, float* o) {
  const int qh = blockIdx.x, d = threadIdx.x;
  float M = -INFINITY;
  for (int s = 0; s < NSPLIT; ++s) M = fmaxf(M, pml[(qh * NSPLIT + s) * 2]);
  float L = 0.f, a = 0.f;
  for (int s = 0; s < NSPLIT; ++s) { const float e = __expf(pml[(qh * NSPLIT + s) * 2] - M); L += e * pml[(qh * NSPLIT + s) * 2 + 1]; a += e * po[((size_t)qh * NSPLIT + s) * DH + d]; }
  o[qh * DH + d] = rbf(a / L);
}

__global__ void __launch_bounds__(256) k_out(const u16* x, const float* o, const float* hf, const u16* wo, const u16* wd, u16* out) {
  __shared__ float red[2][4][8];
  const int tid = threadIdx.x, c0 = blockIdx.x * 8;
  float a[8], b[8];
#pragma unroll
  for (int j = 0; j < 8; ++j) { a[j] = 0.f; b[j] = 0.f; }
  for (int r = tid; r < NQ; r += 256) {
    const v4u w = *reinterpret_cast<const v4u*>(wo + (size_t)r * D + c0);
    const float ov = o[r];
#pragma unroll
    for (int j = 0; j < 4; ++j) { a[2 * j] = fmaf(lo16(w[j]), ov, a[2 * j]); a[2 * j + 1] = fmaf(hi16(w[j]), ov, a[2 * j + 1]); }
  }
  for (int r = tid; r < F; r += 256) {
    const v4u w = *reinterpret_cast<const v4u*>(wd + (size_t)r * D + c0);
    const float hv = hf[r];
#pragma unroll
    for (int j = 0; j < 4; ++j) { b[2 * j] = fmaf(lo16(w[j]), hv, b[2 * j]); b[2 * j + 1] = fmaf(hi16(w[j]), hv, b[2 * j + 1]); }
  }
#pragma unroll
  for (int j = 0; j < 8; ++j) { a[j] = wave_sum(a[j]); b[j] = wave_sum(b[j]); }
  const int lane = tid & 63, wave = tid >> 6;
  if (lane == 0) {
#pragma unroll
    for (int j = 0; j < 8; ++j) { red[0][wave][j] = a[j]; red[1][wave][j] = b[j]; }
  }
  __syncthreads();
  if (tid < 8) {
    const float A = rbf(red[0][0][tid] + red[0][1][tid] + red[0][2][tid] + red[0][3][tid]);
    const float B = rbf(red[1][0][tid] + red[1][1][tid] + red[1][2][tid] + red[1][3][tid]);
    const float xv = bf2f(x[c0 + tid]);
    out[c0 + tid] = f2bf(rbf(xv + A) + B);
  }
}

extern "C" void kernel_launch(void* const* d_in, const int* in_sizes, int n_in,
                              void* d_out, int out_size, void* d_ws, size_t ws_size,
                              hipStream_t stream) {
  const u16* x = (const u16*)d_in[0];
  const u16* kc = (const u16*)d_in[1];
  const u16* vc = (const u16*)d_in[2];
  const u16* wq = (const u16*)d_in[3];
  const u16* wo = (const u16*)d_in[4];
  const u16* wg = (const u16*)d_in[6];
  const u16* wu = (const u16*)d_in[7];
  const u16* wd = (const u16*)d_in[8];
  char* ws = (char*)d_ws;
  u16* q = (u16*)ws;
  float* hf = (float*)(ws + 8192);
  float* o = (float*)(ws + 16384);
  float* pml = (float*)(ws + 32768);
  float* po = (float*)(ws + 65536);
  k_pre<<<NQ / 32 + F / 32, 256, 0, stream>>>(x, wq, wg, wu, q, hf);
  k_attn<<<256, 512, 0, stream>>>(kc, vc, q, pml, po);
  k_comb<<<16, 256, 0, stream>>>(pml, po, o);
  k_out<<<56, 256, 0, stream>>>(x, o, hf, wo, wd, (u16*)d_out);
}
```

```cpp
#include <hip/hip_runtime.h>
#include <hip/hip_bf16.h>
#include <stdint.h>

#define PRE0 8
#define PACE 4
#define W_AUX 0
#define UNIT_PAT 0
#define DIAG 0

typedef unsigned v4u __attribute__((ext_vector_type(4)));
typedef unsigned v2u __attribute__((ext_vector_type(2)));
typedef float f32x4 __attribute__((ext_vector_type(4)));
typedef float f32x2 __attribute__((ext_vector_type(2)));
typedef __bf16 bf16x2 __attribute__((ext_vector_type(2)));
typedef unsigned short u16;
typedef __amdgpu_buffer_rsrc_t rsrc_t;

constexpr int D = 448, SK = 32768, DH = 256, F = 1792, NQ = 4096;
constexpr int NWG = 256, NT = 512;
constexpr unsigned OOB = 0x80000000u;
constexpr unsigned SENT = 0xFFFFFFFFu;
constexpr unsigned AREA = 8192;
constexpr unsigned A_Q = 0;
constexpr unsigned A_O1 = 1024;
constexpr unsigned A_ML = 3072;
constexpr unsigned A_Y2 = 4096;
constexpr unsigned WS_REQ = NWG * AREA;
constexpr unsigned WS_ECHO = WS_REQ + 1024;
constexpr unsigned WS_GO = WS_ECHO + 1024;
constexpr unsigned WS_END = WS_GO + 256;
constexpr int SPIN_CAP = 1 << 18;

__device__ __forceinline__ float asf(unsigned u) { return __builtin_bit_cast(float, u); }
__device__ __forceinline__ unsigned asu(float f) { return __builtin_bit_cast(unsigned, f); }
__device__ __forceinline__ float rbf(float f) { __bf16 b = (__bf16)f; return (float)b; }
__device__ __forceinline__ unsigned f2bf(float f) { __bf16 b = (__bf16)f; return (unsigned)__builtin_bit_cast(u16, b); }
__device__ __forceinline__ float dot2(unsigned k, unsigned q, float acc) {
  return __builtin_amdgcn_fdot2_f32_bf16(__builtin_bit_cast(bf16x2, k), __builtin_bit_cast(bf16x2, q), acc, false);
}
__device__ __forceinline__ f32x2 unp(unsigned u) { f32x2 r; r.x = asf(u << 16); r.y = asf(u & 0xffff0000u); return r; }
template <int CTRL> __device__ __forceinline__ float dpp(float x) {
  return asf((unsigned)__builtin_amdgcn_update_dpp(0, (int)asu(x), CTRL, 0xf, 0xf, true));
}
constexpr int XOR1 = 0xB1, XOR2 = 0x4E, XOR7 = 0x141, ROR8 = 0x128, ROR4 = 0x124, ROR2 = 0x122, BCAST = 0x150;
constexpr int QP_EVEN = 0xA0, QP_ODD = 0xF5;
__device__ __forceinline__ float x16_add(float x) { auto s = __builtin_amdgcn_permlane16_swap(asu(x), asu(x), false, false); return asf(s[0]) + asf(s[1]); }
__device__ __forceinline__ float x32_add(float x) { auto s = __builtin_amdgcn_permlane32_swap(asu(x), asu(x), false, false); return asf(s[0]) + asf(s[1]); }
__device__ __forceinline__ float x16_max(float x) { auto s = __builtin_amdgcn_permlane16_swap(asu(x), asu(x), false, false); return fmaxf(asf(s[0]), asf(s[1])); }
__device__ __forceinline__ float x32_max(float x) { auto s = __builtin_amdgcn_permlane32_swap(asu(x), asu(x), false, false); return fmaxf(asf(s[0]), asf(s[1])); }
__device__ __forceinline__ float tr32(float a, float b) { auto s = __builtin_amdgcn_permlane32_swap(asu(a), asu(b), false, false); return asf(s[0]) + asf(s[1]); }
__device__ __forceinline__ float tr16(float a, float b) { auto s = __builtin_amdgcn_permlane16_swap(asu(a), asu(b), false, false); return asf(s[0]) + asf(s[1]); }
__device__ __forceinline__ float wave_sum(float v) {
  v += dpp<XOR1>(v); v += dpp<XOR2>(v); v += dpp<XOR7>(v); v += dpp<ROR8>(v);
  v = x16_add(v); return x32_add(v);
}
__device__ __forceinline__ float wave_max(float v) {
  v = fmaxf(v, dpp<XOR1>(v)); v = fmaxf(v, dpp<XOR2>(v)); v = fmaxf(v, dpp<XOR7>(v)); v = fmaxf(v, dpp<ROR8>(v));
  v = x16_max(v); return x32_max(v);
}
__device__ __forceinline__ rsrc_t mkrs(const void* p, unsigned bytes) { return __builtin_amdgcn_make_buffer_rsrc((void*)p, 0, (int)bytes, 0x00020000); }
template <int AUX> __device__ __forceinline__ v4u ld4(rsrc_t r, unsigned vo, unsigned so) { return __builtin_amdgcn_raw_buffer_load_b128(r, (int)vo, (int)so, AUX); }
template <int AUX> __device__ __forceinline__ v2u ld2(rsrc_t r, unsigned vo, unsigned so) { return __builtin_amdgcn_raw_buffer_load_b64(r, (int)vo, (int)so, AUX); }
__device__ __forceinline__ void st4c(v4u v, rsrc_t r, unsigned vo) { __builtin_amdgcn_raw_buffer_store_b128(v, r, (int)vo, 0, 16); }
__device__ __forceinline__ void st2c(v2u v, rsrc_t r, unsigned vo) { __builtin_amdgcn_raw_buffer_store_b64(v, r, (int)vo, 0, 16); }
__device__ __forceinline__ void st1c(unsigned v, rsrc_t r, unsigned vo) { __builtin_amdgcn_raw_buffer_store_b32(v, r, (int)vo, 0, 16); }
__device__ __forceinline__ unsigned canon(float f) { unsigned u = asu(f); return u < 0xFFFFFFFEu ? u : 0xFFFFFFFEu; }
__device__ __forceinline__ bool full4(v4u v) { return v[0] != SENT && v[1] != SENT && v[2] != SENT && v[3] != SENT; }
#define SB() __builtin_amdgcn_sched_barrier(0)
__device__ __forceinline__ void bar() { __builtin_amdgcn_s_waitcnt(0xC07F); __builtin_amdgcn_s_barrier(); }
#define WAITV(n) __builtin_amdgcn_s_waitcnt(((n) & 15) | (((n) >> 4) << 14) | 0x0F70)
__device__ __forceinline__ float reduce16(float (&y)[16], int lane) {
#pragma unroll
  for (int v = 0; v < 8; ++v) y[v] = tr32(y[v], y[v + 8]);
#pragma unroll
  for (int v = 0; v < 4; ++v) y[v] = tr16(y[v], y[v + 4]);
#pragma unroll
  for (int v = 0; v < 2; ++v) { const bool s_ = lane & 8; y[v] = (s_ ? y[v + 2] : y[v]) + dpp<ROR8>(s_ ? y[v] : y[v + 2]); }
  { const bool s_ = lane & 4; y[0] = (s_ ? y[1] : y[0]) + dpp<XOR7>(s_ ? y[0] : y[1]); }
  y[0] += dpp<XOR2>(y[0]); y[0] += dpp<XOR1>(y[0]);
  return y[0];
}
#define LDS_FENCE() asm volatile("" ::: "memory")

constexpr float EXP_C = 0.0625f * 1.4426950408889634f;

struct Att {
  float m, l;
  f32x2 a0[4], a1[4];
};

template <class ISSUE>
__device__ __forceinline__ float phase_k(v4u (&kb)[8], const v4u& q0, const v4u& q1, Att& st, int lane, ISSUE issue) {
  float x[16];
#pragma unroll
  for (int i = 0; i < 8; ++i) {
    float a = 0.f, b = 0.f;
#pragma unroll
    for (int j = 0; j < 4; ++j) { a = dot2(kb[i][j], q0[j], a); b = dot2(kb[i][j], q1[j], b); }
    x[2 * i] = a; x[2 * i + 1] = b;
    asm volatile("" : "+v"(x[2 * i]), "+v"(x[2 * i + 1]));
    issue(i);
    SB();
  }
#define TR_STEP(HALF, CTRL, BIT) _Pragma("unroll") for (int v = 0; v < (HALF); ++v) { \
    const float lo_ = x[v], hi_ = x[v + (HALF)]; const bool s_ = lane & (BIT); \
    x[v] = (s_ ? hi_ : lo_) + dpp<CTRL>(s_ ? lo_ : hi_); }
  TR_STEP(8, ROR8, 8) TR_STEP(4, XOR7, 4) TR_STEP(2, XOR2, 2) TR_STEP(1, XOR1, 1)
#undef TR_STEP
  const float s = rbf(x16_add(x[0]));
  float mx = fmaxf(s, dpp<ROR2>(s)); mx = fmaxf(mx, dpp<ROR4>(mx)); mx = fmaxf(mx, dpp<ROR8>(mx));
  mx = x32_max(mx);
  const float mn = fmaxf(st.m, mx);
  const float al = __builtin_amdgcn_exp2f((st.m - mn) * EXP_C);
  const float p = __builtin_amdgcn_exp2f((s - mn) * EXP_C);
  st.m = mn; st.l = st.l * al + p;
  const float al0 = dpp<QP_EVEN>(al), al1 = dpp<QP_ODD>(al);
#pragma unroll
  for (int j = 0; j < 4; ++j) { st.a0[j] *= al0; st.a1[j] *= al1; }
  return p;
}
template <int I> struct Bc { static __device__ __forceinline__ float get(float p) { return dpp<BCAST + I>(p); } };
template <int I, class ISSUE>
__device__ __forceinline__ void pv_step(v4u (&vb)[8], float p, Att& st, ISSUE& issue) {
  const float p0 = dpp<BCAST + 2 * I>(p), p1 = dpp<BCAST + 2 * I + 1>(p);
#pragma unroll
  for (int j = 0; j < 4; ++j) { const f32x2 v = unp(vb[I][j]); st.a0[j] += v * p0; st.a1[j] += v * p1; }
  asm volatile("" : "+v"(st.a0[0]), "+v"(st.a0[1]), "+v"(st.a0[2]), "+v"(st.a0[3]), "+v"(st.a1[0]), "+v"(st.a1[1]), "+v"(st.a1[2]), "+v"(st.a1[3]));
  issue(I);
  SB();
}
template <class ISSUE>
__device__ __forceinline__ void phase_v(v4u (&vb)[8], float p, Att& st, ISSUE issue) {
  pv_step<0>(vb, p, st, issue); pv_step<1>(vb, p, st, issue); pv_step<2>(vb, p, st, issue); pv_step<3>(vb, p, st, issue);
  pv_step<4>(vb, p, st, issue); pv_step<5>(vb, p, st, issue); pv_step<6>(vb, p, st, issue); pv_step<7>(vb, p, st, issue);
}

#define STAMP(k) do {} while (0)

__global__ void __launch_bounds__(NT, 2)
fused_kernel(const u16* __restrict__ xg, const u16* __restrict__ kc, const u16* __restrict__ vc, const u16* __restrict__ wq,
             const u16* __restrict__ wo, const u16* __restrict__ wg, const u16* __restrict__ wu, const u16* __restrict__ wd,
             u16* __restrict__ out, char* ws) {
  __shared__ __attribute__((aligned(16))) float s_part[8][512];
  __shared__ __attribute__((aligned(16))) float s_qp[8][16];
  __shared__ __attribute__((aligned(16))) float s_fp[8][16];
  __shared__ __attribute__((aligned(16))) float s_ml[8][4];
  __shared__ __attribute__((aligned(16))) float s_o[16];
  const int tid = threadIdx.x, lane = tid & 63, w = __builtin_amdgcn_readfirstlane(tid >> 6);
  const int b = blockIdx.x, h = b & 7, s = b >> 3;
  asm volatile("" :: "s"(xg), "s"(kc), "s"(vc), "s"(wq), "s"(wo), "s"(wg), "s"(wu), "s"(wd), "s"(out), "s"(ws));
  STAMP(0);
  const rsrc_t rx = mkrs(xg, D * 2);
  const rsrc_t rk = mkrs(kc + (size_t)h * SK * DH, SK * DH * 2);
  const rsrc_t rv = mkrs(vc + (size_t)h * SK * DH, SK * DH * 2);
  const rsrc_t rwq = mkrs(wq, D * NQ * 2);
  const rsrc_t rws = mkrs(ws, WS_END);
  const unsigned l16 = lane * 16;
  auto ubase = [&](int u) -> unsigned {
    return (unsigned)(s * 1024 + w * 128 + u * 16) * 512u;
  };
  auto ldk = [&](unsigned base, int i) { return ld4<2>(rk, l16 + (i & 3) * 1024, base + (i >> 2) * 4096); };
  auto ldv = [&](unsigned base, int i) { return ld4<2>(rv, l16 + (i & 3) * 1024, base + (i >> 2) * 4096); };

  const unsigned area = (unsigned)b * AREA;
  auto area_of = [&](int ss) -> unsigned { return (unsigned)(ss * 8 + h) * AREA; };
  const int myrow = w * 56 + lane;
  const unsigned lmask = lane < 56 ? 0u : OOB;
  const v4u xa = ld4<0>(rx, l16, 0);
  const unsigned xw = __builtin_amdgcn_raw_buffer_load_b16(rx, (int)((unsigned)myrow * 2 | lmask), 0, 0);
  const unsigned wqo = (unsigned)(myrow * NQ + h * 512 + s * 16) * 2 | lmask;
  const v4u wqa = ld4<W_AUX>(rwq, wqo, 0), wqb = ld4<W_AUX>(rwq, wqo + 16, 0);
  SB();
  st4c((v4u){SENT, SENT, SENT, SENT}, rws, (area + tid * 16) | ((tid < 208 || (b < 112 && tid >= 256)) ? 0u : OOB));
  SB();
  v4u pre[4][8];
  const unsigned ub0 = ubase(0), ub1 = ubase(1);
  auto ldpre = [&](int n) { pre[n >> 3][n & 7] = (n & 8) ? ldv((n & 16) ? ub1 : ub0, n & 7) : ldk((n & 16) ? ub1 : ub0, n & 7); };
#pragma unroll
  for (int n = 0; n < PRE0; ++n) ldpre(n);
  SB();
  const bool leader = (b == 0 && w == 1), member = (w == 0);
  unsigned poll_off = tid == 1 ? WS_GO : OOB;
  poll_off = tid == 0 ? WS_REQ + (b >> 2) * 16 : poll_off;
  poll_off = leader ? WS_ECHO + l16 : poll_off;
  const v4u hs0 = ld4<16>(rws, poll_off, 0);
  const unsigned go_old_l = __builtin_amdgcn_raw_buffer_load_b32(rws, leader ? (int)WS_GO : (int)OOB, 0, 16);
  const int fg = h * 28 + s;
  const bool fok = s < 28;
  const unsigned guo = (unsigned)(myrow * F + fg * 8) * 2 | (fok ? lmask : OOB);
  const v4u gw = ld4<0>(mkrs(wg, D * F * 2), guo, 0);
  const v4u uw = ld4<0>(mkrs(wu, D * F * 2), guo, 0);
  SB();
  float xn;
  {
    f32x2 xv[4];
    float sm = 0.f;
#pragma unroll
    for (int j = 0; j < 4; ++j) { xv[j] = unp(xa[j]); sm += xv[j].x + xv[j].y; }
    const float mean = wave_sum(sm) * (1.0f / D);
    float sq = 0.f;
#pragma unroll
    for (int j = 0; j < 4; ++j) { const f32x2 d = xv[j] - mean; sq += d.x * d.x + d.y * d.y; }
    sq = lane < 56 ? sq : 0.f;
    const float var = rbf(wave_sum(sq) * (1.0f / D));
    const float sd = rbf(sqrtf(rbf(var + 1e-5f)));
    xn = lane < 56 ? rbf(rbf(asf(xw << 16) - rbf(mean)) / sd) : 0.f;
  }
  {
    float y[16];
#pragma unroll
    for (int j = 0; j < 4; ++j) {
      const f32x2 a = unp(wqa[j]) * xn, c = unp(wqb[j]) * xn;
      y[2 * j] = a.x; y[2 * j + 1] = a.y; y[8 + 2 * j] = c.x; y[8 + 2 * j + 1] = c.y;
    }
    const float r = reduce16(y, lane);
    if ((lane & 3) == 0) s_qp[w][lane >> 2] = r;
  }
  SB();
  WAITV(PRE0 + 4);
  LDS_FENCE(); bar(); LDS_FENCE();
  v4u qsend;
  const unsigned send_off = member ? area_of((lane & 15) + 16 * (lane >> 5)) + A_Q + s * 32 + ((lane >> 4) & 1) * 16 : OOB;
  {
    const int vi = ((lane >> 4) & 1) * 8 + (lane & 7);
    float a = 0.f;
#pragma unroll
    for (int k = 0; k < 8; ++k) a += s_qp[k][vi];
    const unsigned qb = f2bf(a);
    unsigned pk = qb | ((unsigned)__builtin_amdgcn_update_dpp(0, (int)qb, XOR1, 0xf, 0xf, true) << 16);
    pk = pk < 0xFFFFFFFEu ? pk : 0xFFFFFFFEu;
    qsend[0] = (unsigned)__builtin_amdgcn_update_dpp(0, (int)pk, BCAST + 0, 0xf, 0xf, true);
    qsend[1] = (unsigned)__builtin_amdgcn_update_dpp(0, (int)pk, BCAST + 2, 0xf, 0xf, true);
    qsend[2] = (unsigned)__builtin_amdgcn_update_dpp(0, (int)pk, BCAST + 4, 0xf, 0xf, true);
    qsend[3] = (unsigned)__builtin_amdgcn_update_dpp(0, (int)pk, BCAST + 6, 0xf, 0xf, true);
    st4c(qsend, rws, send_off);
  }
  STAMP(5);
  SB();
#pragma unroll
  for (int n = PRE0; n < PRE0 + 4; ++n) { WAITV(PACE + 4); ldpre(n); SB(); }
  const v4u e1 = hs0 + 1u;
  st4c(e1, rws, leader ? WS_REQ + l16 : OOB);
  const unsigned go_old = (unsigned)__builtin_amdgcn_readlane((int)hs0[0], 1);
  const unsigned go_new = go_old_l + 1u;
  bool seen = false;
  unsigned seenm = 0;
  {
    float y[16];
#pragma unroll
    for (int j = 0; j < 4; ++j) {
      const f32x2 g = unp(gw[j]) * xn, u = unp(uw[j]) * xn;
      y[4 * j] = g.x; y[4 * j + 1] = u.x; y[4 * j + 2] = g.y; y[4 * j + 3] = u.y;
    }
    const float r = reduce16(y, lane);
    if ((lane & 3) == 0) s_fp[w][lane >> 2] = r;
  }
  SB();
#pragma unroll
  for (int n = PRE0 + 4; n < 20; ++n) { WAITV(PACE - 1); ldpre(n); SB(); }
  const int cq = lane >> 2, rg = lane & 3;
  v2u wov[4], wdv[2];
  {
    const rsrc_t ro = mkrs(wo, NQ * D * 2), rd = mkrs(wd, F * D * 2);
    const unsigned oo = (unsigned)((h * 512 + s * 16 + rg * 4) * D + w * 56 + cq * 4) * 2 | (cq < 14 ? 0u : OOB);
    const unsigned od = (unsigned)((fg * 8 + rg * 2) * D + w * 56 + cq * 4) * 2 | ((cq < 14 && fok) ? 0u : OOB);
#pragma unroll
    for (int i = 0; i < 4; ++i) wov[i] = ld2<0>(ro, oo + i * D * 2, 0);
#pragma unroll
    for (int i = 0; i < 2; ++i) wdv[i] = ld2<0>(rd, od + i * D * 2, 0);
  }
  SB();
#pragma unroll
  for (int n = 20; n < 20 + PACE; ++n) { WAITV(PACE + 5); ldpre(n); SB(); }
#pragma unroll
  for (int n = 20 + PACE; n < 32 - PACE; ++n) { WAITV(PACE - 1); ldpre(n); SB(); }
  const unsigned qo = area + A_Q + (lane & 31) * 16;
  v4u q0 = ld4<16>(rws, qo, 0), q1 = ld4<16>(rws, qo + 512, 0);
  v4u pv = ld4<16>(rws, poll_off, 0);
  SB();
#pragma unroll
  for (int n = 32 - PACE; n < 32; ++n) { WAITV(PACE + 2); ldpre(n); SB(); }
  LDS_FENCE(); bar(); LDS_FENCE();
  for (int it = 0; it < SPIN_CAP; ++it) {
    if (__builtin_amdgcn_ballot_w64(!(full4(q0) && full4(q1))) == 0) break;
    st4c(qsend, rws, send_off);
    __builtin_amdgcn_s_sleep(2);
    q0 = ld4<16>(rws, qo, 0); q1 = ld4<16>(rws, qo + 512, 0);
    WAITV(0);
  }
  STAMP(1);
  SB();

  auto service = [&]() {
    seenm |= (pv[0] == e1[0] ? 1u : 0u) | (pv[1] == e1[1] ? 2u : 0u) | (pv[2] == e1[2] ? 4u : 0u) | (pv[3] == e1[3] ? 8u : 0u);
    const bool allseen = __builtin_amdgcn_ballot_w64(seenm != 15u) == 0;
    const unsigned req = (b & 2) ? ((b & 1) ? pv[3] : pv[2]) : ((b & 1) ? pv[1] : pv[0]);
    const unsigned gonow = (unsigned)__builtin_amdgcn_readlane((int)pv[0], 1);
    const unsigned so = lane != 0 || seen ? OOB : leader ? (allseen ? WS_GO : OOB) : member ? WS_ECHO + b * 4 : OOB;
    st1c(leader ? go_new : req, rws, so);
    seen = seen || (leader ? allseen : gonow != go_old);
    st4c(qsend, rws, seen ? OOB : send_off);
    pv = ld4<16>(rws, seen ? OOB : poll_off, 0);
    SB();
  };

  Att st;
  st.m = -3.0e38f; st.l = 0.f;
#pragma unroll
  for (int j = 0; j < 4; ++j) { st.a0[j] = (f32x2){0.f, 0.f}; st.a1[j] = (f32x2){0.f, 0.f}; }
  v4u ring[8];
  f32x4 yf;
  {
    const unsigned ub2 = ubase(2);
    float p = phase_k(pre[0], q0, q1, st, lane, [&](int i) { ring[i] = ldk(ub2, i); });
    phase_v(pre[1], p, st, [&](int) {});
    p = phase_k(pre[2], q0, q1, st, lane, [&](int) {});
    phase_v(pre[3], p, st, [&](int) {});
    service();
    f32x4 gu = {0.f, 0.f, 0.f, 0.f};
#pragma unroll
    for (int k = 0; k < 8; ++k) gu += *reinterpret_cast<const f32x4*>(&s_fp[k][rg * 4]);
#pragma unroll
    for (int k = 0; k < 4; ++k) gu[k] = rbf(gu[k]);
    const float h0 = rbf(rbf(gu[0] / (1.0f + __expf(-gu[0]))) * gu[1]);
    const float h1 = rbf(rbf(gu[2] / (1.0f + __expf(-gu[2]))) * gu[3]);
    const f32x2 ya = unp(wdv[0][0]) * h0 + unp(wdv[1][0]) * h1, yb = unp(wdv[0][1]) * h0 + unp(wdv[1][1]) * h1;
    yf = (f32x4){ya.x, ya.y, yb.x, yb.y};
    asm volatile("" : "+v"(yf));
    SB();
  }
#pragma unroll 1
  for (int u = 2; u < 8; ++u) {
    const unsigned ub = ubase(u), un = u < 7 ? ubase(u + 1) : OOB;
    const float p = phase_k(ring, q0, q1, st, lane, [&](int i) { ring[i] = ldv(ub, i); });
    service();
    phase_v(ring, p, st, [&](int i) { ring[i] = ldk(un, i); });
    service();
  }
  {
    float l = st.l;
    l += dpp<ROR2>(l); l += dpp<ROR4>(l); l += dpp<ROR8>(l); l = x32_add(l);
    float o[8];
#pragma unroll
    for (int j = 0; j < 4; ++j) { o[2 * j] = tr32(st.a0[j].x, st.a1[j].x); o[2 * j + 1] = tr32(st.a0[j].y, st.a1[j].y); }
    *reinterpret_cast<f32x4*>(&s_part[w][lane * 8]) = (f32x4){o[0], o[1], o[2], o[3]};
    *reinterpret_cast<f32x4*>(&s_part[w][lane * 8 + 4]) = (f32x4){o[4], o[5], o[6], o[7]};
    if (lane < 2) *reinterpret_cast<f32x2*>(&s_ml[w][lane * 2]) = (f32x2){st.m, l};
  }
  if (member || leader) {
    for (int it = 0; !seen && it < SPIN_CAP; ++it) { service(); __builtin_amdgcn_s_sleep(2); }
  }
  LDS_FENCE(); bar(); LDS_FENCE();
  STAMP(2);
  if (w == 2 || w == 3 || w == 4) {
    const int t = tid - 128, hd = (t >> 6) & 1;
    f32x4 ml[8];
#pragma unroll
    for (int k = 0; k < 8; ++k) ml[k] = *reinterpret_cast<const f32x4*>(&s_ml[k][0]);
    if (w < 4) {
      float M = -3.0e38f;
#pragma unroll
      for (int k = 0; k < 8; ++k) M = fmaxf(M, hd ? ml[k][2] : ml[k][0]);
      f32x4 o = {0.f, 0.f, 0.f, 0.f};
#pragma unroll
      for (int k = 0; k < 8; ++k) o += *reinterpret_cast<const f32x4*>(&s_part[k][t * 4]) * __builtin_amdgcn_exp2f(((hd ? ml[k][2] : ml[k][0]) - M) * EXP_C);
      st4c((v4u){canon(o[0]), canon(o[1]), canon(o[2]), canon(o[3])}, rws, area_of(t >> 2) + A_O1 + s * 64 + (t & 3) * 16);
    } else {
      float M0 = -3.0e38f, M1 = -3.0e38f, L0 = 0.f, L1 = 0.f;
#pragma unroll
      for (int k = 0; k < 8; ++k) { M0 = fmaxf(M0, ml[k][0]); M1 = fmaxf(M1, ml[k][2]); }
#pragma unroll
      for (int k = 0; k < 8; ++k) { L0 += ml[k][1] * __builtin_amdgcn_exp2f((ml[k][0] - M0) * EXP_C); L1 += ml[k][3] * __builtin_amdgcn_exp2f((ml[k][2] - M1) * EXP_C); }
      st2c((v2u){canon((lane & 16) ? M1 : M0), canon((lane & 16) ? L1 : L0)}, rws, (area_of(lane & 31) + A_ML + s * 8) | (lane < 32 ? 0u : OOB));
    }
  }
  if (w == 0) {
    const unsigned oa = area + A_O1 + l16;
    const unsigned ma = area + A_ML + cq * 8;
    v4u da, db; v2u mA, mB;
    bool okA = false, okB = false;
    for (int it = 0; it < SPIN_CAP; ++it) {
      if (!okA) { da = ld4<16>(rws, oa, 0); mA = ld2<16>(rws, ma, 0); }
      if (!okB) { db = ld4<16>(rws, oa + 1024, 0); mB = ld2<16>(rws, ma + 128, 0); }
      okA = full4(da) && mA[0] != SENT && mA[1] != SENT;
      okB = full4(db) && mB[0] != SENT && mB[1] != SENT;
      if (__builtin_amdgcn_ballot_w64(!(okA && okB)) == 0) break;
      __builtin_amdgcn_s_sleep(2);
    }
    const float M = wave_max(fmaxf(asf(mA[0]), asf(mB[0])));
    const float ea = __builtin_amdgcn_exp2f((asf(mA[0]) - M) * EXP_C), eb = __builtin_amdgcn_exp2f((asf(mB[0]) - M) * EXP_C);
    float v[5];
#pragma unroll
    for (int j = 0; j < 4; ++j) v[j] = asf(da[j]) * ea + asf(db[j]) * eb;
    v[4] = asf(mA[1]) * ea + asf(mB[1]) * eb;
#pragma unroll
    for (int j = 0; j < 5; ++j) { v[j] += dpp<ROR4>(v[j]); v[j] += dpp<ROR8>(v[j]); v[j] = x16_add(v[j]); v[j] = x32_add(v[j]); }
    if (lane < 4) *reinterpret_cast<f32x4*>(&s_o[lane * 4]) = (f32x4){rbf(v[0] / v[4]), rbf(v[1] / v[4]), rbf(v[2] / v[4]), rbf(v[3] / v[4])};
  }
  LDS_FENCE(); bar(); LDS_FENCE();
  STAMP(3);
  {
    const f32x4 ov = *reinterpret_cast<const f32x4*>(&s_o[rg * 4]);
    f32x2 ya = {yf[0], yf[1]}, yb = {yf[2], yf[3]};
#pragma unroll
    for (int i = 0; i < 4; ++i) { ya += unp(wov[i][0]) * ov[i]; yb += unp(wov[i][1]) * ov[i]; }
    float y[4] = {ya.x, ya.y, yb.x, yb.y};
#pragma unroll
    for (int j = 0; j < 4; ++j) { y[j] += dpp<XOR1>(y[j]); y[j] += dpp<XOR2>(y[j]); }
    st4c((v4u){canon(y[0]), canon(y[1]), canon(y[2]), canon(y[3])}, rws, ((unsigned)(w * 14 + cq) * AREA + A_Y2 + b * 16) | ((rg == 0 && cq < 14) ? 0u : OOB));
  }
  if (w == 0 && b < 112) {
    const v2u xo = ld2<0>(rx, b * 8, 0);
    const unsigned ya = area + A_Y2 + l16;
    v4u d[4];
    bool ok[4] = {false, false, false, false};
    for (int it = 0; it < SPIN_CAP; ++it) {
#pragma unroll
      for (int k = 0; k < 4; ++k) if (!ok[k]) d[k] = ld4<16>(rws, ya + k * 1024, 0);
#pragma unroll
      for (int k = 0; k < 4; ++k) ok[k] = full4(d[k]);
      if (__builtin_amdgcn_ballot_w64(!(ok[0] && ok[1] && ok[2] && ok[3])) == 0) break;
      __builtin_amdgcn_s_sleep(1);
    }
    float y[4];
#pragma unroll
    for (int j = 0; j < 4; ++j) y[j] = wave_sum((asf(d[0][j]) + asf(d[1][j])) + (asf(d[2][j]) + asf(d[3][j])));
    const f32x2 x01 = unp(xo[0]), x23 = unp(xo[1]);
    if (lane == 0) {
      v2u r;
      r[0] = f2bf(x01.x + y[0]) | (f2bf(x01.y + y[1]) << 16);
      r[1] = f2bf(x23.x + y[2]) | (f2bf(x23.y + y[3]) << 16);
      *reinterpret_cast<v2u*>(out + b * 4) = r;
    }
  }
  STAMP(4);
}

extern "C" void kernel_launch(void* const* d_in, const int* in_sizes, int n_in,
                              void* d_out, int out_size, void* d_ws, size_t ws_size,
                              hipStream_t stream) {
  fused_kernel<<<NWG, NT, 0, stream>>>((const u16*)d_in[0], (const u16*)d_in[1], (const u16*)d_in[2], (const u16*)d_in[3],
                                       (const u16*)d_in[4], (const u16*)d_in[6], (const u16*)d_in[7], (const u16*)d_in[8],
                                       (u16*)d_out, (char*)d_ws);
}
```

```cpp
#include <hip/hip_runtime.h>
#include <hip/hip_bf16.h>
#include <stdint.h>

#define QPOS 24
#define PACE 8
#define QTRIES 8
#define DIAG 0

typedef unsigned v4u __attribute__((ext_vector_type(4)));
typedef unsigned v2u __attribute__((ext_vector_type(2)));
typedef float f32x4 __attribute__((ext_vector_type(4)));
typedef float f32x2 __attribute__((ext_vector_type(2)));
typedef __bf16 bf16x2 __attribute__((ext_vector_type(2)));
typedef unsigned short u16;
typedef __amdgpu_buffer_rsrc_t rsrc_t;

constexpr int D = 448, SK = 32768, DH = 256, F = 1792, NQ = 4096;
constexpr int NWG = 256, NT = 512;
constexpr unsigned OOB = 0x80000000u;
constexpr unsigned SENT = 0xFFFFFFFFu;
constexpr unsigned AREA = 8192;
constexpr unsigned A_Q = 0;
constexpr unsigned A_O1 = 1024;
constexpr unsigned A_ML = 3072;
constexpr unsigned A_F = 3328;
constexpr unsigned A_Y2 = 4096;
constexpr unsigned WS_REQ = NWG * AREA;
constexpr unsigned WS_ECHO = WS_REQ + 1024;
constexpr unsigned WS_GO = WS_ECHO + 1024;
constexpr unsigned WS_END = WS_GO + 256;
constexpr int RED0 = NWG - 112;
constexpr int SPIN_CAP = 1 << 18;
constexpr int WROW = 912;

__device__ __forceinline__ float asf(unsigned u) { return __builtin_bit_cast(float, u); }
__device__ __forceinline__ unsigned asu(float f) { return __builtin_bit_cast(unsigned, f); }
__device__ __forceinline__ float rbf(float f) { __bf16 b = (__bf16)f; return (float)b; }
__device__ __forceinline__ unsigned f2bf(float f) { __bf16 b = (__bf16)f; return (unsigned)__builtin_bit_cast(u16, b); }
__device__ __forceinline__ float dot2(unsigned k, unsigned q, float acc) {
  return __builtin_amdgcn_fdot2_f32_bf16(__builtin_bit_cast(bf16x2, k), __builtin_bit_cast(bf16x2, q), acc, false);
}
__device__ __forceinline__ f32x2 unp(unsigned u) { f32x2 r; r.x = asf(u << 16); r.y = asf(u & 0xffff0000u); return r; }
template <int CTRL> __device__ __forceinline__ float dpp(float x) {
  return asf((unsigned)__builtin_amdgcn_update_dpp(0, (int)asu(x), CTRL, 0xf, 0xf, true));
}
constexpr int XOR1 = 0xB1, XOR2 = 0x4E, XOR7 = 0x141, ROR8 = 0x128, ROR4 = 0x124, ROR2 = 0x122, BCAST = 0x150;
constexpr int QP_EVEN = 0xA0, QP_ODD = 0xF5;
__device__ __forceinline__ float x16_add(float x) { auto s = __builtin_amdgcn_permlane16_swap(asu(x), asu(x), false, false); return asf(s[0]) + asf(s[1]); }
__device__ __forceinline__ float x32_add(float x) { auto s = __builtin_amdgcn_permlane32_swap(asu(x), asu(x), false, false); return asf(s[0]) + asf(s[1]); }
__device__ __forceinline__ float x16_max(float x) { auto s = __builtin_amdgcn_permlane16_swap(asu(x), asu(x), false, false); return fmaxf(asf(s[0]), asf(s[1])); }
__device__ __forceinline__ float x32_max(float x) { auto s = __builtin_amdgcn_permlane32_swap(asu(x), asu(x), false, false); return fmaxf(asf(s[0]), asf(s[1])); }
__device__ __forceinline__ float tr32(float a, float b) { auto s = __builtin_amdgcn_permlane32_swap(asu(a), asu(b), false, false); return asf(s[0]) + asf(s[1]); }
__device__ __forceinline__ float tr16(float a, float b) { auto s = __builtin_amdgcn_permlane16_swap(asu(a), asu(b), false, false); return asf(s[0]) + asf(s[1]); }
__device__ __forceinline__ float wave_sum(float v) {
  v += dpp<XOR1>(v); v += dpp<XOR2>(v); v += dpp<XOR7>(v); v += dpp<ROR8>(v);
  v = x16_add(v); return x32_add(v);
}
__device__ __forceinline__ float wave_max(float v) {
  v = fmaxf(v, dpp<XOR1>(v)); v = fmaxf(v, dpp<XOR2>(v)); v = fmaxf(v, dpp<XOR7>(v)); v = fmaxf(v, dpp<ROR8>(v));
  v = x16_max(v); return x32_max(v);
}
__device__ __forceinline__ rsrc_t mkrs(const void* p, unsigned bytes) { return __builtin_amdgcn_make_buffer_rsrc((void*)p, 0, (int)bytes, 0x00020000); }
template <int AUX> __device__ __forceinline__ v4u ld4(rsrc_t r, unsigned vo, unsigned so) { return __builtin_amdgcn_raw_buffer_load_b128(r, (int)vo, (int)so, AUX); }
template <int AUX> __device__ __forceinline__ v2u ld2(rsrc_t r, unsigned vo, unsigned so) { return __builtin_amdgcn_raw_buffer_load_b64(r, (int)vo, (int)so, AUX); }
__device__ __forceinline__ void st4c(v4u v, rsrc_t r, unsigned vo) { __builtin_amdgcn_raw_buffer_store_b128(v, r, (int)vo, 0, 16); }
__device__ __forceinline__ void st2c(v2u v, rsrc_t r, unsigned vo) { __builtin_amdgcn_raw_buffer_store_b64(v, r, (int)vo, 0, 16); }
__device__ __forceinline__ void st1c(unsigned v, rsrc_t r, unsigned vo) { __builtin_amdgcn_raw_buffer_store_b32(v, r, (int)vo, 0, 16); }
__device__ __forceinline__ unsigned canon(float f) { unsigned u = asu(f); return u < 0xFFFFFFFEu ? u : 0xFFFFFFFEu; }
__device__ __forceinline__ bool full4(v4u v) { return v[0] != SENT && v[1] != SENT && v[2] != SENT && v[3] != SENT; }
#define SB() __builtin_amdgcn_sched_barrier(0)
__device__ __forceinline__ void bar() { __builtin_amdgcn_s_waitcnt(0xC07F); __builtin_amdgcn_s_barrier(); }
#define WAITV(n) __builtin_amdgcn_s_waitcnt(((n) & 15) | (((n) >> 4) << 14) | 0x0F70)
template <int V> struct IC { static constexpr int value = V; constexpr operator int() const { return V; } };
template <int A, int B, class Fn> __device__ __forceinline__ void static_for(Fn&& f) { if constexpr (A < B) { f(IC<A>{}); static_for<A + 1, B>(f); } }
#define LDS_FENCE() asm volatile("" ::: "memory")

constexpr float EXP_C = 0.0625f * 1.4426950408889634f;

struct Att {
  float m, l;
  f32x2 a0[4], a1[4];
};

template <class ISSUE>
__device__ __forceinline__ float phase_k(v4u (&kb)[8], const v4u& q0, const v4u& q1, Att& st, int lane, ISSUE issue) {
  float x[16];
#pragma unroll
  for (int i = 0; i < 8; ++i) {
    float a = 0.f, b = 0.f;
#pragma unroll
    for (int j = 0; j < 4; ++j) { a = dot2(kb[i][j], q0[j], a); b = dot2(kb[i][j], q1[j], b); }
    x[2 * i] = a; x[2 * i + 1] = b;
    asm volatile("" : "+v"(x[2 * i]), "+v"(x[2 * i + 1]));
    issue(i);
    SB();
  }
#define TR_STEP(HALF, CTRL, BIT) _Pragma("unroll") for (int v = 0; v < (HALF); ++v) { \
    const float lo_ = x[v], hi_ = x[v + (HALF)]; const bool s_ = lane & (BIT); \
    x[v] = (s_ ? hi_ : lo_) + dpp<CTRL>(s_ ? lo_ : hi_); }
  TR_STEP(8, ROR8, 8) TR_STEP(4, XOR7, 4) TR_STEP(2, XOR2, 2) TR_STEP(1, XOR1, 1)
#undef TR_STEP
  const float s = rbf(x16_add(x[0]));
  float mx = fmaxf(s, dpp<ROR2>(s)); mx = fmaxf(mx, dpp<ROR4>(mx)); mx = fmaxf(mx, dpp<ROR8>(mx));
  mx = x32_max(mx);
  const float mn = fmaxf(st.m, mx);
  const float al = __builtin_amdgcn_exp2f((st.m - mn) * EXP_C);
  const float p = __builtin_amdgcn_exp2f((s - mn) * EXP_C);
  st.m = mn; st.l = st.l * al + p;
  const float al0 = dpp<QP_EVEN>(al), al1 = dpp<QP_ODD>(al);
#pragma unroll
  for (int j = 0; j < 4; ++j) { st.a0[j] *= al0; st.a1[j] *= al1; }
  return p;
}
template <int I> struct Bc { static __device__ __forceinline__ float get(float p) { return dpp<BCAST + I>(p); } };
template <int I, class ISSUE>
__device__ __forceinline__ void pv_step(v4u (&vb)[8], float p, Att& st, ISSUE& issue) {
  const float p0 = dpp<BCAST + 2 * I>(p), p1 = dpp<BCAST + 2 * I + 1>(p);
#pragma unroll
  for (int j = 0; j < 4; ++j) { const f32x2 v = unp(vb[I][j]); st.a0[j] += v * p0; st.a1[j] += v * p1; }
  asm volatile("" : "+v"(st.a0[0]), "+v"(st.a0[1]), "+v"(st.a0[2]), "+v"(st.a0[3]), "+v"(st.a1[0]), "+v"(st.a1[1]), "+v"(st.a1[2]), "+v"(st.a1[3]));
  issue(I);
  SB();
}
template <class ISSUE>
__device__ __forceinline__ void phase_v(v4u (&vb)[8], float p, Att& st, ISSUE issue) {
  pv_step<0>(vb, p, st, issue); pv_step<1>(vb, p, st, issue); pv_step<2>(vb, p, st, issue); pv_step<3>(vb, p, st, issue);
  pv_step<4>(vb, p, st, issue); pv_step<5>(vb, p, st, issue); pv_step<6>(vb, p, st, issue); pv_step<7>(vb, p, st, issue);
}

#define STAMP(k) do {} while (0)
#define STAMP_OUT() do {} while (0)

__device__ __forceinline__ f32x2 ln_stats(const v4u& xa, int lane) {
  f32x2 xv[4];
  float sm = 0.f;
#pragma unroll
  for (int j = 0; j < 4; ++j) { xv[j] = unp(xa[j]); sm += xv[j].x + xv[j].y; }
  const float mean = wave_sum(sm) * (1.0f / D);
  float sq = 0.f;
#pragma unroll
  for (int j = 0; j < 4; ++j) { const f32x2 d = xv[j] - mean; sq += d.x * d.x + d.y * d.y; }
  sq = lane < 56 ? sq : 0.f;
  const float var = rbf(wave_sum(sq) * (1.0f / D));
  const float sd = rbf(sqrtf(rbf(var + 1e-5f)));
  return (f32x2){rbf(mean), 1.0f / sd};
}
__device__ __forceinline__ float ln_apply(float x, f32x2 st) { return rbf(rbf(x - st.x) * st.y); }
__device__ __forceinline__ unsigned pack2(float a, float b) { const unsigned u = f2bf(a) | (f2bf(b) << 16); return u < 0xFFFFFFFEu ? u : 0xFFFFFFFEu; }

__global__ void __launch_bounds__(NT, 2)
fused_kernel(const u16* __restrict__ xg, const u16* __restrict__ kc, const u16* __restrict__ vc, const u16* __restrict__ wq,
             const u16* __restrict__ wo, const u16* __restrict__ wg, const u16* __restrict__ wu, const u16* __restrict__ wd,
             u16* __restrict__ out, char* ws) {
  __shared__ __attribute__((aligned(16))) float s_part[8][512];
  __shared__ __attribute__((aligned(16))) char s_w[32 * WROW];
  __shared__ __attribute__((aligned(16))) float s_mv[8][64];
  __shared__ __attribute__((aligned(16))) unsigned s_q[256];
  __shared__ __attribute__((aligned(16))) u16 s_qs[64];
  __shared__ __attribute__((aligned(16))) float s_ml[8][4];
  __shared__ __attribute__((aligned(16))) float s_o[32];
  __shared__ int s_flag;
  const int tid = threadIdx.x, lane = tid & 63, w = __builtin_amdgcn_readfirstlane(tid >> 6);
  const int b = blockIdx.x, h = b & 7, s = b >> 3;
  asm volatile("" :: "s"(xg), "s"(kc), "s"(vc), "s"(wq), "s"(wo), "s"(wg), "s"(wu), "s"(wd), "s"(out), "s"(ws));
  STAMP(0);
  const rsrc_t rx = mkrs(xg, D * 2);
  const rsrc_t rk = mkrs(kc + (size_t)h * SK * DH, SK * DH * 2);
  const rsrc_t rv = mkrs(vc + (size_t)h * SK * DH, SK * DH * 2);
  const rsrc_t rws = mkrs(ws, WS_END);
  const unsigned l16 = lane * 16;
  auto ubase = [&](int u) -> unsigned { return (unsigned)(s * 1024 + w * 128 + u * 16) * 512u; };
  auto ldk = [&](unsigned base, int i) { return ld4<2>(rk, l16 + (i & 3) * 1024, base + (i >> 2) * 4096); };
  auto ldv = [&](unsigned base, int i) { return ld4<2>(rv, l16 + (i & 3) * 1024, base + (i >> 2) * 4096); };
  const unsigned area = (unsigned)b * AREA;
  auto area_of = [&](int ss) -> unsigned { return (unsigned)(ss * 8 + h) * AREA; };
  const unsigned lmask = lane < 56 ? 0u : OOB;

  const bool isq = s < 8, isf = s >= 8 && s < 22;
  const int f = h * 14 + s - 8;
  const bool tile = isq || (isf && w < 4);
  const bool leader = (b == NWG - 1 && w == 1), member = (w == 0);

  const v4u sent = {SENT, SENT, SENT, SENT};
  unsigned poll_off = lane == 1 ? WS_GO : OOB;
  poll_off = lane == 0 ? WS_REQ + (b >> 2) * 16 : poll_off;
  poll_off = leader ? WS_ECHO + l16 : poll_off;
  v4u pv = {0u, 0u, 0u, 0u}, e1 = {0u, 0u, 0u, 0u};
  unsigned go_ref = 0;
  if (member) {
    st4c(sent, rws, area + A_Q + l16);
    __hip_atomic_store(&s_flag, 0, __ATOMIC_RELAXED, __HIP_MEMORY_SCOPE_WORKGROUP);
  }
  SB();
  if (tile) {
    v4u tw[8];
    const int oct = isq ? 7 * w : (f & 3) * 14 + (w >> 1) * 7;
    const int stride = isq ? NQ * 2 : F * 2;
    const rsrc_t rt = mkrs(isq ? wq : (w & 1) ? wu : wg, D * stride);
    const unsigned to = (unsigned)((oct + (lane >> 3)) * 8 * stride + (isq ? h * 8 + s : f >> 2) * 128 + (lane & 7) * 16) | lmask;
    const v4u xa = ld4<0>(rx, l16, 0);
    const v4u xb = ld4<0>(rx, (unsigned)(oct + (lane >> 3)) * 16 | lmask, 0);
    SB();
#pragma unroll
    for (int i = 0; i < 8; ++i) { tw[i] = ld4<2>(rt, to, i * stride); SB(); }
    LDS_FENCE(); bar(); LDS_FENCE();
    SB();
    const f32x2 ls = ln_stats(xa, lane);
    f32x2 acc[4];
#pragma unroll
    for (int i = 0; i < 8; ++i) {
      const float xn = ln_apply(asf((i & 1) ? (xb[i >> 1] & 0xffff0000u) : (xb[i >> 1] << 16)), ls);
#pragma unroll
      for (int j = 0; j < 4; ++j) acc[j] = i ? acc[j] + unp(tw[i][j]) * xn : unp(tw[i][j]) * xn;
    }
    float y0 = tr32(acc[0].x, acc[2].x), y1 = tr32(acc[0].y, acc[2].y), y2 = tr32(acc[1].x, acc[3].x), y3 = tr32(acc[1].y, acc[3].y);
    y0 = tr16(y0, y2); y1 = tr16(y1, y3);
    const bool s_ = lane & 8;
    y0 = (s_ ? y1 : y0) + dpp<ROR8>(s_ ? y0 : y1);
    s_mv[w][(lane & 7) * 8 + (lane >> 3)] = y0;
    STAMP(6);
    if (isq) {
      LDS_FENCE(); bar(); LDS_FENCE();
      if (w < 4) {
        float a = 0.f;
#pragma unroll
        for (int k = 0; k < 8; ++k) a += s_mv[k][lane];
        s_qs[lane] = (u16)f2bf(a);
        LDS_FENCE();
        v4u qs = *reinterpret_cast<const v4u*>(&s_qs[(lane & 7) * 8]);
#pragma unroll
        for (int j = 0; j < 4; ++j) qs[j] = qs[j] < 0xFFFFFFFEu ? qs[j] : 0xFFFFFFFEu;
        st4c(qs, rws, area_of(w * 8 + (lane >> 3)) + A_Q + s * 128 + (lane & 7) * 16);
      }
    }
  } else if (leader) {
    e1 = ld4<16>(rws, poll_off, 0);
    go_ref = __builtin_amdgcn_raw_buffer_load_b32(rws, (int)WS_GO, 0, 16);
    SB();
    LDS_FENCE(); bar(); LDS_FENCE();
    SB();
    e1 += 1u;
    go_ref += 1u;
    st4c(e1, rws, WS_REQ + l16);
  } else {
    LDS_FENCE(); bar(); LDS_FENCE();
  }
  SB();
  if (member) {
#pragma unroll
    for (int i = 1; i < 4; ++i) st4c(sent, rws, (area + i * 1024 + l16) | ((i < 3 || lane < 48) ? 0u : OOB));
    if (b >= RED0) {
#pragma unroll
      for (int i = 4; i < 8; ++i) st4c(sent, rws, area + i * 1024 + l16);
    }
  }
  STAMP(5);
  SB();
  v4u pre[4][8];
  v4u wov[2], wdv[2], qv;
  const unsigned ub0 = ubase(0), ub1 = ubase(1);
  static_for<0, 32>([&](auto N) {
    constexpr int n = N;
    if constexpr (n == 4) {
      const rsrc_t ro = mkrs(wo, NQ * D * 2), rd = mkrs(wd, F * D * 2);
      const unsigned oo = (unsigned)((h * 512 + s * 16 + w * 2) * (D * 2) + l16) | lmask;
      const unsigned od = (unsigned)((f * 16 + w * 2) * (D * 2) + l16) | (isf ? lmask : OOB);
      wov[0] = ld4<2>(ro, oo, 0); wov[1] = ld4<2>(ro, oo + D * 2, 0);
      wdv[0] = ld4<2>(rd, od, 0); wdv[1] = ld4<2>(rd, od + D * 2, 0);
      SB();
    }
    if constexpr (n == QPOS) {
      STAMP(9);
      qv = ld4<16>(rws, member ? area + A_Q + l16 : OOB, 0);
      pv = ld4<16>(rws, (member || leader) ? poll_off : OOB, 0);
      SB();
    }
    if constexpr (n >= PACE) WAITV(PACE - 1 + ((n >= QPOS && n < QPOS + PACE) ? 2 : 0) + ((n < 4 + PACE) ? 4 : 0));
    pre[n >> 3][n & 7] = (n & 8) ? ldv((n & 16) ? ub1 : ub0, n & 7) : ldk((n & 16) ? ub1 : ub0, n & 7);
    SB();
    if constexpr (n - PACE == 7 || n - PACE == 15 || n - PACE == 23) STAMP(16 + (n - PACE) / 8);
  });
  STAMP(10);
  if (member) {
    int tries = 0;
    bool ok;
    for (;; ++tries) {
      ok = __builtin_amdgcn_ballot_w64(!full4(qv)) == 0;
      if (ok || tries >= QTRIES) break;
      __builtin_amdgcn_s_sleep(4);
      qv = ld4<16>(rws, area + A_Q + l16, 0);
      WAITV(0);
    }
    if (!ok) {
      const v4u xc = ld4<0>(rx, l16, 0);
      const f32x2 ls = ln_stats(xc, lane);
      const rsrc_t rwq = mkrs(wq, D * NQ * 2);
      f32x2 acc[4] = {{0.f, 0.f}, {0.f, 0.f}, {0.f, 0.f}, {0.f, 0.f}};
#pragma unroll 1
      for (int r = 0; r < D / 2; ++r) {
        unsigned xp = xc[0];
        xp = (r & 3) == 1 ? xc[1] : xp; xp = (r & 3) == 2 ? xc[2] : xp; xp = (r & 3) == 3 ? xc[3] : xp;
        const f32x2 xr = unp((unsigned)__builtin_amdgcn_readlane((int)xp, r >> 2));
        const v4u wa = ld4<0>(rwq, h * 1024 + l16, r * 2 * NQ * 2), wb = ld4<0>(rwq, h * 1024 + l16, (r * 2 + 1) * NQ * 2);
        const float x0 = ln_apply(xr.x, ls), x1 = ln_apply(xr.y, ls);
#pragma unroll
        for (int j = 0; j < 4; ++j) acc[j] += unp(wa[j]) * x0 + unp(wb[j]) * x1;
      }
#pragma unroll
      for (int j = 0; j < 4; ++j) qv[j] = pack2(acc[j].x, acc[j].y);
      WAITV(0);
    }
    *reinterpret_cast<v4u*>(&s_q[lane * 4]) = qv;
    LDS_FENCE();
    __builtin_amdgcn_s_waitcnt(0xC07F);
    __hip_atomic_store(&s_flag, 1, __ATOMIC_RELAXED, __HIP_MEMORY_SCOPE_WORKGROUP);
  } else {
    while (__builtin_amdgcn_readfirstlane(__hip_atomic_load(&s_flag, __ATOMIC_RELAXED, __HIP_MEMORY_SCOPE_WORKGROUP)) == 0) __builtin_amdgcn_s_sleep(1);
  }
  LDS_FENCE();
  const v4u q0 = *reinterpret_cast<const v4u*>(&s_q[(lane & 31) * 4]), q1 = *reinterpret_cast<const v4u*>(&s_q[128 + (lane & 31) * 4]);
  STAMP(1);
  SB();

  bool svc = member || leader;
  unsigned seenm = 0, last_echo = 0;
  bool echoed = false;
  auto service = [&]() {
    if (leader) {
      seenm |= (pv[0] == e1[0] ? 1u : 0u) | (pv[1] == e1[1] ? 2u : 0u) | (pv[2] == e1[2] ? 4u : 0u) | (pv[3] == e1[3] ? 8u : 0u);
      svc = __builtin_amdgcn_ballot_w64(seenm != 15u) != 0;
      st1c(go_ref, rws, (lane == 0 && !svc) ? WS_GO : OOB);
    } else {
      const unsigned req = (unsigned)__builtin_amdgcn_readfirstlane((int)((b & 2) ? ((b & 1) ? pv[3] : pv[2]) : ((b & 1) ? pv[1] : pv[0])));
      const unsigned go = (unsigned)__builtin_amdgcn_readlane((int)pv[0], 1);
      go_ref = echoed ? go_ref : go;
      svc = go == (unsigned)__builtin_amdgcn_readfirstlane((int)go_ref);
      st1c(req, rws, (lane == 0 && svc && !(echoed && req == last_echo)) ? WS_ECHO + b * 4 : OOB);
      echoed = true; last_echo = req;
    }
    pv = ld4<16>(rws, svc ? poll_off : OOB, 0);
    SB();
  };

  Att st;
  st.m = -3.0e38f; st.l = 0.f;
#pragma unroll
  for (int j = 0; j < 4; ++j) { st.a0[j] = (f32x2){0.f, 0.f}; st.a1[j] = (f32x2){0.f, 0.f}; }
  v4u ring[8];
  {
    const unsigned ub2 = ubase(2);
    if (svc) service();
    float p = phase_k(pre[0], q0, q1, st, lane, [&](int i) { ring[i] = ldk(ub2, i); });
    phase_v(pre[1], p, st, [&](int) {});
    p = phase_k(pre[2], q0, q1, st, lane, [&](int) {});
    phase_v(pre[3], p, st, [&](int) {});
    STAMP(19);
    if (lane < 56) {
      *reinterpret_cast<v4u*>(&s_w[(w * 2) * WROW + l16]) = wov[0];
      *reinterpret_cast<v4u*>(&s_w[(w * 2 + 1) * WROW + l16]) = wov[1];
      *reinterpret_cast<v4u*>(&s_w[(16 + w * 2) * WROW + l16]) = wdv[0];
      *reinterpret_cast<v4u*>(&s_w[(17 + w * 2) * WROW + l16]) = wdv[1];
    }
    SB();
  }
#pragma unroll 1
  for (int u = 2; u < 8; ++u) {
    const unsigned ub = ubase(u), un = u < 7 ? ubase(u + 1) : OOB;
    const float p = phase_k(ring, q0, q1, st, lane, [&](int i) { ring[i] = ldv(ub, i); });
    STAMP(16 + 2 * u);
    if (svc) service();
    phase_v(ring, p, st, [&](int i) { ring[i] = ldk(un, i); });
    STAMP(17 + 2 * u);
    if (svc) service();
  }
  STAMP(12);
  {
    float l = st.l;
    l += dpp<ROR2>(l); l += dpp<ROR4>(l); l += dpp<ROR8>(l); l = x32_add(l);
    float o[8];
#pragma unroll
    for (int j = 0; j < 4; ++j) { o[2 * j] = tr32(st.a0[j].x, st.a1[j].x); o[2 * j + 1] = tr32(st.a0[j].y, st.a1[j].y); }
    *reinterpret_cast<f32x4*>(&s_part[w][lane * 8]) = (f32x4){o[0], o[1], o[2], o[3]};
    *reinterpret_cast<f32x4*>(&s_part[w][lane * 8 + 4]) = (f32x4){o[4], o[5], o[6], o[7]};
    if (lane < 2) *reinterpret_cast<f32x2*>(&s_ml[w][lane * 2]) = (f32x2){st.m, l};
  }
  for (int it = 0; svc && it < SPIN_CAP; ++it) { service(); __builtin_amdgcn_s_sleep(2); }
  WAITV(0);
  LDS_FENCE(); bar(); LDS_FENCE();
  STAMP(2);
  const int cq = lane >> 2, rg = lane & 3;
  if (w == 2 || w == 3 || w == 4) {
    const int t = tid - 128, hd = (t >> 6) & 1;
    f32x4 ml[8];
#pragma unroll
    for (int k = 0; k < 8; ++k) ml[k] = *reinterpret_cast<const f32x4*>(&s_ml[k][0]);
    if (w < 4) {
      float M = -3.0e38f;
#pragma unroll
      for (int k = 0; k < 8; ++k) M = fmaxf(M, hd ? ml[k][2] : ml[k][0]);
      f32x4 o = {0.f, 0.f, 0.f, 0.f};
#pragma unroll
      for (int k = 0; k < 8; ++k) o += *reinterpret_cast<const f32x4*>(&s_part[k][t * 4]) * __builtin_amdgcn_exp2f(((hd ? ml[k][2] : ml[k][0]) - M) * EXP_C);
      st4c((v4u){canon(o[0]), canon(o[1]), canon(o[2]), canon(o[3])}, rws, area_of(t >> 2) + A_O1 + s * 64 + (t & 3) * 16);
    } else {
      float M0 = -3.0e38f, M1 = -3.0e38f, L0 = 0.f, L1 = 0.f;
#pragma unroll
      for (int k = 0; k < 8; ++k) { M0 = fmaxf(M0, ml[k][0]); M1 = fmaxf(M1, ml[k][2]); }
#pragma unroll
      for (int k = 0; k < 8; ++k) { L0 += ml[k][1] * __builtin_amdgcn_exp2f((ml[k][0] - M0) * EXP_C); L1 += ml[k][3] * __builtin_amdgcn_exp2f((ml[k][2] - M1) * EXP_C); }
      st2c((v2u){canon((lane & 16) ? M1 : M0), canon((lane & 16) ? L1 : L0)}, rws, (area_of(lane & 31) + A_ML + s * 8) | (lane < 32 ? 0u : OOB));
    }
  }
  if (w == 5 && isf) {
    const int j = (lane >> 3) & 3, pc = lane & 7, m = pc >> 2, col = j * 16 + (pc & 3) * 4;
    const f32x4 v = *reinterpret_cast<const f32x4*>(&s_mv[m][col]) + *reinterpret_cast<const f32x4*>(&s_mv[m + 2][col]);
    const int fd = (f & ~3) + j, hd = fd / 14;
    st4c((v4u){canon(v[0]), canon(v[1]), canon(v[2]), canon(v[3])}, rws,
         ((unsigned)((fd - hd * 14 + 8) * 8 + hd) * AREA + A_F + (f & 3) * 128 + pc * 16) | (lane < 32 ? 0u : OOB));
  }
  v2u wt[8];
#pragma unroll
  for (int i = 0; i < 8; ++i) wt[i] = *reinterpret_cast<const v2u*>(&s_w[((i >> 2) * 16 + rg * 4 + (i & 3)) * WROW + w * 112 + (cq < 14 ? cq : 0) * 8]);
  if (w == 0) {
    const unsigned oa = area + A_O1 + l16;
    const unsigned ma = area + A_ML + cq * 8;
    v4u da, db; v2u mA, mB;
    bool okA = false, okB = false;
    for (int it = 0; it < SPIN_CAP; ++it) {
      if (!okA) { da = ld4<16>(rws, oa, 0); mA = ld2<16>(rws, ma, 0); }
      if (!okB) { db = ld4<16>(rws, oa + 1024, 0); mB = ld2<16>(rws, ma + 128, 0); }
      okA = full4(da) && mA[0] != SENT && mA[1] != SENT;
      okB = full4(db) && mB[0] != SENT && mB[1] != SENT;
      if (__builtin_amdgcn_ballot_w64(!(okA && okB)) == 0) break;
      __builtin_amdgcn_s_sleep(2);
    }
    const float M = wave_max(fmaxf(asf(mA[0]), asf(mB[0])));
    const float ea = __builtin_amdgcn_exp2f((asf(mA[0]) - M) * EXP_C), eb = __builtin_amdgcn_exp2f((asf(mB[0]) - M) * EXP_C);
    float v[5];
#pragma unroll
    for (int j = 0; j < 4; ++j) v[j] = asf(da[j]) * ea + asf(db[j]) * eb;
    v[4] = asf(mA[1]) * ea + asf(mB[1]) * eb;
#pragma unroll
    for (int j = 0; j < 5; ++j) { v[j] += dpp<ROR4>(v[j]); v[j] += dpp<ROR8>(v[j]); v[j] = x16_add(v[j]); v[j] = x32_add(v[j]); }
    if (lane < 4) *reinterpret_cast<f32x4*>(&s_o[lane * 4]) = (f32x4){rbf(v[0] / v[4]), rbf(v[1] / v[4]), rbf(v[2] / v[4]), rbf(v[3] / v[4])};
  }
  if (w == 1) {
    f32x4 hv = {0.f, 0.f, 0.f, 0.f};
    if (isf) {
      v4u d;
      for (int it = 0; it < SPIN_CAP; ++it) {
        d = ld4<16>(rws, area + A_F + (lane & 31) * 16, 0);
        if (__builtin_amdgcn_ballot_w64(!full4(d)) == 0) break;
        __builtin_amdgcn_s_sleep(2);
      }
#pragma unroll
      for (int j = 0; j < 4; ++j) {
        float g = asf(d[j]);
        g += dpp<ROR8>(g); g = rbf(x16_add(g));
        const float u = dpp<ROR4>(g);
        hv[j] = rbf(rbf(g / (1.0f + __expf(-g))) * u);
      }
    }
    if (lane < 4) *reinterpret_cast<f32x4*>(&s_o[16 + lane * 4]) = hv;
  }
  LDS_FENCE(); bar(); LDS_FENCE();
  STAMP(3);
  {
    const f32x4 ov = *reinterpret_cast<const f32x4*>(&s_o[rg * 4]), hv = *reinterpret_cast<const f32x4*>(&s_o[16 + rg * 4]);
    f32x2 ya = {0.f, 0.f}, yb = {0.f, 0.f};
#pragma unroll
    for (int i = 0; i < 4; ++i) { ya += unp(wt[i][0]) * ov[i] + unp(wt[4 + i][0]) * hv[i]; yb += unp(wt[i][1]) * ov[i] + unp(wt[4 + i][1]) * hv[i]; }
    float y[4] = {ya.x, ya.y, yb.x, yb.y};
#pragma unroll
    for (int j = 0; j < 4; ++j) { y[j] += dpp<XOR1>(y[j]); y[j] += dpp<XOR2>(y[j]); }
    st4c((v4u){canon(y[0]), canon(y[1]), canon(y[2]), canon(y[3])}, rws, ((unsigned)(RED0 + w * 14 + cq) * AREA + A_Y2 + b * 16) | ((rg == 0 && cq < 14) ? 0u : OOB));
  }
  if (w == 0 && b >= RED0) {
    const v2u xo = ld2<0>(rx, (b - RED0) * 8, 0);
    const unsigned ya = area + A_Y2 + l16;
    v4u d[4];
    bool ok[4] = {false, false, false, false};
    for (int it = 0; it < SPIN_CAP; ++it) {
#pragma unroll
      for (int k = 0; k < 4; ++k) if (!ok[k]) d[k] = ld4<16>(rws, ya + k * 1024, 0);
#pragma unroll
      for (int k = 0; k < 4; ++k) ok[k] = full4(d[k]);
      if (__builtin_amdgcn_ballot_w64(!(ok[0] && ok[1] && ok[2] && ok[3])) == 0) break;
      __builtin_amdgcn_s_sleep(1);
    }
    float y[4];
#pragma unroll
    for (int j = 0; j < 4; ++j) y[j] = wave_sum((asf(d[0][j]) + asf(d[1][j])) + (asf(d[2][j]) + asf(d[3][j])));
    const f32x2 x01 = unp(xo[0]), x23 = unp(xo[1]);
    if (lane == 0) {
      v2u r;
      r[0] = f2bf(x01.x + y[0]) | (f2bf(x01.y + y[1]) << 16);
      r[1] = f2bf(x23.x + y[2]) | (f2bf(x23.y + y[3]) << 16);
      *reinterpret_cast<v2u*>(out + (b - RED0) * 4) = r;
    }
  }
  STAMP(4);
  STAMP_OUT();
}

extern "C" void kernel_launch(void* const* d_in, const int* in_sizes, int n_in,
                              void* d_out, int out_size, void* d_ws, size_t ws_size,
                              hipStream_t stream) {
  fused_kernel<<<NWG, NT, 0, stream>>>((const u16*)d_in[0], (const u16*)d_in[1], (const u16*)d_in[2], (const u16*)d_in[3],
                                       (const u16*)d_in[4], (const u16*)d_in[6], (const u16*)d_in[7], (const u16*)d_in[8],
                                       (u16*)d_out, (char*)d_ws);
}
```

```cpp
#include <hip/hip_runtime.h>
#include <hip/hip_bf16.h>
#include <stdint.h>

#define QPOS 24
#define PACE 8
#define QTRIES 8
#define PQ 122
#define PF 130
#define PO 130
#define DLT 4
#define SWEEP 1
#define HM 4
#define DIAG 0

typedef unsigned v4u __attribute__((ext_vector_type(4)));
typedef unsigned v2u __attribute__((ext_vector_type(2)));
typedef float f32x4 __attribute__((ext_vector_type(4)));
typedef float f32x2 __attribute__((ext_vector_type(2)));
typedef __bf16 bf16x2 __attribute__((ext_vector_type(2)));
typedef unsigned short u16;
typedef __amdgpu_buffer_rsrc_t rsrc_t;

constexpr int D = 448, SK = 32768, DH = 256, F = 1792, NQ = 4096;
constexpr int NWG = 256, NT = 512;
constexpr unsigned OOB = 0x80000000u;
constexpr unsigned SENT = 0xFFFFFFFFu;
constexpr unsigned AREA = 8192;
constexpr unsigned A_Q = 0;
constexpr unsigned A_O1 = 1024;
constexpr unsigned A_ML = 3072;
constexpr unsigned A_F = 3328;
constexpr unsigned A_Y2 = 4096;
constexpr unsigned WS_REQ = NWG * AREA;
constexpr unsigned WS_ECHO = WS_REQ + 1024;
constexpr unsigned WS_GO = WS_ECHO + 1024;
constexpr unsigned WS_END = WS_GO + 256;
constexpr int RED0 = NWG - 112;
constexpr int SPIN_CAP = 1 << 18;
constexpr int WROW = 912;

__device__ __forceinline__ float asf(unsigned u) { return __builtin_bit_cast(float, u); }
__device__ __forceinline__ unsigned asu(float f) { return __builtin_bit_cast(unsigned, f); }
__device__ __forceinline__ float rbf(float f) { __bf16 b = (__bf16)f; return (float)b; }
__device__ __forceinline__ unsigned f2bf(float f) { __bf16 b = (__bf16)f; return (unsigned)__builtin_bit_cast(u16, b); }
__device__ __forceinline__ float dot2(unsigned k, unsigned q, float acc) {
  return __builtin_amdgcn_fdot2_f32_bf16(__builtin_bit_cast(bf16x2, k), __builtin_bit_cast(bf16x2, q), acc, false);
}
__device__ __forceinline__ f32x2 unp(unsigned u) { f32x2 r; r.x = asf(u << 16); r.y = asf(u & 0xffff0000u); return r; }
template <int CTRL> __device__ __forceinline__ float dpp(float x) {
  return asf((unsigned)__builtin_amdgcn_update_dpp(0, (int)asu(x), CTRL, 0xf, 0xf, true));
}
constexpr int XOR1 = 0xB1, XOR2 = 0x4E, XOR7 = 0x141, ROR8 = 0x128, ROR4 = 0x124, ROR2 = 0x122, BCAST = 0x150;
constexpr int QP_EVEN = 0xA0, QP_ODD = 0xF5;
__device__ __forceinline__ float x16_add(float x) { auto s = __builtin_amdgcn_permlane16_swap(asu(x), asu(x), false, false); return asf(s[0]) + asf(s[1]); }
__device__ __forceinline__ float x32_add(float x) { auto s = __builtin_amdgcn_permlane32_swap(asu(x), asu(x), false, false); return asf(s[0]) + asf(s[1]); }
__device__ __forceinline__ float x16_max(float x) { auto s = __builtin_amdgcn_permlane16_swap(asu(x), asu(x), false, false); return fmaxf(asf(s[0]), asf(s[1])); }
__device__ __forceinline__ float x32_max(float x) { auto s = __builtin_amdgcn_permlane32_swap(asu(x), asu(x), false, false); return fmaxf(asf(s[0]), asf(s[1])); }
__device__ __forceinline__ float tr32(float a, float b) { auto s = __builtin_amdgcn_permlane32_swap(asu(a), asu(b), false, false); return asf(s[0]) + asf(s[1]); }
__device__ __forceinline__ float tr16(float a, float b) { auto s = __builtin_amdgcn_permlane16_swap(asu(a), asu(b), false, false); return asf(s[0]) + asf(s[1]); }
__device__ __forceinline__ float wave_sum(float v) {
  v += dpp<XOR1>(v); v += dpp<XOR2>(v); v += dpp<XOR7>(v); v += dpp<ROR8>(v);
  v = x16_add(v); return x32_add(v);
}
__device__ __forceinline__ float wave_max(float v) {
  v = fmaxf(v, dpp<XOR1>(v)); v = fmaxf(v, dpp<XOR2>(v)); v = fmaxf(v, dpp<XOR7>(v)); v = fmaxf(v, dpp<ROR8>(v));
  v = x16_max(v); return x32_max(v);
}
__device__ __forceinline__ rsrc_t mkrs(const void* p, unsigned bytes) { return __builtin_amdgcn_make_buffer_rsrc((void*)p, 0, (int)bytes, 0x00020000); }
template <int AUX> __device__ __forceinline__ v4u ld4(rsrc_t r, unsigned vo, unsigned so) { return __builtin_amdgcn_raw_buffer_load_b128(r, (int)vo, (int)so, AUX); }
template <int AUX> __device__ __forceinline__ v2u ld2(rsrc_t r, unsigned vo, unsigned so) { return __builtin_amdgcn_raw_buffer_load_b64(r, (int)vo, (int)so, AUX); }
__device__ __forceinline__ void st4c(v4u v, rsrc_t r, unsigned vo) { __builtin_amdgcn_raw_buffer_store_b128(v, r, (int)vo, 0, 16); }
__device__ __forceinline__ void st2c(v2u v, rsrc_t r, unsigned vo) { __builtin_amdgcn_raw_buffer_store_b64(v, r, (int)vo, 0, 16); }
__device__ __forceinline__ void st1c(unsigned v, rsrc_t r, unsigned vo) { __builtin_amdgcn_raw_buffer_store_b32(v, r, (int)vo, 0, 16); }
__device__ __forceinline__ unsigned canon(float f) { unsigned u = asu(f); return u < 0xFFFFFFFEu ? u : 0xFFFFFFFEu; }
__device__ __forceinline__ bool full4(v4u v) { return v[0] != SENT && v[1] != SENT && v[2] != SENT && v[3] != SENT; }
#define SB() __builtin_amdgcn_sched_barrier(0)
__device__ __forceinline__ void bar() { __builtin_amdgcn_s_waitcnt(0xC07F); __builtin_amdgcn_s_barrier(); }
#define WAITV(n) __builtin_amdgcn_s_waitcnt(((n) & 15) | (((n) >> 4) << 14) | 0x0F70)
template <int V> struct IC { static constexpr int value = V; constexpr operator int() const { return V; } };
template <int A, int B, class Fn> __device__ __forceinline__ void static_for(Fn&& f) { if constexpr (A < B) { f(IC<A>{}); static_for<A + 1, B>(f); } }
#define LDS_FENCE() asm volatile("" ::: "memory")

constexpr float EXP_C = 0.0625f * 1.4426950408889634f;

struct Att {
  float m, l;
  f32x2 a0[4], a1[4];
};

template <class ISSUE>
__device__ __forceinline__ float phase_k(v4u (&kb)[8], const v4u& q0, const v4u& q1, Att& st, int lane, ISSUE issue) {
  float x[16];
#pragma unroll
  for (int i = 0; i < 8; ++i) {
    float a = 0.f, b = 0.f;
#pragma unroll
    for (int j = 0; j < 4; ++j) { a = dot2(kb[i][j], q0[j], a); b = dot2(kb[i][j], q1[j], b); }
    x[2 * i] = a; x[2 * i + 1] = b;
    asm volatile("" : "+v"(x[2 * i]), "+v"(x[2 * i + 1]));
    issue(i);
    SB();
  }
#define TR_STEP(HALF, CTRL, BIT) _Pragma("unroll") for (int v = 0; v < (HALF); ++v) { \
    const float lo_ = x[v], hi_ = x[v + (HALF)]; const bool s_ = lane & (BIT); \
    x[v] = (s_ ? hi_ : lo_) + dpp<CTRL>(s_ ? lo_ : hi_); }
  TR_STEP(8, ROR8, 8) TR_STEP(4, XOR7, 4) TR_STEP(2, XOR2, 2) TR_STEP(1, XOR1, 1)
#undef TR_STEP
  const float s = rbf(x16_add(x[0]));
  float mx = fmaxf(s, dpp<ROR2>(s)); mx = fmaxf(mx, dpp<ROR4>(mx)); mx = fmaxf(mx, dpp<ROR8>(mx));
  mx = x32_max(mx);
  const float mn = fmaxf(st.m, mx);
  const float al = __builtin_amdgcn_exp2f((st.m - mn) * EXP_C);
  const float p = __builtin_amdgcn_exp2f((s - mn) * EXP_C);
  st.m = mn; st.l = st.l * al + p;
  const float al0 = dpp<QP_EVEN>(al), al1 = dpp<QP_ODD>(al);
#pragma unroll
  for (int j = 0; j < 4; ++j) { st.a0[j] *= al0; st.a1[j] *= al1; }
  return p;
}
template <int I> struct Bc { static __device__ __forceinline__ float get(float p) { return dpp<BCAST + I>(p); } };
template <int I, class ISSUE>
__device__ __forceinline__ void pv_step(v4u (&vb)[8], float p, Att& st, ISSUE& issue) {
  const float p0 = dpp<BCAST + 2 * I>(p), p1 = dpp<BCAST + 2 * I + 1>(p);
#pragma unroll
  for (int j = 0; j < 4; ++j) { const f32x2 v = unp(vb[I][j]); st.a0[j] += v * p0; st.a1[j] += v * p1; }
  asm volatile("" : "+v"(st.a0[0]), "+v"(st.a0[1]), "+v"(st.a0[2]), "+v"(st.a0[3]), "+v"(st.a1[0]), "+v"(st.a1[1]), "+v"(st.a1[2]), "+v"(st.a1[3]));
  issue(I);
  SB();
}
template <class ISSUE>
__device__ __forceinline__ void phase_v(v4u (&vb)[8], float p, Att& st, ISSUE issue) {
  pv_step<0>(vb, p, st, issue); pv_step<1>(vb, p, st, issue); pv_step<2>(vb, p, st, issue); pv_step<3>(vb, p, st, issue);
  pv_step<4>(vb, p, st, issue); pv_step<5>(vb, p, st, issue); pv_step<6>(vb, p, st, issue); pv_step<7>(vb, p, st, issue);
}

#define STAMP(k) do {} while (0)
#define STAMP_OUT() do {} while (0)

__device__ __forceinline__ f32x2 ln_stats(const v4u& xa, int lane) {
  f32x2 xv[4];
  float sm = 0.f;
#pragma unroll
  for (int j = 0; j < 4; ++j) { xv[j] = unp(xa[j]); sm += xv[j].x + xv[j].y; }
  const float mean = wave_sum(sm) * (1.0f / D);
  float sq = 0.f;
#pragma unroll
  for (int j = 0; j < 4; ++j) { const f32x2 d = xv[j] - mean; sq += d.x * d.x + d.y * d.y; }
  sq = lane < 56 ? sq : 0.f;
  const float var = rbf(wave_sum(sq) * (1.0f / D));
  const float sd = rbf(sqrtf(rbf(var + 1e-5f)));
  return (f32x2){rbf(mean), 1.0f / sd};
}
__device__ __forceinline__ float ln_apply(float x, f32x2 st) { return rbf(rbf(x - st.x) * st.y); }
__device__ __forceinline__ unsigned pack2(float a, float b) { const unsigned u = f2bf(a) | (f2bf(b) << 16); return u < 0xFFFFFFFEu ? u : 0xFFFFFFFEu; }

__global__ void __launch_bounds__(NT, 2)
fused_kernel(const u16* __restrict__ xg, const u16* __restrict__ kc, const u16* __restrict__ vc, const u16* __restrict__ wq,
             const u16* __restrict__ wo, const u16* __restrict__ wg, const u16* __restrict__ wu, const u16* __restrict__ wd,
             u16* __restrict__ out, char* ws) {
  __shared__ __attribute__((aligned(16))) float s_part[8][512];
  __shared__ __attribute__((aligned(16))) char s_w[32 * WROW];
  __shared__ __attribute__((aligned(16))) float s_mv[8][64];
  __shared__ __attribute__((aligned(16))) unsigned s_q[256];
  __shared__ __attribute__((aligned(16))) u16 s_qs[64];
  __shared__ __attribute__((aligned(16))) float s_ml[8][4];
  __shared__ __attribute__((aligned(16))) float s_o[32];
  __shared__ int s_flag, s_ctr;
  const int tid = threadIdx.x, lane = tid & 63, w = __builtin_amdgcn_readfirstlane(tid >> 6);
  const int bi = blockIdx.x, h = (bi & 6) | (bi >> 7), s = ((bi >> 3) & 15) * 2 + (bi & 1), b = s * 8 + h;
  static_assert(4 * PQ + 7 * PF + 5 * PO == 2048, "units");
  const int pk = s >> 1;
  const int pn = pk < 4 ? PQ : pk < 11 ? PF : PO, pe = (pn - DLT) >> 1;
  const int first = (pk < 4 ? pk * PQ : pk < 11 ? 4 * PQ + (pk - 4) * PF : 4 * PQ + 7 * PF + (pk - 11) * PO) + ((s & 1) ? pe : 0);
  const int cnt = (s & 1) ? pn - pe : pe;
  asm volatile("" :: "s"(xg), "s"(kc), "s"(vc), "s"(wq), "s"(wo), "s"(wg), "s"(wu), "s"(wd), "s"(out), "s"(ws));
  STAMP(0);
  const rsrc_t rx = mkrs(xg, D * 2);
  const rsrc_t rk = mkrs(kc + (size_t)h * SK * DH, SK * DH * 2);
  const rsrc_t rv = mkrs(vc + (size_t)h * SK * DH, SK * DH * 2);
  const rsrc_t rws = mkrs(ws, WS_END);
  const unsigned l16 = lane * 16;
  constexpr int T0 = ((PQ - (DLT < 0 ? -DLT : DLT)) >> 1) & ~7;
  auto ubase = [&](int u) -> unsigned { return (unsigned)(u < T0 ? (u >> 3) * 256 + s * 8 + (u & 7) : (31 - s) * T0 + first + u) * 8192u; };
  auto ldk = [&](unsigned base, int i) { return ld4<2>(rk, l16 + (i & 3) * 1024, base + (i >> 2) * 4096); };
  auto ldv = [&](unsigned base, int i) { return ld4<2>(rv, l16 + (i & 3) * 1024, base + (i >> 2) * 4096); };
  const unsigned area = (unsigned)b * AREA;
  auto area_of = [&](int ss) -> unsigned { return (unsigned)(ss * 8 + h) * AREA; };
  const unsigned lmask = lane < 56 ? 0u : OOB;

  const bool isq = s < 8, isf = s >= 8 && s < 22;
  const int f = h * 14 + s - 8;
  const bool tile = isq || (isf && w < 4);
  const bool leader = (b == NWG - 1 && w == 1), member = (w == 0);

  const v4u sent = {SENT, SENT, SENT, SENT};
  unsigned poll_off = lane == 1 ? WS_GO : OOB;
  poll_off = lane == 0 ? WS_REQ + (b >> 2) * 16 : poll_off;
  poll_off = leader ? WS_ECHO + l16 : poll_off;
  v4u pv = {0u, 0u, 0u, 0u}, e1 = {0u, 0u, 0u, 0u};
  unsigned go_ref = 0;
  if (member) {
    st4c(sent, rws, area + A_Q + l16);
    __hip_atomic_store(&s_flag, 0, __ATOMIC_RELAXED, __HIP_MEMORY_SCOPE_WORKGROUP);
    __hip_atomic_store(&s_ctr, 24, __ATOMIC_RELAXED, __HIP_MEMORY_SCOPE_WORKGROUP);
  }
  SB();
  if (tile) {
    v4u tw[8];
    const int oct = isq ? 7 * w : (f & 3) * 14 + (w >> 1) * 7;
    const int stride = isq ? NQ * 2 : F * 2;
    const rsrc_t rt = mkrs(isq ? wq : (w & 1) ? wu : wg, D * stride);
    const unsigned to = (unsigned)((oct + (lane >> 3)) * 8 * stride + (isq ? h * 8 + s : f >> 2) * 128 + (lane & 7) * 16) | lmask;
    const v4u xa = ld4<0>(rx, l16, 0);
    const v4u xb = ld4<0>(rx, (unsigned)(oct + (lane >> 3)) * 16 | lmask, 0);
    SB();
#pragma unroll
    for (int i = 0; i < 8; ++i) { tw[i] = ld4<2>(rt, to, i * stride); SB(); }
    LDS_FENCE(); bar(); LDS_FENCE();
    SB();
    const f32x2 ls = ln_stats(xa, lane);
    f32x2 acc[4];
#pragma unroll
    for (int i = 0; i < 8; ++i) {
      const float xn = ln_apply(asf((i & 1) ? (xb[i >> 1] & 0xffff0000u) : (xb[i >> 1] << 16)), ls);
#pragma unroll
      for (int j = 0; j < 4; ++j) acc[j] = i ? acc[j] + unp(tw[i][j]) * xn : unp(tw[i][j]) * xn;
    }
    float y0 = tr32(acc[0].x, acc[2].x), y1 = tr32(acc[0].y, acc[2].y), y2 = tr32(acc[1].x, acc[3].x), y3 = tr32(acc[1].y, acc[3].y);
    y0 = tr16(y0, y2); y1 = tr16(y1, y3);
    const bool s_ = lane & 8;
    y0 = (s_ ? y1 : y0) + dpp<ROR8>(s_ ? y0 : y1);
    s_mv[w][(lane & 7) * 8 + (lane >> 3)] = y0;
    STAMP(6);
    if (isq) {
      LDS_FENCE(); bar(); LDS_FENCE();
      if (w < 4) {
        float a = 0.f;
#pragma unroll
        for (int k = 0; k < 8; ++k) a += s_mv[k][lane];
        s_qs[lane] = (u16)f2bf(a);
        LDS_FENCE();
        v4u qs = *reinterpret_cast<const v4u*>(&s_qs[(lane & 7) * 8]);
#pragma unroll
        for (int j = 0; j < 4; ++j) qs[j] = qs[j] < 0xFFFFFFFEu ? qs[j] : 0xFFFFFFFEu;
        st4c(qs, rws, area_of(w * 8 + (lane >> 3)) + A_Q + s * 128 + (lane & 7) * 16);
      }
    }
  } else if (leader) {
    e1 = ld4<16>(rws, poll_off, 0);
    go_ref = __builtin_amdgcn_raw_buffer_load_b32(rws, (int)WS_GO, 0, 16);
    SB();
    LDS_FENCE(); bar(); LDS_FENCE();
    SB();
    e1 += 1u;
    go_ref += 1u;
    st4c(e1, rws, WS_REQ + l16);
  } else {
    LDS_FENCE(); bar(); LDS_FENCE();
  }
  SB();
  if (member) {
#pragma unroll
    for (int i = 1; i < 4; ++i) st4c(sent, rws, (area + i * 1024 + l16) | ((i < 3 || lane < 48) ? 0u : OOB));
    if (b >= RED0) {
#pragma unroll
      for (int i = 4; i < 8; ++i) st4c(sent, rws, area + i * 1024 + l16);
    }
  }
  STAMP(5);
  SB();
  v4u pre[4][8];
  v4u wov[2], wdv[2], qv;
  const unsigned ub0 = ubase(w), ub1 = ubase(8 + w);
  static_for<0, 32>([&](auto N) {
    constexpr int n = N;
    if constexpr (n == 4) {
      const rsrc_t ro = mkrs(wo, NQ * D * 2), rd = mkrs(wd, F * D * 2);
      const unsigned oo = (unsigned)((h * 512 + s * 16 + w * 2) * (D * 2) + l16) | lmask;
      const unsigned od = (unsigned)((f * 16 + w * 2) * (D * 2) + l16) | (isf ? lmask : OOB);
      wov[0] = ld4<2>(ro, oo, 0); wov[1] = ld4<2>(ro, oo + D * 2, 0);
      wdv[0] = ld4<2>(rd, od, 0); wdv[1] = ld4<2>(rd, od + D * 2, 0);
      SB();
    }
    if constexpr (n == QPOS) {
      STAMP(9);
      qv = ld4<16>(rws, member ? area + A_Q + l16 : OOB, 0);
      pv = ld4<16>(rws, (member || leader) ? poll_off : OOB, 0);
      SB();
    }
    if constexpr (n >= PACE) WAITV(PACE - 1 + ((n >= QPOS && n < QPOS + PACE) ? 2 : 0) + ((n < 4 + PACE) ? 4 : 0));
    pre[n >> 3][n & 7] = (n & 8) ? ldv((n & 16) ? ub1 : ub0, n & 7) : ldk((n & 16) ? ub1 : ub0, n & 7);
    SB();
    if constexpr (n - PACE == 7 || n - PACE == 15 || n - PACE == 23) STAMP(16 + (n - PACE) / 8);
  });
  STAMP(10);
  if (member) {
    int tries = 0;
    bool ok;
    for (;; ++tries) {
      ok = __builtin_amdgcn_ballot_w64(!full4(qv)) == 0;
      if (ok || tries >= QTRIES) break;
      __builtin_amdgcn_s_sleep(4);
      qv = ld4<16>(rws, area + A_Q + l16, 0);
      WAITV(0);
    }
    if (!ok) {
      const v4u xc = ld4<0>(rx, l16, 0);
      const f32x2 ls = ln_stats(xc, lane);
      const rsrc_t rwq = mkrs(wq, D * NQ * 2);
      f32x2 acc[4] = {{0.f, 0.f}, {0.f, 0.f}, {0.f, 0.f}, {0.f, 0.f}};
#pragma unroll 1
      for (int r = 0; r < D / 2; ++r) {
        unsigned xp = xc[0];
        xp = (r & 3) == 1 ? xc[1] : xp; xp = (r & 3) == 2 ? xc[2] : xp; xp = (r & 3) == 3 ? xc[3] : xp;
        const f32x2 xr = unp((unsigned)__builtin_amdgcn_readlane((int)xp, r >> 2));
        const v4u wa = ld4<0>(rwq, h * 1024 + l16, r * 2 * NQ * 2), wb = ld4<0>(rwq, h * 1024 + l16, (r * 2 + 1) * NQ * 2);
        const float x0 = ln_apply(xr.x, ls), x1 = ln_apply(xr.y, ls);
#pragma unroll
        for (int j = 0; j < 4; ++j) acc[j] += unp(wa[j]) * x0 + unp(wb[j]) * x1;
      }
#pragma unroll
      for (int j = 0; j < 4; ++j) qv[j] = pack2(acc[j].x, acc[j].y);
      WAITV(0);
    }
    *reinterpret_cast<v4u*>(&s_q[lane * 4]) = qv;
    LDS_FENCE();
    __builtin_amdgcn_s_waitcnt(0xC07F);
    __hip_atomic_store(&s_flag, 1, __ATOMIC_RELAXED, __HIP_MEMORY_SCOPE_WORKGROUP);
  } else {
    while (__builtin_amdgcn_readfirstlane(__hip_atomic_load(&s_flag, __ATOMIC_RELAXED, __HIP_MEMORY_SCOPE_WORKGROUP)) == 0) __builtin_amdgcn_s_sleep(1);
  }
  LDS_FENCE();
  const v4u q0 = *reinterpret_cast<const v4u*>(&s_q[(lane & 31) * 4]), q1 = *reinterpret_cast<const v4u*>(&s_q[128 + (lane & 31) * 4]);
  STAMP(1);
  SB();

  bool svc = member || leader;
  unsigned seenm = 0, last_echo = 0;
  bool echoed = false;
  auto service = [&]() {
    if (leader) {
      seenm |= (pv[0] == e1[0] ? 1u : 0u) | (pv[1] == e1[1] ? 2u : 0u) | (pv[2] == e1[2] ? 4u : 0u) | (pv[3] == e1[3] ? 8u : 0u);
      svc = __builtin_amdgcn_ballot_w64(seenm != 15u) != 0;
      st1c(go_ref, rws, (lane == 0 && !svc) ? WS_GO : OOB);
    } else {
      const unsigned req = (unsigned)__builtin_amdgcn_readfirstlane((int)((b & 2) ? ((b & 1) ? pv[3] : pv[2]) : ((b & 1) ? pv[1] : pv[0])));
      const unsigned go = (unsigned)__builtin_amdgcn_readlane((int)pv[0], 1);
      go_ref = echoed ? go_ref : go;
      svc = go == (unsigned)__builtin_amdgcn_readfirstlane((int)go_ref);
      st1c(req, rws, (lane == 0 && svc && !(echoed && req == last_echo)) ? WS_ECHO + b * 4 : OOB);
      echoed = true; last_echo = req;
    }
    pv = ld4<16>(rws, svc ? poll_off : OOB, 0);
    SB();
  };

  Att st;
  st.m = -3.0e38f; st.l = 0.f;
#pragma unroll
  for (int j = 0; j < 4; ++j) { st.a0[j] = (f32x2){0.f, 0.f}; st.a1[j] = (f32x2){0.f, 0.f}; }
  v4u ring[8];
  unsigned ub = ubase(16 + w);
  {
    const unsigned ub2 = ub;
    if (svc) service();
    float p = phase_k(pre[0], q0, q1, st, lane, [&](int i) { ring[i] = ldk(ub2, i); });
    phase_v(pre[1], p, st, [&](int) {});
    p = phase_k(pre[2], q0, q1, st, lane, [&](int) {});
    phase_v(pre[3], p, st, [&](int) {});
    STAMP(19);
    if (lane < 56) {
      *reinterpret_cast<v4u*>(&s_w[(w * 2) * WROW + l16]) = wov[0];
      *reinterpret_cast<v4u*>(&s_w[(w * 2 + 1) * WROW + l16]) = wov[1];
      *reinterpret_cast<v4u*>(&s_w[(16 + w * 2) * WROW + l16]) = wdv[0];
      *reinterpret_cast<v4u*>(&s_w[(17 + w * 2) * WROW + l16]) = wdv[1];
    }
    SB();
  }
#pragma unroll 1
  for (int u = 2;; ++u) {
    int tk = 0;
    if (lane == 0) tk = __hip_atomic_fetch_add(&s_ctr, 1, __ATOMIC_RELAXED, __HIP_MEMORY_SCOPE_WORKGROUP);
    SB();
    const float p = phase_k(ring, q0, q1, st, lane, [&](int i) { ring[i] = ldv(ub, i); });
    asm volatile("" : "+v"(tk));
    tk = __builtin_amdgcn_readfirstlane(tk);
    const bool more = tk < cnt;
    const unsigned un = more ? ubase(tk) : OOB;
    STAMP(16 + 2 * u);
    if (svc) service();
    phase_v(ring, p, st, [&](int i) { ring[i] = ldk(un, i); });
    STAMP(17 + 2 * u);
    if (svc) service();
    if (!more) break;
    ub = un;
  }
  STAMP(12);
  {
    float l = st.l;
    l += dpp<ROR2>(l); l += dpp<ROR4>(l); l += dpp<ROR8>(l); l = x32_add(l);
    float o[8];
#pragma unroll
    for (int j = 0; j < 4; ++j) { o[2 * j] = tr32(st.a0[j].x, st.a1[j].x); o[2 * j + 1] = tr32(st.a0[j].y, st.a1[j].y); }
    *reinterpret_cast<f32x4*>(&s_part[w][lane * 8]) = (f32x4){o[0], o[1], o[2], o[3]};
    *reinterpret_cast<f32x4*>(&s_part[w][lane * 8 + 4]) = (f32x4){o[4], o[5], o[6], o[7]};
    if (lane < 2) *reinterpret_cast<f32x2*>(&s_ml[w][lane * 2]) = (f32x2){st.m, l};
  }
  for (int it = 0; svc && it < SPIN_CAP; ++it) { service(); __builtin_amdgcn_s_sleep(2); }
  WAITV(0);
  LDS_FENCE(); bar(); LDS_FENCE();
  STAMP(2);
  const int cq = lane >> 2, rg = lane & 3;
  if (w == 2 || w == 3 || w == 4) {
    const int t = tid - 128, hd = (t >> 6) & 1;
    f32x4 ml[8];
#pragma unroll
    for (int k = 0; k < 8; ++k) ml[k] = *reinterpret_cast<const f32x4*>(&s_ml[k][0]);
    if (w < 4) {
      float M = -3.0e38f;
#pragma unroll
      for (int k = 0; k < 8; ++k) M = fmaxf(M, hd ? ml[k][2] : ml[k][0]);
      f32x4 o = {0.f, 0.f, 0.f, 0.f};
#pragma unroll
      for (int k = 0; k < 8; ++k) o += *reinterpret_cast<const f32x4*>(&s_part[k][t * 4]) * __builtin_amdgcn_exp2f(((hd ? ml[k][2] : ml[k][0]) - M) * EXP_C);
      st4c((v4u){canon(o[0]), canon(o[1]), canon(o[2]), canon(o[3])}, rws, area_of(t >> 2) + A_O1 + s * 64 + (t & 3) * 16);
    } else {
      float M0 = -3.0e38f, M1 = -3.0e38f, L0 = 0.f, L1 = 0.f;
#pragma unroll
      for (int k = 0; k < 8; ++k) { M0 = fmaxf(M0, ml[k][0]); M1 = fmaxf(M1, ml[k][2]); }
#pragma unroll
      for (int k = 0; k < 8; ++k) { L0 += ml[k][1] * __builtin_amdgcn_exp2f((ml[k][0] - M0) * EXP_C); L1 += ml[k][3] * __builtin_amdgcn_exp2f((ml[k][2] - M1) * EXP_C); }
      st2c((v2u){canon((lane & 16) ? M1 : M0), canon((lane & 16) ? L1 : L0)}, rws, (area_of(lane & 31) + A_ML + s * 8) | (lane < 32 ? 0u : OOB));
    }
  }
  if (w == 5 && isf) {
    const int j = (lane >> 3) & 3, pc = lane & 7, m = pc >> 2, col = j * 16 + (pc & 3) * 4;
    const f32x4 v = *reinterpret_cast<const f32x4*>(&s_mv[m][col]) + *reinterpret_cast<const f32x4*>(&s_mv[m + 2][col]);
    const int fd = (f & ~3) + j, hd = fd / 14;
    st4c((v4u){canon(v[0]), canon(v[1]), canon(v[2]), canon(v[3])}, rws,
         ((unsigned)((fd - hd * 14 + 8) * 8 + hd) * AREA + A_F + (f & 3) * 128 + pc * 16) | (lane < 32 ? 0u : OOB));
  }
  v2u wt[8];
#pragma unroll
  for (int i = 0; i < 8; ++i) wt[i] = *reinterpret_cast<const v2u*>(&s_w[((i >> 2) * 16 + rg * 4 + (i & 3)) * WROW + w * 112 + (cq < 14 ? cq : 0) * 8]);
  if (w == 0) {
    const unsigned oa = area + A_O1 + l16;
    const unsigned ma = area + A_ML + cq * 8;
    v4u da, db; v2u mA, mB;
    bool okA = false, okB = false;
    for (int it = 0; it < SPIN_CAP; ++it) {
      if (!okA) { da = ld4<16>(rws, oa, 0); mA = ld2<16>(rws, ma, 0); }
      if (!okB) { db = ld4<16>(rws, oa + 1024, 0); mB = ld2<16>(rws, ma + 128, 0); }
      okA = full4(da) && mA[0] != SENT && mA[1] != SENT;
      okB = full4(db) && mB[0] != SENT && mB[1] != SENT;
      if (__builtin_amdgcn_ballot_w64(!(okA && okB)) == 0) break;
      __builtin_amdgcn_s_sleep(2);
    }
    const float M = wave_max(fmaxf(asf(mA[0]), asf(mB[0])));
    const float ea = __builtin_amdgcn_exp2f((asf(mA[0]) - M) * EXP_C), eb = __builtin_amdgcn_exp2f((asf(mB[0]) - M) * EXP_C);
    float v[5];
#pragma unroll
    for (int j = 0; j < 4; ++j) v[j] = asf(da[j]) * ea + asf(db[j]) * eb;
    v[4] = asf(mA[1]) * ea + asf(mB[1]) * eb;
#pragma unroll
    for (int j = 0; j < 5; ++j) { v[j] += dpp<ROR4>(v[j]); v[j] += dpp<ROR8>(v[j]); v[j] = x16_add(v[j]); v[j] = x32_add(v[j]); }
    if (lane < 4) *reinterpret_cast<f32x4*>(&s_o[lane * 4]) = (f32x4){rbf(v[0] / v[4]), rbf(v[1] / v[4]), rbf(v[2] / v[4]), rbf(v[3] / v[4])};
  }
  if (w == 1) {
    f32x4 hv = {0.f, 0.f, 0.f, 0.f};
    if (isf) {
      v4u d;
      for (int it = 0; it < SPIN_CAP; ++it) {
        d = ld4<16>(rws, area + A_F + (lane & 31) * 16, 0);
        if (__builtin_amdgcn_ballot_w64(!full4(d)) == 0) break;
        __builtin_amdgcn_s_sleep(2);
      }
#pragma unroll
      for (int j = 0; j < 4; ++j) {
        float g = asf(d[j]);
        g += dpp<ROR8>(g); g = rbf(x16_add(g));
        const float u = dpp<ROR4>(g);
        hv[j] = rbf(rbf(g / (1.0f + __expf(-g))) * u);
      }
    }
    if (lane < 4) *reinterpret_cast<f32x4*>(&s_o[16 + lane * 4]) = hv;
  }
  LDS_FENCE(); bar(); LDS_FENCE();
  STAMP(3);
  {
    const f32x4 ov = *reinterpret_cast<const f32x4*>(&s_o[rg * 4]), hv = *reinterpret_cast<const f32x4*>(&s_o[16 + rg * 4]);
    f32x2 ya = {0.f, 0.f}, yb = {0.f, 0.f};
#pragma unroll
    for (int i = 0; i < 4; ++i) { ya += unp(wt[i][0]) * ov[i] + unp(wt[4 + i][0]) * hv[i]; yb += unp(wt[i][1]) * ov[i] + unp(wt[4 + i][1]) * hv[i]; }
    float y[4] = {ya.x, ya.y, yb.x, yb.y};
#pragma unroll
    for (int j = 0; j < 4; ++j) { y[j] += dpp<XOR1>(y[j]); y[j] += dpp<XOR2>(y[j]); }
    st4c((v4u){canon(y[0]), canon(y[1]), canon(y[2]), canon(y[3])}, rws, ((unsigned)(RED0 + w * 14 + cq) * AREA + A_Y2 + b * 16) | ((rg == 0 && cq < 14) ? 0u : OOB));
  }
  if (w == 0 && b >= RED0) {
    const v2u xo = ld2<0>(rx, (b - RED0) * 8, 0);
    const unsigned ya = area + A_Y2 + l16;
    v4u d[4];
    bool ok[4] = {false, false, false, false};
    for (int it = 0; it < SPIN_CAP; ++it) {
#pragma unroll
      for (int k = 0; k < 4; ++k) if (!ok[k]) d[k] = ld4<16>(rws, ya + k * 1024, 0);
#pragma unroll
      for (int k = 0; k < 4; ++k) ok[k] = full4(d[k]);
      if (__builtin_amdgcn_ballot_w64(!(ok[0] && ok[1] && ok[2] && ok[3])) == 0) break;
      __builtin_amdgcn_s_sleep(1);
    }
    float y[4];
#pragma unroll
    for (int j = 0; j < 4; ++j) y[j] = wave_sum((asf(d[0][j]) + asf(d[1][j])) + (asf(d[2][j]) + asf(d[3][j])));
    const f32x2 x01 = unp(xo[0]), x23 = unp(xo[1]);
    if (lane == 0) {
      v2u r;
      r[0] = f2bf(x01.x + y[0]) | (f2bf(x01.y + y[1]) << 16);
      r[1] = f2bf(x23.x + y[2]) | (f2bf(x23.y + y[3]) << 16);
      *reinterpret_cast<v2u*>(out + (b - RED0) * 4) = r;
    }
  }
  STAMP(4);
  STAMP_OUT();
}

extern "C" void kernel_launch(void* const* d_in, const int* in_sizes, int n_in,
                              void* d_out, int out_size, void* d_ws, size_t ws_size,
                              hipStream_t stream) {
  fused_kernel<<<NWG, NT, 0, stream>>>((const u16*)d_in[0], (const u16*)d_in[1], (const u16*)d_in[2], (const u16*)d_in[3],
                                       (const u16*)d_in[4], (const u16*)d_in[6], (const u16*)d_in[7], (const u16*)d_in[8],
                                       (u16*)d_out, (char*)d_ws);
}
```

```cpp
#include <hip/hip_runtime.h>
#include <hip/hip_bf16.h>
#include <stdint.h>

#define QPOS 24
#define PACE 8
#define QTRIES 8
#define POOL 32
#define NG 16
#define PQ (122 - POOL)
#define PF (130 - POOL)
#define PO (130 - POOL)
#define DLT 0
#define SWEEP 0
#define HM 5
#define DIAG 0

typedef unsigned v4u __attribute__((ext_vector_type(4)));
typedef unsigned v2u __attribute__((ext_vector_type(2)));
typedef float f32x4 __attribute__((ext_vector_type(4)));
typedef float f32x2 __attribute__((ext_vector_type(2)));
typedef __bf16 bf16x2 __attribute__((ext_vector_type(2)));
typedef unsigned short u16;
typedef __amdgpu_buffer_rsrc_t rsrc_t;

constexpr int D = 448, SK = 32768, DH = 256, F = 1792, NQ = 4096;
constexpr int NWG = 256, NT = 512;
constexpr unsigned OOB = 0x80000000u;
constexpr unsigned SENT = 0xFFFFFFFFu;
constexpr unsigned AREA = 8192;
constexpr unsigned A_Q = 0;
constexpr unsigned A_O1 = 1024;
constexpr unsigned A_ML = 3072;
constexpr unsigned A_F = 3328;
constexpr unsigned A_Y2 = 4096;
constexpr unsigned WS_REQ = NWG * AREA;
constexpr unsigned WS_ECHO = WS_REQ + 1024;
constexpr unsigned WS_GO = WS_ECHO + 1024;
constexpr unsigned WS_POOL = WS_GO + 256;
constexpr unsigned WS_END = WS_POOL + 8 * NG * 256;
constexpr int RED0 = NWG - 112;
constexpr int SPIN_CAP = 1 << 18;
constexpr int WROW = 912;

__device__ __forceinline__ float asf(unsigned u) { return __builtin_bit_cast(float, u); }
__device__ __forceinline__ unsigned asu(float f) { return __builtin_bit_cast(unsigned, f); }
__device__ __forceinline__ float rbf(float f) { __bf16 b = (__bf16)f; return (float)b; }
__device__ __forceinline__ unsigned f2bf(float f) { __bf16 b = (__bf16)f; return (unsigned)__builtin_bit_cast(u16, b); }
__device__ __forceinline__ float dot2(unsigned k, unsigned q, float acc) {
  return __builtin_amdgcn_fdot2_f32_bf16(__builtin_bit_cast(bf16x2, k), __builtin_bit_cast(bf16x2, q), acc, false);
}
__device__ __forceinline__ f32x2 unp(unsigned u) { f32x2 r; r.x = asf(u << 16); r.y = asf(u & 0xffff0000u); return r; }
template <int CTRL> __device__ __forceinline__ float dpp(float x) {
  return asf((unsigned)__builtin_amdgcn_update_dpp(0, (int)asu(x), CTRL, 0xf, 0xf, true));
}
constexpr int XOR1 = 0xB1, XOR2 = 0x4E, XOR7 = 0x141, ROR8 = 0x128, ROR4 = 0x124, ROR2 = 0x122, BCAST = 0x150;
constexpr int QP_EVEN = 0xA0, QP_ODD = 0xF5;
__device__ __forceinline__ float x16_add(float x) { auto s = __builtin_amdgcn_permlane16_swap(asu(x), asu(x), false, false); return asf(s[0]) + asf(s[1]); }
__device__ __forceinline__ float x32_add(float x) { auto s = __builtin_amdgcn_permlane32_swap(asu(x), asu(x), false, false); return asf(s[0]) + asf(s[1]); }
__device__ __forceinline__ float x16_max(float x) { auto s = __builtin_amdgcn_permlane16_swap(asu(x), asu(x), false, false); return fmaxf(asf(s[0]), asf(s[1])); }
__device__ __forceinline__ float x32_max(float x) { auto s = __builtin_amdgcn_permlane32_swap(asu(x), asu(x), false, false); return fmaxf(asf(s[0]), asf(s[1])); }
__device__ __forceinline__ float tr32(float a, float b) { auto s = __builtin_amdgcn_permlane32_swap(asu(a), asu(b), false, false); return asf(s[0]) + asf(s[1]); }
__device__ __forceinline__ float tr16(float a, float b) { auto s = __builtin_amdgcn_permlane16_swap(asu(a), asu(b), false, false); return asf(s[0]) + asf(s[1]); }
__device__ __forceinline__ float wave_sum(float v) {
  v += dpp<XOR1>(v); v += dpp<XOR2>(v); v += dpp<XOR7>(v); v += dpp<ROR8>(v);
  v = x16_add(v); return x32_add(v);
}
__device__ __forceinline__ float wave_max(float v) {
  v = fmaxf(v, dpp<XOR1>(v)); v = fmaxf(v, dpp<XOR2>(v)); v = fmaxf(v, dpp<XOR7>(v)); v = fmaxf(v, dpp<ROR8>(v));
  v = x16_max(v); return x32_max(v);
}
__device__ __forceinline__ rsrc_t mkrs(const void* p, unsigned bytes) { return __builtin_amdgcn_make_buffer_rsrc((void*)p, 0, (int)bytes, 0x00020000); }
template <int AUX> __device__ __forceinline__ v4u ld4(rsrc_t r, unsigned vo, unsigned so) { return __builtin_amdgcn_raw_buffer_load_b128(r, (int)vo, (int)so, AUX); }
template <int AUX> __device__ __forceinline__ v2u ld2(rsrc_t r, unsigned vo, unsigned so) { return __builtin_amdgcn_raw_buffer_load_b64(r, (int)vo, (int)so, AUX); }
__device__ __forceinline__ void st4c(v4u v, rsrc_t r, unsigned vo) { __builtin_amdgcn_raw_buffer_store_b128(v, r, (int)vo, 0, 16); }
__device__ __forceinline__ void st2c(v2u v, rsrc_t r, unsigned vo) { __builtin_amdgcn_raw_buffer_store_b64(v, r, (int)vo, 0, 16); }
__device__ __forceinline__ void st1c(unsigned v, rsrc_t r, unsigned vo) { __builtin_amdgcn_raw_buffer_store_b32(v, r, (int)vo, 0, 16); }
__device__ __forceinline__ unsigned canon(float f) { unsigned u = asu(f); return u < 0xFFFFFFFEu ? u : 0xFFFFFFFEu; }
__device__ __forceinline__ bool full4(v4u v) { return v[0] != SENT && v[1] != SENT && v[2] != SENT && v[3] != SENT; }
#define SB() __builtin_amdgcn_sched_barrier(0)
__device__ __forceinline__ void bar() { __builtin_amdgcn_s_waitcnt(0xC07F); __builtin_amdgcn_s_barrier(); }
#define WAITV(n) __builtin_amdgcn_s_waitcnt(((n) & 15) | (((n) >> 4) << 14) | 0x0F70)
template <int V> struct IC { static constexpr int value = V; constexpr operator int() const { return V; } };
template <int A, int B, class Fn> __device__ __forceinline__ void static_for(Fn&& f) { if constexpr (A < B) { f(IC<A>{}); static_for<A + 1, B>(f); } }
#define LDS_FENCE() asm volatile("" ::: "memory")

constexpr float EXP_C = 0.0625f * 1.4426950408889634f;

struct Att {
  float m, l;
  f32x2 a0[4], a1[4];
};

template <class ISSUE>
__device__ __forceinline__ float phase_k(v4u (&kb)[8], const v4u& q0, const v4u& q1, Att& st, int lane, ISSUE issue) {
  float x[16];
#pragma unroll
  for (int i = 0; i < 8; ++i) {
    float a = 0.f, b = 0.f;
#pragma unroll
    for (int j = 0; j < 4; ++j) { a = dot2(kb[i][j], q0[j], a); b = dot2(kb[i][j], q1[j], b); }
    x[2 * i] = a; x[2 * i + 1] = b;
    asm volatile("" : "+v"(x[2 * i]), "+v"(x[2 * i + 1]));
    issue(i);
    SB();
  }
#define TR_STEP(HALF, CTRL, BIT) _Pragma("unroll") for (int v = 0; v < (HALF); ++v) { \
    const float lo_ = x[v], hi_ = x[v + (HALF)]; const bool s_ = lane & (BIT); \
    x[v] = (s_ ? hi_ : lo_) + dpp<CTRL>(s_ ? lo_ : hi_); }
  TR_STEP(8, ROR8, 8) TR_STEP(4, XOR7, 4) TR_STEP(2, XOR2, 2) TR_STEP(1, XOR1, 1)
#undef TR_STEP
  const float s = rbf(x16_add(x[0]));
  float mx = fmaxf(s, dpp<ROR2>(s)); mx = fmaxf(mx, dpp<ROR4>(mx)); mx = fmaxf(mx, dpp<ROR8>(mx));
  mx = x32_max(mx);
  const float mn = fmaxf(st.m, mx);
  const float al = __builtin_amdgcn_exp2f((st.m - mn) * EXP_C);
  const float p = __builtin_amdgcn_exp2f((s - mn) * EXP_C);
  st.m = mn; st.l = st.l * al + p;
  const float al0 = dpp<QP_EVEN>(al), al1 = dpp<QP_ODD>(al);
#pragma unroll
  for (int j = 0; j < 4; ++j) { st.a0[j] *= al0; st.a1[j] *= al1; }
  return p;
}
template <int I> struct Bc { static __device__ __forceinline__ float get(float p) { return dpp<BCAST + I>(p); } };
template <int I, class ISSUE>
__device__ __forceinline__ void pv_step(v4u (&vb)[8], float p, Att& st, ISSUE& issue) {
  const float p0 = dpp<BCAST + 2 * I>(p), p1 = dpp<BCAST + 2 * I + 1>(p);
#pragma unroll
  for (int j = 0; j < 4; ++j) { const f32x2 v = unp(vb[I][j]); st.a0[j] += v * p0; st.a1[j] += v * p1; }
  asm volatile("" : "+v"(st.a0[0]), "+v"(st.a0[1]), "+v"(st.a0[2]), "+v"(st.a0[3]), "+v"(st.a1[0]), "+v"(st.a1[1]), "+v"(st.a1[2]), "+v"(st.a1[3]));
  issue(I);
  SB();
}
template <class ISSUE>
__device__ __forceinline__ void phase_v(v4u (&vb)[8], float p, Att& st, ISSUE issue) {
  pv_step<0>(vb, p, st, issue); pv_step<1>(vb, p, st, issue); pv_step<2>(vb, p, st, issue); pv_step<3>(vb, p, st, issue);
  pv_step<4>(vb, p, st, issue); pv_step<5>(vb, p, st, issue); pv_step<6>(vb, p, st, issue); pv_step<7>(vb, p, st, issue);
}

#define STAMP(k) do {} while (0)
#define STAMP_OUT() do {} while (0)

__device__ __forceinline__ f32x2 ln_stats(const v4u& xa, int lane) {
  f32x2 xv[4];
  float sm = 0.f;
#pragma unroll
  for (int j = 0; j < 4; ++j) { xv[j] = unp(xa[j]); sm += xv[j].x + xv[j].y; }
  const float mean = wave_sum(sm) * (1.0f / D);
  float sq = 0.f;
#pragma unroll
  for (int j = 0; j < 4; ++j) { const f32x2 d = xv[j] - mean; sq += d.x * d.x + d.y * d.y; }
  sq = lane < 56 ? sq : 0.f;
  const float var = rbf(wave_sum(sq) * (1.0f / D));
  const float sd = rbf(sqrtf(rbf(var + 1e-5f)));
  return (f32x2){rbf(mean), 1.0f / sd};
}
__device__ __forceinline__ float ln_apply(float x, f32x2 st) { return rbf(rbf(x - st.x) * st.y); }
__device__ __forceinline__ unsigned pack2(float a, float b) { const unsigned u = f2bf(a) | (f2bf(b) << 16); return u < 0xFFFFFFFEu ? u : 0xFFFFFFFEu; }

__global__ void __launch_bounds__(NT, 2)
fused_kernel(const u16* __restrict__ xg, const u16* __restrict__ kc, const u16* __restrict__ vc, const u16* __restrict__ wq,
             const u16* __restrict__ wo, const u16* __restrict__ wg, const u16* __restrict__ wu, const u16* __restrict__ wd,
             u16* __restrict__ out, char* ws) {
  __shared__ __attribute__((aligned(16))) float s_part[8][512];
  __shared__ __attribute__((aligned(16))) char s_w[32 * WROW];
  __shared__ __attribute__((aligned(16))) float s_mv[8][64];
  __shared__ __attribute__((aligned(16))) unsigned s_q[256];
  __shared__ __attribute__((aligned(16))) u16 s_qs[64];
  __shared__ __attribute__((aligned(16))) float s_ml[8][4];
  __shared__ __attribute__((aligned(16))) float s_o[32];
  __shared__ int s_flag, s_ctr, s_go;
  const int tid = threadIdx.x, lane = tid & 63, w = __builtin_amdgcn_readfirstlane(tid >> 6);
  const int bi = blockIdx.x, h = (((bi & 1) ? bi + 5 : bi) & 6) | (bi >> 7), s = ((bi >> 3) & 15) * 2 + (bi & 1), b = s * 8 + h;
  static_assert(4 * PQ + 7 * PF + 5 * PO + 16 * POOL == 2048, "units");
  constexpr int PSZ = 16 * POOL / NG, POOL0 = 2048 - 16 * POOL;
  const int pk = s >> 1;
  const int pn = pk < 4 ? PQ : pk < 11 ? PF : PO, pe = (pn - DLT) >> 1;
  const int first = (pk < 4 ? pk * PQ : pk < 11 ? 4 * PQ + (pk - 4) * PF : 4 * PQ + 7 * PF + (pk - 11) * PO) + ((s & 1) ? pe : 0);
  const int cnt = (s & 1) ? pn - pe : pe;
  const int pg = pk % NG;
  asm volatile("" :: "s"(xg), "s"(kc), "s"(vc), "s"(wq), "s"(wo), "s"(wg), "s"(wu), "s"(wd), "s"(out), "s"(ws));
  STAMP(0);
  const rsrc_t rx = mkrs(xg, D * 2);
  const rsrc_t rk = mkrs(kc + (size_t)h * SK * DH, SK * DH * 2);
  const rsrc_t rv = mkrs(vc + (size_t)h * SK * DH, SK * DH * 2);
  const rsrc_t rws = mkrs(ws, WS_END);
  const unsigned l16 = lane * 16;
  auto ubase = [&](int u) -> unsigned { return (unsigned)(first + u) * 8192u; };
  auto ldk = [&](unsigned base, int i) { return ld4<2>(rk, l16 + (i & 3) * 1024, base + (i >> 2) * 4096); };
  auto ldv = [&](unsigned base, int i) { return ld4<2>(rv, l16 + (i & 3) * 1024, base + (i >> 2) * 4096); };
  const unsigned area = (unsigned)b * AREA;
  auto area_of = [&](int ss) -> unsigned { return (unsigned)(ss * 8 + h) * AREA; };
  const unsigned lmask = lane < 56 ? 0u : OOB;

  const bool isq = s < 8, isf = s >= 8 && s < 22;
  const int f = h * 14 + s - 8;
  const bool tile = isq || (isf && w < 4);
  const bool leader = (b == NWG - 1 && w == 1), member = (w == 0);

  const v4u sent = {SENT, SENT, SENT, SENT};
  unsigned poll_off = lane == 1 ? WS_GO : OOB;
  poll_off = lane == 0 ? WS_REQ + (b >> 2) * 16 : poll_off;
  poll_off = leader ? WS_ECHO + l16 : poll_off;
  v4u pv = {0u, 0u, 0u, 0u}, e1 = {0u, 0u, 0u, 0u};
  unsigned go_ref = 0;
  if (member) {
    st4c(sent, rws, area + A_Q + l16);
    __hip_atomic_store(&s_flag, 0, __ATOMIC_RELAXED, __HIP_MEMORY_SCOPE_WORKGROUP);
    __hip_atomic_store(&s_ctr, 24, __ATOMIC_RELAXED, __HIP_MEMORY_SCOPE_WORKGROUP);
    __hip_atomic_store(&s_go, 0, __ATOMIC_RELAXED, __HIP_MEMORY_SCOPE_WORKGROUP);
    st1c(0u, rws, (lane == 0 && s == 2 * pg) ? WS_POOL + (h * NG + pg) * 256 : OOB);
  }
  SB();
  if (tile) {
    v4u tw[8];
    const int oct = isq ? 7 * w : (f & 3) * 14 + (w >> 1) * 7;
    const int stride = isq ? NQ * 2 : F * 2;
    const rsrc_t rt = mkrs(isq ? wq : (w & 1) ? wu : wg, D * stride);
    const unsigned to = (unsigned)((oct + (lane >> 3)) * 8 * stride + (isq ? h * 8 + s : f >> 2) * 128 + (lane & 7) * 16) | lmask;
    const v4u xa = ld4<0>(rx, l16, 0);
    const v4u xb = ld4<0>(rx, (unsigned)(oct + (lane >> 3)) * 16 | lmask, 0);
    SB();
#pragma unroll
    for (int i = 0; i < 8; ++i) { tw[i] = ld4<2>(rt, to, i * stride); SB(); }
    LDS_FENCE(); bar(); LDS_FENCE();
    SB();
    const f32x2 ls = ln_stats(xa, lane);
    f32x2 acc[4];
#pragma unroll
    for (int i = 0; i < 8; ++i) {
      const float xn = ln_apply(asf((i & 1) ? (xb[i >> 1] & 0xffff0000u) : (xb[i >> 1] << 16)), ls);
#pragma unroll
      for (int j = 0; j < 4; ++j) acc[j] = i ? acc[j] + unp(tw[i][j]) * xn : unp(tw[i][j]) * xn;
    }
    float y0 = tr32(acc[0].x, acc[2].x), y1 = tr32(acc[0].y, acc[2].y), y2 = tr32(acc[1].x, acc[3].x), y3 = tr32(acc[1].y, acc[3].y);
    y0 = tr16(y0, y2); y1 = tr16(y1, y3);
    const bool s_ = lane & 8;
    y0 = (s_ ? y1 : y0) + dpp<ROR8>(s_ ? y0 : y1);
    s_mv[w][(lane & 7) * 8 + (lane >> 3)] = y0;
    STAMP(6);
    if (isq) {
      LDS_FENCE(); bar(); LDS_FENCE();
      if (w < 4) {
        float a = 0.f;
#pragma unroll
        for (int k = 0; k < 8; ++k) a += s_mv[k][lane];
        s_qs[lane] = (u16)f2bf(a);
        LDS_FENCE();
        v4u qs = *reinterpret_cast<const v4u*>(&s_qs[(lane & 7) * 8]);
#pragma unroll
        for (int j = 0; j < 4; ++j) qs[j] = qs[j] < 0xFFFFFFFEu ? qs[j] : 0xFFFFFFFEu;
        st4c(qs, rws, area_of(w * 8 + (lane >> 3)) + A_Q + s * 128 + (lane & 7) * 16);
      }
    }
  } else if (leader) {
    e1 = ld4<16>(rws, poll_off, 0);
    go_ref = __builtin_amdgcn_raw_buffer_load_b32(rws, (int)WS_GO, 0, 16);
    SB();
    LDS_FENCE(); bar(); LDS_FENCE();
    SB();
    e1 += 1u;
    go_ref += 1u;
    st4c(e1, rws, WS_REQ + l16);
  } else {
    LDS_FENCE(); bar(); LDS_FENCE();
  }
  SB();
  if (member) {
#pragma unroll
    for (int i = 1; i < 4; ++i) st4c(sent, rws, (area + i * 1024 + l16) | ((i < 3 || lane < 48) ? 0u : OOB));
    if (b >= RED0) {
#pragma unroll
      for (int i = 4; i < 8; ++i) st4c(sent, rws, area + i * 1024 + l16);
    }
  }
  STAMP(5);
  SB();
  v4u pre[4][8];
  v4u wov[2], wdv[2], qv;
  const unsigned ub0 = ubase(w), ub1 = ubase(8 + w);
  static_for<0, 32>([&](auto N) {
    constexpr int n = N;
    if constexpr (n == 4) {
      const rsrc_t ro = mkrs(wo, NQ * D * 2), rd = mkrs(wd, F * D * 2);
      const unsigned oo = (unsigned)((h * 512 + s * 16 + w * 2) * (D * 2) + l16) | lmask;
      const unsigned od = (unsigned)((f * 16 + w * 2) * (D * 2) + l16) | (isf ? lmask : OOB);
      wov[0] = ld4<2>(ro, oo, 0); wov[1] = ld4<2>(ro, oo + D * 2, 0);
      wdv[0] = ld4<2>(rd, od, 0); wdv[1] = ld4<2>(rd, od + D * 2, 0);
      SB();
    }
    if constexpr (n == QPOS) {
      STAMP(9);
      qv = ld4<16>(rws, member ? area + A_Q + l16 : OOB, 0);
      pv = ld4<16>(rws, (member || leader) ? poll_off : OOB, 0);
      SB();
    }
    if constexpr (n >= PACE) WAITV(PACE - 1 + ((n >= QPOS && n < QPOS + PACE) ? 2 : 0) + ((n < 4 + PACE) ? 4 : 0));
    pre[n >> 3][n & 7] = (n & 8) ? ldv((n & 16) ? ub1 : ub0, n & 7) : ldk((n & 16) ? ub1 : ub0, n & 7);
    SB();
    if constexpr (n - PACE == 7 || n - PACE == 15 || n - PACE == 23) STAMP(16 + (n - PACE) / 8);
  });
  STAMP(10);
  if (member) {
    int tries = 0;
    bool ok;
    for (;; ++tries) {
      ok = __builtin_amdgcn_ballot_w64(!full4(qv)) == 0;
      if (ok || tries >= QTRIES) break;
      __builtin_amdgcn_s_sleep(4);
      qv = ld4<16>(rws, area + A_Q + l16, 0);
      WAITV(0);
    }
    if (!ok) {
      const v4u xc = ld4<0>(rx, l16, 0);
      const f32x2 ls = ln_stats(xc, lane);
      const rsrc_t rwq = mkrs(wq, D * NQ * 2);
      f32x2 acc[4] = {{0.f, 0.f}, {0.f, 0.f}, {0.f, 0.f}, {0.f, 0.f}};
#pragma unroll 1
      for (int r = 0; r < D / 2; ++r) {
        unsigned xp = xc[0];
        xp = (r & 3) == 1 ? xc[1] : xp; xp = (r & 3) == 2 ? xc[2] : xp; xp = (r & 3) == 3 ? xc[3] : xp;
        const f32x2 xr = unp((unsigned)__builtin_amdgcn_readlane((int)xp, r >> 2));
        const v4u wa = ld4<0>(rwq, h * 1024 + l16, r * 2 * NQ * 2), wb = ld4<0>(rwq, h * 1024 + l16, (r * 2 + 1) * NQ * 2);
        const float x0 = ln_apply(xr.x, ls), x1 = ln_apply(xr.y, ls);
#pragma unroll
        for (int j = 0; j < 4; ++j) acc[j] += unp(wa[j]) * x0 + unp(wb[j]) * x1;
      }
#pragma unroll
      for (int j = 0; j < 4; ++j) qv[j] = pack2(acc[j].x, acc[j].y);
      WAITV(0);
    }
    *reinterpret_cast<v4u*>(&s_q[lane * 4]) = qv;
    LDS_FENCE();
    __builtin_amdgcn_s_waitcnt(0xC07F);
    __hip_atomic_store(&s_flag, 1, __ATOMIC_RELAXED, __HIP_MEMORY_SCOPE_WORKGROUP);
  } else {
    while (__builtin_amdgcn_readfirstlane(__hip_atomic_load(&s_flag, __ATOMIC_RELAXED, __HIP_MEMORY_SCOPE_WORKGROUP)) == 0) __builtin_amdgcn_s_sleep(1);
  }
  LDS_FENCE();
  const v4u q0 = *reinterpret_cast<const v4u*>(&s_q[(lane & 31) * 4]), q1 = *reinterpret_cast<const v4u*>(&s_q[128 + (lane & 31) * 4]);
  STAMP(1);
  SB();

  bool svc = member || leader;
  unsigned seenm = 0, last_echo = 0;
  bool echoed = false;
  auto service = [&]() {
    if (leader) {
      seenm |= (pv[0] == e1[0] ? 1u : 0u) | (pv[1] == e1[1] ? 2u : 0u) | (pv[2] == e1[2] ? 4u : 0u) | (pv[3] == e1[3] ? 8u : 0u);
      svc = __builtin_amdgcn_ballot_w64(seenm != 15u) != 0;
      st1c(go_ref, rws, (lane == 0 && !svc) ? WS_GO : OOB);
    } else {
      const unsigned req = (unsigned)__builtin_amdgcn_readfirstlane((int)((b & 2) ? ((b & 1) ? pv[3] : pv[2]) : ((b & 1) ? pv[1] : pv[0])));
      const unsigned go = (unsigned)__builtin_amdgcn_readlane((int)pv[0], 1);
      go_ref = echoed ? go_ref : go;
      svc = go == (unsigned)__builtin_amdgcn_readfirstlane((int)go_ref);
      st1c(req, rws, (lane == 0 && svc && !(echoed && req == last_echo)) ? WS_ECHO + b * 4 : OOB);
      echoed = true; last_echo = req;
      if (!svc) __hip_atomic_store(&s_go, 1, __ATOMIC_RELAXED, __HIP_MEMORY_SCOPE_WORKGROUP);
    }
    pv = ld4<16>(rws, svc ? poll_off : OOB, 0);
    SB();
  };

  Att st;
  st.m = -3.0e38f; st.l = 0.f;
#pragma unroll
  for (int j = 0; j < 4; ++j) { st.a0[j] = (f32x2){0.f, 0.f}; st.a1[j] = (f32x2){0.f, 0.f}; }
  v4u ring[8];
  unsigned ub = ubase(16 + w);
  {
    const unsigned ub2 = ub;
    if (svc) service();
    float p = phase_k(pre[0], q0, q1, st, lane, [&](int i) { ring[i] = ldk(ub2, i); });
    phase_v(pre[1], p, st, [&](int) {});
    p = phase_k(pre[2], q0, q1, st, lane, [&](int) {});
    phase_v(pre[3], p, st, [&](int) {});
    STAMP(19);
    if (lane < 56) {
      *reinterpret_cast<v4u*>(&s_w[(w * 2) * WROW + l16]) = wov[0];
      *reinterpret_cast<v4u*>(&s_w[(w * 2 + 1) * WROW + l16]) = wov[1];
      *reinterpret_cast<v4u*>(&s_w[(16 + w * 2) * WROW + l16]) = wdv[0];
      *reinterpret_cast<v4u*>(&s_w[(17 + w * 2) * WROW + l16]) = wdv[1];
    }
    SB();
  }
  unsigned pco = WS_POOL + (h * NG + pg) * 256, lco = 0;
  asm volatile("" : "+v"(pco), "+v"(lco));
  unsigned* const pctr = (unsigned*)(ws + pco);
  int* const lctr = &s_ctr + lco;
  int tk = 0;
  if (lane == 0) tk = __hip_atomic_fetch_add(lctr, 1, __ATOMIC_RELAXED, __HIP_MEMORY_SCOPE_WORKGROUP);
  bool dyn = false;
#pragma unroll 1
  for (int u = 2;; ++u) {
    if (!dyn) {
      tk = __builtin_amdgcn_readfirstlane(tk);
      if (tk >= cnt) {
        dyn = true;
        STAMP(7);
        for (int it = 0; it < SPIN_CAP; ++it) {
          if (__builtin_amdgcn_readfirstlane(__hip_atomic_load(&s_go, __ATOMIC_RELAXED, __HIP_MEMORY_SCOPE_WORKGROUP))) break;
          if (svc) service();
          __builtin_amdgcn_s_sleep(2);
        }
        STAMP(8);
      }
    }
    if (dyn) { if (lane == 0) tk = (int)__hip_atomic_fetch_add(pctr, 1u, __ATOMIC_RELAXED, __HIP_MEMORY_SCOPE_AGENT); }
    SB();
    const float p = phase_k(ring, q0, q1, st, lane, [&](int i) { ring[i] = ldv(ub, i); });
    asm volatile("" : "+v"(tk));
    tk = __builtin_amdgcn_readfirstlane(tk);
    const bool more = !dyn || (unsigned)tk < (unsigned)PSZ;
    const unsigned un = dyn ? (more ? (unsigned)(POOL0 + pg * PSZ + tk) * 8192u : OOB) : ubase(tk);
    STAMP(16 + 2 * u);
    if (svc) service();
    if (!dyn) { if (lane == 0) tk = __hip_atomic_fetch_add(lctr, 1, __ATOMIC_RELAXED, __HIP_MEMORY_SCOPE_WORKGROUP); }
    SB();
    phase_v(ring, p, st, [&](int i) { ring[i] = ldk(un, i); });
    asm volatile("" : "+v"(tk));
    STAMP(17 + 2 * u);
    if (svc) service();
    if (!more) break;
    ub = un;
  }
  STAMP(12);
  {
    float l = st.l;
    l += dpp<ROR2>(l); l += dpp<ROR4>(l); l += dpp<ROR8>(l); l = x32_add(l);
    float o[8];
#pragma unroll
    for (int j = 0; j < 4; ++j) { o[2 * j] = tr32(st.a0[j].x, st.a1[j].x); o[2 * j + 1] = tr32(st.a0[j].y, st.a1[j].y); }
    *reinterpret_cast<f32x4*>(&s_part[w][lane * 8]) = (f32x4){o[0], o[1], o[2], o[3]};
    *reinterpret_cast<f32x4*>(&s_part[w][lane * 8 + 4]) = (f32x4){o[4], o[5], o[6], o[7]};
    if (lane < 2) *reinterpret_cast<f32x2*>(&s_ml[w][lane * 2]) = (f32x2){st.m, l};
  }
  for (int it = 0; svc && it < SPIN_CAP; ++it) { service(); __builtin_amdgcn_s_sleep(2); }
  WAITV(0);
  LDS_FENCE(); bar(); LDS_FENCE();
  STAMP(2);
  const int cq = lane >> 2, rg = lane & 3;
  if (w == 2 || w == 3 || w == 4) {
    const int t = tid - 128, hd = (t >> 6) & 1;
    f32x4 ml[8];
#pragma unroll
    for (int k = 0; k < 8; ++k) ml[k] = *reinterpret_cast<const f32x4*>(&s_ml[k][0]);
    if (w < 4) {
      float M = -3.0e38f;
#pragma unroll
      for (int k = 0; k < 8; ++k) M = fmaxf(M, hd ? ml[k][2] : ml[k][0]);
      f32x4 o = {0.f, 0.f, 0.f, 0.f};
#pragma unroll
      for (int k = 0; k < 8; ++k) o += *reinterpret_cast<const f32x4*>(&s_part[k][t * 4]) * __builtin_amdgcn_exp2f(((hd ? ml[k][2] : ml[k][0]) - M) * EXP_C);
      st4c((v4u){canon(o[0]), canon(o[1]), canon(o[2]), canon(o[3])}, rws, area_of(t >> 2) + A_O1 + s * 64 + (t & 3) * 16);
    } else {
      float M0 = -3.0e38f, M1 = -3.0e38f, L0 = 0.f, L1 = 0.f;
#pragma unroll
      for (int k = 0; k < 8; ++k) { M0 = fmaxf(M0, ml[k][0]); M1 = fmaxf(M1, ml[k][2]); }
#pragma unroll
      for (int k = 0; k < 8; ++k) { L0 += ml[k][1] * __builtin_amdgcn_exp2f((ml[k][0] - M0) * EXP_C); L1 += ml[k][3] * __builtin_amdgcn_exp2f((ml[k][2] - M1) * EXP_C); }
      st2c((v2u){canon((lane & 16) ? M1 : M0), canon((lane & 16) ? L1 : L0)}, rws, (area_of(lane & 31) + A_ML + s * 8) | (lane < 32 ? 0u : OOB));
    }
  }
  if (w == 5 && isf) {
    const int j = (lane >> 3) & 3, pc = lane & 7, m = pc >> 2, col = j * 16 + (pc & 3) * 4;
    const f32x4 v = *reinterpret_cast<const f32x4*>(&s_mv[m][col]) + *reinterpret_cast<const f32x4*>(&s_mv[m + 2][col]);
    const int fd = (f & ~3) + j, hd = fd / 14;
    st4c((v4u){canon(v[0]), canon(v[1]), canon(v[2]), canon(v[3])}, rws,
         ((unsigned)((fd - hd * 14 + 8) * 8 + hd) * AREA + A_F + (f & 3) * 128 + pc * 16) | (lane < 32 ? 0u : OOB));
  }
  v2u wt[8];
#pragma unroll
  for (int i = 0; i < 8; ++i) wt[i] = *reinterpret_cast<const v2u*>(&s_w[((i >> 2) * 16 + rg * 4 + (i & 3)) * WROW + w * 112 + (cq < 14 ? cq : 0) * 8]);
  if (w == 0) {
    const unsigned oa = area + A_O1 + l16;
    const unsigned ma = area + A_ML + cq * 8;
    v4u da, db; v2u mA, mB;
    bool okA = false, okB = false;
    for (int it = 0; it < SPIN_CAP; ++it) {
      if (!okA) { da = ld4<16>(rws, oa, 0); mA = ld2<16>(rws, ma, 0); }
      if (!okB) { db = ld4<16>(rws, oa + 1024, 0); mB = ld2<16>(rws, ma + 128, 0); }
      okA = full4(da) && mA[0] != SENT && mA[1] != SENT;
      okB = full4(db) && mB[0] != SENT && mB[1] != SENT;
      if (__builtin_amdgcn_ballot_w64(!(okA && okB)) == 0) break;
      __builtin_amdgcn_s_sleep(2);
    }
    const float M = wave_max(fmaxf(asf(mA[0]), asf(mB[0])));
    const float ea = __builtin_amdgcn_exp2f((asf(mA[0]) - M) * EXP_C), eb = __builtin_amdgcn_exp2f((asf(mB[0]) - M) * EXP_C);
    float v[5];
#pragma unroll
    for (int j = 0; j < 4; ++j) v[j] = asf(da[j]) * ea + asf(db[j]) * eb;
    v[4] = asf(mA[1]) * ea + asf(mB[1]) * eb;
#pragma unroll
    for (int j = 0; j < 5; ++j) { v[j] += dpp<ROR4>(v[j]); v[j] += dpp<ROR8>(v[j]); v[j] = x16_add(v[j]); v[j] = x32_add(v[j]); }
    if (lane < 4) *reinterpret_cast<f32x4*>(&s_o[lane * 4]) = (f32x4){rbf(v[0] / v[4]), rbf(v[1] / v[4]), rbf(v[2] / v[4]), rbf(v[3] / v[4])};
  }
  if (w == 1) {
    f32x4 hv = {0.f, 0.f, 0.f, 0.f};
    if (isf) {
      v4u d;
      for (int it = 0; it < SPIN_CAP; ++it) {
        d = ld4<16>(rws, area + A_F + (lane & 31) * 16, 0);
        if (__builtin_amdgcn_ballot_w64(!full4(d)) == 0) break;
        __builtin_amdgcn_s_sleep(2);
      }
#pragma unroll
      for (int j = 0; j < 4; ++j) {
        float g = asf(d[j]);
        g += dpp<ROR8>(g); g = rbf(x16_add(g));
        const float u = dpp<ROR4>(g);
        hv[j] = rbf(rbf(g / (1.0f + __expf(-g))) * u);
      }
    }
    if (lane < 4) *reinterpret_cast<f32x4*>(&s_o[16 + lane * 4]) = hv;
  }
  LDS_FENCE(); bar(); LDS_FENCE();
  STAMP(3);
  {
    const f32x4 ov = *reinterpret_cast<const f32x4*>(&s_o[rg * 4]), hv = *reinterpret_cast<const f32x4*>(&s_o[16 + rg * 4]);
    f32x2 ya = {0.f, 0.f}, yb = {0.f, 0.f};
#pragma unroll
    for (int i = 0; i < 4; ++i) { ya += unp(wt[i][0]) * ov[i] + unp(wt[4 + i][0]) * hv[i]; yb += unp(wt[i][1]) * ov[i] + unp(wt[4 + i][1]) * hv[i]; }
    float y[4] = {ya.x, ya.y, yb.x, yb.y};
#pragma unroll
    for (int j = 0; j < 4; ++j) { y[j] += dpp<XOR1>(y[j]); y[j] += dpp<XOR2>(y[j]); }
    st4c((v4u){canon(y[0]), canon(y[1]), canon(y[2]), canon(y[3])}, rws, ((unsigned)(RED0 + w * 14 + cq) * AREA + A_Y2 + b * 16) | ((rg == 0 && cq < 14) ? 0u : OOB));
  }
  if (w == 0 && b >= RED0) {
    const v2u xo = ld2<0>(rx, (b - RED0) * 8, 0);
    const unsigned ya = area + A_Y2 + l16;
    v4u d[4];
    bool ok[4] = {false, false, false, false};
    for (int it = 0; it < SPIN_CAP; ++it) {
#pragma unroll
      for (int k = 0; k < 4; ++k) if (!ok[k]) d[k] = ld4<16>(rws, ya + k * 1024, 0);
#pragma unroll
      for (int k = 0; k < 4; ++k) ok[k] = full4(d[k]);
      if (__builtin_amdgcn_ballot_w64(!(ok[0] && ok[1] && ok[2] && ok[3])) == 0) break;
      __builtin_amdgcn_s_sleep(1);
    }
    float y[4];
#pragma unroll
    for (int j = 0; j < 4; ++j) y[j] = wave_sum((asf(d[0][j]) + asf(d[1][j])) + (asf(d[2][j]) + asf(d[3][j])));
    const f32x2 x01 = unp(xo[0]), x23 = unp(xo[1]);
    if (lane == 0) {
      v2u r;
      r[0] = f2bf(x01.x + y[0]) | (f2bf(x01.y + y[1]) << 16);
      r[1] = f2bf(x23.x + y[2]) | (f2bf(x23.y + y[3]) << 16);
      *reinterpret_cast<v2u*>(out + (b - RED0) * 4) = r;
    }
  }
  STAMP(4);
  STAMP_OUT();
}

extern "C" void kernel_launch(void* const* d_in, const int* in_sizes, int n_in,
                              void* d_out, int out_size, void* d_ws, size_t ws_size,
                              hipStream_t stream) {
  fused_kernel<<<NWG, NT, 0, stream>>>((const u16*)d_in[0], (const u16*)d_in[1], (const u16*)d_in[2], (const u16*)d_in[3],
                                       (const u16*)d_in[4], (const u16*)d_in[6], (const u16*)d_in[7], (const u16*)d_in[8],
                                       (u16*)d_out, (char*)d_ws);
}
```

```cpp
#include <hip/hip_runtime.h>
#include <hip/hip_bf16.h>
#include <stdint.h>

#define QPOS 24
#define PACE 8
#define QTRIES 8
#define NG 16
#define CQ 46
#define CF 48
#define CO 50
#define HM 5
#define LATE 1
#define RSLEEP 0
#define DIAG 0

typedef unsigned v4u __attribute__((ext_vector_type(4)));
typedef unsigned v2u __attribute__((ext_vector_type(2)));
typedef float f32x4 __attribute__((ext_vector_type(4)));
typedef float f32x2 __attribute__((ext_vector_type(2)));
typedef __bf16 bf16x2 __attribute__((ext_vector_type(2)));
typedef unsigned short u16;
typedef __amdgpu_buffer_rsrc_t rsrc_t;

constexpr int D = 448, SK = 32768, DH = 256, F = 1792, NQ = 4096;
constexpr int NWG = 256, NT = 512;
constexpr unsigned OOB = 0x80000000u;
constexpr unsigned SENT = 0xFFFFFFFFu;
constexpr unsigned AREA = 9216;
constexpr unsigned A_Q = 0;
constexpr unsigned A_O1 = 1024;
constexpr unsigned A_F = 5120;
constexpr unsigned A_Y2 = 1024;
constexpr unsigned WS_REQ = NWG * AREA;
constexpr unsigned WS_ECHO = WS_REQ + 1024;
constexpr unsigned WS_GO = WS_ECHO + 1024;
constexpr unsigned WS_POOL = WS_GO + 256;
constexpr unsigned WS_END = WS_POOL + 8 * NG * 256;
constexpr int SPIN_CAP = 1 << 18;

__device__ __forceinline__ float asf(unsigned u) { return __builtin_bit_cast(float, u); }
__device__ __forceinline__ unsigned asu(float f) { return __builtin_bit_cast(unsigned, f); }
__device__ __forceinline__ float rbf(float f) { __bf16 b = (__bf16)f; return (float)b; }
__device__ __forceinline__ unsigned f2bf(float f) { __bf16 b = (__bf16)f; return (unsigned)__builtin_bit_cast(u16, b); }
__device__ __forceinline__ float dot2(unsigned k, unsigned q, float acc) {
  return __builtin_amdgcn_fdot2_f32_bf16(__builtin_bit_cast(bf16x2, k), __builtin_bit_cast(bf16x2, q), acc, false);
}
__device__ __forceinline__ f32x2 unp(unsigned u) { f32x2 r; r.x = asf(u << 16); r.y = asf(u & 0xffff0000u); return r; }
template <int CTRL> __device__ __forceinline__ float dpp(float x) {
  return asf((unsigned)__builtin_amdgcn_update_dpp(0, (int)asu(x), CTRL, 0xf, 0xf, true));
}
constexpr int XOR1 = 0xB1, XOR2 = 0x4E, XOR7 = 0x141, ROR8 = 0x128, ROR4 = 0x124, ROR2 = 0x122, BCAST = 0x150;
constexpr int QP_EVEN = 0xA0, QP_ODD = 0xF5;
__device__ __forceinline__ float x16_add(float x) { auto s = __builtin_amdgcn_permlane16_swap(asu(x), asu(x), false, false); return asf(s[0]) + asf(s[1]); }
__device__ __forceinline__ float x32_add(float x) { auto s = __builtin_amdgcn_permlane32_swap(asu(x), asu(x), false, false); return asf(s[0]) + asf(s[1]); }
__device__ __forceinline__ float x16_max(float x) { auto s = __builtin_amdgcn_permlane16_swap(asu(x), asu(x), false, false); return fmaxf(asf(s[0]), asf(s[1])); }
__device__ __forceinline__ float x32_max(float x) { auto s = __builtin_amdgcn_permlane32_swap(asu(x), asu(x), false, false); return fmaxf(asf(s[0]), asf(s[1])); }
__device__ __forceinline__ float tr32(float a, float b) { auto s = __builtin_amdgcn_permlane32_swap(asu(a), asu(b), false, false); return asf(s[0]) + asf(s[1]); }
__device__ __forceinline__ float tr16(float a, float b) { auto s = __builtin_amdgcn_permlane16_swap(asu(a), asu(b), false, false); return asf(s[0]) + asf(s[1]); }
__device__ __forceinline__ float wave_sum(float v) {
  v += dpp<XOR1>(v); v += dpp<XOR2>(v); v += dpp<XOR7>(v); v += dpp<ROR8>(v);
  v = x16_add(v); return x32_add(v);
}
__device__ __forceinline__ float wave_max(float v) {
  v = fmaxf(v, dpp<XOR1>(v)); v = fmaxf(v, dpp<XOR2>(v)); v = fmaxf(v, dpp<XOR7>(v)); v = fmaxf(v, dpp<ROR8>(v));
  v = x16_max(v); return x32_max(v);
}
__device__ __forceinline__ rsrc_t mkrs(const void* p, unsigned bytes) { return __builtin_amdgcn_make_buffer_rsrc((void*)p, 0, (int)bytes, 0x00020000); }
template <int AUX> __device__ __forceinline__ v4u ld4(rsrc_t r, unsigned vo, unsigned so) { return __builtin_amdgcn_raw_buffer_load_b128(r, (int)vo, (int)so, AUX); }
template <int AUX> __device__ __forceinline__ v2u ld2(rsrc_t r, unsigned vo, unsigned so) { return __builtin_amdgcn_raw_buffer_load_b64(r, (int)vo, (int)so, AUX); }
__device__ __forceinline__ void st4c(v4u v, rsrc_t r, unsigned vo) { __builtin_amdgcn_raw_buffer_store_b128(v, r, (int)vo, 0, 16); }
__device__ __forceinline__ void st2c(v2u v, rsrc_t r, unsigned vo) { __builtin_amdgcn_raw_buffer_store_b64(v, r, (int)vo, 0, 16); }
__device__ __forceinline__ void st1c(unsigned v, rsrc_t r, unsigned vo) { __builtin_amdgcn_raw_buffer_store_b32(v, r, (int)vo, 0, 16); }
__device__ __forceinline__ unsigned canon(float f) { unsigned u = asu(f); return u < 0xFFFFFFFEu ? u : 0xFFFFFFFEu; }
__device__ __forceinline__ bool full4(v4u v) { return v[0] != SENT && v[1] != SENT && v[2] != SENT && v[3] != SENT; }
#define SB() __builtin_amdgcn_sched_barrier(0)
__device__ __forceinline__ void bar() { __builtin_amdgcn_s_waitcnt(0xC07F); __builtin_amdgcn_s_barrier(); }
#define WAITV(n) __builtin_amdgcn_s_waitcnt(((n) & 15) | (((n) >> 4) << 14) | 0x0F70)
template <int V> struct IC { static constexpr int value = V; constexpr operator int() const { return V; } };
template <int A, int B, class Fn> __device__ __forceinline__ void static_for(Fn&& f) { if constexpr (A < B) { f(IC<A>{}); static_for<A + 1, B>(f); } }
#define LDS_FENCE() asm volatile("" ::: "memory")

constexpr float EXP_C = 0.0625f * 1.4426950408889634f;

struct Att {
  float m, l;
  f32x2 a0[4], a1[4];
};

template <int NL, class ISSUE>
__device__ __forceinline__ float phase_k(v4u (&kb)[8], const v4u& q0, const v4u& q1, Att& st, int lane, ISSUE issue) {
  float x[2 * NL];
#pragma unroll
  for (int i = 0; i < NL; ++i) {
    float a = 0.f, b = 0.f;
#pragma unroll
    for (int j = 0; j < 4; ++j) { a = dot2(kb[i][j], q0[j], a); b = dot2(kb[i][j], q1[j], b); }
    x[2 * i] = a; x[2 * i + 1] = b;
    asm volatile("" : "+v"(x[2 * i]), "+v"(x[2 * i + 1]));
    issue(i);
    SB();
  }
#define TR_STEP(HALF, CTRL, BIT) _Pragma("unroll") for (int v = 0; v < (HALF); ++v) { \
    const float lo_ = x[v], hi_ = x[v + (HALF)]; const bool s_ = lane & (BIT); \
    x[v] = (s_ ? hi_ : lo_) + dpp<CTRL>(s_ ? lo_ : hi_); }
  if constexpr (NL == 8) { TR_STEP(8, ROR8, 8) }
  TR_STEP(4, XOR7, 4) TR_STEP(2, XOR2, 2) TR_STEP(1, XOR1, 1)
#undef TR_STEP
  if constexpr (NL == 4) x[0] += dpp<ROR8>(x[0]);
  const float s = rbf(x16_add(x[0]));
  float mx = fmaxf(s, dpp<ROR2>(s)); mx = fmaxf(mx, dpp<ROR4>(mx));
  if constexpr (NL == 8) mx = fmaxf(mx, dpp<ROR8>(mx));
  mx = x32_max(mx);
  const float mn = fmaxf(st.m, mx);
  const float al = __builtin_amdgcn_exp2f((st.m - mn) * EXP_C);
  const float p = __builtin_amdgcn_exp2f((s - mn) * EXP_C);
  st.m = mn; st.l = st.l * al + (NL == 8 ? p : p * 0.5f);
  const float al0 = dpp<QP_EVEN>(al), al1 = dpp<QP_ODD>(al);
#pragma unroll
  for (int j = 0; j < 4; ++j) { st.a0[j] *= al0; st.a1[j] *= al1; }
  SB();
  return p;
}
template <int I, int O, class ISSUE>
__device__ __forceinline__ void pv_step(v4u (&vb)[8], float p, Att& st, ISSUE& issue) {
  const float p0 = dpp<BCAST + 2 * I>(p), p1 = dpp<BCAST + 2 * I + 1>(p);
#pragma unroll
  for (int j = 0; j < 4; ++j) { const f32x2 v = unp(vb[O + I][j]); st.a0[j] += v * p0; st.a1[j] += v * p1; }
  asm volatile("" : "+v"(st.a0[0]), "+v"(st.a0[1]), "+v"(st.a0[2]), "+v"(st.a0[3]), "+v"(st.a1[0]), "+v"(st.a1[1]), "+v"(st.a1[2]), "+v"(st.a1[3]));
  issue(O + I);
  SB();
}
template <int NL, class ISSUE>
__device__ __forceinline__ void phase_v(v4u (&vb)[8], float p, Att& st, ISSUE issue) {
  constexpr int O = 8 - NL;
  pv_step<0, O>(vb, p, st, issue); pv_step<1, O>(vb, p, st, issue); pv_step<2, O>(vb, p, st, issue); pv_step<3, O>(vb, p, st, issue);
  if constexpr (NL == 8) { pv_step<4, O>(vb, p, st, issue); pv_step<5, O>(vb, p, st, issue); pv_step<6, O>(vb, p, st, issue); pv_step<7, O>(vb, p, st, issue); }
}

#define STAMP(k) do {} while (0)
#define STAMP_OUT() do {} while (0)

__device__ __forceinline__ f32x2 ln_stats(const v4u& xa, int lane) {
  f32x2 xv[4];
  float sm = 0.f;
#pragma unroll
  for (int j = 0; j < 4; ++j) { xv[j] = unp(xa[j]); sm += xv[j].x + xv[j].y; }
  const float mean = wave_sum(sm) * (1.0f / D);
  float sq = 0.f;
#pragma unroll
  for (int j = 0; j < 4; ++j) { const f32x2 d = xv[j] - mean; sq += d.x * d.x + d.y * d.y; }
  sq = lane < 56 ? sq : 0.f;
  const float var = rbf(wave_sum(sq) * (1.0f / D));
  const float sd = rbf(sqrtf(rbf(var + 1e-5f)));
  return (f32x2){rbf(mean), 1.0f / sd};
}
__device__ __forceinline__ float ln_apply(float x, f32x2 st) { return rbf(rbf(x - st.x) * st.y); }
__device__ __forceinline__ unsigned pack2(float a, float b) { const unsigned u = f2bf(a) | (f2bf(b) << 16); return u < 0xFFFFFFFEu ? u : 0xFFFFFFFEu; }

__global__ void __launch_bounds__(NT, 2)
fused_kernel(const u16* __restrict__ xg, const u16* __restrict__ kc, const u16* __restrict__ vc, const u16* __restrict__ wq,
             const u16* __restrict__ wo, const u16* __restrict__ wg, const u16* __restrict__ wu, const u16* __restrict__ wd,
             u16* __restrict__ out, char* ws) {
  __shared__ __attribute__((aligned(16))) float s_part[8][512];
  __shared__ __attribute__((aligned(16))) float s_mv[8][64];
  __shared__ __attribute__((aligned(16))) unsigned s_q[256];
  __shared__ __attribute__((aligned(16))) u16 s_qs[64];
  __shared__ __attribute__((aligned(16))) float s_ml[8][4];
  __shared__ __attribute__((aligned(16))) float s_o[4][32];
  __shared__ __attribute__((aligned(16))) float s_ml2[8];
  __shared__ __attribute__((aligned(16))) float s_h[16];
  __shared__ int s_flag, s_ctr, s_go, s_fc;
  const int tid = threadIdx.x, lane = tid & 63, w = __builtin_amdgcn_readfirstlane(tid >> 6);
  const int bi = blockIdx.x, h = (((bi & 1) ? bi + 5 : bi) & 6) | (bi >> 7), s = ((bi >> 3) & 15) * 2 + (bi & 1);
  const int b = h * 32 + s;
  constexpr int PH = 2 * (2048 - 8 * CQ - 14 * CF - 10 * CO), GS = 32 / NG;
  auto prefix = [](int n) { return n < 8 ? n * CQ : n < 22 ? 8 * CQ + (n - 8) * CF : 8 * CQ + 14 * CF + (n - 22) * CO; };
  const int pg = s / GS;
  const int first = 2 * prefix(s) + pg * PH / NG, cnt = s < 8 ? CQ : s < 22 ? CF : CO;
  const int pool0 = 2 * prefix((pg + 1) * GS) + pg * PH / NG, psz = (pg + 1) * PH / NG - pg * PH / NG;
  asm volatile("" :: "s"(xg), "s"(kc), "s"(vc), "s"(wq), "s"(wo), "s"(wg), "s"(wu), "s"(wd), "s"(out), "s"(ws));
  STAMP(0);
  const rsrc_t rx = mkrs(xg, D * 2);
  const rsrc_t rk = mkrs(kc + (size_t)h * SK * DH, SK * DH * 2);
  const rsrc_t rv = mkrs(vc + (size_t)h * SK * DH, SK * DH * 2);
  const rsrc_t rws = mkrs(ws, WS_END);
  const unsigned l16 = lane * 16;
  auto ubase = [&](int u) -> unsigned { return (unsigned)(first + 2 * u) * 4096u; };
  auto ldk = [&](unsigned base, int i) { return ld4<2>(rk, l16 + (i & 3) * 1024, base + (i >> 2) * 4096); };
  auto ldv = [&](unsigned base, int i) { return ld4<2>(rv, l16 + (i & 3) * 1024, base + (i >> 2) * 4096); };
  const unsigned area = (unsigned)b * AREA;
  auto area_of = [&](int ss) -> unsigned { return (unsigned)(h * 32 + ss) * AREA; };
  const unsigned lmask = lane < 56 ? 0u : OOB;

  const bool isq = s < 8, isf = s >= 8 && s < 22, isy = s >= 16;
  const int f = h * 14 + s - 8, y = h * 16 + s - 16, ro = h * 4 + (s >> 2);
  const bool isr = s < 16 && ((s + h) & 3) == 0 && ro < 28;
  const rsrc_t rwo = mkrs(wo + (size_t)(isy ? y : 0) * 32 * D, (isy && w < 7) ? 32 * D * 2 : 0);
  const rsrc_t rwd = mkrs(wd + (size_t)((isy && y < 112) ? y : 0) * 16 * D, (isy && y < 112 && w < 7) ? 16 * D * 2 : 0);
  const unsigned wo_off = (lane >> 3) * (4 * D * 2) + w * 128 + (lane & 7) * 16, wd_off = (lane >> 3) * (D * 2) + w * 128 + (lane & 7) * 16;
  const bool tile = isq || (isf && w < 4);
  const bool leader = (b == NWG - 1 && w == 1), member = (w == 0);

  const v4u sent = {SENT, SENT, SENT, SENT};
  unsigned poll_off = lane == 1 ? WS_GO : OOB;
  poll_off = lane == 0 ? WS_REQ + (b >> 2) * 16 : poll_off;
  poll_off = leader ? WS_ECHO + l16 : poll_off;
  v4u pv = {0u, 0u, 0u, 0u}, e1 = {0u, 0u, 0u, 0u};
  unsigned go_ref = 0;
  if (member) {
    st4c(sent, rws, area + A_Q + l16);
    __hip_atomic_store(&s_flag, 0, __ATOMIC_RELAXED, __HIP_MEMORY_SCOPE_WORKGROUP);
    __hip_atomic_store(&s_ctr, 24, __ATOMIC_RELAXED, __HIP_MEMORY_SCOPE_WORKGROUP);
    __hip_atomic_store(&s_go, 0, __ATOMIC_RELAXED, __HIP_MEMORY_SCOPE_WORKGROUP);
    __hip_atomic_store(&s_fc, 0, __ATOMIC_RELAXED, __HIP_MEMORY_SCOPE_WORKGROUP);
    st1c(0u, rws, (lane == 0 && s == pg * GS) ? WS_POOL + (h * NG + pg) * 256 : OOB);
  }
  SB();
  if (tile) {
    v4u tw[8];
    const int oct = isq ? 7 * w : (f & 3) * 14 + (w >> 1) * 7;
    const int stride = isq ? NQ * 2 : F * 2;
    const rsrc_t rt = mkrs(isq ? wq : (w & 1) ? wu : wg, D * stride);
    const unsigned to = (unsigned)((oct + (lane >> 3)) * 8 * stride + (isq ? h * 8 + s : f >> 2) * 128 + (lane & 7) * 16) | lmask;
    const v4u xa = ld4<0>(rx, l16, 0);
    const v4u xb = ld4<0>(rx, (unsigned)(oct + (lane >> 3)) * 16 | lmask, 0);
    SB();
#pragma unroll
    for (int i = 0; i < 8; ++i) { tw[i] = ld4<2>(rt, to, i * stride); SB(); }
    LDS_FENCE(); bar(); LDS_FENCE();
    SB();
    const f32x2 ls = ln_stats(xa, lane);
    f32x2 acc[4];
#pragma unroll
    for (int i = 0; i < 8; ++i) {
      const float xn = ln_apply(asf((i & 1) ? (xb[i >> 1] & 0xffff0000u) : (xb[i >> 1] << 16)), ls);
#pragma unroll
      for (int j = 0; j < 4; ++j) acc[j] = i ? acc[j] + unp(tw[i][j]) * xn : unp(tw[i][j]) * xn;
    }
    float y0 = tr32(acc[0].x, acc[2].x), y1 = tr32(acc[0].y, acc[2].y), y2 = tr32(acc[1].x, acc[3].x), y3 = tr32(acc[1].y, acc[3].y);
    y0 = tr16(y0, y2); y1 = tr16(y1, y3);
    const bool s_ = lane & 8;
    y0 = (s_ ? y1 : y0) + dpp<ROR8>(s_ ? y0 : y1);
    s_mv[w][(lane & 7) * 8 + (lane >> 3)] = y0;
    STAMP(6);
    if (isq) {
      LDS_FENCE(); bar(); LDS_FENCE();
      if (w < 4) {
        float a = 0.f;
#pragma unroll
        for (int k = 0; k < 8; ++k) a += s_mv[k][lane];
        s_qs[lane] = (u16)f2bf(a);
        LDS_FENCE();
        v4u qs = *reinterpret_cast<const v4u*>(&s_qs[(lane & 7) * 8]);
#pragma unroll
        for (int j = 0; j < 4; ++j) qs[j] = qs[j] < 0xFFFFFFFEu ? qs[j] : 0xFFFFFFFEu;
        st4c(qs, rws, area_of(w * 8 + (lane >> 3)) + A_Q + s * 128 + (lane & 7) * 16);
      }
    } else {
      LDS_FENCE();
      __builtin_amdgcn_s_waitcnt(0xC07F);
      if (lane == 0) __hip_atomic_fetch_add(&s_fc, 1, __ATOMIC_RELAXED, __HIP_MEMORY_SCOPE_WORKGROUP);
    }
  } else if (leader) {
    e1 = ld4<16>(rws, poll_off, 0);
    go_ref = __builtin_amdgcn_raw_buffer_load_b32(rws, (int)WS_GO, 0, 16);
    SB();
    LDS_FENCE(); bar(); LDS_FENCE();
    SB();
    e1 += 1u;
    go_ref += 1u;
    st4c(e1, rws, WS_REQ + l16);
  } else {
    LDS_FENCE(); bar(); LDS_FENCE();
  }
  SB();
  if (member && (isy || isr)) {
#pragma unroll
    for (int i = 1; i < 6; ++i) st4c(sent, rws, area + i * 1024 + l16);
    if (isr) {
#pragma unroll
      for (int i = 6; i < 9; ++i) st4c(sent, rws, area + i * 1024 + l16);
    }
  }
  STAMP(5);
  SB();
  v4u pre[4][8];
  v4u qv;
  const unsigned ub0 = ubase(w), ub1 = ubase(8 + w);
  static_for<0, 32>([&](auto N) {
    constexpr int n = N;
    if constexpr (n == QPOS) {
      STAMP(9);
      qv = ld4<16>(rws, member ? area + A_Q + l16 : OOB, 0);
      pv = ld4<16>(rws, (member || leader) ? poll_off : OOB, 0);
      SB();
    }
    if constexpr (n >= PACE) WAITV(PACE - 1 + ((n >= QPOS && n < QPOS + PACE) ? 2 : 0) + ((!LATE && n < 4 + PACE) ? 6 : 0));
    pre[n >> 3][n & 7] = (n & 8) ? ldv((n & 16) ? ub1 : ub0, n & 7) : ldk((n & 16) ? ub1 : ub0, n & 7);
    SB();
    if constexpr (n - PACE == 7 || n - PACE == 15 || n - PACE == 23) STAMP(16 + (n - PACE) / 8);
  });
  STAMP(10);
  if (member) {
    int tries = 0;
    bool ok;
    for (;; ++tries) {
      ok = __builtin_amdgcn_ballot_w64(!full4(qv)) == 0;
      if (ok || tries >= QTRIES) break;
      __builtin_amdgcn_s_sleep(4);
      qv = ld4<16>(rws, area + A_Q + l16, 0);
      WAITV(0);
    }
    if (!ok) {
      const v4u xc = ld4<0>(rx, l16, 0);
      const f32x2 ls = ln_stats(xc, lane);
      const rsrc_t rwq = mkrs(wq, D * NQ * 2);
      f32x2 acc[4] = {{0.f, 0.f}, {0.f, 0.f}, {0.f, 0.f}, {0.f, 0.f}};
#pragma unroll 1
      for (int r = 0; r < D / 2; ++r) {
        unsigned xp = xc[0];
        xp = (r & 3) == 1 ? xc[1] : xp; xp = (r & 3) == 2 ? xc[2] : xp; xp = (r & 3) == 3 ? xc[3] : xp;
        const f32x2 xr = unp((unsigned)__builtin_amdgcn_readlane((int)xp, r >> 2));
        const v4u wa = ld4<0>(rwq, h * 1024 + l16, r * 2 * NQ * 2), wb = ld4<0>(rwq, h * 1024 + l16, (r * 2 + 1) * NQ * 2);
        const float x0 = ln_apply(xr.x, ls), x1 = ln_apply(xr.y, ls);
#pragma unroll
        for (int j = 0; j < 4; ++j) acc[j] += unp(wa[j]) * x0 + unp(wb[j]) * x1;
      }
#pragma unroll
      for (int j = 0; j < 4; ++j) qv[j] = pack2(acc[j].x, acc[j].y);
      WAITV(0);
    }
    *reinterpret_cast<v4u*>(&s_q[lane * 4]) = qv;
    LDS_FENCE();
    __builtin_amdgcn_s_waitcnt(0xC07F);
    __hip_atomic_store(&s_flag, 1, __ATOMIC_RELAXED, __HIP_MEMORY_SCOPE_WORKGROUP);
  } else {
    while (__builtin_amdgcn_readfirstlane(__hip_atomic_load(&s_flag, __ATOMIC_RELAXED, __HIP_MEMORY_SCOPE_WORKGROUP)) == 0) __builtin_amdgcn_s_sleep(1);
  }
  LDS_FENCE();
  const v4u q0 = *reinterpret_cast<const v4u*>(&s_q[(lane & 31) * 4]), q1 = *reinterpret_cast<const v4u*>(&s_q[128 + (lane & 31) * 4]);
  STAMP(1);
  SB();

  bool svc = member || leader;
  unsigned seenm = 0, last_echo = 0;
  bool echoed = false;
  auto service = [&]() {
    if (leader) {
      seenm |= (pv[0] == e1[0] ? 1u : 0u) | (pv[1] == e1[1] ? 2u : 0u) | (pv[2] == e1[2] ? 4u : 0u) | (pv[3] == e1[3] ? 8u : 0u);
      svc = __builtin_amdgcn_ballot_w64(seenm != 15u) != 0;
      st1c(go_ref, rws, (lane == 0 && !svc) ? WS_GO : OOB);
    } else {
      const unsigned req = (unsigned)__builtin_amdgcn_readfirstlane((int)((b & 2) ? ((b & 1) ? pv[3] : pv[2]) : ((b & 1) ? pv[1] : pv[0])));
      const unsigned go = (unsigned)__builtin_amdgcn_readlane((int)pv[0], 1);
      go_ref = echoed ? go_ref : go;
      svc = go == (unsigned)__builtin_amdgcn_readfirstlane((int)go_ref);
      st1c(req, rws, (lane == 0 && svc && !(echoed && req == last_echo)) ? WS_ECHO + b * 4 : OOB);
      echoed = true; last_echo = req;
      if (!svc) {
        __hip_atomic_store(&s_go, 1, __ATOMIC_RELAXED, __HIP_MEMORY_SCOPE_WORKGROUP);
        if (isf) {
          while (__builtin_amdgcn_readfirstlane(__hip_atomic_load(&s_fc, __ATOMIC_RELAXED, __HIP_MEMORY_SCOPE_WORKGROUP)) != 4) __builtin_amdgcn_s_sleep(1);
          LDS_FENCE();
          const int j = (lane >> 3) & 3, pc = lane & 7, m = pc >> 2, col = j * 16 + (pc & 3) * 4;
          const f32x4 v = *reinterpret_cast<const f32x4*>(&s_mv[m][col]) + *reinterpret_cast<const f32x4*>(&s_mv[m + 2][col]);
          const int fd = (f & ~3) + j;
          st4c((v4u){canon(v[0]), canon(v[1]), canon(v[2]), canon(v[3])}, rws,
               ((unsigned)((fd >> 4) * 32 + 16 + (fd & 15)) * AREA + A_F + (f & 3) * 128 + pc * 16) | (lane < 32 ? 0u : OOB));
        }
      }
    }
    pv = ld4<16>(rws, svc ? poll_off : OOB, 0);
    SB();
  };

  Att st;
  st.m = -3.0e38f; st.l = 0.f;
#pragma unroll
  for (int j = 0; j < 4; ++j) { st.a0[j] = (f32x2){0.f, 0.f}; st.a1[j] = (f32x2){0.f, 0.f}; }
  v4u ring[8];
  unsigned ub = ubase(16 + w);
  {
    const unsigned ub2 = ub;
    if (svc) service();
    float p = phase_k<8>(pre[0], q0, q1, st, lane, [&](int i) { ring[i] = ldk(ub2, i); });
    phase_v<8>(pre[1], p, st, [&](int) {});
    p = phase_k<8>(pre[2], q0, q1, st, lane, [&](int) {});
    phase_v<8>(pre[3], p, st, [&](int) {});
    STAMP(19);
    SB();
  }
  unsigned lco = 0;
  asm volatile("" : "+v"(lco));
  int* const lctr = &s_ctr + lco;
  const unsigned pco = lane == 0 ? WS_POOL + (h * NG + pg) * 256 : OOB;
  int tk = 0, sx = 20;
  if (lane == 0) tk = __hip_atomic_fetch_add(lctr, 1, __ATOMIC_RELAXED, __HIP_MEMORY_SCOPE_WORKGROUP);
#pragma unroll 1
  for (;;) {
    const int t = __builtin_amdgcn_readfirstlane(tk);
    if (t >= cnt) break;
    const unsigned un = ubase(t);
    SB();
    const float p = phase_k<8>(ring, q0, q1, st, lane, [&](int i) { ring[i] = ldv(ub, i); });
    STAMP(sx); ++sx;
    if (svc) service();
    if (lane == 0) tk = __hip_atomic_fetch_add(lctr, 1, __ATOMIC_RELAXED, __HIP_MEMORY_SCOPE_WORKGROUP);
    SB();
    phase_v<8>(ring, p, st, [&](int i) { ring[i] = ldk(un, i); });
    asm volatile("" : "+v"(tk));
    STAMP(sx); ++sx;
    if (svc) service();
    ub = un;
  }
  STAMP(7);
  for (int it = 0; it < SPIN_CAP; ++it) {
    if (__builtin_amdgcn_readfirstlane(__hip_atomic_load(&s_go, __ATOMIC_RELAXED, __HIP_MEMORY_SCOPE_WORKGROUP))) break;
    if (svc) service();
    __builtin_amdgcn_s_sleep(2);
  }
  STAMP(8);
  {
    rsrc_t nra, nrb;
    unsigned nva, nvb, nb, nsa, nsb;
    bool more;
    int gt = __builtin_amdgcn_raw_ptr_buffer_atomic_add_i32(1, rws, (int)pco, 0, 0);
    auto next_half = [&]() {
      const int t = __builtin_amdgcn_readfirstlane(gt);
      more = (unsigned)t < (unsigned)psz;
      nb = more ? (unsigned)(pool0 + t) * 4096u : LATE ? 0u : OOB;
      nra = more ? rk : rwo; nrb = more ? rv : rwd;
      nva = more ? l16 : wo_off; nvb = more ? l16 : wd_off;
      nsa = more ? 1024 : D * 2; nsb = more ? 1024 : 8 * D * 2;
      gt = __builtin_amdgcn_raw_ptr_buffer_atomic_add_i32(1, rws, (int)(more ? pco : OOB), 0, 0);
      SB();
    };
    auto ldn = [&](int i) { ring[i] = i < 4 ? ld4<2>(nra, nva, nb + i * nsa) : ld4<2>(nrb, nvb, nb + (i - 4) * nsb); };
    SB();
    const float p = phase_k<8>(ring, q0, q1, st, lane, [&](int i) { ring[i] = ldv(ub, i); });
    STAMP(sx); ++sx;
    next_half();
    phase_v<8>(ring, p, st, ldn);
    STAMP(sx); ++sx;
#pragma unroll 1
    while (more) {
      next_half();
      const float p = phase_k<4>(ring, q0, q1, st, lane, ldn);
      phase_v<4>(ring, p, st, ldn);
      STAMP(sx); ++sx;
    }
    asm volatile("" :: "v"(gt));
  }
  STAMP(12);
  {
    float l = st.l;
    l += dpp<ROR2>(l); l += dpp<ROR4>(l); l += dpp<ROR8>(l); l = x32_add(l);
    float o[8];
#pragma unroll
    for (int j = 0; j < 4; ++j) { o[2 * j] = tr32(st.a0[j].x, st.a1[j].x); o[2 * j + 1] = tr32(st.a0[j].y, st.a1[j].y); }
    *reinterpret_cast<f32x4*>(&s_part[w][lane * 8]) = (f32x4){o[0], o[1], o[2], o[3]};
    *reinterpret_cast<f32x4*>(&s_part[w][lane * 8 + 4]) = (f32x4){o[4], o[5], o[6], o[7]};
    if (lane < 2) *reinterpret_cast<f32x2*>(&s_ml[w][lane * 2]) = (f32x2){st.m, l};
  }
  LDS_FENCE(); bar(); LDS_FENCE();
  STAMP(2);
  if (w == 4 || w == 5) {
    const int t = tid - 256;
    f32x2 ml[8];
#pragma unroll
    for (int k = 0; k < 8; ++k) ml[k] = *reinterpret_cast<const f32x2*>(&s_ml[k][(w - 4) * 2]);
    float M = -3.0e38f, L = 0.f;
#pragma unroll
    for (int k = 0; k < 8; ++k) M = fmaxf(M, ml[k].x);
    f32x4 o = {0.f, 0.f, 0.f, 0.f};
#pragma unroll
    for (int k = 0; k < 8; ++k) {
      const float e = __builtin_amdgcn_exp2f((ml[k].x - M) * EXP_C);
      o += *reinterpret_cast<const f32x4*>(&s_part[k][t * 4]) * e; L += ml[k].y * e;
    }
    v4u msg;
#pragma unroll
    for (int j = 0; j < 3; ++j) { const unsigned u = asu(o[j]); msg[j] = __builtin_amdgcn_perm(u < 0xFFFFFE00u ? u : 0xFFFFFE00u, asu(o[3]), 0x07060501u + j); }
    msg[3] = canon((t & 1) ? L : M);
    st4c(msg, rws, area_of(16 + (t >> 3)) + A_O1 + s * 128 + (t & 7) * 16);
    SB();
    asm volatile("" :: "v"(ring[0]), "v"(ring[1]), "v"(ring[2]), "v"(ring[3]), "v"(ring[4]), "v"(ring[5]), "v"(ring[6]), "v"(ring[7]), "v"(pv));
  }
  if (isy) {
    const unsigned pa = w < 4 ? area + A_O1 + w * 1024 + l16 : (w == 7 && y < 112) ? area + A_F + (lane & 31) * 16 : OOB;
    v4u d = ld4<16>(rws, pa, 0);
    SB();
    f32x2 wf[6][4];
#pragma unroll
    for (int i = 0; i < 6; ++i)
#pragma unroll
      for (int j = 0; j < 4; ++j) {
        wf[i][j] = unp(ring[i][j]);
        asm volatile("" : "+v"(wf[i][j]));
      }
    SB();
    STAMP(14);
    for (int it = 0; it < SPIN_CAP; ++it) {
      if (__builtin_amdgcn_ballot_w64(!full4(d)) == 0) break;
      __builtin_amdgcn_s_sleep(1);
      d = ld4<16>(rws, pa, 0);
    }
    STAMP(15);
    if (w < 4) {
      const float ot = dpp<XOR1>(asf(d[3]));
      const float m = (lane & 1) ? ot : asf(d[3]), l = (lane & 1) ? asf(d[3]) : ot;
      float M = fmaxf(m, dpp<ROR8>(m)); M = x16_max(M); M = x32_max(M);
      const float e = __builtin_amdgcn_exp2f((m - M) * EXP_C);
      const float v3 = asf(__builtin_amdgcn_perm(__builtin_amdgcn_perm(d[2], d[1], 0x04000c0cu), d[0], 0x0706000cu));
      float y0 = tr32(asf(d[0]) * e, asf(d[2]) * e), y1 = tr32(asf(d[1]) * e, v3 * e), L = x32_add(l * e);
      y0 = tr16(y0, y1); L = x16_add(L);
      const bool s_ = lane & 8;
      y0 = (s_ ? L : y0) + dpp<ROR8>(s_ ? y0 : L);
      if (!s_) s_o[w][(lane & 7) * 4 + (lane >> 5) * 2 + ((lane >> 4) & 1)] = y0;
      if (lane == 8) *reinterpret_cast<f32x2*>(&s_ml2[w * 2]) = (f32x2){M, y0};
    } else if (w == 7) {
      float hv[4];
#pragma unroll
      for (int j = 0; j < 4; ++j) {
        float g = asf(d[j]);
        g += dpp<ROR8>(g); g = rbf(x16_add(g));
        const float u = dpp<ROR4>(g);
        hv[j] = rbf(rbf(g / (1.0f + __expf(-g))) * u);
      }
      if (lane < 4) {
#pragma unroll
        for (int j = 0; j < 4; ++j) s_h[((lane & 1) * 4 + j) * 2 + (lane >> 1)] = hv[j];
      }
    }
    LDS_FENCE(); bar(); LDS_FENCE();
    STAMP(3);
    if (w < 7) {
      const f32x4 ma = *reinterpret_cast<const f32x4*>(&s_ml2[0]), mb = *reinterpret_cast<const f32x4*>(&s_ml2[4]);
      f32x4 pq[4];
#pragma unroll
      for (int k = 0; k < 4; ++k) pq[k] = *reinterpret_cast<const f32x4*>(&s_o[k][(lane >> 3) * 4]);
      const f32x2 hv = *reinterpret_cast<const f32x2*>(&s_h[(lane >> 3) * 2]);
      const float M = fmaxf(fmaxf(ma[0], ma[2]), fmaxf(mb[0], mb[2]));
      const float e0 = __builtin_amdgcn_exp2f((ma[0] - M) * EXP_C), e1_ = __builtin_amdgcn_exp2f((ma[2] - M) * EXP_C);
      const float e2 = __builtin_amdgcn_exp2f((mb[0] - M) * EXP_C), e3 = __builtin_amdgcn_exp2f((mb[2] - M) * EXP_C);
      const float inv = __builtin_amdgcn_rcpf((ma[1] * e0 + ma[3] * e1_) + (mb[1] * e2 + mb[3] * e3));
      const f32x4 av = ((pq[0] * e0 + pq[1] * e1_) + (pq[2] * e2 + pq[3] * e3)) * inv;
      f32x2 acc[4];
#pragma unroll
      for (int j = 0; j < 4; ++j) acc[j] = wf[4][j] * hv.x + wf[5][j] * hv.y;
#pragma unroll
      for (int i = 0; i < 4; ++i) {
        const float a = rbf(av[i]);
#pragma unroll
        for (int j = 0; j < 4; ++j) acc[j] += wf[i][j] * a;
      }
      float z[4] = {tr32(acc[0].x, acc[2].x), tr32(acc[0].y, acc[2].y), tr32(acc[1].x, acc[3].x), tr32(acc[1].y, acc[3].y)};
#pragma unroll
      for (int j = 0; j < 4; ++j) { z[j] = x16_add(z[j]); z[j] += dpp<ROR8>(z[j]); }
      const unsigned ra = (unsigned)(w * 32 + ((lane >> 1) & 3) * 4 + ((-w) & 3)) * AREA;
      st4c((v4u){canon(z[0]), canon(z[1]), canon(z[2]), canon(z[3])}, rws, (ra + A_Y2 + y * 64 + (lane & 1) * 32 + (lane >> 5) * 16) | ((lane & 24) ? OOB : 0u));
    }
  } else if (isr) {
    const float xo = asf((unsigned)xg[ro * 16 + (lane & 15)] << 16);
    const unsigned pa = area + A_Y2 + w * 1024 + l16;
    v4u d;
    if (RSLEEP) __builtin_amdgcn_s_sleep(RSLEEP);
    for (int it = 0; it < SPIN_CAP; ++it) {
      d = ld4<16>(rws, pa, 0);
      if (__builtin_amdgcn_ballot_w64(!full4(d)) == 0) break;
      __builtin_amdgcn_s_sleep(1);
    }
    float y0 = tr32(asf(d[0]), asf(d[2])), y1 = tr32(asf(d[1]), asf(d[3]));
    y0 = tr16(y0, y1);
    y0 += dpp<ROR8>(y0); y0 += dpp<ROR4>(y0);
    if (!(lane & 12)) s_mv[w][(lane & 3) * 4 + (lane >> 5) * 2 + ((lane >> 4) & 1)] = y0;
    LDS_FENCE(); bar(); LDS_FENCE();
    if (w == 0) {
      float a = s_mv[lane >> 4][lane & 15] + s_mv[4 + (lane >> 4)][lane & 15];
      a = x16_add(a); a = x32_add(a);
      if (lane < 16) out[ro * 16 + lane] = (u16)f2bf(xo + a);
    }
  }
  STAMP(4);
  STAMP_OUT();
}

extern "C" void kernel_launch(void* const* d_in, const int* in_sizes, int n_in,
                              void* d_out, int out_size, void* d_ws, size_t ws_size,
                              hipStream_t stream) {
  fused_kernel<<<NWG, NT, 0, stream>>>((const u16*)d_in[0], (const u16*)d_in[1], (const u16*)d_in[2], (const u16*)d_in[3],
                                       (const u16*)d_in[4], (const u16*)d_in[6], (const u16*)d_in[7], (const u16*)d_in[8],
                                       (u16*)d_out, (char*)d_ws);
}
```

```cpp
#include <hip/hip_runtime.h>
#include <hip/hip_bf16.h>
#include <stdint.h>

#define QPOS 24
#define PACE 8
#define QTRIES 8
#define NG 16
#define CQ 46
#define CF 48
#define CO 50
#define HM 5
#define P2 20
#define TAP 8
#define RSLEEP 0
#define DIAG 0

typedef unsigned v4u __attribute__((ext_vector_type(4)));
typedef unsigned v2u __attribute__((ext_vector_type(2)));
typedef float f32x4 __attribute__((ext_vector_type(4)));
typedef float f32x2 __attribute__((ext_vector_type(2)));
typedef __bf16 bf16x2 __attribute__((ext_vector_type(2)));
typedef unsigned short u16;
typedef __amdgpu_buffer_rsrc_t rsrc_t;

constexpr int D = 448, SK = 32768, DH = 256, F = 1792, NQ = 4096;
constexpr int NWG = 256, NT = 512;
constexpr unsigned OOB = 0x80000000u;
constexpr unsigned SENT = 0xFFFFFFFFu;
constexpr unsigned AREA = 9216;
constexpr unsigned A_Q = 0;
constexpr unsigned A_O1 = 1024;
constexpr unsigned A_F = 5120;
constexpr unsigned A_Y2 = 1024;
constexpr unsigned WS_REQ = NWG * AREA;
constexpr unsigned WS_ECHO = WS_REQ + 1024;
constexpr unsigned WS_GO = WS_ECHO + 1024;
constexpr unsigned WS_POOL = WS_GO + 256;
constexpr unsigned WS_END = WS_POOL + 8 * NG * 512;
constexpr int SPIN_CAP = 1 << 18;

__device__ __forceinline__ float asf(unsigned u) { return __builtin_bit_cast(float, u); }
__device__ __forceinline__ unsigned asu(float f) { return __builtin_bit_cast(unsigned, f); }
__device__ __forceinline__ float rbf(float f) { __bf16 b = (__bf16)f; return (float)b; }
__device__ __forceinline__ unsigned f2bf(float f) { __bf16 b = (__bf16)f; return (unsigned)__builtin_bit_cast(u16, b); }
__device__ __forceinline__ float dot2(unsigned k, unsigned q, float acc) {
  return __builtin_amdgcn_fdot2_f32_bf16(__builtin_bit_cast(bf16x2, k), __builtin_bit_cast(bf16x2, q), acc, false);
}
__device__ __forceinline__ f32x2 unp(unsigned u) { f32x2 r; r.x = asf(u << 16); r.y = asf(u & 0xffff0000u); return r; }
template <int CTRL> __device__ __forceinline__ float dpp(float x) {
  return asf((unsigned)__builtin_amdgcn_update_dpp(0, (int)asu(x), CTRL, 0xf, 0xf, true));
}
constexpr int XOR1 = 0xB1, XOR2 = 0x4E, XOR7 = 0x141, ROR8 = 0x128, ROR4 = 0x124, ROR2 = 0x122, BCAST = 0x150;
constexpr int QP_EVEN = 0xA0, QP_ODD = 0xF5;
__device__ __forceinline__ float x16_add(float x) { auto s = __builtin_amdgcn_permlane16_swap(asu(x), asu(x), false, false); return asf(s[0]) + asf(s[1]); }
__device__ __forceinline__ float x32_add(float x) { auto s = __builtin_amdgcn_permlane32_swap(asu(x), asu(x), false, false); return asf(s[0]) + asf(s[1]); }
__device__ __forceinline__ float x16_max(float x) { auto s = __builtin_amdgcn_permlane16_swap(asu(x), asu(x), false, false); return fmaxf(asf(s[0]), asf(s[1])); }
__device__ __forceinline__ float x32_max(float x) { auto s = __builtin_amdgcn_permlane32_swap(asu(x), asu(x), false, false); return fmaxf(asf(s[0]), asf(s[1])); }
__device__ __forceinline__ float tr32(float a, float b) { auto s = __builtin_amdgcn_permlane32_swap(asu(a), asu(b), false, false); return asf(s[0]) + asf(s[1]); }
__device__ __forceinline__ float tr16(float a, float b) { auto s = __builtin_amdgcn_permlane16_swap(asu(a), asu(b), false, false); return asf(s[0]) + asf(s[1]); }
__device__ __forceinline__ float wave_sum(float v) {
  v += dpp<XOR1>(v); v += dpp<XOR2>(v); v += dpp<XOR7>(v); v += dpp<ROR8>(v);
  v = x16_add(v); return x32_add(v);
}
__device__ __forceinline__ float wave_max(float v) {
  v = fmaxf(v, dpp<XOR1>(v)); v = fmaxf(v, dpp<XOR2>(v)); v = fmaxf(v, dpp<XOR7>(v)); v = fmaxf(v, dpp<ROR8>(v));
  v = x16_max(v); return x32_max(v);
}
__device__ __forceinline__ rsrc_t mkrs(const void* p, unsigned bytes) { return __builtin_amdgcn_make_buffer_rsrc((void*)p, 0, (int)bytes, 0x00020000); }
template <int AUX> __device__ __forceinline__ v4u ld4(rsrc_t r, unsigned vo, unsigned so) { return __builtin_amdgcn_raw_buffer_load_b128(r, (int)vo, (int)so, AUX); }
template <int AUX> __device__ __forceinline__ v2u ld2(rsrc_t r, unsigned vo, unsigned so) { return __builtin_amdgcn_raw_buffer_load_b64(r, (int)vo, (int)so, AUX); }
__device__ __forceinline__ void st4c(v4u v, rsrc_t r, unsigned vo) { __builtin_amdgcn_raw_buffer_store_b128(v, r, (int)vo, 0, 16); }
__device__ __forceinline__ void st2c(v2u v, rsrc_t r, unsigned vo) { __builtin_amdgcn_raw_buffer_store_b64(v, r, (int)vo, 0, 16); }
__device__ __forceinline__ void st1c(unsigned v, rsrc_t r, unsigned vo) { __builtin_amdgcn_raw_buffer_store_b32(v, r, (int)vo, 0, 16); }
__device__ __forceinline__ unsigned canon(float f) { unsigned u = asu(f); return u < 0xFFFFFFFEu ? u : 0xFFFFFFFEu; }
__device__ __forceinline__ bool full4(v4u v) { return v[0] != SENT && v[1] != SENT && v[2] != SENT && v[3] != SENT; }
#define SB() __builtin_amdgcn_sched_barrier(0)
__device__ __forceinline__ void bar() { __builtin_amdgcn_s_waitcnt(0xC07F); __builtin_amdgcn_s_barrier(); }
#define WAITV(n) __builtin_amdgcn_s_waitcnt(((n) & 15) | (((n) >> 4) << 14) | 0x0F70)
template <int V> struct IC { static constexpr int value = V; constexpr operator int() const { return V; } };
template <int A, int B, class Fn> __device__ __forceinline__ void static_for(Fn&& f) { if constexpr (A < B) { f(IC<A>{}); static_for<A + 1, B>(f); } }
#define LDS_FENCE() asm volatile("" ::: "memory")

constexpr float EXP_C = 0.0625f * 1.4426950408889634f;

struct Att {
  float m, l;
  f32x2 a0[4], a1[4];
};

template <int NL, class ISSUE>
__device__ __forceinline__ float scores(v4u (&kb)[8], const v4u& q0, const v4u& q1, int lane, ISSUE issue) {
  float x[2 * NL];
#pragma unroll
  for (int i = 0; i < NL; ++i) {
    float a = 0.f, b = 0.f;
#pragma unroll
    for (int j = 0; j < 4; ++j) { a = dot2(kb[i][j], q0[j], a); b = dot2(kb[i][j], q1[j], b); }
    x[2 * i] = a; x[2 * i + 1] = b;
    asm volatile("" : "+v"(x[2 * i]), "+v"(x[2 * i + 1]));
    issue(i);
    SB();
  }
#define TR_STEP(HALF, CTRL, BIT) _Pragma("unroll") for (int v = 0; v < (HALF); ++v) { \
    const float lo_ = x[v], hi_ = x[v + (HALF)]; const bool s_ = lane & (BIT); \
    x[v] = (s_ ? hi_ : lo_) + dpp<CTRL>(s_ ? lo_ : hi_); }
  if constexpr (NL == 8) { TR_STEP(8, ROR8, 8) }
  TR_STEP(4, XOR7, 4) TR_STEP(2, XOR2, 2) TR_STEP(1, XOR1, 1)
#undef TR_STEP
  if constexpr (NL == 4) x[0] += dpp<ROR8>(x[0]);
  return rbf(x16_add(x[0]));
}
template <class ISSUE>
__device__ __forceinline__ float phase_k4(v4u (&kb)[8], const v4u& q0, const v4u& q1, Att& st, int lane, ISSUE issue) {
  const float s = scores<4>(kb, q0, q1, lane, issue);
  float mx = fmaxf(s, dpp<ROR2>(s)); mx = fmaxf(mx, dpp<ROR4>(mx));
  mx = x32_max(mx);
  const float mn = fmaxf(st.m, mx);
  const float al = __builtin_amdgcn_exp2f((st.m - mn) * EXP_C);
  const float p = __builtin_amdgcn_exp2f((s - mn) * EXP_C);
  st.m = mn; st.l = st.l * al + p * 0.5f;
  const float al0 = dpp<QP_EVEN>(al), al1 = dpp<QP_ODD>(al);
#pragma unroll
  for (int j = 0; j < 4; ++j) { st.a0[j] *= al0; st.a1[j] *= al1; }
  SB();
  return p;
}
template <int I, int O, class ISSUE>
__device__ __forceinline__ void pv_step(v4u (&vb)[8], float p, Att& st, ISSUE& issue) {
  const float p0 = dpp<BCAST + 2 * I>(p), p1 = dpp<BCAST + 2 * I + 1>(p);
#pragma unroll
  for (int j = 0; j < 4; ++j) { const f32x2 v = unp(vb[O + I][j]); st.a0[j] += v * p0; st.a1[j] += v * p1; }
  asm volatile("" : "+v"(st.a0[0]), "+v"(st.a0[1]), "+v"(st.a0[2]), "+v"(st.a0[3]), "+v"(st.a1[0]), "+v"(st.a1[1]), "+v"(st.a1[2]), "+v"(st.a1[3]));
  issue(O + I);
  SB();
}
template <int NL, class ISSUE>
__device__ __forceinline__ void phase_v(v4u (&vb)[8], float p, Att& st, ISSUE issue) {
  constexpr int O = 8 - NL;
  pv_step<0, O>(vb, p, st, issue); pv_step<1, O>(vb, p, st, issue); pv_step<2, O>(vb, p, st, issue); pv_step<3, O>(vb, p, st, issue);
  if constexpr (NL == 8) { pv_step<4, O>(vb, p, st, issue); pv_step<5, O>(vb, p, st, issue); pv_step<6, O>(vb, p, st, issue); pv_step<7, O>(vb, p, st, issue); }
}

#define STAMP(k) do {} while (0)
#define STAMP_OUT() do {} while (0)

__device__ __forceinline__ f32x2 ln_stats(const v4u& xa, int lane) {
  f32x2 xv[4];
  float sm = 0.f;
#pragma unroll
  for (int j = 0; j < 4; ++j) { xv[j] = unp(xa[j]); sm += xv[j].x + xv[j].y; }
  const float mean = wave_sum(sm) * (1.0f / D);
  float sq = 0.f;
#pragma unroll
  for (int j = 0; j < 4; ++j) { const f32x2 d = xv[j] - mean; sq += d.x * d.x + d.y * d.y; }
  sq = lane < 56 ? sq : 0.f;
  const float var = rbf(wave_sum(sq) * (1.0f / D));
  const float sd = rbf(sqrtf(rbf(var + 1e-5f)));
  return (f32x2){rbf(mean), 1.0f / sd};
}
__device__ __forceinline__ float ln_apply(float x, f32x2 st) { return rbf(rbf(x - st.x) * st.y); }
__device__ __forceinline__ unsigned pack2(float a, float b) { const unsigned u = f2bf(a) | (f2bf(b) << 16); return u < 0xFFFFFFFEu ? u : 0xFFFFFFFEu; }

__global__ void __launch_bounds__(NT, 2)
fused_kernel(const u16* __restrict__ xg, const u16* __restrict__ kc, const u16* __restrict__ vc, const u16* __restrict__ wq,
             const u16* __restrict__ wo, const u16* __restrict__ wg, const u16* __restrict__ wu, const u16* __restrict__ wd,
             u16* __restrict__ out, char* ws) {
  __shared__ __attribute__((aligned(16))) float s_part[8][512];
  __shared__ __attribute__((aligned(16))) float s_mv[8][64];
  __shared__ __attribute__((aligned(16))) unsigned s_q[256];
  __shared__ __attribute__((aligned(16))) u16 s_qs[64];
  __shared__ __attribute__((aligned(16))) float s_ml[8][4];
  __shared__ __attribute__((aligned(16))) float s_o[4][32];
  __shared__ __attribute__((aligned(16))) float s_ml2[8];
  __shared__ __attribute__((aligned(16))) float s_h[16];
  __shared__ int s_flag, s_go, s_fc;
  const int tid = threadIdx.x, lane = tid & 63, w = __builtin_amdgcn_readfirstlane(tid >> 6);
  const int bi = blockIdx.x, h = (((bi & 1) ? bi + 5 : bi) & 6) | (bi >> 7), s = ((bi >> 3) & 15) * 2 + (bi & 1);
  const int b = h * 32 + s;
  constexpr int PH = 2 * (2048 - 8 * CQ - 14 * CF - 10 * CO), GS = 32 / NG, CAP = 16;
  auto prefix = [](int n) { return n < 8 ? n * CQ : n < 22 ? 8 * CQ + (n - 8) * CF : 8 * CQ + 14 * CF + (n - 22) * CO; };
  const int pg = s / GS;
  const int reg0 = 2 * prefix(pg * GS) + pg * PH / NG, reg1 = 2 * prefix((pg + 1) * GS) + (pg + 1) * PH / NG;
  const int first = reg0 + (s - pg * GS) * 80;
  const int pool1 = reg0 + GS * 80, psz1 = (reg1 - pool1 - GS * P2) >> 1;
  const int pool0 = pool1 + 2 * psz1, psz = reg1 - pool0;
  static_assert(2 * CO + PH / 32 + 1 - P2 - 80 <= 2 * 8 * (CAP - 5), "the waves can take the whole pool");
  asm volatile("" :: "s"(xg), "s"(kc), "s"(vc), "s"(wq), "s"(wo), "s"(wg), "s"(wu), "s"(wd), "s"(out), "s"(ws));
  STAMP(0);
  const rsrc_t rx = mkrs(xg, D * 2);
  const rsrc_t rk = mkrs(kc + (size_t)h * SK * DH, SK * DH * 2);
  const rsrc_t rv = mkrs(vc + (size_t)h * SK * DH, SK * DH * 2);
  const rsrc_t rws = mkrs(ws, WS_END);
  const unsigned l16 = lane * 16;
  auto ubase = [&](int u) -> unsigned { return (unsigned)(first + 2 * u) * 4096u; };
  auto ldk = [&](unsigned base, int i) { return ld4<2>(rk, l16 + (i & 3) * 1024, base + (i >> 2) * 4096); };
  auto ldv = [&](unsigned base, int i) { return ld4<2>(rv, l16 + (i & 3) * 1024, base + (i >> 2) * 4096); };
  const unsigned area = (unsigned)b * AREA;
  auto area_of = [&](int ss) -> unsigned { return (unsigned)(h * 32 + ss) * AREA; };
  const unsigned lmask = lane < 56 ? 0u : OOB;

  const bool isq = s < 8, isf = s >= 8 && s < 22, isy = s >= 16;
  const int f = h * 14 + s - 8, y = h * 16 + s - 16, ro = h * 4 + (s >> 2);
  const bool isr = s < 16 && ((s + h) & 3) == 0 && ro < 28;
  const rsrc_t rwo = mkrs(wo + (size_t)(isy ? y : 0) * 32 * D, (isy && w < 7) ? 32 * D * 2 : 0);
  const rsrc_t rwd = mkrs(wd + (size_t)((isy && y < 112) ? y : 0) * 16 * D, (isy && y < 112 && w < 7) ? 16 * D * 2 : 0);
  const unsigned wo_off = (lane >> 3) * (4 * D * 2) + w * 128 + (lane & 7) * 16, wd_off = (lane >> 3) * (D * 2) + w * 128 + (lane & 7) * 16;
  const bool tile = isq || (isf && w < 4);
  const bool leader = (b == NWG - 1 && w == 1), member = (w == 0);

  const v4u sent = {SENT, SENT, SENT, SENT};
  unsigned poll_off = lane == 1 ? WS_GO : OOB;
  poll_off = lane == 0 ? WS_REQ + (b >> 2) * 16 : poll_off;
  poll_off = leader ? WS_ECHO + l16 : poll_off;
  v4u pv = {0u, 0u, 0u, 0u}, e1 = {0u, 0u, 0u, 0u};
  unsigned go_ref = 0;
  if (member) {
    st4c(sent, rws, area + A_Q + l16);
    __hip_atomic_store(&s_flag, 0, __ATOMIC_RELAXED, __HIP_MEMORY_SCOPE_WORKGROUP);
    __hip_atomic_store(&s_go, 0, __ATOMIC_RELAXED, __HIP_MEMORY_SCOPE_WORKGROUP);
    __hip_atomic_store(&s_fc, 0, __ATOMIC_RELAXED, __HIP_MEMORY_SCOPE_WORKGROUP);
    st1c(0u, rws, (lane < 2 * NG && (lane & 7) == s) ? WS_POOL + (h * NG * 2 + lane) * 256 : OOB);
  }
  SB();
  if (tile) {
    v4u tw[8];
    const int oct = isq ? 7 * w : (f & 3) * 14 + (w >> 1) * 7;
    const int stride = isq ? NQ * 2 : F * 2;
    const rsrc_t rt = mkrs(isq ? wq : (w & 1) ? wu : wg, D * stride);
    const unsigned to = (unsigned)((oct + (lane >> 3)) * 8 * stride + (isq ? h * 8 + s : f >> 2) * 128 + (lane & 7) * 16) | lmask;
    const v4u xa = ld4<0>(rx, l16, 0);
    const v4u xb = ld4<0>(rx, (unsigned)(oct + (lane >> 3)) * 16 | lmask, 0);
    SB();
#pragma unroll
    for (int i = 0; i < 8; ++i) { tw[i] = ld4<2>(rt, to, i * stride); SB(); }
    LDS_FENCE(); bar(); LDS_FENCE();
    SB();
    const f32x2 ls = ln_stats(xa, lane);
    f32x2 acc[4];
#pragma unroll
    for (int i = 0; i < 8; ++i) {
      const float xn = ln_apply(asf((i & 1) ? (xb[i >> 1] & 0xffff0000u) : (xb[i >> 1] << 16)), ls);
#pragma unroll
      for (int j = 0; j < 4; ++j) acc[j] = i ? acc[j] + unp(tw[i][j]) * xn : unp(tw[i][j]) * xn;
    }
    float y0 = tr32(acc[0].x, acc[2].x), y1 = tr32(acc[0].y, acc[2].y), y2 = tr32(acc[1].x, acc[3].x), y3 = tr32(acc[1].y, acc[3].y);
    y0 = tr16(y0, y2); y1 = tr16(y1, y3);
    const bool s_ = lane & 8;
    y0 = (s_ ? y1 : y0) + dpp<ROR8>(s_ ? y0 : y1);
    s_mv[w][(lane & 7) * 8 + (lane >> 3)] = y0;
    STAMP(6);
    if (isq) {
      LDS_FENCE(); bar(); LDS_FENCE();
      if (w < 4) {
        float a = 0.f;
#pragma unroll
        for (int k = 0; k < 8; ++k) a += s_mv[k][lane];
        s_qs[lane] = (u16)f2bf(a);
        LDS_FENCE();
        v4u qs = *reinterpret_cast<const v4u*>(&s_qs[(lane & 7) * 8]);
#pragma unroll
        for (int j = 0; j < 4; ++j) qs[j] = qs[j] < 0xFFFFFFFEu ? qs[j] : 0xFFFFFFFEu;
        st4c(qs, rws, area_of(w * 8 + (lane >> 3)) + A_Q + s * 128 + (lane & 7) * 16);
      }
    } else {
      LDS_FENCE();
      __builtin_amdgcn_s_waitcnt(0xC07F);
      if (lane == 0) __hip_atomic_fetch_add(&s_fc, 1, __ATOMIC_RELAXED, __HIP_MEMORY_SCOPE_WORKGROUP);
    }
  } else if (leader) {
    e1 = ld4<16>(rws, poll_off, 0);
    go_ref = __builtin_amdgcn_raw_buffer_load_b32(rws, (int)WS_GO, 0, 16);
    SB();
    LDS_FENCE(); bar(); LDS_FENCE();
    SB();
    e1 += 1u;
    go_ref += 1u;
    st4c(e1, rws, WS_REQ + l16);
  } else {
    LDS_FENCE(); bar(); LDS_FENCE();
  }
  SB();
  if (member && (isy || isr)) {
#pragma unroll
    for (int i = 1; i < 6; ++i) st4c(sent, rws, area + i * 1024 + l16);
    if (isr) {
#pragma unroll
      for (int i = 6; i < 9; ++i) st4c(sent, rws, area + i * 1024 + l16);
    }
  }
  STAMP(5);
  SB();
  v4u pre[4][8];
  v4u qv;
  static_for<0, 32>([&](auto N) {
    constexpr int n = N;
    if constexpr (n == QPOS) {
      STAMP(9);
      qv = ld4<16>(rws, member ? area + A_Q + l16 : OOB, 0);
      pv = ld4<16>(rws, (member || leader) ? poll_off : OOB, 0);
      SB();
    }
    if constexpr (n >= PACE) WAITV(PACE - 1 + ((n >= QPOS && n < QPOS + PACE) ? 2 : 0));
    pre[n >> 3][n & 7] = ldk(ubase((n & 24) + w), n & 7);
    SB();
    if constexpr (n - PACE == 7 || n - PACE == 15 || n - PACE == 23) STAMP(16 + (n - PACE) / 8);
  });
  STAMP(10);
  int qsrc;
  if (member) {
    int tries = 0;
    bool ok;
    for (;; ++tries) {
      ok = __builtin_amdgcn_ballot_w64(!full4(qv)) == 0;
      if (ok || tries >= QTRIES) break;
      __builtin_amdgcn_s_sleep(4);
      qv = ld4<16>(rws, area + A_Q + l16, 0);
      WAITV(0);
    }
    if (!ok) {
      const v4u xc = ld4<0>(rx, l16, 0);
      const f32x2 ls = ln_stats(xc, lane);
      const rsrc_t rwq = mkrs(wq, D * NQ * 2);
      f32x2 acc[4] = {{0.f, 0.f}, {0.f, 0.f}, {0.f, 0.f}, {0.f, 0.f}};
#pragma unroll 1
      for (int r = 0; r < D / 2; ++r) {
        unsigned xp = xc[0];
        xp = (r & 3) == 1 ? xc[1] : xp; xp = (r & 3) == 2 ? xc[2] : xp; xp = (r & 3) == 3 ? xc[3] : xp;
        const f32x2 xr = unp((unsigned)__builtin_amdgcn_readlane((int)xp, r >> 2));
        const v4u wa = ld4<0>(rwq, h * 1024 + l16, r * 2 * NQ * 2), wb = ld4<0>(rwq, h * 1024 + l16, (r * 2 + 1) * NQ * 2);
        const float x0 = ln_apply(xr.x, ls), x1 = ln_apply(xr.y, ls);
#pragma unroll
        for (int j = 0; j < 4; ++j) acc[j] += unp(wa[j]) * x0 + unp(wb[j]) * x1;
      }
#pragma unroll
      for (int j = 0; j < 4; ++j) qv[j] = pack2(acc[j].x, acc[j].y);
      WAITV(0);
    }
    *reinterpret_cast<v4u*>(&s_q[lane * 4]) = qv;
    LDS_FENCE();
    __builtin_amdgcn_s_waitcnt(0xC07F);
    qsrc = ok ? 1 : 2;
    __hip_atomic_store(&s_flag, qsrc, __ATOMIC_RELAXED, __HIP_MEMORY_SCOPE_WORKGROUP);
  } else {
    while ((qsrc = __builtin_amdgcn_readfirstlane(__hip_atomic_load(&s_flag, __ATOMIC_RELAXED, __HIP_MEMORY_SCOPE_WORKGROUP))) == 0) __builtin_amdgcn_s_sleep(1);
  }
  LDS_FENCE();
  const v4u q0 = *reinterpret_cast<const v4u*>(&s_q[(lane & 31) * 4]), q1 = *reinterpret_cast<const v4u*>(&s_q[128 + (lane & 31) * 4]);
  STAMP(1);
  SB();

  bool svc = member || leader;
  unsigned seenm = 0, last_echo = 0;
  bool echoed = false;
  auto service = [&]() {
    if (leader) {
      seenm |= (pv[0] == e1[0] ? 1u : 0u) | (pv[1] == e1[1] ? 2u : 0u) | (pv[2] == e1[2] ? 4u : 0u) | (pv[3] == e1[3] ? 8u : 0u);
      svc = __builtin_amdgcn_ballot_w64(seenm != 15u) != 0;
      st1c(go_ref, rws, (lane == 0 && !svc) ? WS_GO : OOB);
    } else {
      const unsigned req = (unsigned)__builtin_amdgcn_readfirstlane((int)((b & 2) ? ((b & 1) ? pv[3] : pv[2]) : ((b & 1) ? pv[1] : pv[0])));
      const unsigned go = (unsigned)__builtin_amdgcn_readlane((int)pv[0], 1);
      go_ref = echoed ? go_ref : go;
      svc = go == (unsigned)__builtin_amdgcn_readfirstlane((int)go_ref);
      st1c(req, rws, (lane == 0 && svc && !(echoed && req == last_echo)) ? WS_ECHO + b * 4 : OOB);
      echoed = true; last_echo = req;
      if (!svc) {
        __hip_atomic_store(&s_go, 1, __ATOMIC_RELAXED, __HIP_MEMORY_SCOPE_WORKGROUP);
        if (isf) {
          while (__builtin_amdgcn_readfirstlane(__hip_atomic_load(&s_fc, __ATOMIC_RELAXED, __HIP_MEMORY_SCOPE_WORKGROUP)) != 4) __builtin_amdgcn_s_sleep(1);
          LDS_FENCE();
          const int j = (lane >> 3) & 3, pc = lane & 7, m = pc >> 2, col = j * 16 + (pc & 3) * 4;
          const f32x4 v = *reinterpret_cast<const f32x4*>(&s_mv[m][col]) + *reinterpret_cast<const f32x4*>(&s_mv[m + 2][col]);
          const int fd = (f & ~3) + j;
          st4c((v4u){canon(v[0]), canon(v[1]), canon(v[2]), canon(v[3])}, rws,
               ((unsigned)((fd >> 4) * 32 + 16 + (fd & 15)) * AREA + A_F + (f & 3) * 128 + pc * 16) | (lane < 32 ? 0u : OOB));
        }
      }
    }
    pv = ld4<16>(rws, svc ? poll_off : OOB, 0);
    SB();
  };

  auto wait_go = [&]() {
    for (int it = 0; it < SPIN_CAP; ++it) {
      if (__builtin_amdgcn_readfirstlane(__hip_atomic_load(&s_go, __ATOMIC_RELAXED, __HIP_MEMORY_SCOPE_WORKGROUP))) break;
      if (svc) service();
      __builtin_amdgcn_s_sleep(2);
    }
  };

  Att st;
  v4u ring[8];
  int sx = 20;
  {
    const unsigned pco = lane == 0 ? WS_POOL + (h * NG + pg) * 512 : OOB;
    float* const sc = &s_part[w][(lane >> 5) * 16 + (lane & 15)];
    unsigned uid = ubase(lane * 8 + w);
    float mx;
    int gt, n = 5;
    bool again = true;
    auto claim = [&](unsigned a) { gt = __builtin_amdgcn_raw_ptr_buffer_atomic_add_i32(1, rws, (int)(again ? a : OOB), 0, 0); };
    if (qsrc == 2) wait_go();
    if (svc) service();
    {
      const unsigned ub = ubase(32 + w);
      const float s0 = scores<8>(pre[0], q0, q1, lane, [&](int i) { if (i == 0) claim(pco); ring[i] = ldk(ub, i); });
      const float s1 = scores<8>(pre[1], q0, q1, lane, [&](int) {});
      const float s2 = scores<8>(pre[2], q0, q1, lane, [&](int) {});
      const float s3 = scores<8>(pre[3], q0, q1, lane, [&](int) {});
      sc[0] = s0; sc[32] = s1; sc[64] = s2; sc[96] = s3;
      mx = fmaxf(fmaxf(s0, s1), fmaxf(s2, s3));
      STAMP(19);
      SB();
    }
    for (bool more = true; more;) {
      const int t = __builtin_amdgcn_readfirstlane(gt);
      more = again && (unsigned)t < (unsigned)psz1;
      const unsigned nb = more ? (unsigned)(pool1 + 2 * t) * 4096u : ubase(w);
      const rsrc_t nr = more ? rk : rv;
      uid = lane == n ? nb : uid;
      again = more && n + 1 < CAP;
      SB();
      const float sv = scores<8>(ring, q0, q1, lane, [&](int i) { if (i == 0) claim(pco); ring[i] = ld4<2>(nr, l16 + (i & 3) * 1024, nb + (i >> 2) * 4096); });
      sc[(n - 1) * 32] = sv;
      mx = fmaxf(mx, sv);
      n += more;
      STAMP(sx); ++sx;
      if (svc) service();
      SB();
    }
    STAMP(7);
    mx = fmaxf(mx, dpp<ROR2>(mx)); mx = fmaxf(mx, dpp<ROR4>(mx)); mx = fmaxf(mx, dpp<ROR8>(mx));
    st.m = x32_max(mx); st.l = 0.f;
#pragma unroll
    for (int j = 0; j < 4; ++j) { st.a0[j] = (f32x2){0.f, 0.f}; st.a1[j] = (f32x2){0.f, 0.f}; }
    LDS_FENCE();
    again = false;
#pragma unroll 1
    for (int j = 1; j < n; ++j) {
      const float sv = sc[(j - 1) * 32];
      const unsigned nb = (unsigned)__builtin_amdgcn_readlane((int)uid, j);
      again = j + 1 == n;
      const float p = __builtin_amdgcn_exp2f((sv - st.m) * EXP_C);
      st.l += p;
      SB();
      phase_v<8>(ring, p, st, [&](int i) { if (i == 0) claim(pco + 256); ring[i] = ldv(nb, i); });
      STAMP(sx); ++sx;
      if (svc) service();
      SB();
    }
    wait_go();
    STAMP(8);
    rsrc_t nra, nrb;
    unsigned nva, nvb, nb, nsa, nsb;
    bool more;
    const int lim = psz - TAP * GS * w / 8;
    auto next_half = [&]() {
      const int t = __builtin_amdgcn_readfirstlane(gt);
      more = again && (unsigned)t < (unsigned)psz;
      again = more && t < lim;
      nb = more ? (unsigned)(pool0 + t) * 4096u : 0u;
      nra = more ? rk : rwo; nrb = more ? rv : rwd;
      nva = more ? l16 : wo_off; nvb = more ? l16 : wd_off;
      nsa = more ? 1024 : D * 2; nsb = more ? 1024 : 8 * D * 2;
      SB();
    };
    auto ldn = [&](int i) { if (i == 0) claim(pco + 256); ring[i] = i < 4 ? ld4<2>(nra, nva, nb + i * nsa) : ld4<2>(nrb, nvb, nb + (i - 4) * nsb); };
    {
      const float p = __builtin_amdgcn_exp2f((sc[(n - 1) * 32] - st.m) * EXP_C);
      st.l += p;
      next_half();
      phase_v<8>(ring, p, st, ldn);
      STAMP(sx); ++sx;
    }
#pragma unroll 1
    while (more) {
      next_half();
      const float p = phase_k4(ring, q0, q1, st, lane, ldn);
      phase_v<4>(ring, p, st, ldn);
      STAMP(sx); ++sx;
    }
    asm volatile("" :: "v"(gt));
    LDS_FENCE();
  }
  STAMP(12);
  {
    float l = st.l;
    l += dpp<ROR2>(l); l += dpp<ROR4>(l); l += dpp<ROR8>(l); l = x32_add(l);
    float o[8];
#pragma unroll
    for (int j = 0; j < 4; ++j) { o[2 * j] = tr32(st.a0[j].x, st.a1[j].x); o[2 * j + 1] = tr32(st.a0[j].y, st.a1[j].y); }
    *reinterpret_cast<f32x4*>(&s_part[w][lane * 8]) = (f32x4){o[0], o[1], o[2], o[3]};
    *reinterpret_cast<f32x4*>(&s_part[w][lane * 8 + 4]) = (f32x4){o[4], o[5], o[6], o[7]};
    if (lane < 2) *reinterpret_cast<f32x2*>(&s_ml[w][lane * 2]) = (f32x2){st.m, l};
  }
  LDS_FENCE(); bar(); LDS_FENCE();
  STAMP(2);
  if (w == 4 || w == 5) {
    const int t = tid - 256;
    f32x2 ml[8];
#pragma unroll
    for (int k = 0; k < 8; ++k) ml[k] = *reinterpret_cast<const f32x2*>(&s_ml[k][(w - 4) * 2]);
    float M = -3.0e38f, L = 0.f;
#pragma unroll
    for (int k = 0; k < 8; ++k) M = fmaxf(M, ml[k].x);
    f32x4 o = {0.f, 0.f, 0.f, 0.f};
#pragma unroll
    for (int k = 0; k < 8; ++k) {
      const float e = __builtin_amdgcn_exp2f((ml[k].x - M) * EXP_C);
      o += *reinterpret_cast<const f32x4*>(&s_part[k][t * 4]) * e; L += ml[k].y * e;
    }
    v4u msg;
#pragma unroll
    for (int j = 0; j < 3; ++j) { const unsigned u = asu(o[j]); msg[j] = __builtin_amdgcn_perm(u < 0xFFFFFE00u ? u : 0xFFFFFE00u, asu(o[3]), 0x07060501u + j); }
    msg[3] = canon((t & 1) ? L : M);
    st4c(msg, rws, area_of(16 + (t >> 3)) + A_O1 + s * 128 + (t & 7) * 16);
    SB();
    asm volatile("" :: "v"(ring[0]), "v"(ring[1]), "v"(ring[2]), "v"(ring[3]), "v"(ring[4]), "v"(ring[5]), "v"(ring[6]), "v"(ring[7]), "v"(pv));
  }
  if (isy) {
    const unsigned pa = w < 4 ? area + A_O1 + w * 1024 + l16 : (w == 7 && y < 112) ? area + A_F + (lane & 31) * 16 : OOB;
    v4u d = ld4<16>(rws, pa, 0);
    SB();
    f32x2 wf[6][4];
#pragma unroll
    for (int i = 0; i < 6; ++i)
#pragma unroll
      for (int j = 0; j < 4; ++j) {
        wf[i][j] = unp(ring[i][j]);
        asm volatile("" : "+v"(wf[i][j]));
      }
    SB();
    STAMP(14);
    for (int it = 0; it < SPIN_CAP; ++it) {
      if (__builtin_amdgcn_ballot_w64(!full4(d)) == 0) break;
      __builtin_amdgcn_s_sleep(1);
      d = ld4<16>(rws, pa, 0);
    }
    STAMP(15);
    if (w < 4) {
      const float ot = dpp<XOR1>(asf(d[3]));
      const float m = (lane & 1) ? ot : asf(d[3]), l = (lane & 1) ? asf(d[3]) : ot;
      float M = fmaxf(m, dpp<ROR8>(m)); M = x16_max(M); M = x32_max(M);
      const float e = __builtin_amdgcn_exp2f((m - M) * EXP_C);
      const float v3 = asf(__builtin_amdgcn_perm(__builtin_amdgcn_perm(d[2], d[1], 0x04000c0cu), d[0], 0x0706000cu));
      float y0 = tr32(asf(d[0]) * e, asf(d[2]) * e), y1 = tr32(asf(d[1]) * e, v3 * e), L = x32_add(l * e);
      y0 = tr16(y0, y1); L = x16_add(L);
      const bool s_ = lane & 8;
      y0 = (s_ ? L : y0) + dpp<ROR8>(s_ ? y0 : L);
      if (!s_) s_o[w][(lane & 7) * 4 + (lane >> 5) * 2 + ((lane >> 4) & 1)] = y0;
      if (lane == 8) *reinterpret_cast<f32x2*>(&s_ml2[w * 2]) = (f32x2){M, y0};
    } else if (w == 7) {
      float hv[4];
#pragma unroll
      for (int j = 0; j < 4; ++j) {
        float g = asf(d[j]);
        g += dpp<ROR8>(g); g = rbf(x16_add(g));
        const float u = dpp<ROR4>(g);
        hv[j] = rbf(rbf(g / (1.0f + __expf(-g))) * u);
      }
      if (lane < 4) {
#pragma unroll
        for (int j = 0; j < 4; ++j) s_h[((lane & 1) * 4 + j) * 2 + (lane >> 1)] = hv[j];
      }
    }
    LDS_FENCE(); bar(); LDS_FENCE();
    STAMP(3);
    if (w < 7) {
      const f32x4 ma = *reinterpret_cast<const f32x4*>(&s_ml2[0]), mb = *reinterpret_cast<const f32x4*>(&s_ml2[4]);
      f32x4 pq[4];
#pragma unroll
      for (int k = 0; k < 4; ++k) pq[k] = *reinterpret_cast<const f32x4*>(&s_o[k][(lane >> 3) * 4]);
      const f32x2 hv = *reinterpret_cast<const f32x2*>(&s_h[(lane >> 3) * 2]);
      const float M = fmaxf(fmaxf(ma[0], ma[2]), fmaxf(mb[0], mb[2]));
      const float e0 = __builtin_amdgcn_exp2f((ma[0] - M) * EXP_C), e1_ = __builtin_amdgcn_exp2f((ma[2] - M) * EXP_C);
      const float e2 = __builtin_amdgcn_exp2f((mb[0] - M) * EXP_C), e3 = __builtin_amdgcn_exp2f((mb[2] - M) * EXP_C);
      const float inv = __builtin_amdgcn_rcpf((ma[1] * e0 + ma[3] * e1_) + (mb[1] * e2 + mb[3] * e3));
      const f32x4 av = ((pq[0] * e0 + pq[1] * e1_) + (pq[2] * e2 + pq[3] * e3)) * inv;
      f32x2 acc[4];
#pragma unroll
      for (int j = 0; j < 4; ++j) acc[j] = wf[4][j] * hv.x + wf[5][j] * hv.y;
#pragma unroll
      for (int i = 0; i < 4; ++i) {
        const float a = rbf(av[i]);
#pragma unroll
        for (int j = 0; j < 4; ++j) acc[j] += wf[i][j] * a;
      }
      float z[4] = {tr32(acc[0].x, acc[2].x), tr32(acc[0].y, acc[2].y), tr32(acc[1].x, acc[3].x), tr32(acc[1].y, acc[3].y)};
#pragma unroll
      for (int j = 0; j < 4; ++j) { z[j] = x16_add(z[j]); z[j] += dpp<ROR8>(z[j]); }
      const unsigned ra = (unsigned)(w * 32 + ((lane >> 1) & 3) * 4 + ((-w) & 3)) * AREA;
      st4c((v4u){canon(z[0]), canon(z[1]), canon(z[2]), canon(z[3])}, rws, (ra + A_Y2 + y * 64 + (lane & 1) * 32 + (lane >> 5) * 16) | ((lane & 24) ? OOB : 0u));
    }
  } else if (isr) {
    const float xo = asf((unsigned)xg[ro * 16 + (lane & 15)] << 16);
    const unsigned pa = area + A_Y2 + w * 1024 + l16;
    v4u d;
    if (RSLEEP) __builtin_amdgcn_s_sleep(RSLEEP);
    for (int it = 0; it < SPIN_CAP; ++it) {
      d = ld4<16>(rws, pa, 0);
      if (__builtin_amdgcn_ballot_w64(!full4(d)) == 0) break;
      __builtin_amdgcn_s_sleep(1);
    }
    float y0 = tr32(asf(d[0]), asf(d[2])), y1 = tr32(asf(d[1]), asf(d[3]));
    y0 = tr16(y0, y1);
    y0 += dpp<ROR8>(y0); y0 += dpp<ROR4>(y0);
    if (!(lane & 12)) s_mv[w][(lane & 3) * 4 + (lane >> 5) * 2 + ((lane >> 4) & 1)] = y0;
    LDS_FENCE(); bar(); LDS_FENCE();
    if (w == 0) {
      float a = s_mv[lane >> 4][lane & 15] + s_mv[4 + (lane >> 4)][lane & 15];
      a = x16_add(a); a = x32_add(a);
      if (lane < 16) out[ro * 16 + lane] = (u16)f2bf(xo + a);
    }
  }
  STAMP(4);
  STAMP_OUT();
}

extern "C" void kernel_launch(void* const* d_in, const int* in_sizes, int n_in,
                              void* d_out, int out_size, void* d_ws, size_t ws_size,
                              hipStream_t stream) {
  fused_kernel<<<NWG, NT, 0, stream>>>((const u16*)d_in[0], (const u16*)d_in[1], (const u16*)d_in[2], (const u16*)d_in[3],
                                       (const u16*)d_in[4], (const u16*)d_in[6], (const u16*)d_in[7], (const u16*)d_in[8],
                                       (u16*)d_out, (char*)d_ws);
}
```

```cpp
#include <hip/hip_runtime.h>
#include <hip/hip_bf16.h>
#include <stdint.h>

#define QPOS 24
#define PACE 8
#define QTRIES 8
#define NG 16
#define CQ 56
#define CF (CQ + 2)
#define CO (CQ + 4)
#define TAU 3600
#define TOFF 150
#define EARLY 1
#define HM 5
#define PBURST 0
#define KBURST 0
#define TAP 8
#define RSLEEP 0
#define XW 0
#define XT 0
#define XC 0
#define XH 0
#define XQ 0
#define XK 0
#define XA 1
#define DIAG 0

typedef unsigned v4u __attribute__((ext_vector_type(4)));
typedef unsigned v2u __attribute__((ext_vector_type(2)));
typedef float f32x4 __attribute__((ext_vector_type(4)));
typedef float f32x2 __attribute__((ext_vector_type(2)));
typedef __bf16 bf16x2 __attribute__((ext_vector_type(2)));
typedef unsigned short u16;
typedef __amdgpu_buffer_rsrc_t rsrc_t;

constexpr int D = 448, SK = 32768, DH = 256, F = 1792, NQ = 4096;
constexpr int NWG = 256, NT = 512;
constexpr unsigned OOB = 0x80000000u;
constexpr unsigned SENT = 0xFFFFFFFFu;
constexpr unsigned AREA = 9216;
constexpr unsigned A_Q = 0;
constexpr unsigned A_O1 = 1024;
constexpr unsigned A_F = 5120;
constexpr unsigned A_Y2 = 1024;
constexpr unsigned WS_REQ = NWG * AREA;
constexpr unsigned WS_ECHO = WS_REQ + 1024;
constexpr unsigned WS_GO = WS_ECHO + 1024;
constexpr unsigned WS_POOL = WS_GO + 256;
constexpr unsigned WS_END = WS_POOL + 8 * NG * 256;
constexpr int SPIN_CAP = 1 << 18;
constexpr int C_NEXT = 0, C_TICK = 1, C_GO = 2, C_Q = 3, C_T0 = 4, C_FC = 5;

__device__ __forceinline__ float asf(unsigned u) { return __builtin_bit_cast(float, u); }
__device__ __forceinline__ unsigned asu(float f) { return __builtin_bit_cast(unsigned, f); }
__device__ __forceinline__ float rbf(float f) { __bf16 b = (__bf16)f; return (float)b; }
__device__ __forceinline__ unsigned f2bf(float f) { __bf16 b = (__bf16)f; return (unsigned)__builtin_bit_cast(u16, b); }
__device__ __forceinline__ float dot2(unsigned k, unsigned q, float acc) {
  return __builtin_amdgcn_fdot2_f32_bf16(__builtin_bit_cast(bf16x2, k), __builtin_bit_cast(bf16x2, q), acc, false);
}
__device__ __forceinline__ f32x2 unp(unsigned u) { f32x2 r; r.x = asf(u << 16); r.y = asf(u & 0xffff0000u); return r; }
template <int CTRL> __device__ __forceinline__ float dpp(float x) {
  return asf((unsigned)__builtin_amdgcn_update_dpp(0, (int)asu(x), CTRL, 0xf, 0xf, true));
}
constexpr int XOR1 = 0xB1, XOR2 = 0x4E, XOR7 = 0x141, ROR8 = 0x128, ROR4 = 0x124, ROR2 = 0x122, BCAST = 0x150;
constexpr int QP_EVEN = 0xA0, QP_ODD = 0xF5;
__device__ __forceinline__ float x16_add(float x) { auto s = __builtin_amdgcn_permlane16_swap(asu(x), asu(x), false, false); return asf(s[0]) + asf(s[1]); }
__device__ __forceinline__ float x32_add(float x) { auto s = __builtin_amdgcn_permlane32_swap(asu(x), asu(x), false, false); return asf(s[0]) + asf(s[1]); }
__device__ __forceinline__ float x16_max(float x) { auto s = __builtin_amdgcn_permlane16_swap(asu(x), asu(x), false, false); return fmaxf(asf(s[0]), asf(s[1])); }
__device__ __forceinline__ float x32_max(float x) { auto s = __builtin_amdgcn_permlane32_swap(asu(x), asu(x), false, false); return fmaxf(asf(s[0]), asf(s[1])); }
__device__ __forceinline__ float tr32(float a, float b) { auto s = __builtin_amdgcn_permlane32_swap(asu(a), asu(b), false, false); return asf(s[0]) + asf(s[1]); }
__device__ __forceinline__ float tr16(float a, float b) { auto s = __builtin_amdgcn_permlane16_swap(asu(a), asu(b), false, false); return asf(s[0]) + asf(s[1]); }
__device__ __forceinline__ float wave_sum(float v) {
  v += dpp<XOR1>(v); v += dpp<XOR2>(v); v += dpp<XOR7>(v); v += dpp<ROR8>(v);
  v = x16_add(v); return x32_add(v);
}
__device__ __forceinline__ float wave_max(float v) {
  v = fmaxf(v, dpp<XOR1>(v)); v = fmaxf(v, dpp<XOR2>(v)); v = fmaxf(v, dpp<XOR7>(v)); v = fmaxf(v, dpp<ROR8>(v));
  v = x16_max(v); return x32_max(v);
}
__device__ __forceinline__ rsrc_t mkrs(const void* p, unsigned bytes) { return __builtin_amdgcn_make_buffer_rsrc((void*)p, 0, (int)bytes, 0x00020000); }
__device__ __forceinline__ rsrc_t selr(bool c, rsrc_t a, rsrc_t b) { return c ? a : b; }
template <int AUX> __device__ __forceinline__ v4u ld4(rsrc_t r, unsigned vo, unsigned so) { return __builtin_amdgcn_raw_buffer_load_b128(r, (int)vo, (int)so, AUX); }
template <int AUX> __device__ __forceinline__ v2u ld2(rsrc_t r, unsigned vo, unsigned so) { return __builtin_amdgcn_raw_buffer_load_b64(r, (int)vo, (int)so, AUX); }
__device__ __forceinline__ void st4c(v4u v, rsrc_t r, unsigned vo) { __builtin_amdgcn_raw_buffer_store_b128(v, r, (int)vo, 0, 16); }
__device__ __forceinline__ void st2c(v2u v, rsrc_t r, unsigned vo) { __builtin_amdgcn_raw_buffer_store_b64(v, r, (int)vo, 0, 16); }
__device__ __forceinline__ void st1c(unsigned v, rsrc_t r, unsigned vo) { __builtin_amdgcn_raw_buffer_store_b32(v, r, (int)vo, 0, 16); }
__device__ __forceinline__ unsigned canon(float f) { unsigned u = asu(f); return u < 0xFFFFFFFEu ? u : 0xFFFFFFFEu; }
__device__ __forceinline__ bool full4(v4u v) { return v[0] != SENT && v[1] != SENT && v[2] != SENT && v[3] != SENT; }
#define SB() __builtin_amdgcn_sched_barrier(0)
__device__ __forceinline__ void bar() { __builtin_amdgcn_s_waitcnt(0xC07F); __builtin_amdgcn_s_barrier(); }
#define WAITV(n) __builtin_amdgcn_s_waitcnt(((n) & 15) | (((n) >> 4) << 14) | 0x0F70)
template <int V> struct IC { static constexpr int value = V; constexpr operator int() const { return V; } };
template <int A, int B, class Fn> __device__ __forceinline__ void static_for(Fn&& f) { if constexpr (A < B) { f(IC<A>{}); static_for<A + 1, B>(f); } }
#define LDS_FENCE() asm volatile("" ::: "memory")

constexpr float EXP_C = 0.0625f * 1.4426950408889634f;

struct Att {
  float m, l;
  f32x2 a0[4], a1[4];
};

template <int NL, class ISSUE>
__device__ __forceinline__ float scores(v4u (&kb)[8], const v4u& q0, const v4u& q1, int lane, ISSUE issue) {
  float x[2 * NL];
#pragma unroll
  for (int i = 0; i < NL; ++i) {
    float a = 0.f, b = 0.f;
#pragma unroll
    for (int j = 0; j < (XC ? 1 : 4); ++j) { a = dot2(kb[i][j], q0[j], a); b = dot2(kb[i][j], q1[j], b); }
    x[2 * i] = a; x[2 * i + 1] = b;
    asm volatile("" : "+v"(x[2 * i]), "+v"(x[2 * i + 1]));
    issue(i);
    SB();
  }
#define TR_STEP(HALF, CTRL, BIT) _Pragma("unroll") for (int v = 0; v < (HALF); ++v) { \
    const float lo_ = x[v], hi_ = x[v + (HALF)]; const bool s_ = lane & (BIT); \
    x[v] = (s_ ? hi_ : lo_) + dpp<CTRL>(s_ ? lo_ : hi_); }
  if constexpr (NL == 8) { TR_STEP(8, ROR8, 8) }
  TR_STEP(4, XOR7, 4) TR_STEP(2, XOR2, 2) TR_STEP(1, XOR1, 1)
#undef TR_STEP
  if constexpr (NL == 4) x[0] += dpp<ROR8>(x[0]);
  return rbf(x16_add(x[0]));
}
template <class ISSUE>
__device__ __forceinline__ float phase_k4(v4u (&kb)[8], const v4u& q0, const v4u& q1, Att& st, int lane, ISSUE issue) {
  const float s = scores<4>(kb, q0, q1, lane, issue);
  float mx = fmaxf(s, dpp<ROR2>(s)); mx = fmaxf(mx, dpp<ROR4>(mx));
  mx = x32_max(mx);
  const float mn = fmaxf(st.m, mx);
  const float al = __builtin_amdgcn_exp2f((st.m - mn) * EXP_C);
  const float p = __builtin_amdgcn_exp2f((s - mn) * EXP_C);
  st.m = mn; st.l = st.l * al + p * 0.5f;
  const float al0 = dpp<QP_EVEN>(al), al1 = dpp<QP_ODD>(al);
#pragma unroll
  for (int j = 0; j < 4; ++j) { st.a0[j] *= al0; st.a1[j] *= al1; }
  SB();
  return p;
}
template <int I, int O, class ISSUE>
__device__ __forceinline__ void pv_step(v4u (&vb)[8], float p, Att& st, ISSUE& issue) {
  const float p0 = dpp<BCAST + 2 * I>(p), p1 = dpp<BCAST + 2 * I + 1>(p);
#pragma unroll
  for (int j = 0; j < (XC ? 1 : 4); ++j) { const f32x2 v = unp(vb[O + I][j]); st.a0[j] += v * p0; st.a1[j] += v * p1; }
  asm volatile("" : "+v"(st.a0[0]), "+v"(st.a0[1]), "+v"(st.a0[2]), "+v"(st.a0[3]), "+v"(st.a1[0]), "+v"(st.a1[1]), "+v"(st.a1[2]), "+v"(st.a1[3]));
  issue(O + I);
  SB();
}
template <int NL, class ISSUE>
__device__ __forceinline__ void phase_v(v4u (&vb)[8], float p, Att& st, ISSUE issue) {
  constexpr int O = 8 - NL;
  pv_step<0, O>(vb, p, st, issue); pv_step<1, O>(vb, p, st, issue); pv_step<2, O>(vb, p, st, issue); pv_step<3, O>(vb, p, st, issue);
  if constexpr (NL == 8) { pv_step<4, O>(vb, p, st, issue); pv_step<5, O>(vb, p, st, issue); pv_step<6, O>(vb, p, st, issue); pv_step<7, O>(vb, p, st, issue); }
}

#define STAMP(k) do {} while (0)
#define STAMP_OUT() do {} while (0)

__device__ __forceinline__ f32x2 ln_stats(const v4u& xa, int lane) {
  f32x2 xv[4];
  float sm = 0.f;
#pragma unroll
  for (int j = 0; j < 4; ++j) { xv[j] = unp(xa[j]); sm += xv[j].x + xv[j].y; }
  const float mean = wave_sum(sm) * (1.0f / D);
  float sq = 0.f;
#pragma unroll
  for (int j = 0; j < 4; ++j) { const f32x2 d = xv[j] - mean; sq += d.x * d.x + d.y * d.y; }
  sq = lane < 56 ? sq : 0.f;
  const float var = rbf(wave_sum(sq) * (1.0f / D));
  const float sd = rbf(sqrtf(rbf(var + 1e-5f)));
  return (f32x2){rbf(mean), 1.0f / sd};
}
__device__ __forceinline__ float ln_apply(float x, f32x2 st) { return rbf(rbf(x - st.x) * st.y); }
__device__ __forceinline__ unsigned pack2(float a, float b) { const unsigned u = f2bf(a) | (f2bf(b) << 16); return u < 0xFFFFFFFEu ? u : 0xFFFFFFFEu; }

__global__ void __launch_bounds__(NT, 2)
fused_kernel(const u16* __restrict__ xg, const u16* __restrict__ kc, const u16* __restrict__ vc, const u16* __restrict__ wq,
             const u16* __restrict__ wo, const u16* __restrict__ wg, const u16* __restrict__ wu, const u16* __restrict__ wd,
             u16* __restrict__ out, char* ws) {
  __shared__ __attribute__((aligned(16))) float s_part[8][512];
  __shared__ __attribute__((aligned(16))) float s_mv[8][64];
  __shared__ __attribute__((aligned(16))) unsigned s_q[256];
  __shared__ __attribute__((aligned(16))) u16 s_qs[64];
  __shared__ __attribute__((aligned(16))) float s_ml[8][4];
  __shared__ __attribute__((aligned(16))) float s_o[4][32];
  __shared__ __attribute__((aligned(16))) float s_ml2[8];
  __shared__ __attribute__((aligned(16))) float s_h[16];
  __shared__ __attribute__((aligned(16))) float s_sc[64][32];
  __shared__ int s_ctl[64];
  const int tid = threadIdx.x, lane = tid & 63, w = __builtin_amdgcn_readfirstlane(tid >> 6);
  const int bi = blockIdx.x, h = (((bi & 1) ? bi + 5 : bi) & 6) | (bi >> 7), s = ((bi >> 3) & 15) * 2 + (bi & 1);
  const int b = h * 32 + s;
  constexpr int PH = 2 * (2048 - 8 * CQ - 14 * CF - 10 * CO), GS = 32 / NG;
  static_assert(PH >= 0 && CQ >= 40 && CQ <= 64 && CF >= 40 && CF <= 64 && CO >= 40 && CO <= 64, "unit counts");
  auto prefix = [](int n) { return n * CO - min(n, 22) * (CO - CF) - min(n, 8) * (CF - CQ); };
  const int pg = s / GS, nk = s < 8 ? CQ : s < 22 ? CF : CO;
  const int first = 2 * prefix(s) + pg * PH / NG, pool0 = 2 * prefix((pg + 1) * GS) + pg * PH / NG, psz = (pg + 1) * PH / NG - pg * PH / NG;
  asm volatile("" :: "s"(xg), "s"(kc), "s"(vc), "s"(wq), "s"(wo), "s"(wg), "s"(wu), "s"(wd), "s"(out), "s"(ws));
  STAMP(0);
  const rsrc_t rx = mkrs(xg, D * 2);
  const rsrc_t rk = mkrs(kc + (size_t)h * SK * DH, XK ? 0 : SK * DH * 2);
  const rsrc_t rv = mkrs(vc + (size_t)h * SK * DH, XK ? 0 : SK * DH * 2);
  const rsrc_t rws = mkrs(ws, WS_END);
  const unsigned l16 = lane * 16;
  auto ubase = [&](int u) -> unsigned { return (unsigned)(first + 2 * u) * 4096u; };
  auto ldk = [&](unsigned base, int i) { return ld4<2>(rk, l16 + (i & 3) * 1024, base + (i >> 2) * 4096); };
  auto ldv = [&](unsigned base, int i) { return ld4<2>(rv, l16 + (i & 3) * 1024, base + (i >> 2) * 4096); };
  const unsigned area = (unsigned)b * AREA;
  auto area_of = [&](int ss) -> unsigned { return (unsigned)(h * 32 + ss) * AREA; };
  const unsigned lmask = lane < 56 ? 0u : OOB;

  const bool isq = s < 8, isf = s >= 8 && s < 22, isy = s >= 16;
  const int f = h * 14 + s - 8, y = h * 16 + s - 16, ro = h * 4 + (s >> 2);
  const bool isr = s < 16 && ((s + h) & 3) == 0 && ro < 28;
  const rsrc_t rwo = mkrs(wo + (size_t)(isy ? y : 0) * 32 * D, (isy && w < 7 && !XW) ? 32 * D * 2 : 0);
  const rsrc_t rwd = mkrs(wd + (size_t)((isy && y < 112) ? y : 0) * 16 * D, (isy && y < 112 && w < 7 && !XW) ? 16 * D * 2 : 0);
  const unsigned wo_off = (lane >> 3) * (4 * D * 2) + w * 128 + (lane & 7) * 16, wd_off = (lane >> 3) * (D * 2) + w * 128 + (lane & 7) * 16;
  const bool tile = isq || (isf && w < 4);
  const bool leader = (b == NWG - 1 && w == 1), member = (w == 0);

  const v4u sent = {SENT, SENT, SENT, SENT};
  unsigned poll_off = lane == 1 ? WS_GO : OOB;
  poll_off = lane == 0 ? WS_REQ + (b >> 2) * 16 : poll_off;
  poll_off = leader ? WS_ECHO + l16 : poll_off;
  v4u pv = {0u, 0u, 0u, 0u}, e1 = {0u, 0u, 0u, 0u};
  unsigned go_ref = 0;
  int* const ctl = &s_ctl[lane];
  auto ctl_get = [&](int i) { return __builtin_amdgcn_readfirstlane(__hip_atomic_load(&s_ctl[i], __ATOMIC_RELAXED, __HIP_MEMORY_SCOPE_WORKGROUP)); };
  auto ctl_set = [&](int i) { __hip_atomic_store(&s_ctl[i], 1, __ATOMIC_RELAXED, __HIP_MEMORY_SCOPE_WORKGROUP); };
  if (member) {
    st4c(sent, rws, area + A_Q + l16);
    int c = lane == C_NEXT ? TOFF * 128 : 0;
    c = lane == C_TICK ? 40 : c;
    if (XH) c = lane == C_GO ? 1 : c;
    if (XQ) c = lane == C_Q ? 1 : c;
    c = lane == C_T0 ? (int)(__builtin_amdgcn_s_memrealtime() & 0x7fffffff) : c;
    __hip_atomic_store(ctl, c, __ATOMIC_RELAXED, __HIP_MEMORY_SCOPE_WORKGROUP);
    st1c(0u, rws, (lane == 0 && s == pg * GS) ? WS_POOL + (h * NG + pg) * 256 : OOB);
  }
  *reinterpret_cast<v4u*>(&s_sc[w * 8][lane * 4]) = sent;
  int t0 = 0;
  auto now = [&]() { return ((int)__builtin_amdgcn_s_memrealtime() - t0) & 0x7fffff; };
  const int neg = lane == C_NEXT ? 0 : (int)0x80000000;
  int inc = lane == C_NEXT ? TAU : 0;
  auto token = [&](int burst) {
    const int t = max(now() * 128 - burst, 0);
    __hip_atomic_fetch_max(ctl, t | neg, __ATOMIC_RELAXED, __HIP_MEMORY_SCOPE_WORKGROUP);
    return __hip_atomic_fetch_add(ctl, inc, __ATOMIC_RELAXED, __HIP_MEMORY_SCOPE_WORKGROUP);
  };
  auto due_of = [&](int x) { return __builtin_amdgcn_readlane(x, C_NEXT) >> 7; };
  auto until = [&](int due) {
#pragma unroll 1
    for (int it = 0; it < 4096 && now() < due; ++it) __builtin_amdgcn_s_sleep(1);
    SB();
  };
  auto publish_q = [&](const v4u& q) {
    *reinterpret_cast<v4u*>(&s_q[lane * 4]) = q;
    LDS_FENCE();
    __builtin_amdgcn_s_waitcnt(0xC07F);
    ctl_set(C_Q);
  };
  v4u pre[4][8], ring[8];
  auto unit0 = [&]() {
#pragma unroll
    for (int i = 0; i < 8; ++i) { pre[0][i] = ldk(ubase(w), i); SB(); }
  };
  SB();
  if (tile) {
    v4u tw[8];
    const int oct = isq ? 7 * w : (f & 3) * 14 + (w >> 1) * 7;
    const int stride = isq ? NQ * 2 : F * 2;
    const rsrc_t rt = mkrs(isq ? wq : (w & 1) ? wu : wg, XT ? 0 : D * stride);
    const unsigned to = (unsigned)((oct + (lane >> 3)) * 8 * stride + (isq ? h * 8 + s : f >> 2) * 128 + (lane & 7) * 16) | lmask;
    const v4u xa = ld4<0>(rx, l16, 0);
    const v4u xb = ld4<0>(rx, (unsigned)(oct + (lane >> 3)) * 16 | lmask, 0);
    SB();
#pragma unroll
    for (int i = 0; i < 8; ++i) { tw[i] = ld4<2>(rt, to, i * stride); SB(); }
    if (EARLY == 2) unit0();
    LDS_FENCE(); bar(); LDS_FENCE();
    t0 = ctl_get(C_T0);
    SB();
    const f32x2 ls = ln_stats(xa, lane);
    f32x2 acc[4];
#pragma unroll
    for (int i = 0; i < 8; ++i) {
      const float xn = ln_apply(asf((i & 1) ? (xb[i >> 1] & 0xffff0000u) : (xb[i >> 1] << 16)), ls);
#pragma unroll
      for (int j = 0; j < 4; ++j) acc[j] = i ? acc[j] + unp(tw[i][j]) * xn : unp(tw[i][j]) * xn;
    }
    float y0 = tr32(acc[0].x, acc[2].x), y1 = tr32(acc[0].y, acc[2].y), y2 = tr32(acc[1].x, acc[3].x), y3 = tr32(acc[1].y, acc[3].y);
    y0 = tr16(y0, y2); y1 = tr16(y1, y3);
    const bool s_ = lane & 8;
    y0 = (s_ ? y1 : y0) + dpp<ROR8>(s_ ? y0 : y1);
    s_mv[w][(lane & 7) * 8 + (lane >> 3)] = y0;
    STAMP(6);
    if (EARLY == 1) { token(PBURST); unit0(); }
    if (isq) {
      LDS_FENCE(); bar(); LDS_FENCE();
      if (w < 4) {
        float a = 0.f;
#pragma unroll
        for (int k = 0; k < 8; ++k) a += s_mv[k][lane];
        s_qs[lane] = (u16)f2bf(a);
        LDS_FENCE();
        v4u qs = *reinterpret_cast<const v4u*>(&s_qs[(lane & 7) * 8]);
#pragma unroll
        for (int j = 0; j < 4; ++j) qs[j] = qs[j] < 0xFFFFFFFEu ? qs[j] : 0xFFFFFFFEu;
        st4c(qs, rws, area_of(w * 8 + (lane >> 3)) + A_Q + s * 128 + (lane & 7) * 16);
      }
    } else {
      LDS_FENCE();
      __builtin_amdgcn_s_waitcnt(0xC07F);
      __hip_atomic_fetch_add(ctl, lane == C_FC ? 1 : 0, __ATOMIC_RELAXED, __HIP_MEMORY_SCOPE_WORKGROUP);
    }
  } else if (leader) {
    e1 = ld4<16>(rws, poll_off, 0);
    go_ref = __builtin_amdgcn_raw_buffer_load_b32(rws, (int)WS_GO, 0, 16);
  }
  SB();
  if (member && (isy || isr)) {
#pragma unroll
    for (int i = 1; i < 6; ++i) st4c(sent, rws, area + i * 1024 + l16);
    if (isr) {
#pragma unroll
      for (int i = 6; i < 9; ++i) st4c(sent, rws, area + i * 1024 + l16);
    }
  }
  STAMP(5);
  SB();
  v4u qv;
  bool ok = !member || XQ;
  static_for<0, 40>([&](auto N) {
    constexpr int n = N;
    if constexpr (n == 0) {
      if (EARLY == 0 && tile) until(due_of(token(PBURST)));
      if (EARLY == 0 || !tile) unit0();
    }
    if constexpr (n == 8) {
      if (!tile) {
        LDS_FENCE(); bar(); LDS_FENCE();
        t0 = ctl_get(C_T0);
        if (leader) {
          e1 += 1u;
          go_ref += 1u;
          st4c(e1, rws, WS_REQ + l16);
        }
      }
    }
    if constexpr (n > 0 && n % 8 == 0) {
      const int x = token(PBURST);
      if constexpr (n > 8) {
        if (!ok) {
          ok = __builtin_amdgcn_ballot_w64(!full4(qv)) == 0;
          if (ok) publish_q(qv);
        }
      }
      SB();
      until(due_of(x));
      if (!ok) {
        if constexpr (n == 8) WAITV(7);
        qv = ld4<16>(rws, area + A_Q + l16, 0);
      }
      SB();
    }
    if constexpr (n == QPOS) {
      STAMP(9);
      pv = ld4<16>(rws, (member || leader) ? poll_off : OOB, 0);
      SB();
    }
    if constexpr (n >= 8) {
      if constexpr (n >= PACE) WAITV(PACE + ((n >= QPOS && n < QPOS + PACE) ? 1 : 0));
      if constexpr (n < 32) pre[n >> 3][n & 7] = ldk(ubase((n & 56) + w), n & 7); else ring[n & 7] = ldk(ubase(32 + w), n & 7);
    }
    SB();
    if constexpr (n - PACE == 7 || n - PACE == 15 || n - PACE == 23 || n - PACE == 31) STAMP(16 + (n - PACE) / 8);
  });
  inc = lane == C_TICK ? 1 : inc;
  int tok = token(KBURST);
  STAMP(10);
  if (!ok) {
    int tries = 0;
    for (;; ++tries) {
      ok = __builtin_amdgcn_ballot_w64(!full4(qv)) == 0;
      if (ok || tries >= QTRIES) break;
      __builtin_amdgcn_s_sleep(4);
      qv = ld4<16>(rws, area + A_Q + l16, 0);
      WAITV(0);
    }
    if (!ok) {
      const v4u xc = ld4<0>(rx, l16, 0);
      const f32x2 ls = ln_stats(xc, lane);
      const rsrc_t rwq = mkrs(wq, D * NQ * 2);
      f32x2 acc[4] = {{0.f, 0.f}, {0.f, 0.f}, {0.f, 0.f}, {0.f, 0.f}};
#pragma unroll 1
      for (int r = 0; r < D / 2; ++r) {
        unsigned xp = xc[0];
        xp = (r & 3) == 1 ? xc[1] : xp; xp = (r & 3) == 2 ? xc[2] : xp; xp = (r & 3) == 3 ? xc[3] : xp;
        const f32x2 xr = unp((unsigned)__builtin_amdgcn_readlane((int)xp, r >> 2));
        const v4u wa = ld4<0>(rwq, h * 1024 + l16, r * 2 * NQ * 2), wb = ld4<0>(rwq, h * 1024 + l16, (r * 2 + 1) * NQ * 2);
        const float x0 = ln_apply(xr.x, ls), x1 = ln_apply(xr.y, ls);
#pragma unroll
        for (int j = 0; j < 4; ++j) acc[j] += unp(wa[j]) * x0 + unp(wb[j]) * x1;
      }
#pragma unroll
      for (int j = 0; j < 4; ++j) qv[j] = pack2(acc[j].x, acc[j].y);
      WAITV(0);
    }
    publish_q(qv);
  }
  while (ctl_get(C_Q) == 0) __builtin_amdgcn_s_sleep(1);
  LDS_FENCE();
  const v4u q0 = *reinterpret_cast<const v4u*>(&s_q[(lane & 31) * 4]), q1 = *reinterpret_cast<const v4u*>(&s_q[128 + (lane & 31) * 4]);
  STAMP(1);
  SB();

  bool svc = (member || leader) && !XH;
  unsigned seenm = 0, last_echo = 0;
  bool echoed = false;
  auto service = [&]() {
    if (leader) {
      seenm |= (pv[0] == e1[0] ? 1u : 0u) | (pv[1] == e1[1] ? 2u : 0u) | (pv[2] == e1[2] ? 4u : 0u) | (pv[3] == e1[3] ? 8u : 0u);
      svc = __builtin_amdgcn_ballot_w64(seenm != 15u) != 0;
      st1c(go_ref, rws, (lane == 0 && !svc) ? WS_GO : OOB);
    } else {
      const unsigned req = (unsigned)__builtin_amdgcn_readfirstlane((int)((b & 2) ? ((b & 1) ? pv[3] : pv[2]) : ((b & 1) ? pv[1] : pv[0])));
      const unsigned go = (unsigned)__builtin_amdgcn_readlane((int)pv[0], 1);
      go_ref = echoed ? go_ref : go;
      svc = go == (unsigned)__builtin_amdgcn_readfirstlane((int)go_ref);
      st1c(req, rws, (lane == 0 && svc && !(echoed && req == last_echo)) ? WS_ECHO + b * 4 : OOB);
      echoed = true; last_echo = req;
      if (!svc) {
        ctl_set(C_GO);
        if (isf) {
          while (ctl_get(C_FC) != 4) __builtin_amdgcn_s_sleep(1);
          LDS_FENCE();
          const int j = (lane >> 3) & 3, pc = lane & 7, m = pc >> 2, col = j * 16 + (pc & 3) * 4;
          const f32x4 v = *reinterpret_cast<const f32x4*>(&s_mv[m][col]) + *reinterpret_cast<const f32x4*>(&s_mv[m + 2][col]);
          const int fd = (f & ~3) + j;
          st4c((v4u){canon(v[0]), canon(v[1]), canon(v[2]), canon(v[3])}, rws,
               ((unsigned)((fd >> 4) * 32 + 16 + (fd & 15)) * AREA + A_F + (f & 3) * 128 + pc * 16) | (lane < 32 ? 0u : OOB));
        }
      }
    }
    pv = ld4<16>(rws, svc ? poll_off : OOB, 0);
    SB();
  };

  auto wait_go = [&]() {
    for (int it = 0; it < SPIN_CAP; ++it) {
      if (ctl_get(C_GO)) break;
      if (svc) service();
      __builtin_amdgcn_s_sleep(2);
    }
  };

  Att st;
  int sx = 20;
  {
    const unsigned pco = lane == 0 ? WS_POOL + (h * NG + pg) * 256 : OOB;
    float* const sc = &s_sc[0][(lane >> 5) * 16 + (lane & 15)];
    const int lim = psz - TAP * GS * w / 8;
    rsrc_t nra, nrb;
    unsigned nva, nvb, nba, nbb, nsa, nsb;
    int kind = 0, nu = 32 + w, cu, gt = 0, th = 0;
    bool dc = false, have = false, again = true;
    auto claim = [&](bool on) { gt = __builtin_amdgcn_raw_ptr_buffer_atomic_add_i32(1, rws, (int)(on ? pco : OOB), 0, 0); };
    auto next = [&](bool tickets) {
      const int g = __builtin_amdgcn_readfirstlane(gt);
      th = dc ? g : th;
      have |= dc;
      cu = nu;
      const int t = tickets ? __builtin_amdgcn_readlane(tok, C_TICK) : 2 * nk;
      const int go = tickets ? __builtin_amdgcn_readlane(tok, C_GO) : 1;
      const bool fx = t < 2 * nk;
      if (tickets && !fx && !have && go) {
        claim(true);
        th = __builtin_amdgcn_readfirstlane(gt);
        have = true;
      }
      const bool more = have && again && (unsigned)th < (unsigned)psz, tw = !fx && !more;
      again = fx || (more && th < lim);
      kind = fx ? (t >= nk ? 1 : 0) : more ? 2 : 3;
      nu = t >= nk ? t - nk : t;
      nba = (unsigned)(fx ? first + 2 * nu : more ? pool0 + th : 0) * 4096u;
      nbb = fx ? nba + 4096 : nba;
      nra = selr(tw, rwo, selr(kind == 1, rv, rk));
      nrb = selr(tw, rwd, selr(kind == 0, rk, rv));
      nva = tw ? wo_off : l16; nvb = tw ? wd_off : l16;
      nsa = tw ? D * 2 : 1024; nsb = tw ? 8 * D * 2 : 1024;
      dc = fx ? !have && go && t + 8 >= 2 * nk : again;
      if (tickets) until(fx ? due_of(tok) : 0);
      SB();
    };
    auto ldn = [&](int i) { if (i == 0 && (!XA || dc)) claim(dc); ring[i] = i < 4 ? ld4<2>(nra, nva, nba + i * nsa) : ld4<2>(nrb, nvb, nbb + (i - 4) * nsb); };
    float mx;
    if (svc) service();
    {
      const float s0 = scores<8>(pre[0], q0, q1, lane, [&](int) {});
      const float s1 = scores<8>(pre[1], q0, q1, lane, [&](int) {});
      const float s2 = scores<8>(pre[2], q0, q1, lane, [&](int) {});
      const float s3 = scores<8>(pre[3], q0, q1, lane, [&](int) {});
      sc[w * 32] = s0; sc[(8 + w) * 32] = s1; sc[(16 + w) * 32] = s2; sc[(24 + w) * 32] = s3;
      mx = fmaxf(fmaxf(s0, s1), fmaxf(s2, s3));
      SB();
      STAMP(11);
    }
#pragma unroll 1
    while (kind == 0) {
      next(true);
      const float sv = scores<8>(ring, q0, q1, lane, ldn);
      sc[cu * 32] = sv;
      tok = token(KBURST);
      mx = fmaxf(mx, sv);
      STAMP(sx); ++sx;
      if (svc) service();
      SB();
    }
    STAMP(7);
    mx = fmaxf(mx, dpp<ROR2>(mx)); mx = fmaxf(mx, dpp<ROR4>(mx)); mx = fmaxf(mx, dpp<ROR8>(mx));
    st.m = x32_max(mx); st.l = 0.f;
#pragma unroll
    for (int j = 0; j < 4; ++j) { st.a0[j] = (f32x2){0.f, 0.f}; st.a1[j] = (f32x2){0.f, 0.f}; }
#pragma unroll 1
    while (kind == 1) {
      float sv;
      for (;;) {
        sv = __hip_atomic_load(&sc[nu * 32], __ATOMIC_RELAXED, __HIP_MEMORY_SCOPE_WORKGROUP);
        if (__builtin_amdgcn_ballot_w64(asu(sv) == SENT) == 0) break;
        __builtin_amdgcn_s_sleep(1);
      }
      if (__builtin_amdgcn_ballot_w64(sv > st.m) != 0) {
        float m2 = fmaxf(sv, dpp<ROR2>(sv)); m2 = fmaxf(m2, dpp<ROR4>(m2)); m2 = fmaxf(m2, dpp<ROR8>(m2));
        m2 = fmaxf(st.m, x32_max(m2));
        const float al = __builtin_amdgcn_exp2f((st.m - m2) * EXP_C);
        st.m = m2; st.l *= al;
        const float al0 = dpp<QP_EVEN>(al), al1 = dpp<QP_ODD>(al);
#pragma unroll
        for (int j = 0; j < 4; ++j) { st.a0[j] *= al0; st.a1[j] *= al1; }
      }
      const float p = __builtin_amdgcn_exp2f((sv - st.m) * EXP_C);
      st.l += p;
      SB();
      next(true);
      phase_v<8>(ring, p, st, ldn);
      tok = token(0);
      STAMP(sx); ++sx;
      if (svc) service();
      SB();
    }
    STAMP(8);
    if (!have) {
      wait_go();
      claim(true);
      dc = again = true;
      next(false);
#pragma unroll
      for (int i = 0; i < 8; ++i) ldn(i);
      SB();
    }
#pragma unroll 1
    while (kind == 2) {
      next(false);
      const float p = phase_k4(ring, q0, q1, st, lane, ldn);
      phase_v<4>(ring, p, st, ldn);
      STAMP(sx); ++sx;
    }
    asm volatile("" :: "v"(gt));
    LDS_FENCE();
  }
  STAMP(12);
  {
    float l = st.l;
    l += dpp<ROR2>(l); l += dpp<ROR4>(l); l += dpp<ROR8>(l); l = x32_add(l);
    float o[8];
#pragma unroll
    for (int j = 0; j < 4; ++j) { o[2 * j] = tr32(st.a0[j].x, st.a1[j].x); o[2 * j + 1] = tr32(st.a0[j].y, st.a1[j].y); }
    *reinterpret_cast<f32x4*>(&s_part[w][lane * 8]) = (f32x4){o[0], o[1], o[2], o[3]};
    *reinterpret_cast<f32x4*>(&s_part[w][lane * 8 + 4]) = (f32x4){o[4], o[5], o[6], o[7]};
    if (lane < 2) *reinterpret_cast<f32x2*>(&s_ml[w][lane * 2]) = (f32x2){st.m, l};
  }
  LDS_FENCE(); bar(); LDS_FENCE();
  STAMP(2);
  if (w == 4 || w == 5) {
    const int t = tid - 256;
    f32x2 ml[8];
#pragma unroll
    for (int k = 0; k < 8; ++k) ml[k] = *reinterpret_cast<const f32x2*>(&s_ml[k][(w - 4) * 2]);
    float M = -3.0e38f, L = 0.f;
#pragma unroll
    for (int k = 0; k < 8; ++k) M = fmaxf(M, ml[k].x);
    f32x4 o = {0.f, 0.f, 0.f, 0.f};
#pragma unroll
    for (int k = 0; k < 8; ++k) {
      const float e = __builtin_amdgcn_exp2f((ml[k].x - M) * EXP_C);
      o += *reinterpret_cast<const f32x4*>(&s_part[k][t * 4]) * e; L += ml[k].y * e;
    }
    v4u msg;
#pragma unroll
    for (int j = 0; j < 3; ++j) { const unsigned u = asu(o[j]); msg[j] = __builtin_amdgcn_perm(u < 0xFFFFFE00u ? u : 0xFFFFFE00u, asu(o[3]), 0x07060501u + j); }
    msg[3] = canon((t & 1) ? L : M);
    st4c(msg, rws, area_of(16 + (t >> 3)) + A_O1 + s * 128 + (t & 7) * 16);
    SB();
    asm volatile("" :: "v"(ring[0]), "v"(ring[1]), "v"(ring[2]), "v"(ring[3]), "v"(ring[4]), "v"(ring[5]), "v"(ring[6]), "v"(ring[7]), "v"(pv));
  }
  if (isy) {
    const unsigned pa = w < 4 ? area + A_O1 + w * 1024 + l16 : (w == 7 && y < 112 && !XH) ? area + A_F + (lane & 31) * 16 : OOB;
    v4u d = ld4<16>(rws, pa, 0);
    SB();
    f32x2 wf[6][4];
#pragma unroll
    for (int i = 0; i < 6; ++i)
#pragma unroll
      for (int j = 0; j < 4; ++j) {
        wf[i][j] = unp(ring[i][j]);
        asm volatile("" : "+v"(wf[i][j]));
      }
    SB();
    STAMP(14);
    for (int it = 0; it < SPIN_CAP; ++it) {
      if (__builtin_amdgcn_ballot_w64(!full4(d)) == 0) break;
      __builtin_amdgcn_s_sleep(1);
      d = ld4<16>(rws, pa, 0);
    }
    STAMP(15);
    if (w < 4) {
      const float ot = dpp<XOR1>(asf(d[3]));
      const float m = (lane & 1) ? ot : asf(d[3]), l = (lane & 1) ? asf(d[3]) : ot;
      float M = fmaxf(m, dpp<ROR8>(m)); M = x16_max(M); M = x32_max(M);
      const float e = __builtin_amdgcn_exp2f((m - M) * EXP_C);
      const float v3 = asf(__builtin_amdgcn_perm(__builtin_amdgcn_perm(d[2], d[1], 0x04000c0cu), d[0], 0x0706000cu));
      float y0 = tr32(asf(d[0]) * e, asf(d[2]) * e), y1 = tr32(asf(d[1]) * e, v3 * e), L = x32_add(l * e);
      y0 = tr16(y0, y1); L = x16_add(L);
      const bool s_ = lane & 8;
      y0 = (s_ ? L : y0) + dpp<ROR8>(s_ ? y0 : L);
      if (!s_) s_o[w][(lane & 7) * 4 + (lane >> 5) * 2 + ((lane >> 4) & 1)] = y0;
      if (lane == 8) *reinterpret_cast<f32x2*>(&s_ml2[w * 2]) = (f32x2){M, y0};
    } else if (w == 7) {
      float hv[4];
#pragma unroll
      for (int j = 0; j < 4; ++j) {
        float g = asf(d[j]);
        g += dpp<ROR8>(g); g = rbf(x16_add(g));
        const float u = dpp<ROR4>(g);
        hv[j] = rbf(rbf(g / (1.0f + __expf(-g))) * u);
      }
      if (lane < 4) {
#pragma unroll
        for (int j = 0; j < 4; ++j) s_h[((lane & 1) * 4 + j) * 2 + (lane >> 1)] = hv[j];
      }
    }
    LDS_FENCE(); bar(); LDS_FENCE();
    STAMP(3);
    if (w < 7) {
      const f32x4 ma = *reinterpret_cast<const f32x4*>(&s_ml2[0]), mb = *reinterpret_cast<const f32x4*>(&s_ml2[4]);
      f32x4 pq[4];
#pragma unroll
      for (int k = 0; k < 4; ++k) pq[k] = *reinterpret_cast<const f32x4*>(&s_o[k][(lane >> 3) * 4]);
      const f32x2 hv = *reinterpret_cast<const f32x2*>(&s_h[(lane >> 3) * 2]);
      const float M = fmaxf(fmaxf(ma[0], ma[2]), fmaxf(mb[0], mb[2]));
      const float e0 = __builtin_amdgcn_exp2f((ma[0] - M) * EXP_C), e1_ = __builtin_amdgcn_exp2f((ma[2] - M) * EXP_C);
      const float e2 = __builtin_amdgcn_exp2f((mb[0] - M) * EXP_C), e3 = __builtin_amdgcn_exp2f((mb[2] - M) * EXP_C);
      const float inv = __builtin_amdgcn_rcpf((ma[1] * e0 + ma[3] * e1_) + (mb[1] * e2 + mb[3] * e3));
      const f32x4 av = ((pq[0] * e0 + pq[1] * e1_) + (pq[2] * e2 + pq[3] * e3)) * inv;
      f32x2 acc[4];
#pragma unroll
      for (int j = 0; j < 4; ++j) acc[j] = wf[4][j] * hv.x + wf[5][j] * hv.y;
#pragma unroll
      for (int i = 0; i < 4; ++i) {
        const float a = rbf(av[i]);
#pragma unroll
        for (int j = 0; j < 4; ++j) acc[j] += wf[i][j] * a;
      }
      float z[4] = {tr32(acc[0].x, acc[2].x), tr32(acc[0].y, acc[2].y), tr32(acc[1].x, acc[3].x), tr32(acc[1].y, acc[3].y)};
#pragma unroll
      for (int j = 0; j < 4; ++j) { z[j] = x16_add(z[j]); z[j] += dpp<ROR8>(z[j]); }
      const unsigned ra = (unsigned)(w * 32 + ((lane >> 1) & 3) * 4 + ((-w) & 3)) * AREA;
      st4c((v4u){canon(z[0]), canon(z[1]), canon(z[2]), canon(z[3])}, rws, (ra + A_Y2 + y * 64 + (lane & 1) * 32 + (lane >> 5) * 16) | ((lane & 24) ? OOB : 0u));
    }
  } else if (isr) {
    const float xo = asf((unsigned)xg[ro * 16 + (lane & 15)] << 16);
    const unsigned pa = area + A_Y2 + w * 1024 + l16;
    v4u d;
    if (RSLEEP) __builtin_amdgcn_s_sleep(RSLEEP);
    for (int it = 0; it < SPIN_CAP; ++it) {
      d = ld4<16>(rws, pa, 0);
      if (__builtin_amdgcn_ballot_w64(!full4(d)) == 0) break;
      __builtin_amdgcn_s_sleep(1);
    }
    float y0 = tr32(asf(d[0]), asf(d[2])), y1 = tr32(asf(d[1]), asf(d[3]));
    y0 = tr16(y0, y1);
    y0 += dpp<ROR8>(y0); y0 += dpp<ROR4>(y0);
    if (!(lane & 12)) s_mv[w][(lane & 3) * 4 + (lane >> 5) * 2 + ((lane >> 4) & 1)] = y0;
    LDS_FENCE(); bar(); LDS_FENCE();
    if (w == 0) {
      float a = s_mv[lane >> 4][lane & 15] + s_mv[4 + (lane >> 4)][lane & 15];
      a = x16_add(a); a = x32_add(a);
      if (lane < 16) out[ro * 16 + lane] = (u16)f2bf(xo + a);
    }
  }
  STAMP(4);
  STAMP_OUT();
}

extern "C" void kernel_launch(void* const* d_in, const int* in_sizes, int n_in,
                              void* d_out, int out_size, void* d_ws, size_t ws_size,
                              hipStream_t stream) {
  fused_kernel<<<NWG, NT, 0, stream>>>((const u16*)d_in[0], (const u16*)d_in[1], (const u16*)d_in[2], (const u16*)d_in[3],
                                       (const u16*)d_in[4], (const u16*)d_in[6], (const u16*)d_in[7], (const u16*)d_in[8],
                                       (u16*)d_out, (char*)d_ws);
}
```

```cpp
#include <hip/hip_runtime.h>
#include <hip/hip_bf16.h>
#include <stdint.h>

#define QPOS 24
#define PACE 8
#define QTRIES 8
#define NG 16
#define CQ 56
#define CF (CQ + 2)
#define CO (CQ + 4)
#define TAU 3600
#define TOFF 150
#define EARLY 1
#define HM 5
#define PBURST 0
#define KBURST 0
#define TAP 8
#define RSLEEP 0
#define TWP 0
#define DIAG 0

typedef unsigned v4u __attribute__((ext_vector_type(4)));
typedef unsigned v2u __attribute__((ext_vector_type(2)));
typedef float f32x4 __attribute__((ext_vector_type(4)));
typedef float f32x2 __attribute__((ext_vector_type(2)));
typedef __bf16 bf16x2 __attribute__((ext_vector_type(2)));
typedef __bf16 bf16x8 __attribute__((ext_vector_type(8)));
typedef unsigned short u16;
typedef __amdgpu_buffer_rsrc_t rsrc_t;

constexpr int D = 448, SK = 32768, DH = 256, F = 1792, NQ = 4096;
constexpr int NWG = 256, NT = 512;
constexpr unsigned OOB = 0x80000000u;
constexpr unsigned SENT = 0xFFFFFFFFu;
constexpr unsigned AREA = 9216;
constexpr unsigned A_Q = 0;
constexpr unsigned A_O1 = 1024;
constexpr unsigned A_F = 5120;
constexpr unsigned A_Y2 = 1024;
constexpr unsigned WS_REQ = NWG * AREA;
constexpr unsigned WS_ECHO = WS_REQ + 1024;
constexpr unsigned WS_GO = WS_ECHO + 1024;
constexpr unsigned WS_POOL = WS_GO + 256;
constexpr unsigned WS_END = WS_POOL + 8 * NG * 256;
constexpr int SPIN_CAP = 1 << 18;
constexpr int C_NEXT = 0, C_TICK = 1, C_GO = 2, C_Q = 3, C_T0 = 4, C_FC = 5;

__device__ __forceinline__ float asf(unsigned u) { return __builtin_bit_cast(float, u); }
__device__ __forceinline__ unsigned asu(float f) { return __builtin_bit_cast(unsigned, f); }
__device__ __forceinline__ float rbf(float f) { __bf16 b = (__bf16)f; return (float)b; }
__device__ __forceinline__ unsigned f2bf(float f) { __bf16 b = (__bf16)f; return (unsigned)__builtin_bit_cast(u16, b); }
__device__ __forceinline__ float dot2(unsigned k, unsigned q, float acc) {
  return __builtin_amdgcn_fdot2_f32_bf16(__builtin_bit_cast(bf16x2, k), __builtin_bit_cast(bf16x2, q), acc, false);
}
__device__ __forceinline__ f32x2 unp(unsigned u) { f32x2 r; r.x = asf(u << 16); r.y = asf(u & 0xffff0000u); return r; }
template <int CTRL> __device__ __forceinline__ float dpp(float x) {
  return asf((unsigned)__builtin_amdgcn_update_dpp(0, (int)asu(x), CTRL, 0xf, 0xf, true));
}
constexpr int XOR1 = 0xB1, XOR2 = 0x4E, XOR7 = 0x141, ROR8 = 0x128, ROR4 = 0x124, ROR2 = 0x122, BCAST = 0x150;
constexpr int QP_EVEN = 0xA0, QP_ODD = 0xF5;
__device__ __forceinline__ float x16_add(float x) { auto s = __builtin_amdgcn_permlane16_swap(asu(x), asu(x), false, false); return asf(s[0]) + asf(s[1]); }
__device__ __forceinline__ float x32_add(float x) { auto s = __builtin_amdgcn_permlane32_swap(asu(x), asu(x), false, false); return asf(s[0]) + asf(s[1]); }
__device__ __forceinline__ float x16_max(float x) { auto s = __builtin_amdgcn_permlane16_swap(asu(x), asu(x), false, false); return fmaxf(asf(s[0]), asf(s[1])); }
__device__ __forceinline__ float x32_max(float x) { auto s = __builtin_amdgcn_permlane32_swap(asu(x), asu(x), false, false); return fmaxf(asf(s[0]), asf(s[1])); }
__device__ __forceinline__ float tr32(float a, float b) { auto s = __builtin_amdgcn_permlane32_swap(asu(a), asu(b), false, false); return asf(s[0]) + asf(s[1]); }
__device__ __forceinline__ float tr16(float a, float b) { auto s = __builtin_amdgcn_permlane16_swap(asu(a), asu(b), false, false); return asf(s[0]) + asf(s[1]); }
__device__ __forceinline__ float wave_sum(float v) {
  v += dpp<XOR1>(v); v += dpp<XOR2>(v); v += dpp<XOR7>(v); v += dpp<ROR8>(v);
  v = x16_add(v); return x32_add(v);
}
__device__ __forceinline__ float wave_max(float v) {
  v = fmaxf(v, dpp<XOR1>(v)); v = fmaxf(v, dpp<XOR2>(v)); v = fmaxf(v, dpp<XOR7>(v)); v = fmaxf(v, dpp<ROR8>(v));
  v = x16_max(v); return x32_max(v);
}
__device__ __forceinline__ rsrc_t mkrs(const void* p, unsigned bytes) { return __builtin_amdgcn_make_buffer_rsrc((void*)p, 0, (int)bytes, 0x00020000); }
__device__ __forceinline__ rsrc_t selr(bool c, rsrc_t a, rsrc_t b) { return c ? a : b; }
__device__ __forceinline__ unsigned selu(bool c, unsigned a, unsigned b) { return c ? a : b; }
template <int AUX> __device__ __forceinline__ v4u ld4(rsrc_t r, unsigned vo, unsigned so) { return __builtin_amdgcn_raw_buffer_load_b128(r, (int)vo, (int)so, AUX); }
template <int AUX> __device__ __forceinline__ v2u ld2(rsrc_t r, unsigned vo, unsigned so) { return __builtin_amdgcn_raw_buffer_load_b64(r, (int)vo, (int)so, AUX); }
__device__ __forceinline__ void st4c(v4u v, rsrc_t r, unsigned vo) { __builtin_amdgcn_raw_buffer_store_b128(v, r, (int)vo, 0, 16); }
__device__ __forceinline__ void st2c(v2u v, rsrc_t r, unsigned vo) { __builtin_amdgcn_raw_buffer_store_b64(v, r, (int)vo, 0, 16); }
__device__ __forceinline__ void st1c(unsigned v, rsrc_t r, unsigned vo) { __builtin_amdgcn_raw_buffer_store_b32(v, r, (int)vo, 0, 16); }
__device__ __forceinline__ unsigned canon(float f) { unsigned u = asu(f); return u < 0xFFFFFFFEu ? u : 0xFFFFFFFEu; }
__device__ __forceinline__ bool full4(v4u v) { return v[0] != SENT && v[1] != SENT && v[2] != SENT && v[3] != SENT; }
#define SB() __builtin_amdgcn_sched_barrier(0)
__device__ __forceinline__ void bar() { __builtin_amdgcn_s_waitcnt(0xC07F); __builtin_amdgcn_s_barrier(); }
#define WAITV(n) __builtin_amdgcn_s_waitcnt(((n) & 15) | (((n) >> 4) << 14) | 0x0F70)
template <int V> struct IC { static constexpr int value = V; constexpr operator int() const { return V; } };
template <int A, int B, class Fn> __device__ __forceinline__ void static_for(Fn&& f) { if constexpr (A < B) { f(IC<A>{}); static_for<A + 1, B>(f); } }
#define LDS_FENCE() asm volatile("" ::: "memory")

constexpr float EXP_C = 0.0625f * 1.4426950408889634f;

struct Att {
  float m, l;
  f32x4 a[2];
};
__device__ __forceinline__ f32x4 mfma(const v4u& a, const v4u& b, const f32x4& c) {
  return __builtin_amdgcn_mfma_f32_16x16x32_bf16(__builtin_bit_cast(bf16x8, a), __builtin_bit_cast(bf16x8, b), c, 0, 0, 0);
}
__device__ __forceinline__ float red_max(float v) { v = fmaxf(v, dpp<XOR1>(v)); v = fmaxf(v, dpp<XOR2>(v)); return x32_max(x16_max(v)); }

template <int NL, class ISSUE>
__device__ __forceinline__ float scores(v4u (&kb)[8], const v4u (&qb)[8], int lane, ISSUE issue) {
  f32x4 d = {0.f, 0.f, 0.f, 0.f};
#pragma unroll
  for (int i = 0; i < NL; ++i) {
    d = mfma(kb[i], qb[i], d);
    asm volatile("" : "+v"(d));
    issue(i);
    SB();
  }
  const bool odd = lane & 1;
  return rbf((odd ? d[3] : d[0]) + dpp<XOR1>(odd ? d[1] : d[2]));
}
template <class ISSUE>
__device__ __forceinline__ float phase_k4(v4u (&kb)[8], const v4u (&qb)[8], Att& st, int lane, ISSUE issue) {
  float s = scores<4>(kb, qb, lane, issue);
  s = (lane & 2) ? -__builtin_inff() : s;
  const float mn = fmaxf(st.m, red_max(s));
  const float al = __builtin_amdgcn_exp2f((st.m - mn) * EXP_C);
  const float p = __builtin_amdgcn_exp2f((s - mn) * EXP_C);
  st.m = mn; st.l = st.l * al + p;
  st.a[0] *= al; st.a[1] *= al;
  SB();
  return p;
}
template <int I, int O, class ISSUE>
__device__ __forceinline__ void pv_step(v4u (&vb)[8], unsigned pp, const v4u& pm, Att& st, ISSUE& issue) {
  const v4u pb = pm & (unsigned)__builtin_amdgcn_update_dpp(0, (int)pp, I * 0x55, 0xf, 0xf, true);
  st.a[0] = mfma(vb[O + 2 * I], pb, st.a[0]);
  asm volatile("" : "+v"(st.a[0]));
  issue(O + 2 * I);
  SB();
  st.a[1] = mfma(vb[O + 2 * I + 1], pb, st.a[1]);
  asm volatile("" : "+v"(st.a[1]));
  issue(O + 2 * I + 1);
  SB();
}
template <int NL, class ISSUE>
__device__ __forceinline__ void phase_v(v4u (&vb)[8], float p, const v4u& pm, Att& st, ISSUE issue) {
  constexpr int O = 8 - NL;
  const unsigned pp = __builtin_bit_cast(unsigned, (bf16x2){(__bf16)p, (__bf16)p});
  pv_step<0, O>(vb, pp, pm, st, issue); pv_step<1, O>(vb, pp, pm, st, issue);
  if constexpr (NL == 8) { pv_step<2, O>(vb, pp, pm, st, issue); pv_step<3, O>(vb, pp, pm, st, issue); }
}

#define STAMP(k) do {} while (0)
#define STAMP_OUT() do {} while (0)

__device__ __forceinline__ f32x2 ln_stats(const v4u& xa, int lane) {
  f32x2 xv[4];
  float sm = 0.f;
#pragma unroll
  for (int j = 0; j < 4; ++j) { xv[j] = unp(xa[j]); sm += xv[j].x + xv[j].y; }
  const float mean = wave_sum(sm) * (1.0f / D);
  float sq = 0.f;
#pragma unroll
  for (int j = 0; j < 4; ++j) { const f32x2 d = xv[j] - mean; sq += d.x * d.x + d.y * d.y; }
  sq = lane < 56 ? sq : 0.f;
  const float var = rbf(wave_sum(sq) * (1.0f / D));
  const float sd = rbf(sqrtf(rbf(var + 1e-5f)));
  return (f32x2){rbf(mean), 1.0f / sd};
}
__device__ __forceinline__ float ln_apply(float x, f32x2 st) { return rbf(rbf(x - st.x) * st.y); }
__device__ __forceinline__ unsigned pack2(float a, float b) { const unsigned u = f2bf(a) | (f2bf(b) << 16); return u < 0xFFFFFFFEu ? u : 0xFFFFFFFEu; }

__global__ void __launch_bounds__(NT, 2)
fused_kernel(const u16* __restrict__ xg, const u16* __restrict__ kc, const u16* __restrict__ vc, const u16* __restrict__ wq,
             const u16* __restrict__ wo, const u16* __restrict__ wg, const u16* __restrict__ wu, const u16* __restrict__ wd,
             u16* __restrict__ out, char* ws) {
  __shared__ __attribute__((aligned(16))) float s_part[8][512];
  __shared__ __attribute__((aligned(16))) float s_mv[8][64];
  __shared__ __attribute__((aligned(16))) unsigned s_q[256];
  __shared__ __attribute__((aligned(16))) u16 s_qs[64];
  __shared__ __attribute__((aligned(16))) float s_ml[8][4];
  __shared__ __attribute__((aligned(16))) float s_o[4][32];
  __shared__ __attribute__((aligned(16))) float s_ml2[8];
  __shared__ __attribute__((aligned(16))) float s_h[16];
  __shared__ __attribute__((aligned(16))) float s_sc[64][32];
  __shared__ int s_ctl[64];
  const int tid = threadIdx.x, lane = tid & 63, w = __builtin_amdgcn_readfirstlane(tid >> 6);
  const int bi = blockIdx.x, h = (((bi & 1) ? bi + 5 : bi) & 6) | (bi >> 7), s = ((bi >> 3) & 15) * 2 + (bi & 1);
  const int b = h * 32 + s;
  constexpr int PH = 2 * (2048 - 8 * CQ - 14 * CF - 10 * CO), GS = 32 / NG;
  static_assert(PH >= 0 && CQ >= 40 && CQ <= 64 && CF >= 40 && CF <= 64 && CO >= 40 && CO <= 64, "unit counts");
  auto prefix = [](int n) { return n * CO - min(n, 22) * (CO - CF) - min(n, 8) * (CF - CQ); };
  const int pg = s / GS, nk = s < 8 ? CQ : s < 22 ? CF : CO;
  const int first = 2 * prefix(s) + pg * PH / NG, pool0 = 2 * prefix((pg + 1) * GS) + pg * PH / NG, psz = (pg + 1) * PH / NG - pg * PH / NG;
  asm volatile("" :: "s"(xg), "s"(kc), "s"(vc), "s"(wq), "s"(wo), "s"(wg), "s"(wu), "s"(wd), "s"(out), "s"(ws));
  STAMP(0);
  const rsrc_t rx = mkrs(xg, D * 2);
  const rsrc_t rk = mkrs(kc + (size_t)h * SK * DH, SK * DH * 2);
  const rsrc_t rv = mkrs(vc + (size_t)h * SK * DH, SK * DH * 2);
  const rsrc_t rws = mkrs(ws, WS_END);
  const unsigned l16 = lane * 16;
  auto ubase = [&](int u) -> unsigned { return (unsigned)(first + 2 * u) * 4096u; };
  const unsigned kvo = ((lane >> 1) & 7) * 512 + (lane >> 4) * 32 + (lane & 1) * 16, vvo = (lane >> 4) * 1024 + (lane & 15) * 16;
  auto ldk = [&](unsigned base, int i) { return ld4<2>(rk, kvo + (i & 3) * 128, base + (i >> 2) * 4096); };
  const unsigned area = (unsigned)b * AREA;
  auto area_of = [&](int ss) -> unsigned { return (unsigned)(h * 32 + ss) * AREA; };
  const unsigned lmask = lane < 56 ? 0u : OOB;

  const bool isq = s < 8, isf = s >= 8 && s < 22, isy = s >= 16;
  const int f = h * 14 + s - 8, y = h * 16 + s - 16, ro = h * 4 + (s >> 2);
  const bool isr = s < 16 && ((s + h) & 3) == 0 && ro < 28;
  const rsrc_t rwo = mkrs(wo + (size_t)(isy ? y : 0) * 32 * D, (isy && w < 7) ? 32 * D * 2 : 0);
  const rsrc_t rwd = mkrs(wd + (size_t)((isy && y < 112) ? y : 0) * 16 * D, (isy && y < 112 && w < 7) ? 16 * D * 2 : 0);
  const unsigned wt_off = (lane >> 3) * (D * 2) + w * 128 + (lane & 7) * 16;
  const bool tile = isq || (isf && w < 4);
  const bool leader = (b == NWG - 1 && w == 1), member = (w == 0);

  const v4u sent = {SENT, SENT, SENT, SENT};
  unsigned poll_off = lane == 1 ? WS_GO : OOB;
  poll_off = lane == 0 ? WS_REQ + (b >> 2) * 16 : poll_off;
  poll_off = leader ? WS_ECHO + l16 : poll_off;
  v4u pv = {0u, 0u, 0u, 0u}, e1 = {0u, 0u, 0u, 0u};
  unsigned go_ref = 0;
  int* const ctl = &s_ctl[lane];
  auto ctl_get = [&](int i) { return __builtin_amdgcn_readfirstlane(__hip_atomic_load(&s_ctl[i], __ATOMIC_RELAXED, __HIP_MEMORY_SCOPE_WORKGROUP)); };
  auto ctl_set = [&](int i) { __hip_atomic_store(&s_ctl[i], 1, __ATOMIC_RELAXED, __HIP_MEMORY_SCOPE_WORKGROUP); };
  if (member) {
    st4c(sent, rws, area + A_Q + l16);
    int c = lane == C_NEXT ? TOFF * 128 : 0;
    c = lane == C_TICK ? 40 : c;
    c = lane == C_T0 ? (int)(__builtin_amdgcn_s_memrealtime() & 0x7fffffff) : c;
    __hip_atomic_store(ctl, c, __ATOMIC_RELAXED, __HIP_MEMORY_SCOPE_WORKGROUP);
    st1c(0u, rws, (lane == 0 && s == pg * GS) ? WS_POOL + (h * NG + pg) * 256 : OOB);
  }
  *reinterpret_cast<v4u*>(&s_sc[w * 8][lane * 4]) = sent;
  int t0 = 0;
  auto now = [&]() { return ((int)__builtin_amdgcn_s_memrealtime() - t0) & 0x7fffff; };
  const int neg = lane == C_NEXT ? 0 : (int)0x80000000;
  int inc = lane == C_NEXT ? TAU : 0;
  auto token = [&](int burst) {
    const int t = max(now() * 128 - burst, 0);
    __hip_atomic_fetch_max(ctl, t | neg, __ATOMIC_RELAXED, __HIP_MEMORY_SCOPE_WORKGROUP);
    return __hip_atomic_fetch_add(ctl, inc, __ATOMIC_RELAXED, __HIP_MEMORY_SCOPE_WORKGROUP);
  };
  auto due_of = [&](int x) { return __builtin_amdgcn_readlane(x, C_NEXT) >> 7; };
  auto until = [&](int due) {
#pragma unroll 1
    for (int it = 0; it < 4096 && now() < due; ++it) __builtin_amdgcn_s_sleep(1);
    SB();
  };
  auto publish_q = [&](const v4u& q) {
    *reinterpret_cast<v4u*>(&s_q[lane * 4]) = q;
    LDS_FENCE();
    __builtin_amdgcn_s_waitcnt(0xC07F);
    ctl_set(C_Q);
  };
  v4u pre[4][8], ring[8];
  auto unit0 = [&]() {
#pragma unroll
    for (int i = 0; i < 8; ++i) { pre[0][i] = ldk(ubase(w), i); SB(); }
  };
  SB();
  if (tile) {
    v4u tw[8];
    const int oct = isq ? 7 * w : (f & 3) * 14 + (w >> 1) * 7;
    const int stride = isq ? NQ * 2 : F * 2;
    const rsrc_t rt = mkrs(isq ? wq : (w & 1) ? wu : wg, D * stride);
    const unsigned to = (unsigned)((oct + (lane >> 3)) * 8 * stride + (isq ? h * 8 + s : f >> 2) * 128 + (lane & 7) * 16) | lmask;
    const v4u xa = ld4<0>(rx, l16, 0);
    const v4u xb = ld4<0>(rx, (unsigned)(oct + (lane >> 3)) * 16 | lmask, 0);
    SB();
#pragma unroll
    for (int i = 0; i < 8; ++i) { tw[i] = ld4<2>(rt, to, i * stride); SB(); }
    if (EARLY == 2) unit0();
    LDS_FENCE(); bar(); LDS_FENCE();
    t0 = ctl_get(C_T0);
    SB();
    const f32x2 ls = ln_stats(xa, lane);
    f32x2 acc[4];
#pragma unroll
    for (int i = 0; i < 8; ++i) {
      const float xn = ln_apply(asf((i & 1) ? (xb[i >> 1] & 0xffff0000u) : (xb[i >> 1] << 16)), ls);
#pragma unroll
      for (int j = 0; j < 4; ++j) acc[j] = i ? acc[j] + unp(tw[i][j]) * xn : unp(tw[i][j]) * xn;
    }
    float y0 = tr32(acc[0].x, acc[2].x), y1 = tr32(acc[0].y, acc[2].y), y2 = tr32(acc[1].x, acc[3].x), y3 = tr32(acc[1].y, acc[3].y);
    y0 = tr16(y0, y2); y1 = tr16(y1, y3);
    const bool s_ = lane & 8;
    y0 = (s_ ? y1 : y0) + dpp<ROR8>(s_ ? y0 : y1);
    s_mv[w][(lane & 7) * 8 + (lane >> 3)] = y0;
    STAMP(6);
    if (EARLY == 1) { token(PBURST); unit0(); }
    if (isq) {
      LDS_FENCE(); bar(); LDS_FENCE();
      if (w < 4) {
        float a = 0.f;
#pragma unroll
        for (int k = 0; k < 8; ++k) a += s_mv[k][lane];
        s_qs[lane] = (u16)f2bf(a);
        LDS_FENCE();
        v4u qs = *reinterpret_cast<const v4u*>(&s_qs[(lane & 7) * 8]);
#pragma unroll
        for (int j = 0; j < 4; ++j) qs[j] = qs[j] < 0xFFFFFFFEu ? qs[j] : 0xFFFFFFFEu;
        st4c(qs, rws, area_of(w * 8 + (lane >> 3)) + A_Q + s * 128 + (lane & 7) * 16);
      }
    } else {
      LDS_FENCE();
      __builtin_amdgcn_s_waitcnt(0xC07F);
      __hip_atomic_fetch_add(ctl, lane == C_FC ? 1 : 0, __ATOMIC_RELAXED, __HIP_MEMORY_SCOPE_WORKGROUP);
    }
  } else if (leader) {
    e1 = ld4<16>(rws, poll_off, 0);
    go_ref = __builtin_amdgcn_raw_buffer_load_b32(rws, (int)WS_GO, 0, 16);
  }
  SB();
  if (member && (isy || isr)) {
#pragma unroll
    for (int i = 1; i < 6; ++i) st4c(sent, rws, area + i * 1024 + l16);
    if (isr) {
#pragma unroll
      for (int i = 6; i < 9; ++i) st4c(sent, rws, area + i * 1024 + l16);
    }
  }
  STAMP(5);
  SB();
  v4u qv;
  bool ok = !member;
  static_for<0, 40>([&](auto N) {
    constexpr int n = N;
    if constexpr (n == 0) {
      if (EARLY == 0 && tile) until(due_of(token(PBURST)));
      if (EARLY == 0 || !tile) unit0();
    }
    if constexpr (n == 8) {
      if (!tile) {
        LDS_FENCE(); bar(); LDS_FENCE();
        t0 = ctl_get(C_T0);
        if (leader) {
          e1 += 1u;
          go_ref += 1u;
          st4c(e1, rws, WS_REQ + l16);
        }
      }
    }
    if constexpr (n > 0 && n % 8 == 0) {
      const int x = token(PBURST);
      if constexpr (n > 8) {
        if (!ok) {
          ok = __builtin_amdgcn_ballot_w64(!full4(qv)) == 0;
          if (ok) publish_q(qv);
        }
      }
      SB();
      until(due_of(x));
      if (!ok) {
        if constexpr (n == 8) WAITV(7);
        qv = ld4<16>(rws, area + A_Q + l16, 0);
      }
      SB();
    }
    if constexpr (n == QPOS) {
      STAMP(9);
      pv = ld4<16>(rws, (member || leader) ? poll_off : OOB, 0);
      SB();
    }
    if constexpr (n >= 8) {
      if constexpr (n >= PACE) WAITV(PACE + ((n >= QPOS && n < QPOS + PACE) ? 1 : 0));
      if constexpr (n < 32) pre[n >> 3][n & 7] = ldk(ubase((n & 56) + w), n & 7); else ring[n & 7] = ldk(ubase(32 + w), n & 7);
    }
    SB();
    if constexpr (n - PACE == 7 || n - PACE == 15 || n - PACE == 23 || n - PACE == 31) STAMP(16 + (n - PACE) / 8);
  });
  inc = lane == C_TICK ? 1 : inc;
  int tok = token(KBURST);
  STAMP(10);
  if (!ok) {
    int tries = 0;
    for (;; ++tries) {
      ok = __builtin_amdgcn_ballot_w64(!full4(qv)) == 0;
      if (ok || tries >= QTRIES) break;
      __builtin_amdgcn_s_sleep(4);
      qv = ld4<16>(rws, area + A_Q + l16, 0);
      WAITV(0);
    }
    if (!ok) {
      const v4u xc = ld4<0>(rx, l16, 0);
      const f32x2 ls = ln_stats(xc, lane);
      const rsrc_t rwq = mkrs(wq, D * NQ * 2);
      f32x2 acc[4] = {{0.f, 0.f}, {0.f, 0.f}, {0.f, 0.f}, {0.f, 0.f}};
#pragma unroll 1
      for (int r = 0; r < D / 2; ++r) {
        unsigned xp = xc[0];
        xp = (r & 3) == 1 ? xc[1] : xp; xp = (r & 3) == 2 ? xc[2] : xp; xp = (r & 3) == 3 ? xc[3] : xp;
        const f32x2 xr = unp((unsigned)__builtin_amdgcn_readlane((int)xp, r >> 2));
        const v4u wa = ld4<0>(rwq, h * 1024 + l16, r * 2 * NQ * 2), wb = ld4<0>(rwq, h * 1024 + l16, (r * 2 + 1) * NQ * 2);
        const float x0 = ln_apply(xr.x, ls), x1 = ln_apply(xr.y, ls);
#pragma unroll
        for (int j = 0; j < 4; ++j) acc[j] += unp(wa[j]) * x0 + unp(wb[j]) * x1;
      }
#pragma unroll
      for (int j = 0; j < 4; ++j) qv[j] = pack2(acc[j].x, acc[j].y);
      WAITV(0);
    }
    publish_q(qv);
  }
  while (ctl_get(C_Q) == 0) __builtin_amdgcn_s_sleep(1);
  LDS_FENCE();
  v4u qb[8];
#pragma unroll
  for (int i = 0; i < 4; ++i) {
    const v4u z = {0u, 0u, 0u, 0u}, q = *reinterpret_cast<const v4u*>(&s_q[(lane & 8) * 16 + i * 32 + (lane >> 4) * 8 + (lane & 1) * 4]);
    qb[i] = (lane & 2) ? z : q; qb[4 + i] = (lane & 2) ? q : z;
  }
  v4u pm;
#pragma unroll
  for (int j = 0; j < 4; ++j) pm[j] = ((lane >> 1) & 3) == j ? ((lane & 1) ? 0xffff0000u : 0xffffu) : 0u;
  STAMP(1);
  SB();

  bool svc = member || leader;
  unsigned seenm = 0, last_echo = 0;
  bool echoed = false;
  auto service = [&]() {
    if (leader) {
      seenm |= (pv[0] == e1[0] ? 1u : 0u) | (pv[1] == e1[1] ? 2u : 0u) | (pv[2] == e1[2] ? 4u : 0u) | (pv[3] == e1[3] ? 8u : 0u);
      svc = __builtin_amdgcn_ballot_w64(seenm != 15u) != 0;
      st1c(go_ref, rws, (lane == 0 && !svc) ? WS_GO : OOB);
    } else {
      const unsigned req = (unsigned)__builtin_amdgcn_readfirstlane((int)((b & 2) ? ((b & 1) ? pv[3] : pv[2]) : ((b & 1) ? pv[1] : pv[0])));
      const unsigned go = (unsigned)__builtin_amdgcn_readlane((int)pv[0], 1);
      go_ref = echoed ? go_ref : go;
      svc = go == (unsigned)__builtin_amdgcn_readfirstlane((int)go_ref);
      st1c(req, rws, (lane == 0 && svc && !(echoed && req == last_echo)) ? WS_ECHO + b * 4 : OOB);
      echoed = true; last_echo = req;
      if (!svc) {
        ctl_set(C_GO);
        if (isf) {
          while (ctl_get(C_FC) != 4) __builtin_amdgcn_s_sleep(1);
          LDS_FENCE();
          const int j = (lane >> 3) & 3, pc = lane & 7, m = pc >> 2, col = j * 16 + (pc & 3) * 4;
          const f32x4 v = *reinterpret_cast<const f32x4*>(&s_mv[m][col]) + *reinterpret_cast<const f32x4*>(&s_mv[m + 2][col]);
          const int fd = (f & ~3) + j;
          st4c((v4u){canon(v[0]), canon(v[1]), canon(v[2]), canon(v[3])}, rws,
               ((unsigned)((fd >> 4) * 32 + 16 + (fd & 15)) * AREA + A_F + (f & 3) * 128 + pc * 16) | (lane < 32 ? 0u : OOB));
        }
      }
    }
    pv = ld4<16>(rws, svc ? poll_off : OOB, 0);
    SB();
  };

  auto wait_go = [&]() {
    for (int it = 0; it < SPIN_CAP; ++it) {
      if (ctl_get(C_GO)) break;
      if (svc) service();
      __builtin_amdgcn_s_sleep(2);
    }
  };

  Att st;
  int sx = 20;
  {
    const unsigned pco = lane == 0 ? WS_POOL + (h * NG + pg) * 256 : OOB;
    float* const sc = &s_sc[0][(lane >> 3) * 4 + (lane & 3)];
    const int lim = psz - TAP * GS * w / 8;
    rsrc_t nra, nrb;
    unsigned nva, nvb, nba, nbb, nsa, nsb;
    int kind = 0, nu = 32 + w, cu, gt = 0, th = 0;
    bool dc = false, have = false, again = true;
    auto claim = [&]() { gt = __builtin_amdgcn_raw_ptr_buffer_atomic_add_i32(1, rws, (int)pco, 0, 0); };
    auto next = [&](bool tickets) {
      const int g = __builtin_amdgcn_readfirstlane(gt);
      th = dc ? g : th;
      have |= dc;
      cu = nu;
      const int t = tickets ? __builtin_amdgcn_readlane(tok, C_TICK) : 2 * nk;
      const int go = tickets ? __builtin_amdgcn_readlane(tok, C_GO) : 1;
      const bool fx = t < 2 * nk;
      if (tickets && !fx && !have && go) {
        claim();
        th = __builtin_amdgcn_readfirstlane(gt);
        have = true;
      }
      const bool more = have && again && (unsigned)th < (unsigned)psz, tw = !fx && !more;
      again = fx || (more && th < lim);
      kind = fx ? (t >= nk ? 1 : 0) : more ? 2 : 3;
      nu = t >= nk ? t - nk : t;
      nba = (unsigned)(fx ? first + 2 * nu : more ? pool0 + th : TWP ? 0x80000 : 0) * 4096u;
      nbb = fx ? nba + 4096 : nba;
      nra = selr(tw, rwo, selr(kind == 1, rv, rk));
      nrb = selr(tw, rwd, selr(kind == 0, rk, rv));
      nva = selu(tw, wt_off, selu(kind == 1, vvo, kvo)); nvb = selu(tw, wt_off, selu(kind == 0, kvo, vvo));
      nsa = selu(tw, 8 * D * 2, selu(kind == 1, 256, 128)); nsb = selu(tw, 8 * D * 2, selu(kind == 0, 128, 256));
      dc = fx ? !have && go && t + 8 >= 2 * nk : again;
      if (tickets) until(fx ? due_of(tok) : 0);
      SB();
    };
    auto ldn = [&](int i) { if (i == 0 && dc) claim(); ring[i] = i < 4 ? ld4<2>(nra, nva, nba + i * nsa) : ld4<2>(nrb, nvb, nbb + (i - 4) * nsb); };
    float mx;
    if (svc) service();
    {
      const float s0 = scores<8>(pre[0], qb, lane, [&](int) {});
      const float s1 = scores<8>(pre[1], qb, lane, [&](int) {});
      const float s2 = scores<8>(pre[2], qb, lane, [&](int) {});
      const float s3 = scores<8>(pre[3], qb, lane, [&](int) {});
      sc[w * 32] = s0; sc[(8 + w) * 32] = s1; sc[(16 + w) * 32] = s2; sc[(24 + w) * 32] = s3;
      mx = fmaxf(fmaxf(s0, s1), fmaxf(s2, s3));
      SB();
      STAMP(11);
    }
#pragma unroll 1
    while (kind == 0) {
      next(true);
      const float sv = scores<8>(ring, qb, lane, ldn);
      sc[cu * 32] = sv;
      tok = token(KBURST);
      mx = fmaxf(mx, sv);
      STAMP(sx); ++sx;
      if (svc) service();
      SB();
    }
    STAMP(7);
    st.m = red_max(mx); st.l = 0.f;
    st.a[0] = st.a[1] = (f32x4){0.f, 0.f, 0.f, 0.f};
#pragma unroll 1
    while (kind == 1) {
      float sv;
      for (;;) {
        sv = __hip_atomic_load(&sc[nu * 32], __ATOMIC_RELAXED, __HIP_MEMORY_SCOPE_WORKGROUP);
        if (__builtin_amdgcn_ballot_w64(asu(sv) == SENT) == 0) break;
        __builtin_amdgcn_s_sleep(1);
      }
      if (__builtin_amdgcn_ballot_w64(sv > st.m) != 0) {
        const float m2 = fmaxf(st.m, red_max(sv));
        const float al = __builtin_amdgcn_exp2f((st.m - m2) * EXP_C);
        st.m = m2; st.l *= al;
        st.a[0] *= al; st.a[1] *= al;
      }
      const float p = __builtin_amdgcn_exp2f((sv - st.m) * EXP_C);
      st.l += p;
      SB();
      next(true);
      phase_v<8>(ring, p, pm, st, ldn);
      tok = token(0);
      STAMP(sx); ++sx;
      if (svc) service();
      SB();
    }
    STAMP(8);
    if (!have) {
      wait_go();
      claim();
      dc = again = true;
      next(false);
#pragma unroll
      for (int i = 0; i < 8; ++i) ldn(i);
      SB();
    }
#pragma unroll 1
    while (kind == 2) {
      next(false);
      const float p = phase_k4(ring, qb, st, lane, ldn);
      phase_v<4>(ring, p, pm, st, ldn);
      STAMP(sx); ++sx;
    }
    asm volatile("" :: "v"(gt));
    LDS_FENCE();
  }
  STAMP(12);
  auto tailw = [&]() {
#pragma unroll
    for (int i = 0; i < 6; ++i) ring[i] = i < 4 ? ld4<2>(rwo, wt_off, i * 8 * D * 2) : ld4<2>(rwd, wt_off, (i - 4) * 8 * D * 2);
    SB();
  };
  if (TWP == 1) tailw();
  {
    float l = st.l;
    l += dpp<XOR1>(l); l += dpp<XOR2>(l); l = x32_add(x16_add(l));
    float* const po = &s_part[w][(lane & 8) * 32 + (lane >> 4) * 32 + (lane & 7) * 4];
    *reinterpret_cast<f32x4*>(po) = st.a[0];
    *reinterpret_cast<f32x4*>(po + 128) = st.a[1];
    if ((lane & 55) == 0) *reinterpret_cast<f32x2*>(&s_ml[w][lane >> 2]) = (f32x2){st.m, l};
  }
  LDS_FENCE(); bar(); LDS_FENCE();
  STAMP(2);
  if (w == 4 || w == 5) {
    const int t = tid - 256;
    f32x2 ml[8];
#pragma unroll
    for (int k = 0; k < 8; ++k) ml[k] = *reinterpret_cast<const f32x2*>(&s_ml[k][(w - 4) * 2]);
    float M = -3.0e38f, L = 0.f;
#pragma unroll
    for (int k = 0; k < 8; ++k) M = fmaxf(M, ml[k].x);
    f32x4 o = {0.f, 0.f, 0.f, 0.f};
#pragma unroll
    for (int k = 0; k < 8; ++k) {
      const float e = __builtin_amdgcn_exp2f((ml[k].x - M) * EXP_C);
      o += *reinterpret_cast<const f32x4*>(&s_part[k][t * 4]) * e; L += ml[k].y * e;
    }
    v4u msg;
#pragma unroll
    for (int j = 0; j < 3; ++j) { const unsigned u = asu(o[j]); msg[j] = __builtin_amdgcn_perm(u < 0xFFFFFE00u ? u : 0xFFFFFE00u, asu(o[3]), 0x07060501u + j); }
    msg[3] = canon((t & 1) ? L : M);
    st4c(msg, rws, area_of(16 + (t >> 3)) + A_O1 + s * 128 + (t & 7) * 16);
    SB();
    asm volatile("" :: "v"(ring[0]), "v"(ring[1]), "v"(ring[2]), "v"(ring[3]), "v"(ring[4]), "v"(ring[5]), "v"(ring[6]), "v"(ring[7]), "v"(pv));
  }
  if (isy) {
    const unsigned pa = w < 4 ? area + A_O1 + w * 1024 + l16 : (w == 7 && y < 112) ? area + A_F + (lane & 31) * 16 : OOB;
    if (TWP == 2) tailw();
    v4u d = ld4<16>(rws, pa, 0);
    SB();
    f32x2 wf[6][4];
#pragma unroll
    for (int i = 0; i < 6; ++i)
#pragma unroll
      for (int j = 0; j < 4; ++j) {
        wf[i][j] = unp(ring[i][j]);
        asm volatile("" : "+v"(wf[i][j]));
      }
    SB();
    STAMP(14);
    for (int it = 0; it < SPIN_CAP; ++it) {
      if (__builtin_amdgcn_ballot_w64(!full4(d)) == 0) break;
      __builtin_amdgcn_s_sleep(1);
      d = ld4<16>(rws, pa, 0);
    }
    STAMP(15);
    if (w < 4) {
      const float ot = dpp<XOR1>(asf(d[3]));
      const float m = (lane & 1) ? ot : asf(d[3]), l = (lane & 1) ? asf(d[3]) : ot;
      float M = fmaxf(m, dpp<ROR8>(m)); M = x16_max(M); M = x32_max(M);
      const float e = __builtin_amdgcn_exp2f((m - M) * EXP_C);
      const float v3 = asf(__builtin_amdgcn_perm(__builtin_amdgcn_perm(d[2], d[1], 0x04000c0cu), d[0], 0x0706000cu));
      float y0 = tr32(asf(d[0]) * e, asf(d[2]) * e), y1 = tr32(asf(d[1]) * e, v3 * e), L = x32_add(l * e);
      y0 = tr16(y0, y1); L = x16_add(L);
      const bool s_ = lane & 8;
      y0 = (s_ ? L : y0) + dpp<ROR8>(s_ ? y0 : L);
      if (!s_) s_o[w][(lane & 7) * 4 + (lane >> 5) * 2 + ((lane >> 4) & 1)] = y0;
      if (lane == 8) *reinterpret_cast<f32x2*>(&s_ml2[w * 2]) = (f32x2){M, y0};
    } else if (w == 7) {
      float hv[4];
#pragma unroll
      for (int j = 0; j < 4; ++j) {
        float g = asf(d[j]);
        g += dpp<ROR8>(g); g = rbf(x16_add(g));
        const float u = dpp<ROR4>(g);
        hv[j] = rbf(rbf(g / (1.0f + __expf(-g))) * u);
      }
      if (lane < 4) {
#pragma unroll
        for (int j = 0; j < 4; ++j) s_h[((lane & 1) * 4 + j) * 2 + (lane >> 1)] = hv[j];
      }
    }
    LDS_FENCE(); bar(); LDS_FENCE();
    STAMP(3);
    if (w < 7) {
      const f32x4 ma = *reinterpret_cast<const f32x4*>(&s_ml2[0]), mb = *reinterpret_cast<const f32x4*>(&s_ml2[4]);
      f32x4 pq[4];
#pragma unroll
      for (int k = 0; k < 4; ++k) pq[k] = *reinterpret_cast<const f32x4*>(&s_o[k][(lane >> 3) * 4]);
      const f32x2 hv = *reinterpret_cast<const f32x2*>(&s_h[(lane >> 3) * 2]);
      const float M = fmaxf(fmaxf(ma[0], ma[2]), fmaxf(mb[0], mb[2]));
      const float e0 = __builtin_amdgcn_exp2f((ma[0] - M) * EXP_C), e1_ = __builtin_amdgcn_exp2f((ma[2] - M) * EXP_C);
      const float e2 = __builtin_amdgcn_exp2f((mb[0] - M) * EXP_C), e3 = __builtin_amdgcn_exp2f((mb[2] - M) * EXP_C);
      const float inv = __builtin_amdgcn_rcpf((ma[1] * e0 + ma[3] * e1_) + (mb[1] * e2 + mb[3] * e3));
      const f32x4 av = ((pq[0] * e0 + pq[1] * e1_) + (pq[2] * e2 + pq[3] * e3)) * inv;
      f32x2 acc[4];
#pragma unroll
      for (int j = 0; j < 4; ++j) acc[j] = wf[4][j] * hv.x + wf[5][j] * hv.y;
#pragma unroll
      for (int i = 0; i < 4; ++i) {
        const float a = rbf(av[i]);
#pragma unroll
        for (int j = 0; j < 4; ++j) acc[j] += wf[i][j] * a;
      }
      float z[4] = {tr32(acc[0].x, acc[2].x), tr32(acc[0].y, acc[2].y), tr32(acc[1].x, acc[3].x), tr32(acc[1].y, acc[3].y)};
#pragma unroll
      for (int j = 0; j < 4; ++j) { z[j] = x16_add(z[j]); z[j] += dpp<ROR8>(z[j]); }
      const unsigned ra = (unsigned)(w * 32 + ((lane >> 1) & 3) * 4 + ((-w) & 3)) * AREA;
      st4c((v4u){canon(z[0]), canon(z[1]), canon(z[2]), canon(z[3])}, rws, (ra + A_Y2 + y * 64 + (lane & 1) * 32 + (lane >> 5) * 16) | ((lane & 24) ? OOB : 0u));
    }
  } else if (isr) {
    const float xo = asf((unsigned)xg[ro * 16 + (lane & 15)] << 16);
    const unsigned pa = area + A_Y2 + w * 1024 + l16;
    v4u d;
    if (RSLEEP) __builtin_amdgcn_s_sleep(RSLEEP);
    for (int it = 0; it < SPIN_CAP; ++it) {
      d = ld4<16>(rws, pa, 0);
      if (__builtin_amdgcn_ballot_w64(!full4(d)) == 0) break;
      __builtin_amdgcn_s_sleep(1);
    }
    float y0 = tr32(asf(d[0]), asf(d[2])), y1 = tr32(asf(d[1]), asf(d[3]));
    y0 = tr16(y0, y1);
    y0 += dpp<ROR8>(y0); y0 += dpp<ROR4>(y0);
    if (!(lane & 12)) s_mv[w][(lane & 3) * 4 + (lane >> 5) * 2 + ((lane >> 4) & 1)] = y0;
    LDS_FENCE(); bar(); LDS_FENCE();
    if (w == 0) {
      float a = s_mv[lane >> 4][lane & 15] + s_mv[4 + (lane >> 4)][lane & 15];
      a = x16_add(a); a = x32_add(a);
      if (lane < 16) out[ro * 16 + lane] = (u16)f2bf(xo + a);
    }
  }
  STAMP(4);
  STAMP_OUT();
}

extern "C" void kernel_launch(void* const* d_in, const int* in_sizes, int n_in,
                              void* d_out, int out_size, void* d_ws, size_t ws_size,
                              hipStream_t stream) {
  fused_kernel<<<NWG, NT, 0, stream>>>((const u16*)d_in[0], (const u16*)d_in[1], (const u16*)d_in[2], (const u16*)d_in[3],
                                       (const u16*)d_in[4], (const u16*)d_in[6], (const u16*)d_in[7], (const u16*)d_in[8],
                                       (u16*)d_out, (char*)d_ws);
}
```

```cpp
#include <hip/hip_runtime.h>
#include <hip/hip_bf16.h>
#include <stdint.h>

#define QPOS 24
#define PACE 8
#define QTRIES 8
#define NG 16
#define CQ 56
#define CF (CQ + 2)
#define CO (CQ + 4)
#define TAU 3650
#define TOFF 150
#define EARLY 1
#define HM 5
#define PBURST 0
#define KBURST 0
#define TAP 8
#define RSLEEP 0
#define MRG 0
#define DIAG 0
#define XT 0
#define XW 0
#define PP 0
#define QFIRST 24
#define OPOS 8
#define NOTAG 0
#define LP 0
#define XO 0
#define XM 0xffff
#define XA 19
#define XB 19
#define SA 16
#define SH 16
#define QA 16
#define PX 16
#define HA 16
#define AA 0
#define VBURST 0
#define QBURST 0
#define TWP 0
#define QROT 0
#define XQ 0
#define PY 0
#define XH 0
#define LITE 0

typedef unsigned v4u __attribute__((ext_vector_type(4)));
typedef unsigned v2u __attribute__((ext_vector_type(2)));
typedef float f32x4 __attribute__((ext_vector_type(4)));
typedef float f32x2 __attribute__((ext_vector_type(2)));
typedef __bf16 bf16x2 __attribute__((ext_vector_type(2)));
typedef __bf16 bf16x8 __attribute__((ext_vector_type(8)));
typedef unsigned short u16;
typedef __amdgpu_buffer_rsrc_t rsrc_t;
typedef __attribute__((address_space(3))) f32x4* lds4_t;
typedef __attribute__((address_space(3))) v4u* ldsu_t;
typedef __attribute__((address_space(3))) float* lds1_t;

constexpr int D = 448, SK = 32768, DH = 256, F = 1792, NQ = 4096;
constexpr int NWG = 256, NT = 512;
constexpr unsigned OOB = 0x80000000u;
constexpr unsigned SENT = 0xFFFFFFFFu;
constexpr unsigned AREA = 9216;
constexpr unsigned A_O1 = 1024;
constexpr unsigned A_F = 5120;
constexpr unsigned A_Y2 = 1024;
constexpr unsigned WS_REQ = NWG * AREA;
constexpr unsigned WS_ECHO = WS_REQ + 1024;
constexpr unsigned WS_GO = WS_ECHO + 1024;
constexpr unsigned WS_POOL = WS_GO + 256;
constexpr unsigned WS_Q = WS_POOL + 8 * NG * 256;
constexpr unsigned WS_END = WS_Q + 8 * 2048;
constexpr int SPIN_CAP = 1 << 18;
constexpr int C_NEXT = 0, C_TICK = 1, C_GO = 2, C_Q = 3, C_T0 = 4, C_FC = 5, C_OLD = 6, C_DONE = 7;

__device__ __forceinline__ float asf(unsigned u) { return __builtin_bit_cast(float, u); }
__device__ __forceinline__ unsigned asu(float f) { return __builtin_bit_cast(unsigned, f); }
__device__ __forceinline__ float rbf(float f) { __bf16 b = (__bf16)f; return (float)b; }
__device__ __forceinline__ unsigned f2bf(float f) { __bf16 b = (__bf16)f; return (unsigned)__builtin_bit_cast(u16, b); }
__device__ __forceinline__ float dot2(unsigned k, unsigned q, float acc) {
  return __builtin_amdgcn_fdot2_f32_bf16(__builtin_bit_cast(bf16x2, k), __builtin_bit_cast(bf16x2, q), acc, false);
}
__device__ __forceinline__ f32x2 unp(unsigned u) { f32x2 r; r.x = asf(u << 16); r.y = asf(u & 0xffff0000u); return r; }
template <int CTRL> __device__ __forceinline__ float dpp(float x) {
  return asf((unsigned)__builtin_amdgcn_update_dpp(0, (int)asu(x), CTRL, 0xf, 0xf, true));
}
constexpr int XOR1 = 0xB1, XOR2 = 0x4E, XOR7 = 0x141, ROR8 = 0x128, ROR4 = 0x124, ROR2 = 0x122, BCAST = 0x150;
constexpr int QP_EVEN = 0xA0, QP_ODD = 0xF5;
__device__ __forceinline__ float x16_add(float x) { auto s = __builtin_amdgcn_permlane16_swap(asu(x), asu(x), false, false); return asf(s[0]) + asf(s[1]); }
__device__ __forceinline__ float x32_add(float x) { auto s = __builtin_amdgcn_permlane32_swap(asu(x), asu(x), false, false); return asf(s[0]) + asf(s[1]); }
__device__ __forceinline__ float x16_max(float x) { auto s = __builtin_amdgcn_permlane16_swap(asu(x), asu(x), false, false); return fmaxf(asf(s[0]), asf(s[1])); }
__device__ __forceinline__ float x32_max(float x) { auto s = __builtin_amdgcn_permlane32_swap(asu(x), asu(x), false, false); return fmaxf(asf(s[0]), asf(s[1])); }
__device__ __forceinline__ float tr32(float a, float b) { auto s = __builtin_amdgcn_permlane32_swap(asu(a), asu(b), false, false); return asf(s[0]) + asf(s[1]); }
__device__ __forceinline__ float tr16(float a, float b) { auto s = __builtin_amdgcn_permlane16_swap(asu(a), asu(b), false, false); return asf(s[0]) + asf(s[1]); }
__device__ __forceinline__ float wave_sum(float v) {
  v += dpp<XOR1>(v); v += dpp<XOR2>(v); v += dpp<XOR7>(v); v += dpp<ROR8>(v);
  v = x16_add(v); return x32_add(v);
}
__device__ __forceinline__ float wave_max(float v) {
  v = fmaxf(v, dpp<XOR1>(v)); v = fmaxf(v, dpp<XOR2>(v)); v = fmaxf(v, dpp<XOR7>(v)); v = fmaxf(v, dpp<ROR8>(v));
  v = x16_max(v); return x32_max(v);
}
__device__ __forceinline__ rsrc_t mkrs(const void* p, unsigned bytes) { return __builtin_amdgcn_make_buffer_rsrc((void*)p, 0, (int)bytes, 0x00020000); }
__device__ __forceinline__ rsrc_t selr(bool c, rsrc_t a, rsrc_t b) { return c ? a : b; }
__device__ __forceinline__ unsigned selu(bool c, unsigned a, unsigned b) { return c ? a : b; }
template <int AUX> __device__ __forceinline__ v4u ld4(rsrc_t r, unsigned vo, unsigned so) { return __builtin_amdgcn_raw_buffer_load_b128(r, (int)vo, (int)so, AUX); }
template <int AUX> __device__ __forceinline__ v2u ld2(rsrc_t r, unsigned vo, unsigned so) { return __builtin_amdgcn_raw_buffer_load_b64(r, (int)vo, (int)so, AUX); }
__device__ __forceinline__ void st4c(v4u v, rsrc_t r, unsigned vo) { __builtin_amdgcn_raw_buffer_store_b128(v, r, (int)vo, 0, SA); }
__device__ __forceinline__ void st1c(unsigned v, rsrc_t r, unsigned vo) { __builtin_amdgcn_raw_buffer_store_b32(v, r, (int)vo, 0, SH); }
template <unsigned M> __device__ __forceinline__ unsigned tag(unsigned u, unsigned t) { return (u & ~M) | (t & M); }
__device__ __forceinline__ v4u tag4(const f32x4& v, const v4u& t) { return (v4u){tag<1>(asu(v[0]), t[0]), tag<1>(asu(v[1]), t[1]), tag<1>(asu(v[2]), t[2]), tag<1>(asu(v[3]), t[3])}; }
__device__ __forceinline__ bool arrived(const v4u& v, const v4u& o) { return __builtin_amdgcn_ballot_w64(v[0] == o[0] || v[1] == o[1] || v[2] == o[2] || v[3] == o[3]) == 0; }
__device__ __forceinline__ unsigned lowb(const v4u& v) { return __builtin_amdgcn_perm(v[1], v[0], 0x0c0c0400u) | __builtin_amdgcn_perm(v[3], v[2], 0x04000c0cu); }
#define SB() __builtin_amdgcn_sched_barrier(0)
__device__ __forceinline__ void bar() { __builtin_amdgcn_s_waitcnt(0xC07F); __builtin_amdgcn_s_barrier(); }
#define WAITV(n) __builtin_amdgcn_s_waitcnt(((n) & 15) | (((n) >> 4) << 14) | 0x0F70)
template <int V> struct IC { static constexpr int value = V; constexpr operator int() const { return V; } };
template <int A, int B, class Fn> __device__ __forceinline__ void static_for(Fn&& f) { if constexpr (A < B) { f(IC<A>{}); static_for<A + 1, B>(f); } }
#define LDS_FENCE() asm volatile("" ::: "memory")

constexpr float EXP_C = 0.0625f * 1.4426950408889634f;

struct Att {
  float m, l;
  f32x4 a[2];
};
__device__ __forceinline__ f32x4 mfma(const v4u& a, const v4u& b, const f32x4& c) {
  return __builtin_amdgcn_mfma_f32_16x16x32_bf16(__builtin_bit_cast(bf16x8, a), __builtin_bit_cast(bf16x8, b), c, 0, 0, 0);
}
__device__ __forceinline__ float red_max(float v) { v = fmaxf(v, dpp<XOR1>(v)); v = fmaxf(v, dpp<XOR2>(v)); return x32_max(x16_max(v)); }

template <int NL, class ISSUE>
__device__ __forceinline__ float scores(v4u (&kb)[8], const v4u (&qb)[8], int lane, ISSUE issue) {
  f32x4 d = {0.f, 0.f, 0.f, 0.f};
#pragma unroll
  for (int i = 0; i < NL; ++i) {
    d = mfma(kb[i], qb[i], d);
    asm volatile("" : "+v"(d));
    issue(i);
    SB();
  }
  const bool odd = lane & 1;
  return rbf((odd ? d[3] : d[0]) + dpp<XOR1>(odd ? d[1] : d[2]));
}
template <class ISSUE>
__device__ __forceinline__ float phase_k4(v4u (&kb)[8], const v4u (&qb)[8], Att& st, int lane, ISSUE issue) {
  float s = scores<4>(kb, qb, lane, issue);
  s = (lane & 2) ? -__builtin_inff() : s;
  const float mn = fmaxf(st.m, red_max(s));
  const float al = __builtin_amdgcn_exp2f((st.m - mn) * EXP_C);
  const float p = __builtin_amdgcn_exp2f((s - mn) * EXP_C);
  st.m = mn; st.l = st.l * al + p;
  st.a[0] *= al; st.a[1] *= al;
  SB();
  return p;
}
template <int I, int O, class ISSUE>
__device__ __forceinline__ void pv_step(v4u (&vb)[8], unsigned pp, const v4u& pm, Att& st, ISSUE& issue) {
  const v4u pb = pm & (unsigned)__builtin_amdgcn_update_dpp(0, (int)pp, I * 0x55, 0xf, 0xf, true);
  st.a[0] = mfma(vb[O + 2 * I], pb, st.a[0]);
  asm volatile("" : "+v"(st.a[0]));
  issue(O + 2 * I);
  SB();
  st.a[1] = mfma(vb[O + 2 * I + 1], pb, st.a[1]);
  asm volatile("" : "+v"(st.a[1]));
  issue(O + 2 * I + 1);
  SB();
}
template <int NL, class ISSUE>
__device__ __forceinline__ void phase_v(v4u (&vb)[8], float p, const v4u& pm, Att& st, ISSUE issue) {
  constexpr int O = 8 - NL;
  const unsigned pp = __builtin_bit_cast(unsigned, (bf16x2){(__bf16)p, (__bf16)p});
  pv_step<0, O>(vb, pp, pm, st, issue); pv_step<1, O>(vb, pp, pm, st, issue);
  if constexpr (NL == 8) { pv_step<2, O>(vb, pp, pm, st, issue); pv_step<3, O>(vb, pp, pm, st, issue); }
}

#define STAMP(k) do {} while (0)
#define STAMP_OUT() do {} while (0)

__device__ __forceinline__ f32x2 ln_stats(const v4u& xa, int lane) {
  f32x2 xv[4];
  float sm = 0.f;
#pragma unroll
  for (int j = 0; j < 4; ++j) { xv[j] = unp(xa[j]); sm += xv[j].x + xv[j].y; }
  const float mean = wave_sum(sm) * (1.0f / D);
  float sq = 0.f;
#pragma unroll
  for (int j = 0; j < 4; ++j) { const f32x2 d = xv[j] - mean; sq += d.x * d.x + d.y * d.y; }
  sq = lane < 56 ? sq : 0.f;
  const float var = rbf(wave_sum(sq) * (1.0f / D));
  const float sd = rbf(sqrtf(rbf(var + 1e-5f)));
  return (f32x2){rbf(mean), 1.0f / sd};
}
__device__ __forceinline__ float ln_apply(float x, f32x2 st) { return rbf(rbf(x - st.x) * st.y); }

__global__ void __launch_bounds__(NT, 2)
fused_kernel(const u16* __restrict__ xg, const u16* __restrict__ kc, const u16* __restrict__ vc, const u16* __restrict__ wq,
             const u16* __restrict__ wo, const u16* __restrict__ wg, const u16* __restrict__ wu, const u16* __restrict__ wd,
             u16* __restrict__ out, char* ws) {
  __shared__ __attribute__((aligned(16))) float s_part[8][512];
  __shared__ __attribute__((aligned(16))) float s_mv[8][64];
  __shared__ __attribute__((aligned(16))) unsigned s_q[256];
  __shared__ __attribute__((aligned(16))) unsigned s_qs[64];
  __shared__ __attribute__((aligned(16))) unsigned s_old[8][256];
  __shared__ __attribute__((aligned(16))) unsigned s_dst[8][256];
  __shared__ __attribute__((aligned(16))) float s_ml[2][16];
  __shared__ __attribute__((aligned(16))) float s_o[4][32];
  __shared__ __attribute__((aligned(16))) float s_ml2[8];
  __shared__ __attribute__((aligned(16))) float s_h[16];
  __shared__ __attribute__((aligned(16))) float s_r[8][16];
  __shared__ __attribute__((aligned(16))) float s_sc[64][32];
  __shared__ int s_ctl[64];
  const int tid = threadIdx.x, lane = tid & 63, w = __builtin_amdgcn_readfirstlane(tid >> 6);
  const int bi = blockIdx.x, h = (((bi & 1) ? bi + 5 : bi) & 6) | (bi >> 7), s = ((bi >> 3) & 15) * 2 + (bi & 1);
  const int b = h * 32 + s;
  constexpr int PH = 2 * (2048 - 8 * CQ - 14 * CF - 10 * CO), GS = 32 / NG;
  static_assert(PH >= 0 && CQ >= 40 && CQ <= 64 && CF >= 40 && CF <= 64 && CO >= 40 && CO <= 64, "unit counts");
  auto prefix = [](int n) { return n * CO - min(n, 22) * (CO - CF) - min(n, 8) * (CF - CQ); };
  const int pg = s / GS, nk = s < 8 ? CQ : s < 22 ? CF : CO;
  auto poff = [](int g) { return g * PH / NG + PY * (NG / 2 - abs(g - NG / 2)); };
  const int first = 2 * prefix(s) + poff(pg), pool0 = 2 * prefix((pg + 1) * GS) + poff(pg), psz = poff(pg + 1) - poff(pg);
  asm volatile("" :: "s"(xg), "s"(kc), "s"(vc), "s"(wq), "s"(wo), "s"(wg), "s"(wu), "s"(wd), "s"(out), "s"(ws));
  STAMP(0);
  const rsrc_t rx = mkrs(xg, D * 2);
  const rsrc_t rk = mkrs(kc + (size_t)h * SK * DH, SK * DH * 2);
  const rsrc_t rv = mkrs(vc + (size_t)h * SK * DH, SK * DH * 2);
  const rsrc_t rws = mkrs(ws, WS_END);
  const unsigned l16 = lane * 16;
  auto ubase = [&](int u) -> unsigned { return (unsigned)(first + 2 * u) * 4096u; };
  const unsigned kvo = ((lane >> 1) & 7) * 512 + (lane >> 4) * 32 + (lane & 1) * 16, vvo = (lane >> 4) * 1024 + (lane & 15) * 16;
  auto ldk = [&](unsigned base, int i) { return ld4<2>(rk, kvo + (i & 3) * 128, base + (i >> 2) * 4096); };
  const unsigned area = (unsigned)b * AREA;
  auto area_of = [&](int ss) -> unsigned { return (unsigned)(h * 32 + ss) * AREA; };
  const unsigned lmask = lane < 56 ? 0u : OOB;

  const bool isq = s < 8, isf = s >= 8 && s < 22, isy = s >= 16;
  const int f = h * 14 + s - 8, y = h * 16 + s - 16, ro = h * 4 + (s >> 2);
  const int sq = (s + QROT) & 7;
  const bool isr = s < 16 && ((s + h) & 3) == 0 && ro < 28;
  const rsrc_t rwo = mkrs(wo + (size_t)(isy ? y : 0) * 32 * D, (isy && w < 7 && !XW) ? 32 * D * 2 : 0);
  const rsrc_t rwd = mkrs(wd + (size_t)((isy && y < 112) ? y : 0) * 16 * D, (isy && y < 112 && w < 7 && !XW) ? 16 * D * 2 : 0);
  const unsigned wt_off = (lane >> 3) * (D * 2) + w * 128 + (lane & 7) * 16;
  const bool tile = isq || (isf && w < 4);
  const bool leader = (b == NWG - 1 && w == 1), member = (w == 0);

  const v4u sent = {SENT, SENT, SENT, SENT};
  unsigned poll_off = lane == 1 ? WS_GO : OOB;
  poll_off = lane == 0 ? WS_REQ + (b >> 2) * 16 : poll_off;
  poll_off = leader ? WS_ECHO + l16 : poll_off;
  v4u pv = {0u, 0u, 0u, 0u}, e1 = {0u, 0u, 0u, 0u};
  unsigned go_ref = 0;
  int* const ctl = &s_ctl[lane];
  auto ctl_get = [&](int i) { return __builtin_amdgcn_readfirstlane(__hip_atomic_load(&s_ctl[i], __ATOMIC_RELAXED, __HIP_MEMORY_SCOPE_WORKGROUP)); };
  auto ctl_set = [&](int i) { __hip_atomic_store(&s_ctl[i], 1, __ATOMIC_RELAXED, __HIP_MEMORY_SCOPE_WORKGROUP); };
  const unsigned qoff = WS_Q + h * 2048 + l16;
  v4u qv0, qv1;
  if (member) {
    qv0 = ld4<QA>(rws, qoff, 0);
    qv1 = ld4<QA>(rws, qoff, 1024);
    SB();
    int c = lane == C_NEXT ? TOFF * 128 : 0;
    c = lane == C_TICK ? 40 : c;
    c = lane == C_T0 ? (int)(__builtin_amdgcn_s_memrealtime() & 0x7fffffff) : c;
    __hip_atomic_store(ctl, c, __ATOMIC_RELAXED, __HIP_MEMORY_SCOPE_WORKGROUP);
    st1c(0u, rws, (lane == 0 && s == pg * GS) ? WS_POOL + (h * NG + pg) * 256 : OOB);
  }
  *reinterpret_cast<v4u*>(&s_sc[w * 8][lane * 4]) = sent;
  int t0 = 0;
  auto now = [&]() { return ((int)__builtin_amdgcn_s_memrealtime() - t0) & 0x7fffff; };
  const int neg = lane == C_NEXT ? 0 : (int)0x80000000;
  int inc = lane == C_NEXT ? TAU : 0;
  auto token = [&](int burst) {
    const int t = max(now() * 128 - burst, 0);
    __hip_atomic_fetch_max(ctl, t | neg, __ATOMIC_RELAXED, __HIP_MEMORY_SCOPE_WORKGROUP);
    return __hip_atomic_fetch_add(ctl, inc, __ATOMIC_RELAXED, __HIP_MEMORY_SCOPE_WORKGROUP);
  };
  auto due_of = [&](int x) { return __builtin_amdgcn_readlane(x, C_NEXT) >> 7; };
  auto until = [&](int due) {
#pragma unroll 1
    for (int it = 0; it < 4096 && now() < due; ++it) __builtin_amdgcn_s_sleep(1);
    SB();
  };
  unsigned qs0, qs1;
  auto q_there = [&]() { return XQ || __builtin_amdgcn_ballot_w64(((lowb(qv0) ^ qs0) & (lowb(qv1) ^ qs1)) != 0xFFFFFFFFu) == 0; };
  auto load_q = [&]() { if (XQ & 2) return; qv0 = ld4<QA>(rws, qoff, 0); qv1 = ld4<QA>(rws, qoff, 1024); };
  auto publish_q = [&]() {
    *reinterpret_cast<v2u*>(&s_q[lane * 2]) = (v2u){__builtin_amdgcn_perm(qv0[1], qv0[0], 0x07060302u), __builtin_amdgcn_perm(qv0[3], qv0[2], 0x07060302u)};
    *reinterpret_cast<v2u*>(&s_q[128 + lane * 2]) = (v2u){__builtin_amdgcn_perm(qv1[1], qv1[0], 0x07060302u), __builtin_amdgcn_perm(qv1[3], qv1[2], 0x07060302u)};
    LDS_FENCE();
    __builtin_amdgcn_s_waitcnt(0xC07F);
    ctl_set(C_Q);
  };
  v4u pre[4][8], ring[8];
  auto unit0 = [&]() {
#pragma unroll
    for (int i = 0; i < 8; ++i) { pre[0][i] = ldk(ubase(w), i); SB(); }
  };
  SB();
  if (tile) {
    v4u tw[8];
    const int oct = isq ? 7 * w : (f & 3) * 14 + (w >> 1) * 7;
    const int stride = isq ? NQ * 2 : F * 2;
    const rsrc_t rt = mkrs(isq ? wq : (w & 1) ? wu : wg, XT ? 0 : D * stride);
    const unsigned to = (unsigned)((oct + (lane >> 3)) * 8 * stride + (isq ? h * 8 + sq : f >> 2) * 128 + (lane & 7) * 16) | lmask;
    const v4u xa = ld4<0>(rx, l16, 0);
    const v4u xb = ld4<0>(rx, (unsigned)(oct + (lane >> 3)) * 16 | lmask, 0);
    SB();
#pragma unroll
    for (int i = 0; i < 8; ++i) { tw[i] = ld4<2>(rt, to, i * stride); SB(); }
    if (EARLY == 2) unit0();
    LDS_FENCE(); bar(); LDS_FENCE();
    t0 = ctl_get(C_T0);
    SB();
    const f32x2 ls = ln_stats(xa, lane);
    f32x2 acc[4];
#pragma unroll
    for (int i = 0; i < 8; ++i) {
      const float xn = ln_apply(asf((i & 1) ? (xb[i >> 1] & 0xffff0000u) : (xb[i >> 1] << 16)), ls);
#pragma unroll
      for (int j = 0; j < 4; ++j) acc[j] = i ? acc[j] + unp(tw[i][j]) * xn : unp(tw[i][j]) * xn;
    }
    float y0 = tr32(acc[0].x, acc[2].x), y1 = tr32(acc[0].y, acc[2].y), y2 = tr32(acc[1].x, acc[3].x), y3 = tr32(acc[1].y, acc[3].y);
    y0 = tr16(y0, y2); y1 = tr16(y1, y3);
    const bool s_ = lane & 8;
    y0 = (s_ ? y1 : y0) + dpp<ROR8>(s_ ? y0 : y1);
    s_mv[w][(lane & 7) * 8 + (lane >> 3)] = y0;
    STAMP(6);
    if (EARLY == 1) { token(isq ? QBURST : PBURST); unit0(); }
    if (isq) {
      LDS_FENCE(); bar(); LDS_FENCE();
      if (member) {
        float a = 0.f;
#pragma unroll
        for (int k = 0; k < 8; ++k) a += s_mv[k][lane];
        s_qs[lane] = f2bf(a) << 16;
        LDS_FENCE();
        const v4u qs = *reinterpret_cast<const v4u*>(&s_qs[(lane & 15) * 4]) | (~((sq & 4) ? qv1 : qv0) & 0xffffu);
        st4c(qs, rws, (lane >> 4) == (sq & 3) ? qoff + (sq >> 2) * 1024 : OOB);
      }
    } else {
      LDS_FENCE();
      __builtin_amdgcn_s_waitcnt(0xC07F);
      __hip_atomic_fetch_add(ctl, lane == C_FC ? 1 : 0, __ATOMIC_RELAXED, __HIP_MEMORY_SCOPE_WORKGROUP);
    }
  } else if (leader) {
    e1 = ld4<HA>(rws, poll_off, 0);
    go_ref = __builtin_amdgcn_raw_buffer_load_b32(rws, (int)WS_GO, 0, HA);
  }
  SB();
  STAMP(5);
  SB();
  const unsigned pa = isy ? (w < 4 ? area + A_O1 + w * 1024 + l16 : (w == 7 && y < 112) ? area + A_F + (lane & 31) * 16 : OOB) : isr ? area + A_Y2 + w * 1024 + l16 : OOB;
  const unsigned mo_ = area_of(16 + (lane & 8) + (lane >> 4)) + A_O1 + s * 128 + (lane & 7) * 16;
  const int fd = (f & ~3) + ((lane >> 3) & 3);
  const unsigned mf = ((unsigned)((fd >> 4) * 32 + 16 + (fd & 15)) * AREA + A_F + (f & 3) * 128 + (lane & 7) * 16) | (lane < 32 ? 0u : OOB);
  auto my = [&](int p) -> unsigned { return (unsigned)((p >> 4) * 32 + (p & 12) + ((-(p >> 4)) & 3)) * AREA + A_Y2 + y * 64 + (p & 3) * 16; };
  v4u eo, ed;
  bool ok = !member;
  static_for<0, 40>([&](auto N) {
    constexpr int n = N;
    if constexpr (n == 0) {
      if (EARLY == 0 && tile) until(due_of(token(isq ? QBURST : PBURST)));
      if (EARLY == 0 || !tile) unit0();
    }
    if constexpr (n == 8) {
      if (!tile) {
        LDS_FENCE(); bar(); LDS_FENCE();
        t0 = ctl_get(C_T0);
        if (leader) {
          e1 += 1u;
          go_ref += 1u;
          if (!XH) st4c(e1, rws, WS_REQ + l16);
        }
      }
    }
    if constexpr (n > 0 && n % 8 == 0) {
      const int x = token(isq ? QBURST : PBURST);
      if constexpr (n > QFIRST) {
        if (!ok) {
          ok = q_there();
          if (ok) publish_q();
        }
      }
      if constexpr (n == OPOS + 16) {
        *reinterpret_cast<v4u*>(&s_old[w][lane * 4]) = (int)pa < 0 ? sent : eo;
        *reinterpret_cast<v4u*>(&s_dst[w][lane * 4]) = ~ed;
        LDS_FENCE();
        __hip_atomic_fetch_add(ctl, lane == C_OLD ? 1 : 0, __ATOMIC_RELAXED, __HIP_MEMORY_SCOPE_WORKGROUP);
      }
      SB();
      until(due_of(x));
      if constexpr (n == 8) {
        if (member) { qs0 = lowb(qv0); qs1 = lowb(qv1); }
      }
      if constexpr (n >= QFIRST) {
        if (!ok) load_q();
      }
      if constexpr (n == OPOS) {
        const int p = (w - 2) * 64 + lane;
        unsigned da = (isy && p < 112) ? my(p) : OOB;
        da = (w & 6) == 2 ? da : (w & 6) == 4 ? mo_ + (w - 4) * 4 * AREA : (w == 6 && isf) ? mf : OOB;
        if (XO & 4) {
          eo = ld4<16>(rws, pa & (OOB | XM), 0);
          ed = ld4<16>(rws, da & (OOB | XM), 0);
        } else {
        eo = ld4<XB>(rws, (XO & 1) ? OOB : pa, 0);
        ed = ld4<XA>(rws, NOTAG ? OOB : da, 0);
        }
      }
      SB();
    }
    if constexpr (n == QPOS) {
      STAMP(9);
      pv = ld4<HA>(rws, (member || leader) ? poll_off : OOB, 0);
      SB();
    }
    if constexpr (n >= 8) {
      if constexpr (n >= PACE) WAITV(PACE + ((n >= QPOS && n < QPOS + PACE) ? 1 : 0) + ((n >= OPOS && n < OPOS + PACE) ? 2 : 0));
      if constexpr (n < 32) pre[n >> 3][n & 7] = ldk(ubase((n & 56) + w), n & 7); else ring[n & 7] = ldk(ubase(32 + w), n & 7);
    }
    SB();
    if constexpr (n - PACE == 7 || n - PACE == 15 || n - PACE == 23 || n - PACE == 31) STAMP(16 + (n - PACE) / 8);
  });
  inc = lane == C_TICK ? 1 : inc;
  int tok = token(KBURST);
  STAMP(10);
  bool svc = member || (leader && !XH);
  unsigned seenm = 0, last_echo = 0;
  bool echoed = false;
  int xcnt = 0;
  auto service = [&]() {
    if (leader) {
      seenm |= (pv[0] == e1[0] ? 1u : 0u) | (pv[1] == e1[1] ? 2u : 0u) | (pv[2] == e1[2] ? 4u : 0u) | (pv[3] == e1[3] ? 8u : 0u);
      svc = __builtin_amdgcn_ballot_w64(seenm != 15u) != 0;
      st1c(go_ref, rws, (lane == 0 && !svc) ? WS_GO : OOB);
    } else {
      const unsigned req = (unsigned)__builtin_amdgcn_readfirstlane((int)((b & 2) ? ((b & 1) ? pv[3] : pv[2]) : ((b & 1) ? pv[1] : pv[0])));
      const unsigned go = (unsigned)__builtin_amdgcn_readlane((int)pv[0], 1);
      go_ref = echoed ? go_ref : go;
      svc = !XH && go == (unsigned)__builtin_amdgcn_readfirstlane((int)go_ref);
      if (XH >= 2) svc = ++xcnt < 5;
      const bool rdy = ctl_get(C_OLD) == 8;
      st1c(req, rws, (XH != 3 && XH != 4 && lane == 0 && svc && rdy && !(echoed && req == last_echo)) ? WS_ECHO + b * 4 : OOB);
      echoed |= rdy; last_echo = req;
      if (!svc) {
        ctl_set(C_GO);
        if (isf) {
          while (ctl_get(C_FC) != 4) __builtin_amdgcn_s_sleep(1);
          LDS_FENCE();
          const int j = (lane >> 3) & 3, pc = lane & 7, m = pc >> 2, col = j * 16 + (pc & 3) * 4;
          const f32x4 v = *reinterpret_cast<const f32x4*>(&s_mv[m][col]) + *reinterpret_cast<const f32x4*>(&s_mv[m + 2][col]);
          st4c(tag4(v, *reinterpret_cast<const v4u*>(&s_dst[6][lane * 4])), rws, mf);
        }
      }
    }
    pv = ld4<HA>(rws, (svc && XH != 2 && XH != 4) ? (XH == 5 && lane == 1 ? OOB : poll_off) : OOB, 0);
    SB();
  };

  {
    if (!ok) {
      ok = q_there();
      if (ok) publish_q();
    }
#pragma unroll 1
    for (int it = 0; it < SPIN_CAP && !ctl_get(C_Q); ++it) {
      if (svc && it >= QTRIES) { service(); asm volatile("" :: "v"(pv)); }
      if (!ok) {
        load_q();
        ok = q_there() | !svc;
        if (ok) publish_q();
      } else {
        __builtin_amdgcn_s_sleep(1);
      }
    }
  }
  LDS_FENCE();
  v4u qb[8];
#pragma unroll
  for (int i = 0; i < 4; ++i) {
    const v4u z = {0u, 0u, 0u, 0u}, q = *reinterpret_cast<const v4u*>(&s_q[(lane & 8) * 16 + i * 32 + (lane >> 4) * 8 + (lane & 1) * 4]);
    qb[i] = (lane & 2) ? z : q; qb[4 + i] = (lane & 2) ? q : z;
  }
  v4u pm;
#pragma unroll
  for (int j = 0; j < 4; ++j) pm[j] = ((lane >> 1) & 3) == j ? ((lane & 1) ? 0xffff0000u : 0xffffu) : 0u;
  STAMP(1);
  SB();

  auto wait_go = [&]() {
    for (int it = 0; it < SPIN_CAP; ++it) {
      if (ctl_get(C_GO)) break;
      if (svc) service();
      __builtin_amdgcn_s_sleep(2);
    }
  };

  Att st;
  int sx = 20;
  int gt = 0;
  {
    const unsigned pco = lane == 0 ? WS_POOL + (h * NG + pg) * 256 : OOB;
    float* const sc = &s_sc[0][(lane >> 3) * 4 + (lane & 3)];
    const int lim = psz - TAP * GS * w / 8;
    rsrc_t nra, nrb;
    unsigned nva, nvb, nba, nbb, nsa, nsb;
    int kind = 0, nu = 32 + w, cu, th = 0;
    bool dc = false, have = false, again = true;
    auto claim = [&]() {
      if (LP) gt = __hip_atomic_fetch_add(&s_ctl[8 + lane / 2], 2, __ATOMIC_RELAXED, __HIP_MEMORY_SCOPE_WORKGROUP) / 4 * 2 + (s & 1);
      else gt = __builtin_amdgcn_raw_ptr_buffer_atomic_add_i32(1, rws, (int)pco, 0, AA); };
    auto next = [&](bool tickets) {
      const int g = __builtin_amdgcn_readfirstlane(gt);
      th = dc ? g : th;
      have |= dc;
      cu = nu;
      const int t = tickets ? __builtin_amdgcn_readlane(tok, C_TICK) : 2 * nk;
      const int go = tickets ? __builtin_amdgcn_readlane(tok, C_GO) : 1;
      const bool fx = t < 2 * nk;
      if (tickets && !fx && !have && go) {
        claim();
        th = __builtin_amdgcn_readfirstlane(gt);
        have = true;
      }
      const bool more = have && again && (unsigned)th < (unsigned)psz, tw = !fx && !more;
      again = fx || (more && th < lim);
      kind = fx ? (t >= nk ? 1 : 0) : more ? 2 : 3;
      nu = t >= nk ? t - nk : t;
      nba = (unsigned)(fx ? first + 2 * nu : more ? pool0 + th : TWP ? 0x80000 : 0) * 4096u;
      nbb = fx ? nba + 4096 : nba;
      nra = selr(tw, rwo, selr(kind == 1, rv, rk));
      nrb = selr(tw, rwd, selr(kind == 0, rk, rv));
      nva = selu(tw, wt_off, selu(kind == 1, vvo, kvo)); nvb = selu(tw, wt_off, selu(kind == 0, kvo, vvo));
      nsa = selu(tw, 8 * D * 2, selu(kind == 1, 256, 128)); nsb = selu(tw, 8 * D * 2, selu(kind == 0, 128, 256));
      dc = fx ? !have && go && t + 8 >= 2 * nk : again;
      if (PP) { until((fx || more) ? due_of(tok) : 0); inc = (lane == C_NEXT && t + 8 >= 2 * nk) ? PP : inc; }
      else if (tickets) until(fx ? due_of(tok) : 0);
      SB();
    };
    auto ldn = [&](int i) { if (i == 0 && dc) claim(); ring[i] = i < 4 ? ld4<2>(nra, nva, nba + i * nsa) : ld4<2>(nrb, nvb, nbb + (i - 4) * nsb); };
    float mx;
    if (svc) service();
    {
      const float s0 = scores<8>(pre[0], qb, lane, [&](int) {});
      const float s1 = scores<8>(pre[1], qb, lane, [&](int) {});
      const float s2 = scores<8>(pre[2], qb, lane, [&](int) {});
      const float s3 = scores<8>(pre[3], qb, lane, [&](int) {});
      sc[w * 32] = s0; sc[(8 + w) * 32] = s1; sc[(16 + w) * 32] = s2; sc[(24 + w) * 32] = s3;
      mx = fmaxf(fmaxf(s0, s1), fmaxf(s2, s3));
      SB();
      STAMP(11);
    }
#pragma unroll 1
    while (kind == 0) {
      next(true);
      const float sv = scores<8>(ring, qb, lane, ldn);
      sc[cu * 32] = sv;
      tok = token(KBURST);
      mx = fmaxf(mx, sv);
      STAMP(sx); ++sx;
      if (svc) service();
      SB();
    }
    STAMP(7);
    st.m = red_max(mx); st.l = 0.f;
    st.a[0] = st.a[1] = (f32x4){0.f, 0.f, 0.f, 0.f};
#pragma unroll 1
    while (kind == 1) {
      float sv;
      for (;;) {
        sv = __hip_atomic_load(&sc[nu * 32], __ATOMIC_RELAXED, __HIP_MEMORY_SCOPE_WORKGROUP);
        if (__builtin_amdgcn_ballot_w64(asu(sv) == SENT) == 0) break;
        __builtin_amdgcn_s_sleep(1);
      }
      if (__builtin_amdgcn_ballot_w64(sv > st.m) != 0) {
        const float m2 = fmaxf(st.m, red_max(sv));
        const float al = __builtin_amdgcn_exp2f((st.m - m2) * EXP_C);
        st.m = m2; st.l *= al;
        st.a[0] *= al; st.a[1] *= al;
      }
      const float p = __builtin_amdgcn_exp2f((sv - st.m) * EXP_C);
      st.l += p;
      SB();
      next(true);
      phase_v<8>(ring, p, pm, st, ldn);
      tok = token(VBURST);
      STAMP(sx); ++sx;
      if (svc) service();
      SB();
    }
    STAMP(8);
    if (!have) {
      wait_go();
      claim();
      dc = again = true;
      next(false);
#pragma unroll
      for (int i = 0; i < 8; ++i) ldn(i);
      SB();
    }
#pragma unroll 1
    while (kind == 2) {
      next(false);
      const float p = phase_k4(ring, qb, st, lane, ldn);
      phase_v<4>(ring, p, pm, st, ldn);
      if (PP) tok = token(VBURST);
      STAMP(sx); ++sx;
    }
    if (!MRG) asm volatile("" :: "v"(gt));
    LDS_FENCE();
  }
  STAMP(12);
  auto tailw = [&]() {
#pragma unroll
    for (int i = 0; i < 6; ++i) ring[i] = i < 4 ? ld4<2>(rwo, wt_off, i * 8 * D * 2) : ld4<2>(rwd, wt_off, (i - 4) * 8 * D * 2);
    SB();
  };
  if (TWP == 1) tailw();
  auto send_o = [&](const f32x4& o, float M, float L, const v4u& t, int hf) {
    v4u msg;
#pragma unroll
    for (int j = 0; j < 3; ++j) msg[j] = tag<0x100>(__builtin_amdgcn_perm(asu(o[j]), asu(o[3]), 0x07060501u + j), t[j]);
    msg[3] = tag<1>(asu((lane & 1) ? L : M), t[3]);
    __builtin_amdgcn_raw_buffer_store_b128(msg, rws, (int)mo_, hf * 4 * AREA, SA);
  };
  {
    const lds4_t pw = (lds4_t)&s_part[w][lane * 4], pr = (lds4_t)&s_part[0][lane * 4], pmr = (lds4_t)&s_ml[(lane >> 3) & 1][0];
    const lds1_t pml = (lds1_t)&s_ml[(lane >> 3) & 1][w];
    pw[0] = st.a[0]; pw[64] = st.a[1];
    float l = st.l;
    l += dpp<XOR1>(l); l += dpp<XOR2>(l); l = x32_add(x16_add(l));
    pml[0] = st.m; pml[8] = l;
    LDS_FENCE(); bar(); LDS_FENCE();
    STAMP(2);
    if (w == 4 || w == 5) {
      f32x4 ml[4], pt[8];
#pragma unroll
      for (int k = 0; k < 4; ++k) ml[k] = pmr[k];
#pragma unroll
      for (int k = 0; k < 8; ++k) pt[k] = pr[k * 128 + (w - 4) * 64];
      const v4u dt = *reinterpret_cast<const v4u*>(&s_dst[w][lane * 4]);
      float M = -3.0e38f;
#pragma unroll
      for (int k = 0; k < 8; ++k) M = fmaxf(M, ml[k >> 2][k & 3]);
      f32x4 e[2] = {(ml[0] - M) * EXP_C, (ml[1] - M) * EXP_C};
      f32x4 o = {0.f, 0.f, 0.f, 0.f};
#pragma unroll
      for (int k = 0; k < 8; ++k) {
        e[k >> 2][k & 3] = __builtin_amdgcn_exp2f(e[k >> 2][k & 3]);
        o += pt[k] * e[k >> 2][k & 3];
      }
      const f32x4 l4 = ml[2] * e[0] + ml[3] * e[1];
      const float L = (l4[0] + l4[2]) + (l4[1] + l4[3]);
      send_o(o, M, L, dt, w - 4);
      SB();
      asm volatile("" :: "v"(ring[0]), "v"(ring[1]), "v"(ring[2]), "v"(ring[3]), "v"(ring[4]), "v"(ring[5]), "v"(ring[6]), "v"(ring[7]), "v"(pv));
    }
  }
  const v4u old = *reinterpret_cast<const v4u*>(&s_old[w][lane * 4]);
  if (isy) {
    if (TWP == 2) tailw();
    v4u d = ld4<PX>(rws, pa, 0);
    SB();
    f32x2 wf[6][4];
#pragma unroll
    for (int i = 0; i < 6; ++i)
#pragma unroll
      for (int j = 0; j < 4; ++j) {
        wf[i][j] = unp(ring[i][j]);
        asm volatile("" : "+v"(wf[i][j]));
      }
    SB();
    STAMP(14);
    const v4u dt = *reinterpret_cast<const v4u*>(&s_dst[2][(w * 16 + (lane & 7) * 2 + (lane >> 5)) * 4]);
#pragma unroll 1
    for (int it = 0; it < SPIN_CAP; ++it) {
      if ((XO & 2) || arrived(d, old)) break;
      __builtin_amdgcn_s_sleep(1);
      LDS_FENCE();
      d = ld4<PX>(rws, pa, 0);
    }
    STAMP(15);
    if (w < 4) {
      d &= (v4u){~0x100u, ~0x100u, ~0x100u, ~1u};
      const float ot = dpp<XOR1>(asf(d[3]));
      const float m = (lane & 1) ? ot : asf(d[3]), l = (lane & 1) ? asf(d[3]) : ot;
      float M = fmaxf(m, dpp<ROR8>(m)); M = x16_max(M); M = x32_max(M);
      const float e = __builtin_amdgcn_exp2f((m - M) * EXP_C);
      const float v3 = asf(__builtin_amdgcn_perm(__builtin_amdgcn_perm(d[2], d[1], 0x04000c0cu), d[0], 0x0706000cu));
      float y0 = tr32(asf(d[0]) * e, asf(d[2]) * e), y1 = tr32(asf(d[1]) * e, v3 * e), L = x32_add(l * e);
      y0 = tr16(y0, y1); L = x16_add(L);
      const bool s_ = lane & 8;
      y0 = (s_ ? L : y0) + dpp<ROR8>(s_ ? y0 : L);
      if (!s_) s_o[w][(lane & 7) * 4 + (lane >> 5) * 2 + ((lane >> 4) & 1)] = y0;
      if (lane == 8) *reinterpret_cast<f32x2*>(&s_ml2[w * 2]) = (f32x2){M, y0};
    } else if (w == 7) {
      float hv[4];
#pragma unroll
      for (int j = 0; j < 4; ++j) {
        float g = asf(d[j] & ~1u);
        g += dpp<ROR8>(g); g = rbf(x16_add(g));
        const float u = dpp<ROR4>(g);
        hv[j] = rbf(rbf(g / (1.0f + __expf(-g))) * u);
      }
      if (lane < 4) {
#pragma unroll
        for (int j = 0; j < 4; ++j) s_h[((lane & 1) * 4 + j) * 2 + (lane >> 1)] = hv[j];
      }
    }
    LDS_FENCE(); bar(); LDS_FENCE();
    STAMP(3);
    if (w < 7) {
      const f32x4 ma = *reinterpret_cast<const f32x4*>(&s_ml2[0]), mb = *reinterpret_cast<const f32x4*>(&s_ml2[4]);
      f32x4 pq[4];
#pragma unroll
      for (int k = 0; k < 4; ++k) pq[k] = *reinterpret_cast<const f32x4*>(&s_o[k][(lane >> 3) * 4]);
      const f32x2 hv = *reinterpret_cast<const f32x2*>(&s_h[(lane >> 3) * 2]);
      const float M = fmaxf(fmaxf(ma[0], ma[2]), fmaxf(mb[0], mb[2]));
      const float e0 = __builtin_amdgcn_exp2f((ma[0] - M) * EXP_C), e1_ = __builtin_amdgcn_exp2f((ma[2] - M) * EXP_C);
      const float e2 = __builtin_amdgcn_exp2f((mb[0] - M) * EXP_C), e3 = __builtin_amdgcn_exp2f((mb[2] - M) * EXP_C);
      const float inv = __builtin_amdgcn_rcpf((ma[1] * e0 + ma[3] * e1_) + (mb[1] * e2 + mb[3] * e3));
      const f32x4 av = ((pq[0] * e0 + pq[1] * e1_) + (pq[2] * e2 + pq[3] * e3)) * inv;
      f32x2 acc[4];
#pragma unroll
      for (int j = 0; j < 4; ++j) acc[j] = wf[4][j] * hv.x + wf[5][j] * hv.y;
#pragma unroll
      for (int i = 0; i < 4; ++i) {
        const float a = rbf(av[i]);
#pragma unroll
        for (int j = 0; j < 4; ++j) acc[j] += wf[i][j] * a;
      }
      float z[4] = {tr32(acc[0].x, acc[2].x), tr32(acc[0].y, acc[2].y), tr32(acc[1].x, acc[3].x), tr32(acc[1].y, acc[3].y)};
#pragma unroll
      for (int j = 0; j < 4; ++j) { z[j] = x16_add(z[j]); z[j] += dpp<ROR8>(z[j]); }
      const unsigned ra = (unsigned)(w * 32 + ((lane >> 1) & 3) * 4 + ((-w) & 3)) * AREA;
      st4c(tag4((f32x4){z[0], z[1], z[2], z[3]}, dt), rws, (ra + A_Y2 + y * 64 + (lane & 1) * 32 + (lane >> 5) * 16) | ((lane & 24) ? OOB : 0u));
    }
  } else if (isr) {
    const float xo = asf((unsigned)xg[ro * 16 + (lane & 15)] << 16);
    v4u d;
    if (RSLEEP) __builtin_amdgcn_s_sleep(RSLEEP);
#pragma unroll 1
    for (int it = 0; it < SPIN_CAP; ++it) {
      d = ld4<PX>(rws, pa, 0);
      if ((XO & 2) || arrived(d, old)) break;
      __builtin_amdgcn_s_sleep(1);
      LDS_FENCE();
    }
    d &= ~1u;
    float y0 = tr32(asf(d[0]), asf(d[2])), y1 = tr32(asf(d[1]), asf(d[3]));
    y0 = tr16(y0, y1);
    y0 += dpp<ROR8>(y0); y0 += dpp<ROR4>(y0);
    if (!(lane & 12)) s_r[w][(lane & 3) * 4 + (lane >> 5) * 2 + ((lane >> 4) & 1)] = y0;
    LDS_FENCE(); bar(); LDS_FENCE();
    if (w == 0) {
      float a = s_r[lane >> 4][lane & 15] + s_r[4 + (lane >> 4)][lane & 15];
      a = x16_add(a); a = x32_add(a);
      if (lane < 16) out[ro * 16 + lane] = (u16)f2bf(xo + a);
    }
  }
  STAMP(4);
  STAMP_OUT();
}

extern "C" void kernel_launch(void* const* d_in, const int* in_sizes, int n_in,
                              void* d_out, int out_size, void* d_ws, size_t ws_size,
                              hipStream_t stream) {
  fused_kernel<<<NWG, NT, 0, stream>>>((const u16*)d_in[0], (const u16*)d_in[1], (const u16*)d_in[2], (const u16*)d_in[3],
                                       (const u16*)d_in[4], (const u16*)d_in[6], (const u16*)d_in[7], (const u16*)d_in[8],
                                       (u16*)d_out, (char*)d_ws);
}
```

```cpp
#include <hip/hip_runtime.h>
#include <hip/hip_bf16.h>
#include <stdint.h>

#define QPOS 24
#define PACE 8
#define QTRIES 8
#define TQ 125
#define TF 128
#define TFY 127
#define TY 131
#define NP 4
#define TAU 3650
#define PP 3000
#define PL 8
#define TOFF 150
#define TOFFQ 180
#define QBURST 100000
#define PBURST 0
#define KBURST 100000
#define VBURST 100000
#define QFIRST 24
#define OPOS 8
#define MRG 0
#define TWP 0
#define XW 0
#define WOFF 256
#define XA 19
#define XB 19
#define QROT 0
#define DIAG 0
#define LITE 0

typedef unsigned v4u __attribute__((ext_vector_type(4)));
typedef unsigned v2u __attribute__((ext_vector_type(2)));
typedef float f32x4 __attribute__((ext_vector_type(4)));
typedef float f32x2 __attribute__((ext_vector_type(2)));
typedef __bf16 bf16x2 __attribute__((ext_vector_type(2)));
typedef __bf16 bf16x8 __attribute__((ext_vector_type(8)));
typedef unsigned short u16;
typedef __amdgpu_buffer_rsrc_t rsrc_t;
typedef __attribute__((address_space(3))) f32x4* lds4_t;
typedef __attribute__((address_space(3))) v4u* ldsu_t;
typedef __attribute__((address_space(3))) float* lds1_t;

constexpr int D = 448, SK = 32768, DH = 256, F = 1792, NQ = 4096;
constexpr int NWG = 256, NT = 512;
constexpr unsigned OOB = 0x80000000u;
constexpr unsigned SENT = 0xFFFFFFFFu;
constexpr unsigned AREA = 9216;
constexpr unsigned A_O1 = 1024;
constexpr unsigned A_F = 5120;
constexpr unsigned A_Y2 = 1024;
constexpr unsigned WS_REQ = NWG * AREA;
constexpr unsigned WS_ECHO = WS_REQ + 1024;
constexpr unsigned WS_GO = WS_ECHO + 1024;
constexpr unsigned WS_Q = WS_GO + 256;
constexpr unsigned WS_END = WS_Q + 8 * 2048;
constexpr int SPIN_CAP = 1 << 18;
constexpr int C_NEXT = 0, C_TICK = 1, C_GO = 2, C_Q = 3, C_T0 = 4, C_FC = 5, C_OLD = 6, C_DONE = 7;

__device__ __forceinline__ float asf(unsigned u) { return __builtin_bit_cast(float, u); }
__device__ __forceinline__ unsigned asu(float f) { return __builtin_bit_cast(unsigned, f); }
__device__ __forceinline__ float rbf(float f) { __bf16 b = (__bf16)f; return (float)b; }
__device__ __forceinline__ unsigned f2bf(float f) { __bf16 b = (__bf16)f; return (unsigned)__builtin_bit_cast(u16, b); }
__device__ __forceinline__ float dot2(unsigned k, unsigned q, float acc) {
  return __builtin_amdgcn_fdot2_f32_bf16(__builtin_bit_cast(bf16x2, k), __builtin_bit_cast(bf16x2, q), acc, false);
}
__device__ __forceinline__ f32x2 unp(unsigned u) { f32x2 r; r.x = asf(u << 16); r.y = asf(u & 0xffff0000u); return r; }
template <int CTRL> __device__ __forceinline__ float dpp(float x) {
  return asf((unsigned)__builtin_amdgcn_update_dpp(0, (int)asu(x), CTRL, 0xf, 0xf, true));
}
constexpr int XOR1 = 0xB1, XOR2 = 0x4E, XOR7 = 0x141, ROR8 = 0x128, ROR4 = 0x124, ROR2 = 0x122, BCAST = 0x150;
constexpr int QP_EVEN = 0xA0, QP_ODD = 0xF5;
__device__ __forceinline__ float x16_add(float x) { auto s = __builtin_amdgcn_permlane16_swap(asu(x), asu(x), false, false); return asf(s[0]) + asf(s[1]); }
__device__ __forceinline__ float x32_add(float x) { auto s = __builtin_amdgcn_permlane32_swap(asu(x), asu(x), false, false); return asf(s[0]) + asf(s[1]); }
__device__ __forceinline__ float x16_max(float x) { auto s = __builtin_amdgcn_permlane16_swap(asu(x), asu(x), false, false); return fmaxf(asf(s[0]), asf(s[1])); }
__device__ __forceinline__ float x32_max(float x) { auto s = __builtin_amdgcn_permlane32_swap(asu(x), asu(x), false, false); return fmaxf(asf(s[0]), asf(s[1])); }
__device__ __forceinline__ float tr32(float a, float b) { auto s = __builtin_amdgcn_permlane32_swap(asu(a), asu(b), false, false); return asf(s[0]) + asf(s[1]); }
__device__ __forceinline__ float tr16(float a, float b) { auto s = __builtin_amdgcn_permlane16_swap(asu(a), asu(b), false, false); return asf(s[0]) + asf(s[1]); }
__device__ __forceinline__ float wave_sum(float v) {
  v += dpp<XOR1>(v); v += dpp<XOR2>(v); v += dpp<XOR7>(v); v += dpp<ROR8>(v);
  v = x16_add(v); return x32_add(v);
}
__device__ __forceinline__ float wave_max(float v) {
  v = fmaxf(v, dpp<XOR1>(v)); v = fmaxf(v, dpp<XOR2>(v)); v = fmaxf(v, dpp<XOR7>(v)); v = fmaxf(v, dpp<ROR8>(v));
  v = x16_max(v); return x32_max(v);
}
__device__ __forceinline__ rsrc_t mkrs(const void* p, unsigned bytes) { return __builtin_amdgcn_make_buffer_rsrc((void*)p, 0, (int)bytes, 0x00020000); }
__device__ __forceinline__ rsrc_t selr(bool c, rsrc_t a, rsrc_t b) { return c ? a : b; }
__device__ __forceinline__ unsigned selu(bool c, unsigned a, unsigned b) { return c ? a : b; }
template <int AUX> __device__ __forceinline__ v4u ld4(rsrc_t r, unsigned vo, unsigned so) { return __builtin_amdgcn_raw_buffer_load_b128(r, (int)vo, (int)so, AUX); }
template <int AUX> __device__ __forceinline__ v2u ld2(rsrc_t r, unsigned vo, unsigned so) { return __builtin_amdgcn_raw_buffer_load_b64(r, (int)vo, (int)so, AUX); }
__device__ __forceinline__ void st4c(v4u v, rsrc_t r, unsigned vo) { __builtin_amdgcn_raw_buffer_store_b128(v, r, (int)vo, 0, 16); }
__device__ __forceinline__ void st1c(unsigned v, rsrc_t r, unsigned vo) { __builtin_amdgcn_raw_buffer_store_b32(v, r, (int)vo, 0, 16); }
template <unsigned M> __device__ __forceinline__ unsigned tag(unsigned u, unsigned t) { return (u & ~M) | (t & M); }
__device__ __forceinline__ v4u tag4(const f32x4& v, const v4u& t) { return (v4u){tag<1>(asu(v[0]), t[0]), tag<1>(asu(v[1]), t[1]), tag<1>(asu(v[2]), t[2]), tag<1>(asu(v[3]), t[3])}; }
__device__ __forceinline__ bool arrived(const v4u& v, const v4u& o) { return __builtin_amdgcn_ballot_w64(v[0] == o[0] || v[1] == o[1] || v[2] == o[2] || v[3] == o[3]) == 0; }
__device__ __forceinline__ unsigned lowb(const v4u& v) { return __builtin_amdgcn_perm(v[1], v[0], 0x0c0c0400u) | __builtin_amdgcn_perm(v[3], v[2], 0x04000c0cu); }
#define SB() __builtin_amdgcn_sched_barrier(0)
__device__ __forceinline__ void bar() { __builtin_amdgcn_s_waitcnt(0xC07F); __builtin_amdgcn_s_barrier(); }
#define WAITV(n) __builtin_amdgcn_s_waitcnt(((n) & 15) | (((n) >> 4) << 14) | 0x0F70)
template <int V> struct IC { static constexpr int value = V; constexpr operator int() const { return V; } };
template <int A, int B, class Fn> __device__ __forceinline__ void static_for(Fn&& f) { if constexpr (A < B) { f(IC<A>{}); static_for<A + 1, B>(f); } }
#define LDS_FENCE() asm volatile("" ::: "memory")

constexpr float EXP_C = 0.0625f * 1.4426950408889634f;

struct Att {
  float m, l;
  f32x4 a[2];
};
__device__ __forceinline__ f32x4 mfma(const v4u& a, const v4u& b, const f32x4& c) {
  return __builtin_amdgcn_mfma_f32_16x16x32_bf16(__builtin_bit_cast(bf16x8, a), __builtin_bit_cast(bf16x8, b), c, 0, 0, 0);
}
__device__ __forceinline__ float red_max(float v) { v = fmaxf(v, dpp<XOR1>(v)); v = fmaxf(v, dpp<XOR2>(v)); return x32_max(x16_max(v)); }

template <int NL, class ISSUE>
__device__ __forceinline__ float scores(v4u (&kb)[8], const v4u (&qb)[8], int lane, ISSUE issue) {
  f32x4 d = {0.f, 0.f, 0.f, 0.f};
#pragma unroll
  for (int i = 0; i < NL; ++i) {
    d = mfma(kb[i], qb[i], d);
    asm volatile("" : "+v"(d));
    issue(i);
    SB();
  }
  const bool odd = lane & 1;
  return rbf((odd ? d[3] : d[0]) + dpp<XOR1>(odd ? d[1] : d[2]));
}
template <class ISSUE>
__device__ __forceinline__ float phase_k4(v4u (&kb)[8], const v4u (&qb)[8], Att& st, int lane, ISSUE issue) {
  float s = scores<4>(kb, qb, lane, issue);
  s = (lane & 2) ? -__builtin_inff() : s;
  const float mn = fmaxf(st.m, red_max(s));
  const float al = __builtin_amdgcn_exp2f((st.m - mn) * EXP_C);
  const float p = __builtin_amdgcn_exp2f((s - mn) * EXP_C);
  st.m = mn; st.l = st.l * al + p;
  st.a[0] *= al; st.a[1] *= al;
  SB();
  return p;
}
template <int I, int O, class ISSUE>
__device__ __forceinline__ void pv_step(v4u (&vb)[8], unsigned pp, const v4u& pm, Att& st, ISSUE& issue) {
  const v4u pb = pm & (unsigned)__builtin_amdgcn_update_dpp(0, (int)pp, I * 0x55, 0xf, 0xf, true);
  st.a[0] = mfma(vb[O + 2 * I], pb, st.a[0]);
  asm volatile("" : "+v"(st.a[0]));
  issue(O + 2 * I);
  SB();
  st.a[1] = mfma(vb[O + 2 * I + 1], pb, st.a[1]);
  asm volatile("" : "+v"(st.a[1]));
  issue(O + 2 * I + 1);
  SB();
}
template <int NL, class ISSUE>
__device__ __forceinline__ void phase_v(v4u (&vb)[8], float p, const v4u& pm, Att& st, ISSUE issue) {
  constexpr int O = 8 - NL;
  const unsigned pp = __builtin_bit_cast(unsigned, (bf16x2){(__bf16)p, (__bf16)p});
  pv_step<0, O>(vb, pp, pm, st, issue); pv_step<1, O>(vb, pp, pm, st, issue);
  if constexpr (NL == 8) { pv_step<2, O>(vb, pp, pm, st, issue); pv_step<3, O>(vb, pp, pm, st, issue); }
}

#define STAMP(k) do {} while (0)
#define STAMP_OUT() do {} while (0)

__device__ __forceinline__ f32x2 ln_stats(const v4u& xa, int lane) {
  f32x2 xv[4];
  float sm = 0.f;
#pragma unroll
  for (int j = 0; j < 4; ++j) { xv[j] = unp(xa[j]); sm += xv[j].x + xv[j].y; }
  const float mean = wave_sum(sm) * (1.0f / D);
  float sq = 0.f;
#pragma unroll
  for (int j = 0; j < 4; ++j) { const f32x2 d = xv[j] - mean; sq += d.x * d.x + d.y * d.y; }
  sq = lane < 56 ? sq : 0.f;
  const float var = rbf(wave_sum(sq) * (1.0f / D));
  const float sd = rbf(sqrtf(rbf(var + 1e-5f)));
  return (f32x2){rbf(mean), 1.0f / sd};
}
__device__ __forceinline__ float ln_apply(float x, f32x2 st) { return rbf(rbf(x - st.x) * st.y); }

__global__ void __launch_bounds__(NT, 2)
fused_kernel(const u16* __restrict__ xg, const u16* __restrict__ kc, const u16* __restrict__ vc, const u16* __restrict__ wq,
             const u16* __restrict__ wo, const u16* __restrict__ wg, const u16* __restrict__ wu, const u16* __restrict__ wd,
             u16* __restrict__ out, char* ws) {
  __shared__ __attribute__((aligned(16))) float s_part[8][512];
  __shared__ __attribute__((aligned(16))) float s_mv[8][64];
  __shared__ __attribute__((aligned(16))) unsigned s_q[256];
  __shared__ __attribute__((aligned(16))) unsigned s_qs[64];
  __shared__ __attribute__((aligned(16))) unsigned s_old[8][256];
  __shared__ __attribute__((aligned(16))) unsigned s_dst[8][256];
  __shared__ __attribute__((aligned(16))) float s_ml[2][16];
  __shared__ __attribute__((aligned(16))) float s_o[4][32];
  __shared__ __attribute__((aligned(16))) float s_ml2[8];
  __shared__ __attribute__((aligned(16))) float s_h[16];
  __shared__ __attribute__((aligned(16))) float s_r[8][16];
  __shared__ __attribute__((aligned(16))) float s_sc[64][32];
  __shared__ int s_ctl[64];
  const int tid = threadIdx.x, lane = tid & 63, w = __builtin_amdgcn_readfirstlane(tid >> 6);
  const int bi = blockIdx.x, h = (((bi & 1) ? bi + 5 : bi) & 6) | (bi >> 7), s = ((bi >> 3) & 15) * 2 + (bi & 1);
  const int b = h * 32 + s;
  static_assert(8 * TQ + 8 * TF + 6 * TFY + 10 * TY == 4096 && TQ - NP > 80 && TF - NP > 80 && TFY - NP > 80 && TY - NP > 80, "tickets");
  static_assert(TQ - NP <= 128 && TF - NP <= 128 && TFY - NP <= 128 && TY - NP <= 128, "tickets");
  const int nt = s < 8 ? TQ : s < 16 ? TF : s < 22 ? TFY : TY, nk = (nt - NP) >> 1;
  const int first = s * TY - min(s, 22) * (TY - TFY) - min(s, 16) * (TFY - TF) - min(s, 8) * (TF - TQ);
  asm volatile("" :: "s"(xg), "s"(kc), "s"(vc), "s"(wq), "s"(wo), "s"(wg), "s"(wu), "s"(wd), "s"(out), "s"(ws));
  STAMP(0);
  const rsrc_t rx = mkrs(xg, D * 2);
  const rsrc_t rk = mkrs(kc + (size_t)h * SK * DH, SK * DH * 2);
  const rsrc_t rv = mkrs(vc + (size_t)h * SK * DH, SK * DH * 2);
  const rsrc_t rws = mkrs(ws + (size_t)WOFF * 1048576, WS_END);
  const unsigned l16 = lane * 16;
  auto ubase = [&](int u) -> unsigned { return (unsigned)(first + 2 * u) * 4096u; };
  const unsigned kvo = ((lane >> 1) & 7) * 512 + (lane >> 4) * 32 + (lane & 1) * 16, vvo = (lane >> 4) * 1024 + (lane & 15) * 16;
  auto ldk = [&](unsigned base, int i) { return ld4<2>(rk, kvo + (i & 3) * 128, base + (i >> 2) * 4096); };
  const unsigned area = (unsigned)b * AREA;
  auto area_of = [&](int ss) -> unsigned { return (unsigned)(h * 32 + ss) * AREA; };
  const unsigned lmask = lane < 56 ? 0u : OOB;

  const bool isq = s < 8, isf = s >= 8 && s < 22, isy = s >= 16;
  const int f = h * 14 + s - 8, y = h * 16 + s - 16, ro = h * 4 + (s >> 2);
  const int sq = (s + QROT) & 7;
  const bool isr = s < 16 && ((s + h) & 3) == 0 && ro < 28;
  const rsrc_t rwo = mkrs(wo + (size_t)(isy ? y : 0) * 32 * D, (isy && w < 7 && !XW) ? 32 * D * 2 : 0);
  const rsrc_t rwd = mkrs(wd + (size_t)((isy && y < 112) ? y : 0) * 16 * D, (isy && y < 112 && w < 7 && !XW) ? 16 * D * 2 : 0);
  const unsigned wt_off = (lane >> 3) * (D * 2) + w * 128 + (lane & 7) * 16;
  const bool tile = isq || (isf && w < 4);
  const bool leader = (b == NWG - 1 && w == 1), member = (w == 0);

  const v4u sent = {SENT, SENT, SENT, SENT};
  unsigned poll_off = lane == 1 ? WS_GO : OOB;
  poll_off = lane == 0 ? WS_REQ + (b >> 2) * 16 : poll_off;
  poll_off = leader ? WS_ECHO + l16 : poll_off;
  v4u pv = {0u, 0u, 0u, 0u}, e1 = {0u, 0u, 0u, 0u};
  unsigned go_ref = 0;
  int* const ctl = &s_ctl[lane];
  auto ctl_get = [&](int i) { return __builtin_amdgcn_readfirstlane(__hip_atomic_load(&s_ctl[i], __ATOMIC_RELAXED, __HIP_MEMORY_SCOPE_WORKGROUP)); };
  auto ctl_set = [&](int i) { __hip_atomic_store(&s_ctl[i], 1, __ATOMIC_RELAXED, __HIP_MEMORY_SCOPE_WORKGROUP); };
  const unsigned qoff = WS_Q + h * 2048 + l16;
  v4u qv0, qv1;
  if (member) {
    qv0 = ld4<16>(rws, qoff, 0);
    qv1 = ld4<16>(rws, qoff, 1024);
    SB();
    int c = lane == C_NEXT ? (isq ? TOFFQ : TOFF) * 128 : 0;
    c = lane == C_TICK ? 40 : c;
    c = lane == C_T0 ? (int)(__builtin_amdgcn_s_memrealtime() & 0x7fffffff) : c;
    __hip_atomic_store(ctl, c, __ATOMIC_RELAXED, __HIP_MEMORY_SCOPE_WORKGROUP);
  }
  *reinterpret_cast<v4u*>(&s_sc[w * 8][lane * 4]) = sent;
  int t0 = 0;
  auto now = [&]() { return ((int)__builtin_amdgcn_s_memrealtime() - t0) & 0x7fffff; };
  const int neg = lane == C_NEXT ? 0 : (int)0x80000000;
  int inc = lane == C_NEXT ? TAU : 0;
  auto token = [&](int burst) {
    const int t = max(now() * 128 - burst, 0);
    __hip_atomic_fetch_max(ctl, t | neg, __ATOMIC_RELAXED, __HIP_MEMORY_SCOPE_WORKGROUP);
    return __hip_atomic_fetch_add(ctl, inc, __ATOMIC_RELAXED, __HIP_MEMORY_SCOPE_WORKGROUP);
  };
  auto due_of = [&](int x) { return __builtin_amdgcn_readlane(x, C_NEXT) >> 7; };
  auto until = [&](int due) {
#pragma unroll 1
    for (int it = 0; it < 4096 && now() < due; ++it) __builtin_amdgcn_s_sleep(1);
    SB();
  };
  unsigned qs0, qs1;
  auto q_there = [&]() { return __builtin_amdgcn_ballot_w64(((lowb(qv0) ^ qs0) & (lowb(qv1) ^ qs1)) != 0xFFFFFFFFu) == 0; };
  auto load_q = [&]() { qv0 = ld4<16>(rws, qoff, 0); qv1 = ld4<16>(rws, qoff, 1024); };
  auto publish_q = [&]() {
    *reinterpret_cast<v2u*>(&s_q[lane * 2]) = (v2u){__builtin_amdgcn_perm(qv0[1], qv0[0], 0x07060302u), __builtin_amdgcn_perm(qv0[3], qv0[2], 0x07060302u)};
    *reinterpret_cast<v2u*>(&s_q[128 + lane * 2]) = (v2u){__builtin_amdgcn_perm(qv1[1], qv1[0], 0x07060302u), __builtin_amdgcn_perm(qv1[3], qv1[2], 0x07060302u)};
    LDS_FENCE();
    __builtin_amdgcn_s_waitcnt(0xC07F);
    ctl_set(C_Q);
  };
  v4u pre[4][8], ring[8];
  auto unit0 = [&]() {
#pragma unroll
    for (int i = 0; i < 8; ++i) { pre[0][i] = ldk(ubase(w), i); SB(); }
  };
  SB();
  if (tile) {
    v4u tw[8];
    const int oct = isq ? 7 * w : (f & 3) * 14 + (w >> 1) * 7;
    const int stride = isq ? NQ * 2 : F * 2;
    const rsrc_t rt = mkrs(isq ? wq : (w & 1) ? wu : wg, D * stride);
    const unsigned to = (unsigned)((oct + (lane >> 3)) * 8 * stride + (isq ? h * 8 + sq : f >> 2) * 128 + (lane & 7) * 16) | lmask;
    const v4u xa = ld4<0>(rx, l16, 0);
    const v4u xb = ld4<0>(rx, (unsigned)(oct + (lane >> 3)) * 16 | lmask, 0);
    SB();
#pragma unroll
    for (int i = 0; i < 8; ++i) { tw[i] = ld4<2>(rt, to, i * stride); SB(); }
    LDS_FENCE(); bar(); LDS_FENCE();
    t0 = ctl_get(C_T0);
    SB();
    const f32x2 ls = ln_stats(xa, lane);
    f32x2 acc[4];
#pragma unroll
    for (int i = 0; i < 8; ++i) {
      const float xn = ln_apply(asf((i & 1) ? (xb[i >> 1] & 0xffff0000u) : (xb[i >> 1] << 16)), ls);
#pragma unroll
      for (int j = 0; j < 4; ++j) acc[j] = i ? acc[j] + unp(tw[i][j]) * xn : unp(tw[i][j]) * xn;
    }
    float y0 = tr32(acc[0].x, acc[2].x), y1 = tr32(acc[0].y, acc[2].y), y2 = tr32(acc[1].x, acc[3].x), y3 = tr32(acc[1].y, acc[3].y);
    y0 = tr16(y0, y2); y1 = tr16(y1, y3);
    const bool s_ = lane & 8;
    y0 = (s_ ? y1 : y0) + dpp<ROR8>(s_ ? y0 : y1);
    s_mv[w][(lane & 7) * 8 + (lane >> 3)] = y0;
    STAMP(6);
    token(isq ? QBURST : PBURST);
    unit0();
    if (isq) {
      LDS_FENCE(); bar(); LDS_FENCE();
      if (member) {
        float a = 0.f;
#pragma unroll
        for (int k = 0; k < 8; ++k) a += s_mv[k][lane];
        s_qs[lane] = f2bf(a) << 16;
        LDS_FENCE();
        const v4u qs = *reinterpret_cast<const v4u*>(&s_qs[(lane & 15) * 4]) | (~((sq & 4) ? qv1 : qv0) & 0xffffu);
        st4c(qs, rws, (lane >> 4) == (sq & 3) ? qoff + (sq >> 2) * 1024 : OOB);
      }
    } else {
      LDS_FENCE();
      __builtin_amdgcn_s_waitcnt(0xC07F);
      __hip_atomic_fetch_add(ctl, lane == C_FC ? 1 : 0, __ATOMIC_RELAXED, __HIP_MEMORY_SCOPE_WORKGROUP);
    }
  } else if (leader) {
    e1 = ld4<16>(rws, poll_off, 0);
    go_ref = __builtin_amdgcn_raw_buffer_load_b32(rws, (int)WS_GO, 0, 16);
  }
  SB();
  STAMP(5);
  SB();
  const unsigned pa = isy ? (w < 4 ? area + A_O1 + w * 1024 + l16 : (w == 7 && y < 112) ? area + A_F + (lane & 31) * 16 : OOB) : isr ? area + A_Y2 + w * 1024 + l16 : OOB;
  const unsigned mo_ = area_of(16 + (lane & 8) + (lane >> 4)) + A_O1 + s * 128 + (lane & 7) * 16;
  const int fd = (f & ~3) + ((lane >> 3) & 3);
  const unsigned mf = ((unsigned)((fd >> 4) * 32 + 16 + (fd & 15)) * AREA + A_F + (f & 3) * 128 + (lane & 7) * 16) | (lane < 32 ? 0u : OOB);
  auto my = [&](int p) -> unsigned { return (unsigned)((p >> 4) * 32 + (p & 12) + ((-(p >> 4)) & 3)) * AREA + A_Y2 + y * 64 + (p & 3) * 16; };
  v4u eo, ed;
  bool ok = !member;
  static_for<0, 40>([&](auto N) {
    constexpr int n = N;
    if constexpr (n == 0) {
      if (!tile) unit0();
    }
    if constexpr (n == 8) {
      if (!tile) {
        LDS_FENCE(); bar(); LDS_FENCE();
        t0 = ctl_get(C_T0);
        if (leader) {
          e1 += 1u;
          go_ref += 1u;
          st4c(e1, rws, WS_REQ + l16);
        }
      }
    }
    if constexpr (n > 0 && n % 8 == 0) {
      const int x = token(isq ? QBURST : PBURST);
      if constexpr (n > QFIRST) {
        if (!ok) {
          ok = q_there();
          if (ok) publish_q();
        }
      }
      if constexpr (n == OPOS + 16) {
        *reinterpret_cast<v4u*>(&s_old[w][lane * 4]) = (int)pa < 0 ? sent : eo;
        *reinterpret_cast<v4u*>(&s_dst[w][lane * 4]) = ~ed;
        LDS_FENCE();
        __hip_atomic_fetch_add(ctl, lane == C_OLD ? 1 : 0, __ATOMIC_RELAXED, __HIP_MEMORY_SCOPE_WORKGROUP);
      }
      SB();
      until(due_of(x));
      if constexpr (n == 8) {
        if (member) { qs0 = lowb(qv0); qs1 = lowb(qv1); }
      }
      if constexpr (n >= QFIRST) {
        if (!ok) load_q();
      }
      if constexpr (n == OPOS) {
        const int p = (w - 2) * 64 + lane;
        unsigned da = (isy && p < 112) ? my(p) : OOB;
        da = (w & 6) == 2 ? da : (w & 6) == 4 ? mo_ + (w - 4) * 4 * AREA : (w == 6 && isf) ? mf : OOB;
        eo = ld4<XB>(rws, pa, 0);
        ed = ld4<XA>(rws, da, 0);
      }
      SB();
    }
    if constexpr (n == QPOS) {
      STAMP(9);
      pv = ld4<16>(rws, (member || leader) ? poll_off : OOB, 0);
      SB();
    }
    if constexpr (n >= 8) {
      if constexpr (n >= PACE) WAITV(PACE + ((n >= QPOS && n < QPOS + PACE) ? 1 : 0) + ((n >= OPOS && n < OPOS + PACE) ? 2 : 0));
      if constexpr (n < 32) pre[n >> 3][n & 7] = ldk(ubase((n & 56) + w), n & 7); else ring[n & 7] = ldk(ubase(32 + w), n & 7);
    }
    SB();
    if constexpr (n - PACE == 7 || n - PACE == 15 || n - PACE == 23 || n - PACE == 31) STAMP(16 + (n - PACE) / 8);
  });
  inc = lane == C_TICK ? 1 : inc;
  int tok = token(KBURST);
  STAMP(10);
  bool svc = member || leader;
  unsigned seenm = 0, last_echo = 0;
  bool echoed = false;
  auto service = [&]() {
    if (leader) {
      seenm |= (pv[0] == e1[0] ? 1u : 0u) | (pv[1] == e1[1] ? 2u : 0u) | (pv[2] == e1[2] ? 4u : 0u) | (pv[3] == e1[3] ? 8u : 0u);
      svc = __builtin_amdgcn_ballot_w64(seenm != 15u) != 0;
      st1c(go_ref, rws, (lane == 0 && !svc) ? WS_GO : OOB);
    } else {
      const unsigned req = (unsigned)__builtin_amdgcn_readfirstlane((int)((b & 2) ? ((b & 1) ? pv[3] : pv[2]) : ((b & 1) ? pv[1] : pv[0])));
      const unsigned go = (unsigned)__builtin_amdgcn_readlane((int)pv[0], 1);
      go_ref = echoed ? go_ref : go;
      svc = go == (unsigned)__builtin_amdgcn_readfirstlane((int)go_ref);
      const bool rdy = ctl_get(C_OLD) == 8;
      st1c(req, rws, (lane == 0 && svc && rdy && !(echoed && req == last_echo)) ? WS_ECHO + b * 4 : OOB);
      echoed |= rdy; last_echo = req;
      if (!svc) {
        if (isf) {
          while (ctl_get(C_FC) != 4) __builtin_amdgcn_s_sleep(1);
          LDS_FENCE();
          const int j = (lane >> 3) & 3, pc = lane & 7, m = pc >> 2, col = j * 16 + (pc & 3) * 4;
          const f32x4 v = *reinterpret_cast<const f32x4*>(&s_mv[m][col]) + *reinterpret_cast<const f32x4*>(&s_mv[m + 2][col]);
          st4c(tag4(v, *reinterpret_cast<const v4u*>(&s_dst[6][lane * 4])), rws, mf);
        }
      }
    }
    pv = ld4<16>(rws, svc ? poll_off : OOB, 0);
    SB();
  };

  {
    if (!ok) {
      ok = q_there();
      if (ok) publish_q();
    }
#pragma unroll 1
    for (int it = 0; it < SPIN_CAP && !ctl_get(C_Q); ++it) {
      if (svc && it >= QTRIES) { service(); asm volatile("" :: "v"(pv)); }
      if (!ok) {
        load_q();
        ok = q_there() | !svc;
        if (ok) publish_q();
      } else {
        __builtin_amdgcn_s_sleep(1);
      }
    }
  }
  LDS_FENCE();
  v4u qb[8];
#pragma unroll
  for (int i = 0; i < 4; ++i) {
    const v4u z = {0u, 0u, 0u, 0u}, q = *reinterpret_cast<const v4u*>(&s_q[(lane & 8) * 16 + i * 32 + (lane >> 4) * 8 + (lane & 1) * 4]);
    qb[i] = (lane & 2) ? z : q; qb[4 + i] = (lane & 2) ? q : z;
  }
  v4u pm;
#pragma unroll
  for (int j = 0; j < 4; ++j) pm[j] = ((lane >> 1) & 3) == j ? ((lane & 1) ? 0xffff0000u : 0xffffu) : 0u;
  STAMP(1);
  SB();

  Att st;
  int sx = 20;
  {
    float* const sc = &s_sc[0][(lane >> 3) * 4 + (lane & 3)];
    rsrc_t nra, nrb;
    unsigned nva, nvb, nba, nbb, nsa, nsb;
    int kind = 0, nu = 32 + w, cu;
    auto next = [&]() {
      cu = nu;
      const int t = __builtin_amdgcn_readlane(tok, C_TICK);
      const bool fx = t < 2 * nk, tw = t >= nt;
      kind = fx ? (t >= nk ? 1 : 0) : tw ? 3 : 2;
      nu = t >= nk ? t - nk : t;
      nba = (unsigned)(fx ? first + 2 * nu : tw ? (TWP ? 0x80000 : 0) : first + t) * 4096u;
      nbb = fx ? nba + 4096 : nba;
      nra = selr(tw, rwo, selr(kind == 1, rv, rk));
      nrb = selr(tw, rwd, selr(kind == 0, rk, rv));
      nva = selu(tw, wt_off, selu(kind == 1, vvo, kvo)); nvb = selu(tw, wt_off, selu(kind == 0, kvo, vvo));
      nsa = selu(tw, 8 * D * 2, selu(kind == 1, 256, 128)); nsb = selu(tw, 8 * D * 2, selu(kind == 0, 128, 256));
      until(tw ? 0 : due_of(tok));
      inc = (lane == C_NEXT && t + PL >= 2 * nk) ? PP : inc;
      SB();
    };
    auto ldn = [&](int i) { ring[i] = i < 4 ? ld4<2>(nra, nva, nba + i * nsa) : ld4<2>(nrb, nvb, nbb + (i - 4) * nsb); };
    float mx;
    if (svc) service();
    {
      const float s0 = scores<8>(pre[0], qb, lane, [&](int) {});
      const float s1 = scores<8>(pre[1], qb, lane, [&](int) {});
      const float s2 = scores<8>(pre[2], qb, lane, [&](int) {});
      const float s3 = scores<8>(pre[3], qb, lane, [&](int) {});
      sc[w * 32] = s0; sc[(8 + w) * 32] = s1; sc[(16 + w) * 32] = s2; sc[(24 + w) * 32] = s3;
      mx = fmaxf(fmaxf(s0, s1), fmaxf(s2, s3));
      SB();
      STAMP(11);
    }
#pragma unroll 1
    while (kind == 0) {
      next();
      const float sv = scores<8>(ring, qb, lane, ldn);
      sc[cu * 32] = sv;
      tok = token(KBURST);
      mx = fmaxf(mx, sv);
      STAMP(sx); ++sx;
      if (svc) service();
      SB();
    }
    STAMP(7);
    st.m = red_max(mx); st.l = 0.f;
    st.a[0] = st.a[1] = (f32x4){0.f, 0.f, 0.f, 0.f};
#pragma unroll 1
    while (kind == 1) {
      float sv;
      for (;;) {
        sv = __hip_atomic_load(&sc[nu * 32], __ATOMIC_RELAXED, __HIP_MEMORY_SCOPE_WORKGROUP);
        if (__builtin_amdgcn_ballot_w64(asu(sv) == SENT) == 0) break;
        __builtin_amdgcn_s_sleep(1);
      }
      if (__builtin_amdgcn_ballot_w64(sv > st.m) != 0) {
        const float m2 = fmaxf(st.m, red_max(sv));
        const float al = __builtin_amdgcn_exp2f((st.m - m2) * EXP_C);
        st.m = m2; st.l *= al;
        st.a[0] *= al; st.a[1] *= al;
      }
      const float p = __builtin_amdgcn_exp2f((sv - st.m) * EXP_C);
      st.l += p;
      SB();
      next();
      phase_v<8>(ring, p, pm, st, ldn);
      tok = token(VBURST);
      STAMP(sx); ++sx;
      if (svc) service();
      SB();
    }
    STAMP(8);
#pragma unroll 1
    while (kind == 2) {
      next();
      const float p = phase_k4(ring, qb, st, lane, ldn);
      phase_v<4>(ring, p, pm, st, ldn);
      tok = token(VBURST);
      STAMP(sx); ++sx;
    }
#pragma unroll 1
    for (int it = 0; it < SPIN_CAP && svc; ++it) {
      service();
      asm volatile("" :: "v"(pv));
    }
    LDS_FENCE();
  }
  STAMP(12);
  auto tailw = [&]() {
#pragma unroll
    for (int i = 0; i < 6; ++i) ring[i] = i < 4 ? ld4<2>(rwo, wt_off, i * 8 * D * 2) : ld4<2>(rwd, wt_off, (i - 4) * 8 * D * 2);
    SB();
  };
  if (TWP == 1) tailw();
  auto send_o = [&](const f32x4& o, float M, float L, const v4u& t, int hf) {
    v4u msg;
#pragma unroll
    for (int j = 0; j < 3; ++j) msg[j] = tag<0x100>(__builtin_amdgcn_perm(asu(o[j]), asu(o[3]), 0x07060501u + j), t[j]);
    msg[3] = tag<1>(asu((lane & 1) ? L : M), t[3]);
    __builtin_amdgcn_raw_buffer_store_b128(msg, rws, (int)mo_, hf * 4 * AREA, 16);
  };
  {
    const lds4_t pw = (lds4_t)&s_part[w][lane * 4], pr = (lds4_t)&s_part[0][lane * 4], pmr = (lds4_t)&s_ml[(lane >> 3) & 1][0];
    const lds1_t pml = (lds1_t)&s_ml[(lane >> 3) & 1][w];
    pw[0] = st.a[0]; pw[64] = st.a[1];
    float l = st.l;
    l += dpp<XOR1>(l); l += dpp<XOR2>(l); l = x32_add(x16_add(l));
    pml[0] = st.m; pml[8] = l;
    LDS_FENCE(); bar(); LDS_FENCE();
    STAMP(2);
    if (w == 4 || w == 5) {
      f32x4 ml[4], pt[8];
#pragma unroll
      for (int k = 0; k < 4; ++k) ml[k] = pmr[k];
#pragma unroll
      for (int k = 0; k < 8; ++k) pt[k] = pr[k * 128 + (w - 4) * 64];
      const v4u dt = *reinterpret_cast<const v4u*>(&s_dst[w][lane * 4]);
      float M = -3.0e38f;
#pragma unroll
      for (int k = 0; k < 8; ++k) M = fmaxf(M, ml[k >> 2][k & 3]);
      f32x4 e[2] = {(ml[0] - M) * EXP_C, (ml[1] - M) * EXP_C};
      f32x4 o = {0.f, 0.f, 0.f, 0.f};
#pragma unroll
      for (int k = 0; k < 8; ++k) {
        e[k >> 2][k & 3] = __builtin_amdgcn_exp2f(e[k >> 2][k & 3]);
        o += pt[k] * e[k >> 2][k & 3];
      }
      const f32x4 l4 = ml[2] * e[0] + ml[3] * e[1];
      const float L = (l4[0] + l4[2]) + (l4[1] + l4[3]);
      send_o(o, M, L, dt, w - 4);
      SB();
      asm volatile("" :: "v"(ring[0]), "v"(ring[1]), "v"(ring[2]), "v"(ring[3]), "v"(ring[4]), "v"(ring[5]), "v"(ring[6]), "v"(ring[7]), "v"(pv));
    }
  }
  const v4u old = *reinterpret_cast<const v4u*>(&s_old[w][lane * 4]);
  if (isy) {
    if (TWP == 2) tailw();
    v4u d = ld4<16>(rws, pa, 0);
    SB();
    f32x2 wf[6][4];
#pragma unroll
    for (int i = 0; i < 6; ++i)
#pragma unroll
      for (int j = 0; j < 4; ++j) {
        wf[i][j] = unp(ring[i][j]);
        asm volatile("" : "+v"(wf[i][j]));
      }
    SB();
    STAMP(14);
    const v4u dt = *reinterpret_cast<const v4u*>(&s_dst[2][(w * 16 + (lane & 7) * 2 + (lane >> 5)) * 4]);
#pragma unroll 1
    for (int it = 0; it < SPIN_CAP; ++it) {
      if (arrived(d, old)) break;
      __builtin_amdgcn_s_sleep(1);
      LDS_FENCE();
      d = ld4<16>(rws, pa, 0);
    }
    STAMP(15);
    if (w < 4) {
      d &= (v4u){~0x100u, ~0x100u, ~0x100u, ~1u};
      const float ot = dpp<XOR1>(asf(d[3]));
      const float m = (lane & 1) ? ot : asf(d[3]), l = (lane & 1) ? asf(d[3]) : ot;
      float M = fmaxf(m, dpp<ROR8>(m)); M = x16_max(M); M = x32_max(M);
      const float e = __builtin_amdgcn_exp2f((m - M) * EXP_C);
      const float v3 = asf(__builtin_amdgcn_perm(__builtin_amdgcn_perm(d[2], d[1], 0x04000c0cu), d[0], 0x0706000cu));
      float y0 = tr32(asf(d[0]) * e, asf(d[2]) * e), y1 = tr32(asf(d[1]) * e, v3 * e), L = x32_add(l * e);
      y0 = tr16(y0, y1); L = x16_add(L);
      const bool s_ = lane & 8;
      y0 = (s_ ? L : y0) + dpp<ROR8>(s_ ? y0 : L);
      if (!s_) s_o[w][(lane & 7) * 4 + (lane >> 5) * 2 + ((lane >> 4) & 1)] = y0;
      if (lane == 8) *reinterpret_cast<f32x2*>(&s_ml2[w * 2]) = (f32x2){M, y0};
    } else if (w == 7) {
      float hv[4];
#pragma unroll
      for (int j = 0; j < 4; ++j) {
        float g = asf(d[j] & ~1u);
        g += dpp<ROR8>(g); g = rbf(x16_add(g));
        const float u = dpp<ROR4>(g);
        hv[j] = rbf(rbf(g / (1.0f + __expf(-g))) * u);
      }
      if (lane < 4) {
#pragma unroll
        for (int j = 0; j < 4; ++j) s_h[((lane & 1) * 4 + j) * 2 + (lane >> 1)] = hv[j];
      }
    }
    LDS_FENCE(); bar(); LDS_FENCE();
    STAMP(3);
    if (w < 7) {
      const f32x4 ma = *reinterpret_cast<const f32x4*>(&s_ml2[0]), mb = *reinterpret_cast<const f32x4*>(&s_ml2[4]);
      f32x4 pq[4];
#pragma unroll
      for (int k = 0; k < 4; ++k) pq[k] = *reinterpret_cast<const f32x4*>(&s_o[k][(lane >> 3) * 4]);
      const f32x2 hv = *reinterpret_cast<const f32x2*>(&s_h[(lane >> 3) * 2]);
      const float M = fmaxf(fmaxf(ma[0], ma[2]), fmaxf(mb[0], mb[2]));
      const float e0 = __builtin_amdgcn_exp2f((ma[0] - M) * EXP_C), e1_ = __builtin_amdgcn_exp2f((ma[2] - M) * EXP_C);
      const float e2 = __builtin_amdgcn_exp2f((mb[0] - M) * EXP_C), e3 = __builtin_amdgcn_exp2f((mb[2] - M) * EXP_C);
      const float inv = __builtin_amdgcn_rcpf((ma[1] * e0 + ma[3] * e1_) + (mb[1] * e2 + mb[3] * e3));
      const f32x4 av = ((pq[0] * e0 + pq[1] * e1_) + (pq[2] * e2 + pq[3] * e3)) * inv;
      f32x2 acc[4];
#pragma unroll
      for (int j = 0; j < 4; ++j) acc[j] = wf[4][j] * hv.x + wf[5][j] * hv.y;
#pragma unroll
      for (int i = 0; i < 4; ++i) {
        const float a = rbf(av[i]);
#pragma unroll
        for (int j = 0; j < 4; ++j) acc[j] += wf[i][j] * a;
      }
      float z[4] = {tr32(acc[0].x, acc[2].x), tr32(acc[0].y, acc[2].y), tr32(acc[1].x, acc[3].x), tr32(acc[1].y, acc[3].y)};
#pragma unroll
      for (int j = 0; j < 4; ++j) { z[j] = x16_add(z[j]); z[j] += dpp<ROR8>(z[j]); }
      const unsigned ra = (unsigned)(w * 32 + ((lane >> 1) & 3) * 4 + ((-w) & 3)) * AREA;
      st4c(tag4((f32x4){z[0], z[1], z[2], z[3]}, dt), rws, (ra + A_Y2 + y * 64 + (lane & 1) * 32 + (lane >> 5) * 16) | ((lane & 24) ? OOB : 0u));
    }
  } else if (isr) {
    const float xo = asf((unsigned)xg[ro * 16 + (lane & 15)] << 16);
    v4u d;
#pragma unroll 1
    for (int it = 0; it < SPIN_CAP; ++it) {
      d = ld4<16>(rws, pa, 0);
      if (arrived(d, old)) break;
      __builtin_amdgcn_s_sleep(1);
      LDS_FENCE();
    }
    d &= ~1u;
    float y0 = tr32(asf(d[0]), asf(d[2])), y1 = tr32(asf(d[1]), asf(d[3]));
    y0 = tr16(y0, y1);
    y0 += dpp<ROR8>(y0); y0 += dpp<ROR4>(y0);
    if (!(lane & 12)) s_r[w][(lane & 3) * 4 + (lane >> 5) * 2 + ((lane >> 4) & 1)] = y0;
    LDS_FENCE(); bar(); LDS_FENCE();
    if (w == 0) {
      float a = s_r[lane >> 4][lane & 15] + s_r[4 + (lane >> 4)][lane & 15];
      a = x16_add(a); a = x32_add(a);
      if (lane < 16) out[ro * 16 + lane] = (u16)f2bf(xo + a);
    }
  }
  STAMP(4);
  STAMP_OUT();
}

extern "C" void kernel_launch(void* const* d_in, const int* in_sizes, int n_in,
                              void* d_out, int out_size, void* d_ws, size_t ws_size,
                              hipStream_t stream) {
  fused_kernel<<<NWG, NT, 0, stream>>>((const u16*)d_in[0], (const u16*)d_in[1], (const u16*)d_in[2], (const u16*)d_in[3],
                                       (const u16*)d_in[4], (const u16*)d_in[6], (const u16*)d_in[7], (const u16*)d_in[8],
                                       (u16*)d_out, (char*)d_ws);
}
```

```cpp
#define PADK 99
#define PADC (bi == 0)
#include <hip/hip_runtime.h>
#include <hip/hip_bf16.h>
#include <stdint.h>

#define QPOS 24
#define PACE 8
#define QTRIES 8
#define TQ 125
#define TF 128
#define TFY 127
#define TY 131
#define NP 4
#define TAU 3650
#define PP 3000
#define PL 8
#define TOFF 150
#define TOFFQ 180
#define QBURST 100000
#define PBURST 0
#define KBURST 100000
#define VBURST 100000
#define QFIRST 24
#define OPOS 8
#define MRG 0
#define TWP 0
#define XW 0
#define WOFF 0
#define XA 19
#define XB 19
#define QROT 0
#define DIAG 0
#define LITE 0

typedef unsigned v4u __attribute__((ext_vector_type(4)));
typedef unsigned v2u __attribute__((ext_vector_type(2)));
typedef float f32x4 __attribute__((ext_vector_type(4)));
typedef float f32x2 __attribute__((ext_vector_type(2)));
typedef __bf16 bf16x2 __attribute__((ext_vector_type(2)));
typedef __bf16 bf16x8 __attribute__((ext_vector_type(8)));
typedef unsigned short u16;
typedef __amdgpu_buffer_rsrc_t rsrc_t;
typedef __attribute__((address_space(3))) f32x4* lds4_t;
typedef __attribute__((address_space(3))) v4u* ldsu_t;
typedef __attribute__((address_space(3))) float* lds1_t;

constexpr int D = 448, SK = 32768, DH = 256, F = 1792, NQ = 4096;
constexpr int NWG = 256, NT = 512;
constexpr unsigned OOB = 0x80000000u;
constexpr unsigned SENT = 0xFFFFFFFFu;
constexpr unsigned AREA = 9216;
constexpr unsigned A_O1 = 1024;
constexpr unsigned A_F = 5120;
constexpr unsigned A_Y2 = 1024;
constexpr unsigned WS_REQ = NWG * AREA;
constexpr unsigned WS_ECHO = WS_REQ + 1024;
constexpr unsigned WS_GO = WS_ECHO + 1024;
constexpr unsigned WS_Q = WS_GO + 256;
constexpr unsigned WS_END = WS_Q + 8 * 2048;
constexpr int SPIN_CAP = 1 << 18;
constexpr int C_NEXT = 0, C_TICK = 1, C_GO = 2, C_Q = 3, C_T0 = 4, C_FC = 5, C_OLD = 6, C_DONE = 7;

__device__ __forceinline__ float asf(unsigned u) { return __builtin_bit_cast(float, u); }
__device__ __forceinline__ unsigned asu(float f) { return __builtin_bit_cast(unsigned, f); }
__device__ __forceinline__ float rbf(float f) { __bf16 b = (__bf16)f; return (float)b; }
__device__ __forceinline__ unsigned f2bf(float f) { __bf16 b = (__bf16)f; return (unsigned)__builtin_bit_cast(u16, b); }
__device__ __forceinline__ float dot2(unsigned k, unsigned q, float acc) {
  return __builtin_amdgcn_fdot2_f32_bf16(__builtin_bit_cast(bf16x2, k), __builtin_bit_cast(bf16x2, q), acc, false);
}
__device__ __forceinline__ f32x2 unp(unsigned u) { f32x2 r; r.x = asf(u << 16); r.y = asf(u & 0xffff0000u); return r; }
template <int CTRL> __device__ __forceinline__ float dpp(float x) {
  return asf((unsigned)__builtin_amdgcn_update_dpp(0, (int)asu(x), CTRL, 0xf, 0xf, true));
}
constexpr int XOR1 = 0xB1, XOR2 = 0x4E, XOR7 = 0x141, ROR8 = 0x128, ROR4 = 0x124, ROR2 = 0x122, BCAST = 0x150;
constexpr int QP_EVEN = 0xA0, QP_ODD = 0xF5;
__device__ __forceinline__ float x16_add(float x) { auto s = __builtin_amdgcn_permlane16_swap(asu(x), asu(x), false, false); return asf(s[0]) + asf(s[1]); }
__device__ __forceinline__ float x32_add(float x) { auto s = __builtin_amdgcn_permlane32_swap(asu(x), asu(x), false, false); return asf(s[0]) + asf(s[1]); }
__device__ __forceinline__ float x16_max(float x) { auto s = __builtin_amdgcn_permlane16_swap(asu(x), asu(x), false, false); return fmaxf(asf(s[0]), asf(s[1])); }
__device__ __forceinline__ float x32_max(float x) { auto s = __builtin_amdgcn_permlane32_swap(asu(x), asu(x), false, false); return fmaxf(asf(s[0]), asf(s[1])); }
__device__ __forceinline__ float tr32(float a, float b) { auto s = __builtin_amdgcn_permlane32_swap(asu(a), asu(b), false, false); return asf(s[0]) + asf(s[1]); }
__device__ __forceinline__ float tr16(float a, float b) { auto s = __builtin_amdgcn_permlane16_swap(asu(a), asu(b), false, false); return asf(s[0]) + asf(s[1]); }
__device__ __forceinline__ float wave_sum(float v) {
  v += dpp<XOR1>(v); v += dpp<XOR2>(v); v += dpp<XOR7>(v); v += dpp<ROR8>(v);
  v = x16_add(v); return x32_add(v);
}
__device__ __forceinline__ float wave_max(float v) {
  v = fmaxf(v, dpp<XOR1>(v)); v = fmaxf(v, dpp<XOR2>(v)); v = fmaxf(v, dpp<XOR7>(v)); v = fmaxf(v, dpp<ROR8>(v));
  v = x16_max(v); return x32_max(v);
}
__device__ __forceinline__ rsrc_t mkrs(const void* p, unsigned bytes) { return __builtin_amdgcn_make_buffer_rsrc((void*)p, 0, (int)bytes, 0x00020000); }
__device__ __forceinline__ rsrc_t selr(bool c, rsrc_t a, rsrc_t b) { return c ? a : b; }
__device__ __forceinline__ unsigned selu(bool c, unsigned a, unsigned b) { return c ? a : b; }
template <int AUX> __device__ __forceinline__ v4u ld4(rsrc_t r, unsigned vo, unsigned so) { return __builtin_amdgcn_raw_buffer_load_b128(r, (int)vo, (int)so, AUX); }
template <int AUX> __device__ __forceinline__ v2u ld2(rsrc_t r, unsigned vo, unsigned so) { return __builtin_amdgcn_raw_buffer_load_b64(r, (int)vo, (int)so, AUX); }
__device__ __forceinline__ void st4c(v4u v, rsrc_t r, unsigned vo) { __builtin_amdgcn_raw_buffer_store_b128(v, r, (int)vo, 0, 16); }
__device__ __forceinline__ void st1c(unsigned v, rsrc_t r, unsigned vo) { __builtin_amdgcn_raw_buffer_store_b32(v, r, (int)vo, 0, 16); }
template <unsigned M> __device__ __forceinline__ unsigned tag(unsigned u, unsigned t) { return (u & ~M) | (t & M); }
__device__ __forceinline__ v4u tag4(const f32x4& v, const v4u& t) { return (v4u){tag<1>(asu(v[0]), t[0]), tag<1>(asu(v[1]), t[1]), tag<1>(asu(v[2]), t[2]), tag<1>(asu(v[3]), t[3])}; }
__device__ __forceinline__ bool arrived(const v4u& v, const v4u& o) { return __builtin_amdgcn_ballot_w64(v[0] == o[0] || v[1] == o[1] || v[2] == o[2] || v[3] == o[3]) == 0; }
__device__ __forceinline__ unsigned lowb(const v4u& v) { return __builtin_amdgcn_perm(v[1], v[0], 0x0c0c0400u) | __builtin_amdgcn_perm(v[3], v[2], 0x04000c0cu); }
#define SB() __builtin_amdgcn_sched_barrier(0)
__device__ __forceinline__ void bar() { __builtin_amdgcn_s_waitcnt(0xC07F); __builtin_amdgcn_s_barrier(); }
#define WAITV(n) __builtin_amdgcn_s_waitcnt(((n) & 15) | (((n) >> 4) << 14) | 0x0F70)
template <int V> struct IC { static constexpr int value = V; constexpr operator int() const { return V; } };
template <int A, int B, class Fn> __device__ __forceinline__ void static_for(Fn&& f) { if constexpr (A < B) { f(IC<A>{}); static_for<A + 1, B>(f); } }
#define LDS_FENCE() asm volatile("" ::: "memory")

constexpr float EXP_C = 0.0625f * 1.4426950408889634f;

struct Att {
  float m, l;
  f32x4 a[2];
};
__device__ __forceinline__ f32x4 mfma(const v4u& a, const v4u& b, const f32x4& c) {
  return __builtin_amdgcn_mfma_f32_16x16x32_bf16(__builtin_bit_cast(bf16x8, a), __builtin_bit_cast(bf16x8, b), c, 0, 0, 0);
}
__device__ __forceinline__ float red_max(float v) { v = fmaxf(v, dpp<XOR1>(v)); v = fmaxf(v, dpp<XOR2>(v)); return x32_max(x16_max(v)); }

template <int NL, class ISSUE>
__device__ __forceinline__ float scores(v4u (&kb)[8], const v4u (&qb)[8], int lane, ISSUE issue) {
  f32x4 d = {0.f, 0.f, 0.f, 0.f};
#pragma unroll
  for (int i = 0; i < NL; ++i) {
    d = mfma(kb[i], qb[i], d);
    asm volatile("" : "+v"(d));
    issue(i);
    SB();
  }
  const bool odd = lane & 1;
  return rbf((odd ? d[3] : d[0]) + dpp<XOR1>(odd ? d[1] : d[2]));
}
template <class ISSUE>
__device__ __forceinline__ float phase_k4(v4u (&kb)[8], const v4u (&qb)[8], Att& st, int lane, ISSUE issue) {
  float s = scores<4>(kb, qb, lane, issue);
  s = (lane & 2) ? -__builtin_inff() : s;
  const float mn = fmaxf(st.m, red_max(s));
  const float al = __builtin_amdgcn_exp2f((st.m - mn) * EXP_C);
  const float p = __builtin_amdgcn_exp2f((s - mn) * EXP_C);
  st.m = mn; st.l = st.l * al + p;
  st.a[0] *= al; st.a[1] *= al;
  SB();
  return p;
}
template <int I, int O, class ISSUE>
__device__ __forceinline__ void pv_step(v4u (&vb)[8], unsigned pp, const v4u& pm, Att& st, ISSUE& issue) {
  const v4u pb = pm & (unsigned)__builtin_amdgcn_update_dpp(0, (int)pp, I * 0x55, 0xf, 0xf, true);
  st.a[0] = mfma(vb[O + 2 * I], pb, st.a[0]);
  asm volatile("" : "+v"(st.a[0]));
  issue(O + 2 * I);
  SB();
  st.a[1] = mfma(vb[O + 2 * I + 1], pb, st.a[1]);
  asm volatile("" : "+v"(st.a[1]));
  issue(O + 2 * I + 1);
  SB();
}
template <int NL, class ISSUE>
__device__ __forceinline__ void phase_v(v4u (&vb)[8], float p, const v4u& pm, Att& st, ISSUE issue) {
  constexpr int O = 8 - NL;
  const unsigned pp = __builtin_bit_cast(unsigned, (bf16x2){(__bf16)p, (__bf16)p});
  pv_step<0, O>(vb, pp, pm, st, issue); pv_step<1, O>(vb, pp, pm, st, issue);
  if constexpr (NL == 8) { pv_step<2, O>(vb, pp, pm, st, issue); pv_step<3, O>(vb, pp, pm, st, issue); }
}

#define STAMP(k) do { if ((k) == PADK) { SB(); tpad = __builtin_amdgcn_s_memrealtime(); SB(); } } while (0)
#define STAMP_OUT() do { if (PADC) while (__builtin_amdgcn_s_memrealtime() - tpad < 6000) __builtin_amdgcn_s_sleep(1); } while (0)

__device__ __forceinline__ f32x2 ln_stats(const v4u& xa, int lane) {
  f32x2 xv[4];
  float sm = 0.f;
#pragma unroll
  for (int j = 0; j < 4; ++j) { xv[j] = unp(xa[j]); sm += xv[j].x + xv[j].y; }
  const float mean = wave_sum(sm) * (1.0f / D);
  float sq = 0.f;
#pragma unroll
  for (int j = 0; j < 4; ++j) { const f32x2 d = xv[j] - mean; sq += d.x * d.x + d.y * d.y; }
  sq = lane < 56 ? sq : 0.f;
  const float var = rbf(wave_sum(sq) * (1.0f / D));
  const float sd = rbf(sqrtf(rbf(var + 1e-5f)));
  return (f32x2){rbf(mean), 1.0f / sd};
}
__device__ __forceinline__ float ln_apply(float x, f32x2 st) { return rbf(rbf(x - st.x) * st.y); }

__global__ void __launch_bounds__(NT, 2)
fused_kernel(const u16* __restrict__ xg, const u16* __restrict__ kc, const u16* __restrict__ vc, const u16* __restrict__ wq,
             const u16* __restrict__ wo, const u16* __restrict__ wg, const u16* __restrict__ wu, const u16* __restrict__ wd,
             u16* __restrict__ out, char* ws) {
  unsigned long long tpad = 0;
  int bi_ = blockIdx.x;
  if (PADK == 99) asm volatile("s_memrealtime %0" : "=s"(tpad), "+s"(bi_));
  __shared__ __attribute__((aligned(16))) float s_part[8][512];
  __shared__ __attribute__((aligned(16))) float s_mv[8][64];
  __shared__ __attribute__((aligned(16))) unsigned s_q[256];
  __shared__ __attribute__((aligned(16))) unsigned s_qs[64];
  __shared__ __attribute__((aligned(16))) unsigned s_old[8][256];
  __shared__ __attribute__((aligned(16))) unsigned s_dst[8][256];
  __shared__ __attribute__((aligned(16))) float s_ml[2][16];
  __shared__ __attribute__((aligned(16))) float s_o[4][32];
  __shared__ __attribute__((aligned(16))) float s_ml2[8];
  __shared__ __attribute__((aligned(16))) float s_h[16];
  __shared__ __attribute__((aligned(16))) float s_r[8][16];
  __shared__ __attribute__((aligned(16))) float s_sc[64][32];
  __shared__ int s_ctl[64];
  const int tid = threadIdx.x, lane = tid & 63, w = __builtin_amdgcn_readfirstlane(tid >> 6);
  const int bi = bi_, h = (((bi & 1) ? bi + 5 : bi) & 6) | (bi >> 7), s = ((bi >> 3) & 15) * 2 + (bi & 1);
  const int b = h * 32 + s;
  static_assert(8 * TQ + 8 * TF + 6 * TFY + 10 * TY == 4096 && TQ - NP > 80 && TF - NP > 80 && TFY - NP > 80 && TY - NP > 80, "tickets");
  static_assert(TQ - NP <= 128 && TF - NP <= 128 && TFY - NP <= 128 && TY - NP <= 128, "tickets");
  const int nt = s < 8 ? TQ : s < 16 ? TF : s < 22 ? TFY : TY, nk = (nt - NP) >> 1;
  const int first = s * TY - min(s, 22) * (TY - TFY) - min(s, 16) * (TFY - TF) - min(s, 8) * (TF - TQ);
  asm volatile("" :: "s"(xg), "s"(kc), "s"(vc), "s"(wq), "s"(wo), "s"(wg), "s"(wu), "s"(wd), "s"(out), "s"(ws));
  STAMP(0);
  const rsrc_t rx = mkrs(xg, D * 2);
  const rsrc_t rk = mkrs(kc + (size_t)h * SK * DH, SK * DH * 2);
  const rsrc_t rv = mkrs(vc + (size_t)h * SK * DH, SK * DH * 2);
  const rsrc_t rws = mkrs(ws + (size_t)WOFF * 1048576, WS_END);
  const unsigned l16 = lane * 16;
  auto ubase = [&](int u) -> unsigned { return (unsigned)(first + 2 * u) * 4096u; };
  const unsigned kvo = ((lane >> 1) & 7) * 512 + (lane >> 4) * 32 + (lane & 1) * 16, vvo = (lane >> 4) * 1024 + (lane & 15) * 16;
  auto ldk = [&](unsigned base, int i) { return ld4<2>(rk, kvo + (i & 3) * 128, base + (i >> 2) * 4096); };
  const unsigned area = (unsigned)b * AREA;
  auto area_of = [&](int ss) -> unsigned { return (unsigned)(h * 32 + ss) * AREA; };
  const unsigned lmask = lane < 56 ? 0u : OOB;

  const bool isq = s < 8, isf = s >= 8 && s < 22, isy = s >= 16;
  const int f = h * 14 + s - 8, y = h * 16 + s - 16, ro = h * 4 + (s >> 2);
  const int sq = (s + QROT) & 7;
  const bool isr = s < 16 && ((s + h) & 3) == 0 && ro < 28;
  const rsrc_t rwo = mkrs(wo + (size_t)(isy ? y : 0) * 32 * D, (isy && w < 7 && !XW) ? 32 * D * 2 : 0);
  const rsrc_t rwd = mkrs(wd + (size_t)((isy && y < 112) ? y : 0) * 16 * D, (isy && y < 112 && w < 7 && !XW) ? 16 * D * 2 : 0);
  const unsigned wt_off = (lane >> 3) * (D * 2) + w * 128 + (lane & 7) * 16;
  const bool tile = isq || (isf && w < 4);
  const bool leader = (b == NWG - 1 && w == 1), member = (w == 0);

  const v4u sent = {SENT, SENT, SENT, SENT};
  unsigned poll_off = lane == 1 ? WS_GO : OOB;
  poll_off = lane == 0 ? WS_REQ + (b >> 2) * 16 : poll_off;
  poll_off = leader ? WS_ECHO + l16 : poll_off;
  v4u pv = {0u, 0u, 0u, 0u}, e1 = {0u, 0u, 0u, 0u};
  unsigned go_ref = 0;
  int* const ctl = &s_ctl[lane];
  auto ctl_get = [&](int i) { return __builtin_amdgcn_readfirstlane(__hip_atomic_load(&s_ctl[i], __ATOMIC_RELAXED, __HIP_MEMORY_SCOPE_WORKGROUP)); };
  auto ctl_set = [&](int i) { __hip_atomic_store(&s_ctl[i], 1, __ATOMIC_RELAXED, __HIP_MEMORY_SCOPE_WORKGROUP); };
  const unsigned qoff = WS_Q + h * 2048 + l16;
  v4u qv0, qv1;
  if (member) {
    qv0 = ld4<16>(rws, qoff, 0);
    qv1 = ld4<16>(rws, qoff, 1024);
    SB();
    int c = lane == C_NEXT ? (isq ? TOFFQ : TOFF) * 128 : 0;
    c = lane == C_TICK ? 40 : c;
    c = lane == C_T0 ? (int)(__builtin_amdgcn_s_memrealtime() & 0x7fffffff) : c;
    __hip_atomic_store(ctl, c, __ATOMIC_RELAXED, __HIP_MEMORY_SCOPE_WORKGROUP);
  }
  *reinterpret_cast<v4u*>(&s_sc[w * 8][lane * 4]) = sent;
  int t0 = 0;
  auto now = [&]() { return ((int)__builtin_amdgcn_s_memrealtime() - t0) & 0x7fffff; };
  const int neg = lane == C_NEXT ? 0 : (int)0x80000000;
  int inc = lane == C_NEXT ? TAU : 0;
  auto token = [&](int burst) {
    const int t = max(now() * 128 - burst, 0);
    __hip_atomic_fetch_max(ctl, t | neg, __ATOMIC_RELAXED, __HIP_MEMORY_SCOPE_WORKGROUP);
    return __hip_atomic_fetch_add(ctl, inc, __ATOMIC_RELAXED, __HIP_MEMORY_SCOPE_WORKGROUP);
  };
  auto due_of = [&](int x) { return __builtin_amdgcn_readlane(x, C_NEXT) >> 7; };
  auto until = [&](int due) {
#pragma unroll 1
    for (int it = 0; it < 4096 && now() < due; ++it) __builtin_amdgcn_s_sleep(1);
    SB();
  };
  unsigned qs0, qs1;
  auto q_there = [&]() { return __builtin_amdgcn_ballot_w64(((lowb(qv0) ^ qs0) & (lowb(qv1) ^ qs1)) != 0xFFFFFFFFu) == 0; };
  auto load_q = [&]() { qv0 = ld4<16>(rws, qoff, 0); qv1 = ld4<16>(rws, qoff, 1024); };
  auto publish_q = [&]() {
    *reinterpret_cast<v2u*>(&s_q[lane * 2]) = (v2u){__builtin_amdgcn_perm(qv0[1], qv0[0], 0x07060302u), __builtin_amdgcn_perm(qv0[3], qv0[2], 0x07060302u)};
    *reinterpret_cast<v2u*>(&s_q[128 + lane * 2]) = (v2u){__builtin_amdgcn_perm(qv1[1], qv1[0], 0x07060302u), __builtin_amdgcn_perm(qv1[3], qv1[2], 0x07060302u)};
    LDS_FENCE();
    __builtin_amdgcn_s_waitcnt(0xC07F);
    ctl_set(C_Q);
  };
  v4u pre[4][8], ring[8];
  auto unit0 = [&]() {
#pragma unroll
    for (int i = 0; i < 8; ++i) { pre[0][i] = ldk(ubase(w), i); SB(); }
  };
  SB();
  if (tile) {
    v4u tw[8];
    const int oct = isq ? 7 * w : (f & 3) * 14 + (w >> 1) * 7;
    const int stride = isq ? NQ * 2 : F * 2;
    const rsrc_t rt = mkrs(isq ? wq : (w & 1) ? wu : wg, D * stride);
    const unsigned to = (unsigned)((oct + (lane >> 3)) * 8 * stride + (isq ? h * 8 + sq : f >> 2) * 128 + (lane & 7) * 16) | lmask;
    const v4u xa = ld4<0>(rx, l16, 0);
    const v4u xb = ld4<0>(rx, (unsigned)(oct + (lane >> 3)) * 16 | lmask, 0);
    SB();
#pragma unroll
    for (int i = 0; i < 8; ++i) { tw[i] = ld4<2>(rt, to, i * stride); SB(); }
    LDS_FENCE(); bar(); LDS_FENCE();
    t0 = ctl_get(C_T0);
    SB();
    const f32x2 ls = ln_stats(xa, lane);
    f32x2 acc[4];
#pragma unroll
    for (int i = 0; i < 8; ++i) {
      const float xn = ln_apply(asf((i & 1) ? (xb[i >> 1] & 0xffff0000u) : (xb[i >> 1] << 16)), ls);
#pragma unroll
      for (int j = 0; j < 4; ++j) acc[j] = i ? acc[j] + unp(tw[i][j]) * xn : unp(tw[i][j]) * xn;
    }
    float y0 = tr32(acc[0].x, acc[2].x), y1 = tr32(acc[0].y, acc[2].y), y2 = tr32(acc[1].x, acc[3].x), y3 = tr32(acc[1].y, acc[3].y);
    y0 = tr16(y0, y2); y1 = tr16(y1, y3);
    const bool s_ = lane & 8;
    y0 = (s_ ? y1 : y0) + dpp<ROR8>(s_ ? y0 : y1);
    s_mv[w][(lane & 7) * 8 + (lane >> 3)] = y0;
    STAMP(6);
    token(isq ? QBURST : PBURST);
    unit0();
    if (isq) {
      LDS_FENCE(); bar(); LDS_FENCE();
      if (member) {
        float a = 0.f;
#pragma unroll
        for (int k = 0; k < 8; ++k) a += s_mv[k][lane];
        s_qs[lane] = f2bf(a) << 16;
        LDS_FENCE();
        const v4u qs = *reinterpret_cast<const v4u*>(&s_qs[(lane & 15) * 4]) | (~((sq & 4) ? qv1 : qv0) & 0xffffu);
        st4c(qs, rws, (lane >> 4) == (sq & 3) ? qoff + (sq >> 2) * 1024 : OOB);
      }
    } else {
      LDS_FENCE();
      __builtin_amdgcn_s_waitcnt(0xC07F);
      __hip_atomic_fetch_add(ctl, lane == C_FC ? 1 : 0, __ATOMIC_RELAXED, __HIP_MEMORY_SCOPE_WORKGROUP);
    }
  } else if (leader) {
    e1 = ld4<16>(rws, poll_off, 0);
    go_ref = __builtin_amdgcn_raw_buffer_load_b32(rws, (int)WS_GO, 0, 16);
  }
  SB();
  STAMP(5);
  SB();
  const unsigned pa = isy ? (w < 4 ? area + A_O1 + w * 1024 + l16 : (w == 7 && y < 112) ? area + A_F + (lane & 31) * 16 : OOB) : isr ? area + A_Y2 + w * 1024 + l16 : OOB;
  const unsigned mo_ = area_of(16 + (lane & 8) + (lane >> 4)) + A_O1 + s * 128 + (lane & 7) * 16;
  const int fd = (f & ~3) + ((lane >> 3) & 3);
  const unsigned mf = ((unsigned)((fd >> 4) * 32 + 16 + (fd & 15)) * AREA + A_F + (f & 3) * 128 + (lane & 7) * 16) | (lane < 32 ? 0u : OOB);
  auto my = [&](int p) -> unsigned { return (unsigned)((p >> 4) * 32 + (p & 12) + ((-(p >> 4)) & 3)) * AREA + A_Y2 + y * 64 + (p & 3) * 16; };
  v4u eo, ed;
  bool ok = !member;
  static_for<0, 40>([&](auto N) {
    constexpr int n = N;
    if constexpr (n == 0) {
      if (!tile) unit0();
    }
    if constexpr (n == 8) {
      if (!tile) {
        LDS_FENCE(); bar(); LDS_FENCE();
        t0 = ctl_get(C_T0);
        if (leader) {
          e1 += 1u;
          go_ref += 1u;
          st4c(e1, rws, WS_REQ + l16);
        }
      }
    }
    if constexpr (n > 0 && n % 8 == 0) {
      const int x = token(isq ? QBURST : PBURST);
      if constexpr (n > QFIRST) {
        if (!ok) {
          ok = q_there();
          if (ok) publish_q();
        }
      }
      if constexpr (n == OPOS + 16) {
        *reinterpret_cast<v4u*>(&s_old[w][lane * 4]) = (int)pa < 0 ? sent : eo;
        *reinterpret_cast<v4u*>(&s_dst[w][lane * 4]) = ~ed;
        LDS_FENCE();
        __hip_atomic_fetch_add(ctl, lane == C_OLD ? 1 : 0, __ATOMIC_RELAXED, __HIP_MEMORY_SCOPE_WORKGROUP);
      }
      SB();
      until(due_of(x));
      if constexpr (n == 8) {
        if (member) { qs0 = lowb(qv0); qs1 = lowb(qv1); }
      }
      if constexpr (n >= QFIRST) {
        if (!ok) load_q();
      }
      if constexpr (n == OPOS) {
        const int p = (w - 2) * 64 + lane;
        unsigned da = (isy && p < 112) ? my(p) : OOB;
        da = (w & 6) == 2 ? da : (w & 6) == 4 ? mo_ + (w - 4) * 4 * AREA : (w == 6 && isf) ? mf : OOB;
        eo = ld4<XB>(rws, pa, 0);
        ed = ld4<XA>(rws, da, 0);
      }
      SB();
    }
    if constexpr (n == QPOS) {
      STAMP(9);
      pv = ld4<16>(rws, (member || leader) ? poll_off : OOB, 0);
      SB();
    }
    if constexpr (n >= 8) {
      if constexpr (n >= PACE) WAITV(PACE + ((n >= QPOS && n < QPOS + PACE) ? 1 : 0) + ((n >= OPOS && n < OPOS + PACE) ? 2 : 0));
      if constexpr (n < 32) pre[n >> 3][n & 7] = ldk(ubase((n & 56) + w), n & 7); else ring[n & 7] = ldk(ubase(32 + w), n & 7);
    }
    SB();
    if constexpr (n - PACE == 7 || n - PACE == 15 || n - PACE == 23 || n - PACE == 31) STAMP(16 + (n - PACE) / 8);
  });
  inc = lane == C_TICK ? 1 : inc;
  int tok = token(KBURST);
  STAMP(10);
  bool svc = member || leader;
  unsigned seenm = 0, last_echo = 0;
  bool echoed = false;
  auto service = [&]() {
    if (leader) {
      seenm |= (pv[0] == e1[0] ? 1u : 0u) | (pv[1] == e1[1] ? 2u : 0u) | (pv[2] == e1[2] ? 4u : 0u) | (pv[3] == e1[3] ? 8u : 0u);
      svc = __builtin_amdgcn_ballot_w64(seenm != 15u) != 0;
      st1c(go_ref, rws, (lane == 0 && !svc) ? WS_GO : OOB);
    } else {
      const unsigned req = (unsigned)__builtin_amdgcn_readfirstlane((int)((b & 2) ? ((b & 1) ? pv[3] : pv[2]) : ((b & 1) ? pv[1] : pv[0])));
      const unsigned go = (unsigned)__builtin_amdgcn_readlane((int)pv[0], 1);
      go_ref = echoed ? go_ref : go;
      svc = go == (unsigned)__builtin_amdgcn_readfirstlane((int)go_ref);
      const bool rdy = ctl_get(C_OLD) == 8;
      st1c(req, rws, (lane == 0 && svc && rdy && !(echoed && req == last_echo)) ? WS_ECHO + b * 4 : OOB);
      echoed |= rdy; last_echo = req;
      if (!svc) {
        if (isf) {
          while (ctl_get(C_FC) != 4) __builtin_amdgcn_s_sleep(1);
          LDS_FENCE();
          const int j = (lane >> 3) & 3, pc = lane & 7, m = pc >> 2, col = j * 16 + (pc & 3) * 4;
          const f32x4 v = *reinterpret_cast<const f32x4*>(&s_mv[m][col]) + *reinterpret_cast<const f32x4*>(&s_mv[m + 2][col]);
          st4c(tag4(v, *reinterpret_cast<const v4u*>(&s_dst[6][lane * 4])), rws, mf);
        }
      }
    }
    pv = ld4<16>(rws, svc ? poll_off : OOB, 0);
    SB();
  };

  {
    if (!ok) {
      ok = q_there();
      if (ok) publish_q();
    }
#pragma unroll 1
    for (int it = 0; it < SPIN_CAP && !ctl_get(C_Q); ++it) {
      if (svc && it >= QTRIES) { service(); asm volatile("" :: "v"(pv)); }
      if (!ok) {
        load_q();
        ok = q_there() | !svc;
        if (ok) publish_q();
      } else {
        __builtin_amdgcn_s_sleep(1);
      }
    }
  }
  LDS_FENCE();
  v4u qb[8];
#pragma unroll
  for (int i = 0; i < 4; ++i) {
    const v4u z = {0u, 0u, 0u, 0u}, q = *reinterpret_cast<const v4u*>(&s_q[(lane & 8) * 16 + i * 32 + (lane >> 4) * 8 + (lane & 1) * 4]);
    qb[i] = (lane & 2) ? z : q; qb[4 + i] = (lane & 2) ? q : z;
  }
  v4u pm;
#pragma unroll
  for (int j = 0; j < 4; ++j) pm[j] = ((lane >> 1) & 3) == j ? ((lane & 1) ? 0xffff0000u : 0xffffu) : 0u;
  STAMP(1);
  SB();

  Att st;
  int sx = 20;
  {
    float* const sc = &s_sc[0][(lane >> 3) * 4 + (lane & 3)];
    rsrc_t nra, nrb;
    unsigned nva, nvb, nba, nbb, nsa, nsb;
    int kind = 0, nu = 32 + w, cu;
    auto next = [&]() {
      cu = nu;
      const int t = __builtin_amdgcn_readlane(tok, C_TICK);
      const bool fx = t < 2 * nk, tw = t >= nt;
      kind = fx ? (t >= nk ? 1 : 0) : tw ? 3 : 2;
      nu = t >= nk ? t - nk : t;
      nba = (unsigned)(fx ? first + 2 * nu : tw ? (TWP ? 0x80000 : 0) : first + t) * 4096u;
      nbb = fx ? nba + 4096 : nba;
      nra = selr(tw, rwo, selr(kind == 1, rv, rk));
      nrb = selr(tw, rwd, selr(kind == 0, rk, rv));
      nva = selu(tw, wt_off, selu(kind == 1, vvo, kvo)); nvb = selu(tw, wt_off, selu(kind == 0, kvo, vvo));
      nsa = selu(tw, 8 * D * 2, selu(kind == 1, 256, 128)); nsb = selu(tw, 8 * D * 2, selu(kind == 0, 128, 256));
      until(tw ? 0 : due_of(tok));
      inc = (lane == C_NEXT && t + PL >= 2 * nk) ? PP : inc;
      SB();
    };
    auto ldn = [&](int i) { ring[i] = i < 4 ? ld4<2>(nra, nva, nba + i * nsa) : ld4<2>(nrb, nvb, nbb + (i - 4) * nsb); };
    float mx;
    if (svc) service();
    {
      const float s0 = scores<8>(pre[0], qb, lane, [&](int) {});
      const float s1 = scores<8>(pre[1], qb, lane, [&](int) {});
      const float s2 = scores<8>(pre[2], qb, lane, [&](int) {});
      const float s3 = scores<8>(pre[3], qb, lane, [&](int) {});
      sc[w * 32] = s0; sc[(8 + w) * 32] = s1; sc[(16 + w) * 32] = s2; sc[(24 + w) * 32] = s3;
      mx = fmaxf(fmaxf(s0, s1), fmaxf(s2, s3));
      SB();
      STAMP(11);
    }
#pragma unroll 1
    while (kind == 0) {
      next();
      const float sv = scores<8>(ring, qb, lane, ldn);
      sc[cu * 32] = sv;
      tok = token(KBURST);
      mx = fmaxf(mx, sv);
      STAMP(sx); ++sx;
      if (svc) service();
      SB();
    }
    STAMP(7);
    st.m = red_max(mx); st.l = 0.f;
    st.a[0] = st.a[1] = (f32x4){0.f, 0.f, 0.f, 0.f};
#pragma unroll 1
    while (kind == 1) {
      float sv;
      for (;;) {
        sv = __hip_atomic_load(&sc[nu * 32], __ATOMIC_RELAXED, __HIP_MEMORY_SCOPE_WORKGROUP);
        if (__builtin_amdgcn_ballot_w64(asu(sv) == SENT) == 0) break;
        __builtin_amdgcn_s_sleep(1);
      }
      if (__builtin_amdgcn_ballot_w64(sv > st.m) != 0) {
        const float m2 = fmaxf(st.m, red_max(sv));
        const float al = __builtin_amdgcn_exp2f((st.m - m2) * EXP_C);
        st.m = m2; st.l *= al;
        st.a[0] *= al; st.a[1] *= al;
      }
      const float p = __builtin_amdgcn_exp2f((sv - st.m) * EXP_C);
      st.l += p;
      SB();
      next();
      phase_v<8>(ring, p, pm, st, ldn);
      tok = token(VBURST);
      STAMP(sx); ++sx;
      if (svc) service();
      SB();
    }
    STAMP(8);
#pragma unroll 1
    while (kind == 2) {
      next();
      const float p = phase_k4(ring, qb, st, lane, ldn);
      phase_v<4>(ring, p, pm, st, ldn);
      tok = token(VBURST);
      STAMP(sx); ++sx;
    }
#pragma unroll 1
    for (int it = 0; it < SPIN_CAP && svc; ++it) {
      service();
      asm volatile("" :: "v"(pv));
    }
    LDS_FENCE();
  }
  STAMP(12);
  auto tailw = [&]() {
#pragma unroll
    for (int i = 0; i < 6; ++i) ring[i] = i < 4 ? ld4<2>(rwo, wt_off, i * 8 * D * 2) : ld4<2>(rwd, wt_off, (i - 4) * 8 * D * 2);
    SB();
  };
  if (TWP == 1) tailw();
  auto send_o = [&](const f32x4& o, float M, float L, const v4u& t, int hf) {
    v4u msg;
#pragma unroll
    for (int j = 0; j < 3; ++j) msg[j] = tag<0x100>(__builtin_amdgcn_perm(asu(o[j]), asu(o[3]), 0x07060501u + j), t[j]);
    msg[3] = tag<1>(asu((lane & 1) ? L : M), t[3]);
    __builtin_amdgcn_raw_buffer_store_b128(msg, rws, (int)mo_, hf * 4 * AREA, 16);
  };
  {
    const lds4_t pw = (lds4_t)&s_part[w][lane * 4], pr = (lds4_t)&s_part[0][lane * 4], pmr = (lds4_t)&s_ml[(lane >> 3) & 1][0];
    const lds1_t pml = (lds1_t)&s_ml[(lane >> 3) & 1][w];
    pw[0] = st.a[0]; pw[64] = st.a[1];
    float l = st.l;
    l += dpp<XOR1>(l); l += dpp<XOR2>(l); l = x32_add(x16_add(l));
    pml[0] = st.m; pml[8] = l;
    LDS_FENCE(); bar(); LDS_FENCE();
    STAMP(2);
    if (w == 4 || w == 5) {
      f32x4 ml[4], pt[8];
#pragma unroll
      for (int k = 0; k < 4; ++k) ml[k] = pmr[k];
#pragma unroll
      for (int k = 0; k < 8; ++k) pt[k] = pr[k * 128 + (w - 4) * 64];
      const v4u dt = *reinterpret_cast<const v4u*>(&s_dst[w][lane * 4]);
      float M = -3.0e38f;
#pragma unroll
      for (int k = 0; k < 8; ++k) M = fmaxf(M, ml[k >> 2][k & 3]);
      f32x4 e[2] = {(ml[0] - M) * EXP_C, (ml[1] - M) * EXP_C};
      f32x4 o = {0.f, 0.f, 0.f, 0.f};
#pragma unroll
      for (int k = 0; k < 8; ++k) {
        e[k >> 2][k & 3] = __builtin_amdgcn_exp2f(e[k >> 2][k & 3]);
        o += pt[k] * e[k >> 2][k & 3];
      }
      const f32x4 l4 = ml[2] * e[0] + ml[3] * e[1];
      const float L = (l4[0] + l4[2]) + (l4[1] + l4[3]);
      send_o(o, M, L, dt, w - 4);
      SB();
      asm volatile("" :: "v"(ring[0]), "v"(ring[1]), "v"(ring[2]), "v"(ring[3]), "v"(ring[4]), "v"(ring[5]), "v"(ring[6]), "v"(ring[7]), "v"(pv));
    }
  }
  const v4u old = *reinterpret_cast<const v4u*>(&s_old[w][lane * 4]);
  if (isy) {
    if (TWP == 2) tailw();
    v4u d = ld4<16>(rws, pa, 0);
    SB();
    f32x2 wf[6][4];
#pragma unroll
    for (int i = 0; i < 6; ++i)
#pragma unroll
      for (int j = 0; j < 4; ++j) {
        wf[i][j] = unp(ring[i][j]);
        asm volatile("" : "+v"(wf[i][j]));
      }
    SB();
    STAMP(14);
    const v4u dt = *reinterpret_cast<const v4u*>(&s_dst[2][(w * 16 + (lane & 7) * 2 + (lane >> 5)) * 4]);
#pragma unroll 1
    for (int it = 0; it < SPIN_CAP; ++it) {
      if (arrived(d, old)) break;
      __builtin_amdgcn_s_sleep(1);
      LDS_FENCE();
      d = ld4<16>(rws, pa, 0);
    }
    STAMP(15);
    if (w < 4) {
      d &= (v4u){~0x100u, ~0x100u, ~0x100u, ~1u};
      const float ot = dpp<XOR1>(asf(d[3]));
      const float m = (lane & 1) ? ot : asf(d[3]), l = (lane & 1) ? asf(d[3]) : ot;
      float M = fmaxf(m, dpp<ROR8>(m)); M = x16_max(M); M = x32_max(M);
      const float e = __builtin_amdgcn_exp2f((m - M) * EXP_C);
      const float v3 = asf(__builtin_amdgcn_perm(__builtin_amdgcn_perm(d[2], d[1], 0x04000c0cu), d[0], 0x0706000cu));
      float y0 = tr32(asf(d[0]) * e, asf(d[2]) * e), y1 = tr32(asf(d[1]) * e, v3 * e), L = x32_add(l * e);
      y0 = tr16(y0, y1); L = x16_add(L);
      const bool s_ = lane & 8;
      y0 = (s_ ? L : y0) + dpp<ROR8>(s_ ? y0 : L);
      if (!s_) s_o[w][(lane & 7) * 4 + (lane >> 5) * 2 + ((lane >> 4) & 1)] = y0;
      if (lane == 8) *reinterpret_cast<f32x2*>(&s_ml2[w * 2]) = (f32x2){M, y0};
    } else if (w == 7) {
      float hv[4];
#pragma unroll
      for (int j = 0; j < 4; ++j) {
        float g = asf(d[j] & ~1u);
        g += dpp<ROR8>(g); g = rbf(x16_add(g));
        const float u = dpp<ROR4>(g);
        hv[j] = rbf(rbf(g / (1.0f + __expf(-g))) * u);
      }
      if (lane < 4) {
#pragma unroll
        for (int j = 0; j < 4; ++j) s_h[((lane & 1) * 4 + j) * 2 + (lane >> 1)] = hv[j];
      }
    }
    LDS_FENCE(); bar(); LDS_FENCE();
    STAMP(3);
    if (w < 7) {
      const f32x4 ma = *reinterpret_cast<const f32x4*>(&s_ml2[0]), mb = *reinterpret_cast<const f32x4*>(&s_ml2[4]);
      f32x4 pq[4];
#pragma unroll
      for (int k = 0; k < 4; ++k) pq[k] = *reinterpret_cast<const f32x4*>(&s_o[k][(lane >> 3) * 4]);
      const f32x2 hv = *reinterpret_cast<const f32x2*>(&s_h[(lane >> 3) * 2]);
      const float M = fmaxf(fmaxf(ma[0], ma[2]), fmaxf(mb[0], mb[2]));
      const float e0 = __builtin_amdgcn_exp2f((ma[0] - M) * EXP_C), e1_ = __builtin_amdgcn_exp2f((ma[2] - M) * EXP_C);
      const float e2 = __builtin_amdgcn_exp2f((mb[0] - M) * EXP_C), e3 = __builtin_amdgcn_exp2f((mb[2] - M) * EXP_C);
      const float inv = __builtin_amdgcn_rcpf((ma[1] * e0 + ma[3] * e1_) + (mb[1] * e2 + mb[3] * e3));
      const f32x4 av = ((pq[0] * e0 + pq[1] * e1_) + (pq[2] * e2 + pq[3] * e3)) * inv;
      f32x2 acc[4];
#pragma unroll
      for (int j = 0; j < 4; ++j) acc[j] = wf[4][j] * hv.x + wf[5][j] * hv.y;
#pragma unroll
      for (int i = 0; i < 4; ++i) {
        const float a = rbf(av[i]);
#pragma unroll
        for (int j = 0; j < 4; ++j) acc[j] += wf[i][j] * a;
      }
      float z[4] = {tr32(acc[0].x, acc[2].x), tr32(acc[0].y, acc[2].y), tr32(acc[1].x, acc[3].x), tr32(acc[1].y, acc[3].y)};
#pragma unroll
      for (int j = 0; j < 4; ++j) { z[j] = x16_add(z[j]); z[j] += dpp<ROR8>(z[j]); }
      const unsigned ra = (unsigned)(w * 32 + ((lane >> 1) & 3) * 4 + ((-w) & 3)) * AREA;
      st4c(tag4((f32x4){z[0], z[1], z[2], z[3]}, dt), rws, (ra + A_Y2 + y * 64 + (lane & 1) * 32 + (lane >> 5) * 16) | ((lane & 24) ? OOB : 0u));
    }
  } else if (isr) {
    const float xo = asf((unsigned)xg[ro * 16 + (lane & 15)] << 16);
    v4u d;
#pragma unroll 1
    for (int it = 0; it < SPIN_CAP; ++it) {
      d = ld4<16>(rws, pa, 0);
      if (arrived(d, old)) break;
      __builtin_amdgcn_s_sleep(1);
      LDS_FENCE();
    }
    d &= ~1u;
    float y0 = tr32(asf(d[0]), asf(d[2])), y1 = tr32(asf(d[1]), asf(d[3]));
    y0 = tr16(y0, y1);
    y0 += dpp<ROR8>(y0); y0 += dpp<ROR4>(y0);
    if (!(lane & 12)) s_r[w][(lane & 3) * 4 + (lane >> 5) * 2 + ((lane >> 4) & 1)] = y0;
    LDS_FENCE(); bar(); LDS_FENCE();
    if (w == 0) {
      float a = s_r[lane >> 4][lane & 15] + s_r[4 + (lane >> 4)][lane & 15];
      a = x16_add(a); a = x32_add(a);
      if (lane < 16) out[ro * 16 + lane] = (u16)f2bf(xo + a);
    }
  }
  STAMP(4);
  STAMP_OUT();
}

extern "C" void kernel_launch(void* const* d_in, const int* in_sizes, int n_in,
                              void* d_out, int out_size, void* d_ws, size_t ws_size,
                              hipStream_t stream) {
  fused_kernel<<<NWG, NT, 0, stream>>>((const u16*)d_in[0], (const u16*)d_in[1], (const u16*)d_in[2], (const u16*)d_in[3],
                                       (const u16*)d_in[4], (const u16*)d_in[6], (const u16*)d_in[7], (const u16*)d_in[8],
                                       (u16*)d_out, (char*)d_ws);
}
```

```cpp
#define PADK 2
#include <hip/hip_runtime.h>
#include <hip/hip_bf16.h>
#include <stdint.h>

#define QPOS 24
#define PACE 8
#define QTRIES 8
#define TQ 125
#define TF 128
#define TFY 127
#define TY 131
#define NP 4
#define TAU 3650
#define PP 3000
#define PL 8
#define TOFF 150
#define TOFFQ 180
#define QBURST 100000
#define PBURST 0
#define KBURST 100000
#define VBURST 100000
#define QFIRST 24
#define OPOS 8
#define MRG 0
#define TWP 0
#define XW 0
#define WOFF 0
#define XA 19
#define XB 19
#define QROT 0
#define DIAG 0
#define LITE 0

typedef unsigned v4u __attribute__((ext_vector_type(4)));
typedef unsigned v2u __attribute__((ext_vector_type(2)));
typedef float f32x4 __attribute__((ext_vector_type(4)));
typedef float f32x2 __attribute__((ext_vector_type(2)));
typedef __bf16 bf16x2 __attribute__((ext_vector_type(2)));
typedef __bf16 bf16x8 __attribute__((ext_vector_type(8)));
typedef unsigned short u16;
typedef __amdgpu_buffer_rsrc_t rsrc_t;
typedef __attribute__((address_space(3))) f32x4* lds4_t;
typedef __attribute__((address_space(3))) v4u* ldsu_t;
typedef __attribute__((address_space(3))) float* lds1_t;

constexpr int D = 448, SK = 32768, DH = 256, F = 1792, NQ = 4096;
constexpr int NWG = 256, NT = 512;
constexpr unsigned OOB = 0x80000000u;
constexpr unsigned SENT = 0xFFFFFFFFu;
constexpr unsigned AREA = 9216;
constexpr unsigned A_O1 = 1024;
constexpr unsigned A_F = 5120;
constexpr unsigned A_Y2 = 1024;
constexpr unsigned WS_REQ = NWG * AREA;
constexpr unsigned WS_ECHO = WS_REQ + 1024;
constexpr unsigned WS_GO = WS_ECHO + 1024;
constexpr unsigned WS_Q = WS_GO + 256;
constexpr unsigned WS_END = WS_Q + 8 * 2048;
constexpr int SPIN_CAP = 1 << 18;
constexpr int C_NEXT = 0, C_TICK = 1, C_GO = 2, C_Q = 3, C_T0 = 4, C_FC = 5, C_OLD = 6, C_DONE = 7;

__device__ __forceinline__ float asf(unsigned u) { return __builtin_bit_cast(float, u); }
__device__ __forceinline__ unsigned asu(float f) { return __builtin_bit_cast(unsigned, f); }
__device__ __forceinline__ float rbf(float f) { __bf16 b = (__bf16)f; return (float)b; }
__device__ __forceinline__ unsigned f2bf(float f) { __bf16 b = (__bf16)f; return (unsigned)__builtin_bit_cast(u16, b); }
__device__ __forceinline__ float dot2(unsigned k, unsigned q, float acc) {
  return __builtin_amdgcn_fdot2_f32_bf16(__builtin_bit_cast(bf16x2, k), __builtin_bit_cast(bf16x2, q), acc, false);
}
__device__ __forceinline__ f32x2 unp(unsigned u) { f32x2 r; r.x = asf(u << 16); r.y = asf(u & 0xffff0000u); return r; }
template <int CTRL> __device__ __forceinline__ float dpp(float x) {
  return asf((unsigned)__builtin_amdgcn_update_dpp(0, (int)asu(x), CTRL, 0xf, 0xf, true));
}
constexpr int XOR1 = 0xB1, XOR2 = 0x4E, XOR7 = 0x141, ROR8 = 0x128, ROR4 = 0x124, ROR2 = 0x122, BCAST = 0x150;
constexpr int QP_EVEN = 0xA0, QP_ODD = 0xF5;
__device__ __forceinline__ float x16_add(float x) { auto s = __builtin_amdgcn_permlane16_swap(asu(x), asu(x), false, false); return asf(s[0]) + asf(s[1]); }
__device__ __forceinline__ float x32_add(float x) { auto s = __builtin_amdgcn_permlane32_swap(asu(x), asu(x), false, false); return asf(s[0]) + asf(s[1]); }
__device__ __forceinline__ float x16_max(float x) { auto s = __builtin_amdgcn_permlane16_swap(asu(x), asu(x), false, false); return fmaxf(asf(s[0]), asf(s[1])); }
__device__ __forceinline__ float x32_max(float x) { auto s = __builtin_amdgcn_permlane32_swap(asu(x), asu(x), false, false); return fmaxf(asf(s[0]), asf(s[1])); }
__device__ __forceinline__ float tr32(float a, float b) { auto s = __builtin_amdgcn_permlane32_swap(asu(a), asu(b), false, false); return asf(s[0]) + asf(s[1]); }
__device__ __forceinline__ float tr16(float a, float b) { auto s = __builtin_amdgcn_permlane16_swap(asu(a), asu(b), false, false); return asf(s[0]) + asf(s[1]); }
__device__ __forceinline__ float wave_sum(float v) {
  v += dpp<XOR1>(v); v += dpp<XOR2>(v); v += dpp<XOR7>(v); v += dpp<ROR8>(v);
  v = x16_add(v); return x32_add(v);
}
__device__ __forceinline__ float wave_max(float v) {
  v = fmaxf(v, dpp<XOR1>(v)); v = fmaxf(v, dpp<XOR2>(v)); v = fmaxf(v, dpp<XOR7>(v)); v = fmaxf(v, dpp<ROR8>(v));
  v = x16_max(v); return x32_max(v);
}
__device__ __forceinline__ rsrc_t mkrs(const void* p, unsigned bytes) { return __builtin_amdgcn_make_buffer_rsrc((void*)p, 0, (int)bytes, 0x00020000); }
__device__ __forceinline__ rsrc_t selr(bool c, rsrc_t a, rsrc_t b) { return c ? a : b; }
__device__ __forceinline__ unsigned selu(bool c, unsigned a, unsigned b) { return c ? a : b; }
template <int AUX> __device__ __forceinline__ v4u ld4(rsrc_t r, unsigned vo, unsigned so) { return __builtin_amdgcn_raw_buffer_load_b128(r, (int)vo, (int)so, AUX); }
template <int AUX> __device__ __forceinline__ v2u ld2(rsrc_t r, unsigned vo, unsigned so) { return __builtin_amdgcn_raw_buffer_load_b64(r, (int)vo, (int)so, AUX); }
__device__ __forceinline__ void st4c(v4u v, rsrc_t r, unsigned vo) { __builtin_amdgcn_raw_buffer_store_b128(v, r, (int)vo, 0, 16); }
__device__ __forceinline__ void st1c(unsigned v, rsrc_t r, unsigned vo) { __builtin_amdgcn_raw_buffer_store_b32(v, r, (int)vo, 0, 16); }
template <unsigned M> __device__ __forceinline__ unsigned tag(unsigned u, unsigned t) { return (u & ~M) | (t & M); }
__device__ __forceinline__ v4u tag4(const f32x4& v, const v4u& t) { return (v4u){tag<1>(asu(v[0]), t[0]), tag<1>(asu(v[1]), t[1]), tag<1>(asu(v[2]), t[2]), tag<1>(asu(v[3]), t[3])}; }
__device__ __forceinline__ bool arrived(const v4u& v, const v4u& o) { return __builtin_amdgcn_ballot_w64(v[0] == o[0] || v[1] == o[1] || v[2] == o[2] || v[3] == o[3]) == 0; }
__device__ __forceinline__ unsigned lowb(const v4u& v) { return __builtin_amdgcn_perm(v[1], v[0], 0x0c0c0400u) | __builtin_amdgcn_perm(v[3], v[2], 0x04000c0cu); }
#define SB() __builtin_amdgcn_sched_barrier(0)
__device__ __forceinline__ void bar() { __builtin_amdgcn_s_waitcnt(0xC07F); __builtin_amdgcn_s_barrier(); }
#define WAITV(n) __builtin_amdgcn_s_waitcnt(((n) & 15) | (((n) >> 4) << 14) | 0x0F70)
template <int V> struct IC { static constexpr int value = V; constexpr operator int() const { return V; } };
template <int A, int B, class Fn> __device__ __forceinline__ void static_for(Fn&& f) { if constexpr (A < B) { f(IC<A>{}); static_for<A + 1, B>(f); } }
#define LDS_FENCE() asm volatile("" ::: "memory")

constexpr float EXP_C = 0.0625f * 1.4426950408889634f;

struct Att {
  float m, l;
  f32x4 a[2];
};
__device__ __forceinline__ f32x4 mfma(const v4u& a, const v4u& b, const f32x4& c) {
  return __builtin_amdgcn_mfma_f32_16x16x32_bf16(__builtin_bit_cast(bf16x8, a), __builtin_bit_cast(bf16x8, b), c, 0, 0, 0);
}
__device__ __forceinline__ float red_max(float v) { v = fmaxf(v, dpp<XOR1>(v)); v = fmaxf(v, dpp<XOR2>(v)); return x32_max(x16_max(v)); }

template <int NL, class ISSUE>
__device__ __forceinline__ float scores(v4u (&kb)[8], const v4u (&qb)[8], int lane, ISSUE issue) {
  f32x4 d = {0.f, 0.f, 0.f, 0.f};
#pragma unroll
  for (int i = 0; i < NL; ++i) {
    d = mfma(kb[i], qb[i], d);
    asm volatile("" : "+v"(d));
    issue(i);
    SB();
  }
  const bool odd = lane & 1;
  return rbf((odd ? d[3] : d[0]) + dpp<XOR1>(odd ? d[1] : d[2]));
}
template <class ISSUE>
__device__ __forceinline__ float phase_k4(v4u (&kb)[8], const v4u (&qb)[8], Att& st, int lane, ISSUE issue) {
  float s = scores<4>(kb, qb, lane, issue);
  s = (lane & 2) ? -__builtin_inff() : s;
  const float mn = fmaxf(st.m, red_max(s));
  const float al = __builtin_amdgcn_exp2f((st.m - mn) * EXP_C);
  const float p = __builtin_amdgcn_exp2f((s - mn) * EXP_C);
  st.m = mn; st.l = st.l * al + p;
  st.a[0] *= al; st.a[1] *= al;
  SB();
  return p;
}
template <int I, int O, class ISSUE>
__device__ __forceinline__ void pv_step(v4u (&vb)[8], unsigned pp, const v4u& pm, Att& st, ISSUE& issue) {
  const v4u pb = pm & (unsigned)__builtin_amdgcn_update_dpp(0, (int)pp, I * 0x55, 0xf, 0xf, true);
  st.a[0] = mfma(vb[O + 2 * I], pb, st.a[0]);
  asm volatile("" : "+v"(st.a[0]));
  issue(O + 2 * I);
  SB();
  st.a[1] = mfma(vb[O + 2 * I + 1], pb, st.a[1]);
  asm volatile("" : "+v"(st.a[1]));
  issue(O + 2 * I + 1);
  SB();
}
template <int NL, class ISSUE>
__device__ __forceinline__ void phase_v(v4u (&vb)[8], float p, const v4u& pm, Att& st, ISSUE issue) {
  constexpr int O = 8 - NL;
  const unsigned pp = __builtin_bit_cast(unsigned, (bf16x2){(__bf16)p, (__bf16)p});
  pv_step<0, O>(vb, pp, pm, st, issue); pv_step<1, O>(vb, pp, pm, st, issue);
  if constexpr (NL == 8) { pv_step<2, O>(vb, pp, pm, st, issue); pv_step<3, O>(vb, pp, pm, st, issue); }
}

#define STAMP(k) do { if ((k) == PADK) { SB(); tpad = __builtin_amdgcn_s_memrealtime(); SB(); } } while (0)
#define STAMP_OUT() do { while (__builtin_amdgcn_s_memrealtime() - tpad < 6000) __builtin_amdgcn_s_sleep(1); } while (0)

__device__ __forceinline__ f32x2 ln_stats(const v4u& xa, int lane) {
  f32x2 xv[4];
  float sm = 0.f;
#pragma unroll
  for (int j = 0; j < 4; ++j) { xv[j] = unp(xa[j]); sm += xv[j].x + xv[j].y; }
  const float mean = wave_sum(sm) * (1.0f / D);
  float sq = 0.f;
#pragma unroll
  for (int j = 0; j < 4; ++j) { const f32x2 d = xv[j] - mean; sq += d.x * d.x + d.y * d.y; }
  sq = lane < 56 ? sq : 0.f;
  const float var = rbf(wave_sum(sq) * (1.0f / D));
  const float sd = rbf(sqrtf(rbf(var + 1e-5f)));
  return (f32x2){rbf(mean), 1.0f / sd};
}
__device__ __forceinline__ float ln_apply(float x, f32x2 st) { return rbf(rbf(x - st.x) * st.y); }

__global__ void __launch_bounds__(NT, 2)
fused_kernel(const u16* __restrict__ xg, const u16* __restrict__ kc, const u16* __restrict__ vc, const u16* __restrict__ wq,
             const u16* __restrict__ wo, const u16* __restrict__ wg, const u16* __restrict__ wu, const u16* __restrict__ wd,
             u16* __restrict__ out, char* ws) {
  __shared__ __attribute__((aligned(16))) float s_part[8][512];
  __shared__ __attribute__((aligned(16))) float s_mv[8][64];
  __shared__ __attribute__((aligned(16))) unsigned s_q[256];
  __shared__ __attribute__((aligned(16))) unsigned s_qs[64];
  __shared__ __attribute__((aligned(16))) unsigned s_old[8][256];
  __shared__ __attribute__((aligned(16))) unsigned s_dst[8][256];
  __shared__ __attribute__((aligned(16))) float s_ml[2][16];
  __shared__ __attribute__((aligned(16))) float s_o[4][32];
  __shared__ __attribute__((aligned(16))) float s_ml2[8];
  __shared__ __attribute__((aligned(16))) float s_h[16];
  __shared__ __attribute__((aligned(16))) float s_r[8][16];
  __shared__ __attribute__((aligned(16))) float s_sc[64][32];
  __shared__ int s_ctl[64];
  const int tid = threadIdx.x, lane = tid & 63, w = __builtin_amdgcn_readfirstlane(tid >> 6);
  const int bi = blockIdx.x, h = (((bi & 1) ? bi + 5 : bi) & 6) | (bi >> 7), s = ((bi >> 3) & 15) * 2 + (bi & 1);
  const int b = h * 32 + s;
  static_assert(8 * TQ + 8 * TF + 6 * TFY + 10 * TY == 4096 && TQ - NP > 80 && TF - NP > 80 && TFY - NP > 80 && TY - NP > 80, "tickets");
  static_assert(TQ - NP <= 128 && TF - NP <= 128 && TFY - NP <= 128 && TY - NP <= 128, "tickets");
  const int nt = s < 8 ? TQ : s < 16 ? TF : s < 22 ? TFY : TY, nk = (nt - NP) >> 1;
  const int first = s * TY - min(s, 22) * (TY - TFY) - min(s, 16) * (TFY - TF) - min(s, 8) * (TF - TQ);
  unsigned long long tpad = 0;
  asm volatile("" :: "s"(xg), "s"(kc), "s"(vc), "s"(wq), "s"(wo), "s"(wg), "s"(wu), "s"(wd), "s"(out), "s"(ws));
  STAMP(0);
  const rsrc_t rx = mkrs(xg, D * 2);
  const rsrc_t rk = mkrs(kc + (size_t)h * SK * DH, SK * DH * 2);
  const rsrc_t rv = mkrs(vc + (size_t)h * SK * DH, SK * DH * 2);
  const rsrc_t rws = mkrs(ws + (size_t)WOFF * 1048576, WS_END);
  const unsigned l16 = lane * 16;
  auto ubase = [&](int u) -> unsigned { return (unsigned)(first + 2 * u) * 4096u; };
  const unsigned kvo = ((lane >> 1) & 7) * 512 + (lane >> 4) * 32 + (lane & 1) * 16, vvo = (lane >> 4) * 1024 + (lane & 15) * 16;
  auto ldk = [&](unsigned base, int i) { return ld4<2>(rk, kvo + (i & 3) * 128, base + (i >> 2) * 4096); };
  const unsigned area = (unsigned)b * AREA;
  auto area_of = [&](int ss) -> unsigned { return (unsigned)(h * 32 + ss) * AREA; };
  const unsigned lmask = lane < 56 ? 0u : OOB;

  const bool isq = s < 8, isf = s >= 8 && s < 22, isy = s >= 16;
  const int f = h * 14 + s - 8, y = h * 16 + s - 16, ro = h * 4 + (s >> 2);
  const int sq = (s + QROT) & 7;
  const bool isr = s < 16 && ((s + h) & 3) == 0 && ro < 28;
  const rsrc_t rwo = mkrs(wo + (size_t)(isy ? y : 0) * 32 * D, (isy && w < 7 && !XW) ? 32 * D * 2 : 0);
  const rsrc_t rwd = mkrs(wd + (size_t)((isy && y < 112) ? y : 0) * 16 * D, (isy && y < 112 && w < 7 && !XW) ? 16 * D * 2 : 0);
  const unsigned wt_off = (lane >> 3) * (D * 2) + w * 128 + (lane & 7) * 16;
  const bool tile = isq || (isf && w < 4);
  const bool leader = (b == NWG - 1 && w == 1), member = (w == 0);

  const v4u sent = {SENT, SENT, SENT, SENT};
  unsigned poll_off = lane == 1 ? WS_GO : OOB;
  poll_off = lane == 0 ? WS_REQ + (b >> 2) * 16 : poll_off;
  poll_off = leader ? WS_ECHO + l16 : poll_off;
  v4u pv = {0u, 0u, 0u, 0u}, e1 = {0u, 0u, 0u, 0u};
  unsigned go_ref = 0;
  int* const ctl = &s_ctl[lane];
  auto ctl_get = [&](int i) { return __builtin_amdgcn_readfirstlane(__hip_atomic_load(&s_ctl[i], __ATOMIC_RELAXED, __HIP_MEMORY_SCOPE_WORKGROUP)); };
  auto ctl_set = [&](int i) { __hip_atomic_store(&s_ctl[i], 1, __ATOMIC_RELAXED, __HIP_MEMORY_SCOPE_WORKGROUP); };
  const unsigned qoff = WS_Q + h * 2048 + l16;
  v4u qv0, qv1;
  if (member) {
    qv0 = ld4<16>(rws, qoff, 0);
    qv1 = ld4<16>(rws, qoff, 1024);
    SB();
    int c = lane == C_NEXT ? (isq ? TOFFQ : TOFF) * 128 : 0;
    c = lane == C_TICK ? 40 : c;
    c = lane == C_T0 ? (int)(__builtin_amdgcn_s_memrealtime() & 0x7fffffff) : c;
    __hip_atomic_store(ctl, c, __ATOMIC_RELAXED, __HIP_MEMORY_SCOPE_WORKGROUP);
  }
  *reinterpret_cast<v4u*>(&s_sc[w * 8][lane * 4]) = sent;
  int t0 = 0;
  auto now = [&]() { return ((int)__builtin_amdgcn_s_memrealtime() - t0) & 0x7fffff; };
  const int neg = lane == C_NEXT ? 0 : (int)0x80000000;
  int inc = lane == C_NEXT ? TAU : 0;
  auto token = [&](int burst) {
    const int t = max(now() * 128 - burst, 0);
    __hip_atomic_fetch_max(ctl, t | neg, __ATOMIC_RELAXED, __HIP_MEMORY_SCOPE_WORKGROUP);
    return __hip_atomic_fetch_add(ctl, inc, __ATOMIC_RELAXED, __HIP_MEMORY_SCOPE_WORKGROUP);
  };
  auto due_of = [&](int x) { return __builtin_amdgcn_readlane(x, C_NEXT) >> 7; };
  auto until = [&](int due) {
#pragma unroll 1
    for (int it = 0; it < 4096 && now() < due; ++it) __builtin_amdgcn_s_sleep(1);
    SB();
  };
  unsigned qs0, qs1;
  auto q_there = [&]() { return __builtin_amdgcn_ballot_w64(((lowb(qv0) ^ qs0) & (lowb(qv1) ^ qs1)) != 0xFFFFFFFFu) == 0; };
  auto load_q = [&]() { qv0 = ld4<16>(rws, qoff, 0); qv1 = ld4<16>(rws, qoff, 1024); };
  auto publish_q = [&]() {
    *reinterpret_cast<v2u*>(&s_q[lane * 2]) = (v2u){__builtin_amdgcn_perm(qv0[1], qv0[0], 0x07060302u), __builtin_amdgcn_perm(qv0[3], qv0[2], 0x07060302u)};
    *reinterpret_cast<v2u*>(&s_q[128 + lane * 2]) = (v2u){__builtin_amdgcn_perm(qv1[1], qv1[0], 0x07060302u), __builtin_amdgcn_perm(qv1[3], qv1[2], 0x07060302u)};
    LDS_FENCE();
    __builtin_amdgcn_s_waitcnt(0xC07F);
    ctl_set(C_Q);
  };
  v4u pre[4][8], ring[8];
  auto unit0 = [&]() {
#pragma unroll
    for (int i = 0; i < 8; ++i) { pre[0][i] = ldk(ubase(w), i); SB(); }
  };
  SB();
  if (tile) {
    v4u tw[8];
    const int oct = isq ? 7 * w : (f & 3) * 14 + (w >> 1) * 7;
    const int stride = isq ? NQ * 2 : F * 2;
    const rsrc_t rt = mkrs(isq ? wq : (w & 1) ? wu : wg, D * stride);
    const unsigned to = (unsigned)((oct + (lane >> 3)) * 8 * stride + (isq ? h * 8 + sq : f >> 2) * 128 + (lane & 7) * 16) | lmask;
    const v4u xa = ld4<0>(rx, l16, 0);
    const v4u xb = ld4<0>(rx, (unsigned)(oct + (lane >> 3)) * 16 | lmask, 0);
    SB();
#pragma unroll
    for (int i = 0; i < 8; ++i) { tw[i] = ld4<2>(rt, to, i * stride); SB(); }
    LDS_FENCE(); bar(); LDS_FENCE();
    t0 = ctl_get(C_T0);
    SB();
    const f32x2 ls = ln_stats(xa, lane);
    f32x2 acc[4];
#pragma unroll
    for (int i = 0; i < 8; ++i) {
      const float xn = ln_apply(asf((i & 1) ? (xb[i >> 1] & 0xffff0000u) : (xb[i >> 1] << 16)), ls);
#pragma unroll
      for (int j = 0; j < 4; ++j) acc[j] = i ? acc[j] + unp(tw[i][j]) * xn : unp(tw[i][j]) * xn;
    }
    float y0 = tr32(acc[0].x, acc[2].x), y1 = tr32(acc[0].y, acc[2].y), y2 = tr32(acc[1].x, acc[3].x), y3 = tr32(acc[1].y, acc[3].y);
    y0 = tr16(y0, y2); y1 = tr16(y1, y3);
    const bool s_ = lane & 8;
    y0 = (s_ ? y1 : y0) + dpp<ROR8>(s_ ? y0 : y1);
    s_mv[w][(lane & 7) * 8 + (lane >> 3)] = y0;
    STAMP(6);
    token(isq ? QBURST : PBURST);
    unit0();
    if (isq) {
      LDS_FENCE(); bar(); LDS_FENCE();
      if (member) {
        float a = 0.f;
#pragma unroll
        for (int k = 0; k < 8; ++k) a += s_mv[k][lane];
        s_qs[lane] = f2bf(a) << 16;
        LDS_FENCE();
        const v4u qs = *reinterpret_cast<const v4u*>(&s_qs[(lane & 15) * 4]) | (~((sq & 4) ? qv1 : qv0) & 0xffffu);
        st4c(qs, rws, (lane >> 4) == (sq & 3) ? qoff + (sq >> 2) * 1024 : OOB);
      }
    } else {
      LDS_FENCE();
      __builtin_amdgcn_s_waitcnt(0xC07F);
      __hip_atomic_fetch_add(ctl, lane == C_FC ? 1 : 0, __ATOMIC_RELAXED, __HIP_MEMORY_SCOPE_WORKGROUP);
    }
  } else if (leader) {
    e1 = ld4<16>(rws, poll_off, 0);
    go_ref = __builtin_amdgcn_raw_buffer_load_b32(rws, (int)WS_GO, 0, 16);
  }
  SB();
  STAMP(5);
  SB();
  const unsigned pa = isy ? (w < 4 ? area + A_O1 + w * 1024 + l16 : (w == 7 && y < 112) ? area + A_F + (lane & 31) * 16 : OOB) : isr ? area + A_Y2 + w * 1024 + l16 : OOB;
  const unsigned mo_ = area_of(16 + (lane & 8) + (lane >> 4)) + A_O1 + s * 128 + (lane & 7) * 16;
  const int fd = (f & ~3) + ((lane >> 3) & 3);
  const unsigned mf = ((unsigned)((fd >> 4) * 32 + 16 + (fd & 15)) * AREA + A_F + (f & 3) * 128 + (lane & 7) * 16) | (lane < 32 ? 0u : OOB);
  auto my = [&](int p) -> unsigned { return (unsigned)((p >> 4) * 32 + (p & 12) + ((-(p >> 4)) & 3)) * AREA + A_Y2 + y * 64 + (p & 3) * 16; };
  v4u eo, ed;
  bool ok = !member;
  static_for<0, 40>([&](auto N) {
    constexpr int n = N;
    if constexpr (n == 0) {
      if (!tile) unit0();
    }
    if constexpr (n == 8) {
      if (!tile) {
        LDS_FENCE(); bar(); LDS_FENCE();
        t0 = ctl_get(C_T0);
        if (leader) {
          e1 += 1u;
          go_ref += 1u;
          st4c(e1, rws, WS_REQ + l16);
        }
      }
    }
    if constexpr (n > 0 && n % 8 == 0) {
      const int x = token(isq ? QBURST : PBURST);
      if constexpr (n > QFIRST) {
        if (!ok) {
          ok = q_there();
          if (ok) publish_q();
        }
      }
      if constexpr (n == OPOS + 16) {
        *reinterpret_cast<v4u*>(&s_old[w][lane * 4]) = (int)pa < 0 ? sent : eo;
        *reinterpret_cast<v4u*>(&s_dst[w][lane * 4]) = ~ed;
        LDS_FENCE();
        __hip_atomic_fetch_add(ctl, lane == C_OLD ? 1 : 0, __ATOMIC_RELAXED, __HIP_MEMORY_SCOPE_WORKGROUP);
      }
      SB();
      until(due_of(x));
      if constexpr (n == 8) {
        if (member) { qs0 = lowb(qv0); qs1 = lowb(qv1); }
      }
      if constexpr (n >= QFIRST) {
        if (!ok) load_q();
      }
      if constexpr (n == OPOS) {
        const int p = (w - 2) * 64 + lane;
        unsigned da = (isy && p < 112) ? my(p) : OOB;
        da = (w & 6) == 2 ? da : (w & 6) == 4 ? mo_ + (w - 4) * 4 * AREA : (w == 6 && isf) ? mf : OOB;
        eo = ld4<XB>(rws, pa, 0);
        ed = ld4<XA>(rws, da, 0);
      }
      SB();
    }
    if constexpr (n == QPOS) {
      STAMP(9);
      pv = ld4<16>(rws, (member || leader) ? poll_off : OOB, 0);
      SB();
    }
    if constexpr (n >= 8) {
      if constexpr (n >= PACE) WAITV(PACE + ((n >= QPOS && n < QPOS + PACE) ? 1 : 0) + ((n >= OPOS && n < OPOS + PACE) ? 2 : 0));
      if constexpr (n < 32) pre[n >> 3][n & 7] = ldk(ubase((n & 56) + w), n & 7); else ring[n & 7] = ldk(ubase(32 + w), n & 7);
    }
    SB();
    if constexpr (n - PACE == 7 || n - PACE == 15 || n - PACE == 23 || n - PACE == 31) STAMP(16 + (n - PACE) / 8);
  });
  inc = lane == C_TICK ? 1 : inc;
  int tok = token(KBURST);
  STAMP(10);
  bool svc = member || leader;
  unsigned seenm = 0, last_echo = 0;
  bool echoed = false;
  auto service = [&]() {
    if (leader) {
      seenm |= (pv[0] == e1[0] ? 1u : 0u) | (pv[1] == e1[1] ? 2u : 0u) | (pv[2] == e1[2] ? 4u : 0u) | (pv[3] == e1[3] ? 8u : 0u);
      svc = __builtin_amdgcn_ballot_w64(seenm != 15u) != 0;
      st1c(go_ref, rws, (lane == 0 && !svc) ? WS_GO : OOB);
    } else {
      const unsigned req = (unsigned)__builtin_amdgcn_readfirstlane((int)((b & 2) ? ((b & 1) ? pv[3] : pv[2]) : ((b & 1) ? pv[1] : pv[0])));
      const unsigned go = (unsigned)__builtin_amdgcn_readlane((int)pv[0], 1);
      go_ref = echoed ? go_ref : go;
      svc = go == (unsigned)__builtin_amdgcn_readfirstlane((int)go_ref);
      const bool rdy = ctl_get(C_OLD) == 8;
      st1c(req, rws, (lane == 0 && svc && rdy && !(echoed && req == last_echo)) ? WS_ECHO + b * 4 : OOB);
      echoed |= rdy; last_echo = req;
      if (!svc) {
        if (isf) {
          while (ctl_get(C_FC) != 4) __builtin_amdgcn_s_sleep(1);
          LDS_FENCE();
          const int j = (lane >> 3) & 3, pc = lane & 7, m = pc >> 2, col = j * 16 + (pc & 3) * 4;
          const f32x4 v = *reinterpret_cast<const f32x4*>(&s_mv[m][col]) + *reinterpret_cast<const f32x4*>(&s_mv[m + 2][col]);
          st4c(tag4(v, *reinterpret_cast<const v4u*>(&s_dst[6][lane * 4])), rws, mf);
        }
      }
    }
    pv = ld4<16>(rws, svc ? poll_off : OOB, 0);
    SB();
  };

  {
    if (!ok) {
      ok = q_there();
      if (ok) publish_q();
    }
#pragma unroll 1
    for (int it = 0; it < SPIN_CAP && !ctl_get(C_Q); ++it) {
      if (svc && it >= QTRIES) { service(); asm volatile("" :: "v"(pv)); }
      if (!ok) {
        load_q();
        ok = q_there() | !svc;
        if (ok) publish_q();
      } else {
        __builtin_amdgcn_s_sleep(1);
      }
    }
  }
  LDS_FENCE();
  v4u qb[8];
#pragma unroll
  for (int i = 0; i < 4; ++i) {
    const v4u z = {0u, 0u, 0u, 0u}, q = *reinterpret_cast<const v4u*>(&s_q[(lane & 8) * 16 + i * 32 + (lane >> 4) * 8 + (lane & 1) * 4]);
    qb[i] = (lane & 2) ? z : q; qb[4 + i] = (lane & 2) ? q : z;
  }
  v4u pm;
#pragma unroll
  for (int j = 0; j < 4; ++j) pm[j] = ((lane >> 1) & 3) == j ? ((lane & 1) ? 0xffff0000u : 0xffffu) : 0u;
  STAMP(1);
  SB();

  Att st;
  int sx = 20;
  {
    float* const sc = &s_sc[0][(lane >> 3) * 4 + (lane & 3)];
    rsrc_t nra, nrb;
    unsigned nva, nvb, nba, nbb, nsa, nsb;
    int kind = 0, nu = 32 + w, cu;
    auto next = [&]() {
      cu = nu;
      const int t = __builtin_amdgcn_readlane(tok, C_TICK);
      const bool fx = t < 2 * nk, tw = t >= nt;
      kind = fx ? (t >= nk ? 1 : 0) : tw ? 3 : 2;
      nu = t >= nk ? t - nk : t;
      nba = (unsigned)(fx ? first + 2 * nu : tw ? (TWP ? 0x80000 : 0) : first + t) * 4096u;
      nbb = fx ? nba + 4096 : nba;
      nra = selr(tw, rwo, selr(kind == 1, rv, rk));
      nrb = selr(tw, rwd, selr(kind == 0, rk, rv));
      nva = selu(tw, wt_off, selu(kind == 1, vvo, kvo)); nvb = selu(tw, wt_off, selu(kind == 0, kvo, vvo));
      nsa = selu(tw, 8 * D * 2, selu(kind == 1, 256, 128)); nsb = selu(tw, 8 * D * 2, selu(kind == 0, 128, 256));
      until(tw ? 0 : due_of(tok));
      inc = (lane == C_NEXT && t + PL >= 2 * nk) ? PP : inc;
      SB();
    };
    auto ldn = [&](int i) { ring[i] = i < 4 ? ld4<2>(nra, nva, nba + i * nsa) : ld4<2>(nrb, nvb, nbb + (i - 4) * nsb); };
    float mx;
    if (svc) service();
    {
      const float s0 = scores<8>(pre[0], qb, lane, [&](int) {});
      const float s1 = scores<8>(pre[1], qb, lane, [&](int) {});
      const float s2 = scores<8>(pre[2], qb, lane, [&](int) {});
      const float s3 = scores<8>(pre[3], qb, lane, [&](int) {});
      sc[w * 32] = s0; sc[(8 + w) * 32] = s1; sc[(16 + w) * 32] = s2; sc[(24 + w) * 32] = s3;
      mx = fmaxf(fmaxf(s0, s1), fmaxf(s2, s3));
      SB();
      STAMP(11);
    }
#pragma unroll 1
    while (kind == 0) {
      next();
      const float sv = scores<8>(ring, qb, lane, ldn);
      sc[cu * 32] = sv;
      tok = token(KBURST);
      mx = fmaxf(mx, sv);
      STAMP(sx); ++sx;
      if (svc) service();
      SB();
    }
    STAMP(7);
    st.m = red_max(mx); st.l = 0.f;
    st.a[0] = st.a[1] = (f32x4){0.f, 0.f, 0.f, 0.f};
#pragma unroll 1
    while (kind == 1) {
      float sv;
      for (;;) {
        sv = __hip_atomic_load(&sc[nu * 32], __ATOMIC_RELAXED, __HIP_MEMORY_SCOPE_WORKGROUP);
        if (__builtin_amdgcn_ballot_w64(asu(sv) == SENT) == 0) break;
        __builtin_amdgcn_s_sleep(1);
      }
      if (__builtin_amdgcn_ballot_w64(sv > st.m) != 0) {
        const float m2 = fmaxf(st.m, red_max(sv));
        const float al = __builtin_amdgcn_exp2f((st.m - m2) * EXP_C);
        st.m = m2; st.l *= al;
        st.a[0] *= al; st.a[1] *= al;
      }
      const float p = __builtin_amdgcn_exp2f((sv - st.m) * EXP_C);
      st.l += p;
      SB();
      next();
      phase_v<8>(ring, p, pm, st, ldn);
      tok = token(VBURST);
      STAMP(sx); ++sx;
      if (svc) service();
      SB();
    }
    STAMP(8);
#pragma unroll 1
    while (kind == 2) {
      next();
      const float p = phase_k4(ring, qb, st, lane, ldn);
      phase_v<4>(ring, p, pm, st, ldn);
      tok = token(VBURST);
      STAMP(sx); ++sx;
    }
#pragma unroll 1
    for (int it = 0; it < SPIN_CAP && svc; ++it) {
      service();
      asm volatile("" :: "v"(pv));
    }
    LDS_FENCE();
  }
  STAMP(12);
  auto tailw = [&]() {
#pragma unroll
    for (int i = 0; i < 6; ++i) ring[i] = i < 4 ? ld4<2>(rwo, wt_off, i * 8 * D * 2) : ld4<2>(rwd, wt_off, (i - 4) * 8 * D * 2);
    SB();
  };
  if (TWP == 1) tailw();
  auto send_o = [&](const f32x4& o, float M, float L, const v4u& t, int hf) {
    v4u msg;
#pragma unroll
    for (int j = 0; j < 3; ++j) msg[j] = tag<0x100>(__builtin_amdgcn_perm(asu(o[j]), asu(o[3]), 0x07060501u + j), t[j]);
    msg[3] = tag<1>(asu((lane & 1) ? L : M), t[3]);
    __builtin_amdgcn_raw_buffer_store_b128(msg, rws, (int)mo_, hf * 4 * AREA, 16);
  };
  {
    const lds4_t pw = (lds4_t)&s_part[w][lane * 4], pr = (lds4_t)&s_part[0][lane * 4], pmr = (lds4_t)&s_ml[(lane >> 3) & 1][0];
    const lds1_t pml = (lds1_t)&s_ml[(lane >> 3) & 1][w];
    pw[0] = st.a[0]; pw[64] = st.a[1];
    float l = st.l;
    l += dpp<XOR1>(l); l += dpp<XOR2>(l); l = x32_add(x16_add(l));
    pml[0] = st.m; pml[8] = l;
    LDS_FENCE(); bar(); LDS_FENCE();
    STAMP(2);
    if (w == 4 || w == 5) {
      f32x4 ml[4], pt[8];
#pragma unroll
      for (int k = 0; k < 4; ++k) ml[k] = pmr[k];
#pragma unroll
      for (int k = 0; k < 8; ++k) pt[k] = pr[k * 128 + (w - 4) * 64];
      const v4u dt = *reinterpret_cast<const v4u*>(&s_dst[w][lane * 4]);
      float M = -3.0e38f;
#pragma unroll
      for (int k = 0; k < 8; ++k) M = fmaxf(M, ml[k >> 2][k & 3]);
      f32x4 e[2] = {(ml[0] - M) * EXP_C, (ml[1] - M) * EXP_C};
      f32x4 o = {0.f, 0.f, 0.f, 0.f};
#pragma unroll
      for (int k = 0; k < 8; ++k) {
        e[k >> 2][k & 3] = __builtin_amdgcn_exp2f(e[k >> 2][k & 3]);
        o += pt[k] * e[k >> 2][k & 3];
      }
      const f32x4 l4 = ml[2] * e[0] + ml[3] * e[1];
      const float L = (l4[0] + l4[2]) + (l4[1] + l4[3]);
      send_o(o, M, L, dt, w - 4);
      SB();
      asm volatile("" :: "v"(ring[0]), "v"(ring[1]), "v"(ring[2]), "v"(ring[3]), "v"(ring[4]), "v"(ring[5]), "v"(ring[6]), "v"(ring[7]), "v"(pv));
    }
  }
  const v4u old = *reinterpret_cast<const v4u*>(&s_old[w][lane * 4]);
  if (isy) {
    if (TWP == 2) tailw();
    v4u d = ld4<16>(rws, pa, 0);
    SB();
    f32x2 wf[6][4];
#pragma unroll
    for (int i = 0; i < 6; ++i)
#pragma unroll
      for (int j = 0; j < 4; ++j) {
        wf[i][j] = unp(ring[i][j]);
        asm volatile("" : "+v"(wf[i][j]));
      }
    SB();
    STAMP(14);
    const v4u dt = *reinterpret_cast<const v4u*>(&s_dst[2][(w * 16 + (lane & 7) * 2 + (lane >> 5)) * 4]);
#pragma unroll 1
    for (int it = 0; it < SPIN_CAP; ++it) {
      if (arrived(d, old)) break;
      __builtin_amdgcn_s_sleep(1);
      LDS_FENCE();
      d = ld4<16>(rws, pa, 0);
    }
    STAMP(15);
    if (w < 4) {
      d &= (v4u){~0x100u, ~0x100u, ~0x100u, ~1u};
      const float ot = dpp<XOR1>(asf(d[3]));
      const float m = (lane & 1) ? ot : asf(d[3]), l = (lane & 1) ? asf(d[3]) : ot;
      float M = fmaxf(m, dpp<ROR8>(m)); M = x16_max(M); M = x32_max(M);
      const float e = __builtin_amdgcn_exp2f((m - M) * EXP_C);
      const float v3 = asf(__builtin_amdgcn_perm(__builtin_amdgcn_perm(d[2], d[1], 0x04000c0cu), d[0], 0x0706000cu));
      float y0 = tr32(asf(d[0]) * e, asf(d[2]) * e), y1 = tr32(asf(d[1]) * e, v3 * e), L = x32_add(l * e);
      y0 = tr16(y0, y1); L = x16_add(L);
      const bool s_ = lane & 8;
      y0 = (s_ ? L : y0) + dpp<ROR8>(s_ ? y0 : L);
      if (!s_) s_o[w][(lane & 7) * 4 + (lane >> 5) * 2 + ((lane >> 4) & 1)] = y0;
      if (lane == 8) *reinterpret_cast<f32x2*>(&s_ml2[w * 2]) = (f32x2){M, y0};
    } else if (w == 7) {
      float hv[4];
#pragma unroll
      for (int j = 0; j < 4; ++j) {
        float g = asf(d[j] & ~1u);
        g += dpp<ROR8>(g); g = rbf(x16_add(g));
        const float u = dpp<ROR4>(g);
        hv[j] = rbf(rbf(g / (1.0f + __expf(-g))) * u);
      }
      if (lane < 4) {
#pragma unroll
        for (int j = 0; j < 4; ++j) s_h[((lane & 1) * 4 + j) * 2 + (lane >> 1)] = hv[j];
      }
    }
    LDS_FENCE(); bar(); LDS_FENCE();
    STAMP(3);
    if (w < 7) {
      const f32x4 ma = *reinterpret_cast<const f32x4*>(&s_ml2[0]), mb = *reinterpret_cast<const f32x4*>(&s_ml2[4]);
      f32x4 pq[4];
#pragma unroll
      for (int k = 0; k < 4; ++k) pq[k] = *reinterpret_cast<const f32x4*>(&s_o[k][(lane >> 3) * 4]);
      const f32x2 hv = *reinterpret_cast<const f32x2*>(&s_h[(lane >> 3) * 2]);
      const float M = fmaxf(fmaxf(ma[0], ma[2]), fmaxf(mb[0], mb[2]));
      const float e0 = __builtin_amdgcn_exp2f((ma[0] - M) * EXP_C), e1_ = __builtin_amdgcn_exp2f((ma[2] - M) * EXP_C);
      const float e2 = __builtin_amdgcn_exp2f((mb[0] - M) * EXP_C), e3 = __builtin_amdgcn_exp2f((mb[2] - M) * EXP_C);
      const float inv = __builtin_amdgcn_rcpf((ma[1] * e0 + ma[3] * e1_) + (mb[1] * e2 + mb[3] * e3));
      const f32x4 av = ((pq[0] * e0 + pq[1] * e1_) + (pq[2] * e2 + pq[3] * e3)) * inv;
      f32x2 acc[4];
#pragma unroll
      for (int j = 0; j < 4; ++j) acc[j] = wf[4][j] * hv.x + wf[5][j] * hv.y;
#pragma unroll
      for (int i = 0; i < 4; ++i) {
        const float a = rbf(av[i]);
#pragma unroll
        for (int j = 0; j < 4; ++j) acc[j] += wf[i][j] * a;
      }
      float z[4] = {tr32(acc[0].x, acc[2].x), tr32(acc[0].y, acc[2].y), tr32(acc[1].x, acc[3].x), tr32(acc[1].y, acc[3].y)};
#pragma unroll
      for (int j = 0; j < 4; ++j) { z[j] = x16_add(z[j]); z[j] += dpp<ROR8>(z[j]); }
      const unsigned ra = (unsigned)(w * 32 + ((lane >> 1) & 3) * 4 + ((-w) & 3)) * AREA;
      st4c(tag4((f32x4){z[0], z[1], z[2], z[3]}, dt), rws, (ra + A_Y2 + y * 64 + (lane & 1) * 32 + (lane >> 5) * 16) | ((lane & 24) ? OOB : 0u));
    }
  } else if (isr) {
    const float xo = asf((unsigned)xg[ro * 16 + (lane & 15)] << 16);
    v4u d;
#pragma unroll 1
    for (int it = 0; it < SPIN_CAP; ++it) {
      d = ld4<16>(rws, pa, 0);
      if (arrived(d, old)) break;
      __builtin_amdgcn_s_sleep(1);
      LDS_FENCE();
    }
    d &= ~1u;
    float y0 = tr32(asf(d[0]), asf(d[2])), y1 = tr32(asf(d[1]), asf(d[3]));
    y0 = tr16(y0, y1);
    y0 += dpp<ROR8>(y0); y0 += dpp<ROR4>(y0);
    if (!(lane & 12)) s_r[w][(lane & 3) * 4 + (lane >> 5) * 2 + ((lane >> 4) & 1)] = y0;
    LDS_FENCE(); bar(); LDS_FENCE();
    if (w == 0) {
      float a = s_r[lane >> 4][lane & 15] + s_r[4 + (lane >> 4)][lane & 15];
      a = x16_add(a); a = x32_add(a);
      if (lane < 16) out[ro * 16 + lane] = (u16)f2bf(xo + a);
    }
  }
  STAMP(4);
  STAMP_OUT();
}

extern "C" void kernel_launch(void* const* d_in, const int* in_sizes, int n_in,
                              void* d_out, int out_size, void* d_ws, size_t ws_size,
                              hipStream_t stream) {
  fused_kernel<<<NWG, NT, 0, stream>>>((const u16*)d_in[0], (const u16*)d_in[1], (const u16*)d_in[2], (const u16*)d_in[3],
                                       (const u16*)d_in[4], (const u16*)d_in[6], (const u16*)d_in[7], (const u16*)d_in[8],
                                       (u16*)d_out, (char*)d_ws);
}
```

```cpp
#include <hip/hip_runtime.h>
#include <hip/hip_bf16.h>
#include <stdint.h>

#define QPOS 24
#define PACE 8
#define QTRIES 8
#define TQ 125
#define TF 128
#define TFY 127
#define TY 131
#define NP 4
#define TAU 3550
#define PP 3000
#define PL 8
#define TOFF 150
#define TOFFQ 180
#define QBURST 100000
#define PBURST 0
#define KBURST 100000
#define VBURST 100000
#define QFIRST 24
#define OPOS 8
#define MRG 0
#define TWP 0
#define XW 0
#define WOFF 0
#define XA 19
#define XB 19
#define QROT 0
#define DIAG 0
#define LITE 0

typedef unsigned v4u __attribute__((ext_vector_type(4)));
typedef unsigned v2u __attribute__((ext_vector_type(2)));
typedef float f32x4 __attribute__((ext_vector_type(4)));
typedef float f32x2 __attribute__((ext_vector_type(2)));
typedef __bf16 bf16x2 __attribute__((ext_vector_type(2)));
typedef __bf16 bf16x8 __attribute__((ext_vector_type(8)));
typedef unsigned short u16;
typedef __amdgpu_buffer_rsrc_t rsrc_t;
typedef __attribute__((address_space(3))) f32x4* lds4_t;
typedef __attribute__((address_space(3))) v4u* ldsu_t;
typedef __attribute__((address_space(3))) float* lds1_t;

constexpr int D = 448, SK = 32768, DH = 256, F = 1792, NQ = 4096;
constexpr int NWG = 256, NT = 512;
constexpr unsigned OOB = 0x80000000u;
constexpr unsigned SENT = 0xFFFFFFFFu;
constexpr unsigned AREA = 9216;
constexpr unsigned A_O1 = 1024;
constexpr unsigned A_F = 5120;
constexpr unsigned A_Y2 = 1024;
constexpr unsigned WS_REQ = NWG * AREA;
constexpr unsigned WS_ECHO = WS_REQ + 1024;
constexpr unsigned WS_GO = WS_ECHO + 1024;
constexpr unsigned WS_Q = WS_GO + 256;
constexpr unsigned WS_END = WS_Q + 8 * 2048;
constexpr int SPIN_CAP = 1 << 18;
constexpr int C_NEXT = 0, C_TICK = 1, C_GO = 2, C_Q = 3, C_T0 = 4, C_FC = 5, C_OLD = 6, C_DONE = 7;

__device__ __forceinline__ float asf(unsigned u) { return __builtin_bit_cast(float, u); }
__device__ __forceinline__ unsigned asu(float f) { return __builtin_bit_cast(unsigned, f); }
__device__ __forceinline__ float rbf(float f) { __bf16 b = (__bf16)f; return (float)b; }
__device__ __forceinline__ unsigned f2bf(float f) { __bf16 b = (__bf16)f; return (unsigned)__builtin_bit_cast(u16, b); }
__device__ __forceinline__ float dot2(unsigned k, unsigned q, float acc) {
  return __builtin_amdgcn_fdot2_f32_bf16(__builtin_bit_cast(bf16x2, k), __builtin_bit_cast(bf16x2, q), acc, false);
}
__device__ __forceinline__ f32x2 unp(unsigned u) { f32x2 r; r.x = asf(u << 16); r.y = asf(u & 0xffff0000u); return r; }
template <int CTRL> __device__ __forceinline__ float dpp(float x) {
  return asf((unsigned)__builtin_amdgcn_update_dpp(0, (int)asu(x), CTRL, 0xf, 0xf, true));
}
constexpr int XOR1 = 0xB1, XOR2 = 0x4E, XOR7 = 0x141, ROR8 = 0x128, ROR4 = 0x124, ROR2 = 0x122, BCAST = 0x150;
constexpr int QP_EVEN = 0xA0, QP_ODD = 0xF5;
__device__ __forceinline__ float x16_add(float x) { auto s = __builtin_amdgcn_permlane16_swap(asu(x), asu(x), false, false); return asf(s[0]) + asf(s[1]); }
__device__ __forceinline__ float x32_add(float x) { auto s = __builtin_amdgcn_permlane32_swap(asu(x), asu(x), false, false); return asf(s[0]) + asf(s[1]); }
__device__ __forceinline__ float x16_max(float x) { auto s = __builtin_amdgcn_permlane16_swap(asu(x), asu(x), false, false); return fmaxf(asf(s[0]), asf(s[1])); }
__device__ __forceinline__ float x32_max(float x) { auto s = __builtin_amdgcn_permlane32_swap(asu(x), asu(x), false, false); return fmaxf(asf(s[0]), asf(s[1])); }
__device__ __forceinline__ float tr32(float a, float b) { auto s = __builtin_amdgcn_permlane32_swap(asu(a), asu(b), false, false); return asf(s[0]) + asf(s[1]); }
__device__ __forceinline__ float tr16(float a, float b) { auto s = __builtin_amdgcn_permlane16_swap(asu(a), asu(b), false, false); return asf(s[0]) + asf(s[1]); }
__device__ __forceinline__ float wave_sum(float v) {
  v += dpp<XOR1>(v); v += dpp<XOR2>(v); v += dpp<XOR7>(v); v += dpp<ROR8>(v);
  v = x16_add(v); return x32_add(v);
}
__device__ __forceinline__ float wave_max(float v) {
  v = fmaxf(v, dpp<XOR1>(v)); v = fmaxf(v, dpp<XOR2>(v)); v = fmaxf(v, dpp<XOR7>(v)); v = fmaxf(v, dpp<ROR8>(v));
  v = x16_max(v); return x32_max(v);
}
__device__ __forceinline__ rsrc_t mkrs(const void* p, unsigned bytes) { return __builtin_amdgcn_make_buffer_rsrc((void*)p, 0, (int)bytes, 0x00020000); }
__device__ __forceinline__ rsrc_t selr(bool c, rsrc_t a, rsrc_t b) { return c ? a : b; }
__device__ __forceinline__ unsigned selu(bool c, unsigned a, unsigned b) { return c ? a : b; }
template <int AUX> __device__ __forceinline__ v4u ld4(rsrc_t r, unsigned vo, unsigned so) { return __builtin_amdgcn_raw_buffer_load_b128(r, (int)vo, (int)so, AUX); }
template <int AUX> __device__ __forceinline__ v2u ld2(rsrc_t r, unsigned vo, unsigned so) { return __builtin_amdgcn_raw_buffer_load_b64(r, (int)vo, (int)so, AUX); }
__device__ __forceinline__ void st4c(v4u v, rsrc_t r, unsigned vo) { __builtin_amdgcn_raw_buffer_store_b128(v, r, (int)vo, 0, 16); }
__device__ __forceinline__ void st1c(unsigned v, rsrc_t r, unsigned vo) { __builtin_amdgcn_raw_buffer_store_b32(v, r, (int)vo, 0, 16); }
template <unsigned M> __device__ __forceinline__ unsigned tag(unsigned u, unsigned t) { return (u & ~M) | (t & M); }
__device__ __forceinline__ v4u tag4(const f32x4& v, const v4u& t) { return (v4u){tag<1>(asu(v[0]), t[0]), tag<1>(asu(v[1]), t[1]), tag<1>(asu(v[2]), t[2]), tag<1>(asu(v[3]), t[3])}; }
__device__ __forceinline__ bool arrived(const v4u& v, const v4u& o) { return __builtin_amdgcn_ballot_w64(v[0] == o[0] || v[1] == o[1] || v[2] == o[2] || v[3] == o[3]) == 0; }
__device__ __forceinline__ unsigned lowb(const v4u& v) { return __builtin_amdgcn_perm(v[1], v[0], 0x0c0c0400u) | __builtin_amdgcn_perm(v[3], v[2], 0x04000c0cu); }
#define SB() __builtin_amdgcn_sched_barrier(0)
__device__ __forceinline__ void bar() { __builtin_amdgcn_s_waitcnt(0xC07F); __builtin_amdgcn_s_barrier(); }
#define WAITV(n) __builtin_amdgcn_s_waitcnt(((n) & 15) | (((n) >> 4) << 14) | 0x0F70)
template <int V> struct IC { static constexpr int value = V; constexpr operator int() const { return V; } };
template <int A, int B, class Fn> __device__ __forceinline__ void static_for(Fn&& f) { if constexpr (A < B) { f(IC<A>{}); static_for<A + 1, B>(f); } }
#define LDS_FENCE() asm volatile("" ::: "memory")

constexpr float EXP_C = 0.0625f * 1.4426950408889634f;

struct Att {
  float m, l;
  f32x4 a[2];
};
__device__ __forceinline__ f32x4 mfma(const v4u& a, const v4u& b, const f32x4& c) {
  return __builtin_amdgcn_mfma_f32_16x16x32_bf16(__builtin_bit_cast(bf16x8, a), __builtin_bit_cast(bf16x8, b), c, 0, 0, 0);
}
__device__ __forceinline__ float red_max(float v) { v = fmaxf(v, dpp<XOR1>(v)); v = fmaxf(v, dpp<XOR2>(v)); return x32_max(x16_max(v)); }

template <int NL, class ISSUE>
__device__ __forceinline__ float scores(v4u (&kb)[8], const v4u (&qb)[8], int lane, ISSUE issue) {
  f32x4 d = {0.f, 0.f, 0.f, 0.f};
#pragma unroll
  for (int i = 0; i < NL; ++i) {
    d = mfma(kb[i], qb[i], d);
    asm volatile("" : "+v"(d));
    issue(i);
    SB();
  }
  const bool odd = lane & 1;
  return rbf((odd ? d[3] : d[0]) + dpp<XOR1>(odd ? d[1] : d[2]));
}
template <class ISSUE>
__device__ __forceinline__ float phase_k4(v4u (&kb)[8], const v4u (&qb)[8], Att& st, int lane, ISSUE issue) {
  float s = scores<4>(kb, qb, lane, issue);
  s = (lane & 2) ? -__builtin_inff() : s;
  const float mn = fmaxf(st.m, red_max(s));
  const float al = __builtin_amdgcn_exp2f((st.m - mn) * EXP_C);
  const float p = __builtin_amdgcn_exp2f((s - mn) * EXP_C);
  st.m = mn; st.l = st.l * al + p;
  st.a[0] *= al; st.a[1] *= al;
  SB();
  return p;
}
template <int I, int O, class ISSUE>
__device__ __forceinline__ void pv_step(v4u (&vb)[8], unsigned pp, const v4u& pm, Att& st, ISSUE& issue) {
  const v4u pb = pm & (unsigned)__builtin_amdgcn_update_dpp(0, (int)pp, I * 0x55, 0xf, 0xf, true);
  st.a[0] = mfma(vb[O + 2 * I], pb, st.a[0]);
  asm volatile("" : "+v"(st.a[0]));
  issue(O + 2 * I);
  SB();
  st.a[1] = mfma(vb[O + 2 * I + 1], pb, st.a[1]);
  asm volatile("" : "+v"(st.a[1]));
  issue(O + 2 * I + 1);
  SB();
}
template <int NL, class ISSUE>
__device__ __forceinline__ void phase_v(v4u (&vb)[8], float p, const v4u& pm, Att& st, ISSUE issue) {
  constexpr int O = 8 - NL;
  const unsigned pp = __builtin_bit_cast(unsigned, (bf16x2){(__bf16)p, (__bf16)p});
  pv_step<0, O>(vb, pp, pm, st, issue); pv_step<1, O>(vb, pp, pm, st, issue);
  if constexpr (NL == 8) { pv_step<2, O>(vb, pp, pm, st, issue); pv_step<3, O>(vb, pp, pm, st, issue); }
}

#define STAMP(k) do {} while (0)
#define STAMP_OUT() do {} while (0)

__device__ __forceinline__ f32x2 ln_stats(const v4u& xa, int lane) {
  f32x2 xv[4];
  float sm = 0.f;
#pragma unroll
  for (int j = 0; j < 4; ++j) { xv[j] = unp(xa[j]); sm += xv[j].x + xv[j].y; }
  const float mean = wave_sum(sm) * (1.0f / D);
  float sq = 0.f;
#pragma unroll
  for (int j = 0; j < 4; ++j) { const f32x2 d = xv[j] - mean; sq += d.x * d.x + d.y * d.y; }
  sq = lane < 56 ? sq : 0.f;
  const float var = rbf(wave_sum(sq) * (1.0f / D));
  const float sd = rbf(sqrtf(rbf(var + 1e-5f)));
  return (f32x2){rbf(mean), 1.0f / sd};
}
__device__ __forceinline__ float ln_apply(float x, f32x2 st) { return rbf(rbf(x - st.x) * st.y); }

__global__ void __launch_bounds__(NT, 2)
fused_kernel(const u16* __restrict__ xg, const u16* __restrict__ kc, const u16* __restrict__ vc, const u16* __restrict__ wq,
             const u16* __restrict__ wo, const u16* __restrict__ wg, const u16* __restrict__ wu, const u16* __restrict__ wd,
             u16* __restrict__ out, char* ws) {
  __shared__ __attribute__((aligned(16))) float s_part[8][512];
  __shared__ __attribute__((aligned(16))) float s_mv[8][64];
  __shared__ __attribute__((aligned(16))) unsigned s_q[256];
  __shared__ __attribute__((aligned(16))) unsigned s_qs[64];
  __shared__ __attribute__((aligned(16))) unsigned s_old[8][256];
  __shared__ __attribute__((aligned(16))) unsigned s_dst[8][256];
  __shared__ __attribute__((aligned(16))) float s_ml[2][16];
  __shared__ __attribute__((aligned(16))) float s_o[4][32];
  __shared__ __attribute__((aligned(16))) float s_ml2[8];
  __shared__ __attribute__((aligned(16))) float s_h[16];
  __shared__ __attribute__((aligned(16))) float s_r[8][16];
  __shared__ __attribute__((aligned(16))) float s_sc[64][32];
  __shared__ int s_ctl[64];
  const int tid = threadIdx.x, lane = tid & 63, w = __builtin_amdgcn_readfirstlane(tid >> 6);
  const int bi = blockIdx.x, h = (((bi & 1) ? bi + 5 : bi) & 6) | (bi >> 7), s = ((bi >> 3) & 15) * 2 + (bi & 1);
  const int b = h * 32 + s;
  static_assert(8 * TQ + 8 * TF + 6 * TFY + 10 * TY == 4096 && TQ - NP > 80 && TF - NP > 80 && TFY - NP > 80 && TY - NP > 80, "tickets");
  static_assert(TQ - NP <= 128 && TF - NP <= 128 && TFY - NP <= 128 && TY - NP <= 128, "tickets");
  const int nt = s < 8 ? TQ : s < 16 ? TF : s < 22 ? TFY : TY, nk = (nt - NP) >> 1;
  const int first = s * TY - min(s, 22) * (TY - TFY) - min(s, 16) * (TFY - TF) - min(s, 8) * (TF - TQ);
  asm volatile("" :: "s"(xg), "s"(kc), "s"(vc), "s"(wq), "s"(wo), "s"(wg), "s"(wu), "s"(wd), "s"(out), "s"(ws));
  STAMP(0);
  const rsrc_t rx = mkrs(xg, D * 2);
  const rsrc_t rk = mkrs(kc + (size_t)h * SK * DH, SK * DH * 2);
  const rsrc_t rv = mkrs(vc + (size_t)h * SK * DH, SK * DH * 2);
  const rsrc_t rws = mkrs(ws + (size_t)WOFF * 1048576, WS_END);
  const unsigned l16 = lane * 16;
  auto ubase = [&](int u) -> unsigned { return (unsigned)(first + 2 * u) * 4096u; };
  const unsigned kvo = ((lane >> 1) & 7) * 512 + (lane >> 4) * 32 + (lane & 1) * 16, vvo = (lane >> 4) * 1024 + (lane & 15) * 16;
  auto ldk = [&](unsigned base, int i) { return ld4<2>(rk, kvo + (i & 3) * 128, base + (i >> 2) * 4096); };
  const unsigned area = (unsigned)b * AREA;
  auto area_of = [&](int ss) -> unsigned { return (unsigned)(h * 32 + ss) * AREA; };
  const unsigned lmask = lane < 56 ? 0u : OOB;

  const bool isq = s < 8, isf = s >= 8 && s < 22, isy = s >= 16;
  const int f = h * 14 + s - 8, y = h * 16 + s - 16, ro = h * 4 + (s >> 2);
  const int sq = (s + QROT) & 7;
  const bool isr = s < 16 && ((s + h) & 3) == 0 && ro < 28;
  const rsrc_t rwo = mkrs(wo + (size_t)(isy ? y : 0) * 32 * D, (isy && w < 7 && !XW) ? 32 * D * 2 : 0);
  const rsrc_t rwd = mkrs(wd + (size_t)((isy && y < 112) ? y : 0) * 16 * D, (isy && y < 112 && w < 7 && !XW) ? 16 * D * 2 : 0);
  const unsigned wt_off = (lane >> 3) * (D * 2) + w * 128 + (lane & 7) * 16;
  const bool tile = isq || (isf && w < 4);
  const bool leader = (b == NWG - 1 && w == 1), member = (w == 0);

  const v4u sent = {SENT, SENT, SENT, SENT};
  unsigned poll_off = lane == 1 ? WS_GO : OOB;
  poll_off = lane == 0 ? WS_REQ + (b >> 2) * 16 : poll_off;
  poll_off = leader ? WS_ECHO + l16 : poll_off;
  v4u pv = {0u, 0u, 0u, 0u}, e1 = {0u, 0u, 0u, 0u};
  unsigned go_ref = 0;
  int* const ctl = &s_ctl[lane];
  auto ctl_get = [&](int i) { return __builtin_amdgcn_readfirstlane(__hip_atomic_load(&s_ctl[i], __ATOMIC_RELAXED, __HIP_MEMORY_SCOPE_WORKGROUP)); };
  auto ctl_set = [&](int i) { __hip_atomic_store(&s_ctl[i], 1, __ATOMIC_RELAXED, __HIP_MEMORY_SCOPE_WORKGROUP); };
  const unsigned qoff = WS_Q + h * 2048 + l16;
  v4u qv0, qv1;
  if (member) {
    qv0 = ld4<16>(rws, qoff, 0);
    qv1 = ld4<16>(rws, qoff, 1024);
    SB();
    int c = lane == C_NEXT ? (isq ? TOFFQ : TOFF) * 128 : 0;
    c = lane == C_TICK ? 40 : c;
    c = lane == C_T0 ? (int)(__builtin_amdgcn_s_memrealtime() & 0x7fffffff) : c;
    __hip_atomic_store(ctl, c, __ATOMIC_RELAXED, __HIP_MEMORY_SCOPE_WORKGROUP);
  }
  *reinterpret_cast<v4u*>(&s_sc[w * 8][lane * 4]) = sent;
  int t0 = 0;
  auto now = [&]() { return ((int)__builtin_amdgcn_s_memrealtime() - t0) & 0x7fffff; };
  const int neg = lane == C_NEXT ? 0 : (int)0x80000000;
  int inc = lane == C_NEXT ? TAU : 0;
  auto token = [&](int burst) {
    const int t = max(now() * 128 - burst, 0);
    __hip_atomic_fetch_max(ctl, t | neg, __ATOMIC_RELAXED, __HIP_MEMORY_SCOPE_WORKGROUP);
    return __hip_atomic_fetch_add(ctl, inc, __ATOMIC_RELAXED, __HIP_MEMORY_SCOPE_WORKGROUP);
  };
  auto due_of = [&](int x) { return __builtin_amdgcn_readlane(x, C_NEXT) >> 7; };
  auto until = [&](int due) {
#pragma unroll 1
    for (int it = 0; it < 4096 && now() < due; ++it) __builtin_amdgcn_s_sleep(1);
    SB();
  };
  unsigned qs0, qs1;
  auto q_there = [&]() { return __builtin_amdgcn_ballot_w64(((lowb(qv0) ^ qs0) & (lowb(qv1) ^ qs1)) != 0xFFFFFFFFu) == 0; };
  auto load_q = [&]() { qv0 = ld4<16>(rws, qoff, 0); qv1 = ld4<16>(rws, qoff, 1024); };
  auto publish_q = [&]() {
    *reinterpret_cast<v2u*>(&s_q[lane * 2]) = (v2u){__builtin_amdgcn_perm(qv0[1], qv0[0], 0x07060302u), __builtin_amdgcn_perm(qv0[3], qv0[2], 0x07060302u)};
    *reinterpret_cast<v2u*>(&s_q[128 + lane * 2]) = (v2u){__builtin_amdgcn_perm(qv1[1], qv1[0], 0x07060302u), __builtin_amdgcn_perm(qv1[3], qv1[2], 0x07060302u)};
    LDS_FENCE();
    __builtin_amdgcn_s_waitcnt(0xC07F);
    ctl_set(C_Q);
  };
  v4u pre[4][8], ring[8];
  auto unit0 = [&]() {
#pragma unroll
    for (int i = 0; i < 8; ++i) { pre[0][i] = ldk(ubase(w), i); SB(); }
  };
  SB();
  if (tile) {
    v4u tw[8];
    const int oct = isq ? 7 * w : (f & 3) * 14 + (w >> 1) * 7;
    const int stride = isq ? NQ * 2 : F * 2;
    const rsrc_t rt = mkrs(isq ? wq : (w & 1) ? wu : wg, D * stride);
    const unsigned to = (unsigned)((oct + (lane >> 3)) * 8 * stride + (isq ? h * 8 + sq : f >> 2) * 128 + (lane & 7) * 16) | lmask;
    const v4u xa = ld4<0>(rx, l16, 0);
    const v4u xb = ld4<0>(rx, (unsigned)(oct + (lane >> 3)) * 16 | lmask, 0);
    SB();
#pragma unroll
    for (int i = 0; i < 8; ++i) { tw[i] = ld4<2>(rt, to, i * stride); SB(); }
    LDS_FENCE(); bar(); LDS_FENCE();
    t0 = ctl_get(C_T0);
    SB();
    const f32x2 ls = ln_stats(xa, lane);
    f32x2 acc[4];
#pragma unroll
    for (int i = 0; i < 8; ++i) {
      const float xn = ln_apply(asf((i & 1) ? (xb[i >> 1] & 0xffff0000u) : (xb[i >> 1] << 16)), ls);
#pragma unroll
      for (int j = 0; j < 4; ++j) acc[j] = i ? acc[j] + unp(tw[i][j]) * xn : unp(tw[i][j]) * xn;
    }
    float y0 = tr32(acc[0].x, acc[2].x), y1 = tr32(acc[0].y, acc[2].y), y2 = tr32(acc[1].x, acc[3].x), y3 = tr32(acc[1].y, acc[3].y);
    y0 = tr16(y0, y2); y1 = tr16(y1, y3);
    const bool s_ = lane & 8;
    y0 = (s_ ? y1 : y0) + dpp<ROR8>(s_ ? y0 : y1);
    s_mv[w][(lane & 7) * 8 + (lane >> 3)] = y0;
    STAMP(6);
    token(isq ? QBURST : PBURST);
    unit0();
    if (isq) {
      LDS_FENCE(); bar(); LDS_FENCE();
      if (member) {
        float a = 0.f;
#pragma unroll
        for (int k = 0; k < 8; ++k) a += s_mv[k][lane];
        s_qs[lane] = f2bf(a) << 16;
        LDS_FENCE();
        const v4u qs = *reinterpret_cast<const v4u*>(&s_qs[(lane & 15) * 4]) | (~((sq & 4) ? qv1 : qv0) & 0xffffu);
        st4c(qs, rws, (lane >> 4) == (sq & 3) ? qoff + (sq >> 2) * 1024 : OOB);
      }
    } else {
      LDS_FENCE();
      __builtin_amdgcn_s_waitcnt(0xC07F);
      __hip_atomic_fetch_add(ctl, lane == C_FC ? 1 : 0, __ATOMIC_RELAXED, __HIP_MEMORY_SCOPE_WORKGROUP);
    }
  } else if (leader) {
    e1 = ld4<16>(rws, poll_off, 0);
    go_ref = __builtin_amdgcn_raw_buffer_load_b32(rws, (int)WS_GO, 0, 16);
  }
  SB();
  STAMP(5);
  SB();
  const unsigned pa = isy ? (w < 4 ? area + A_O1 + w * 1024 + l16 : (w == 7 && y < 112) ? area + A_F + (lane & 31) * 16 : OOB) : isr ? area + A_Y2 + w * 1024 + l16 : OOB;
  const unsigned mo_ = area_of(16 + (lane & 8) + (lane >> 4)) + A_O1 + s * 128 + (lane & 7) * 16;
  const int fd = (f & ~3) + ((lane >> 3) & 3);
  const unsigned mf = ((unsigned)((fd >> 4) * 32 + 16 + (fd & 15)) * AREA + A_F + (f & 3) * 128 + (lane & 7) * 16) | (lane < 32 ? 0u : OOB);
  auto my = [&](int p) -> unsigned { return (unsigned)((p >> 4) * 32 + (p & 12) + ((-(p >> 4)) & 3)) * AREA + A_Y2 + y * 64 + (p & 3) * 16; };
  v4u eo, ed;
  bool ok = !member;
  static_for<0, 40>([&](auto N) {
    constexpr int n = N;
    if constexpr (n == 0) {
      if (!tile) unit0();
    }
    if constexpr (n == 8) {
      if (!tile) {
        LDS_FENCE(); bar(); LDS_FENCE();
        t0 = ctl_get(C_T0);
        if (leader) {
          e1 += 1u;
          go_ref += 1u;
          st4c(e1, rws, WS_REQ + l16);
        }
      }
    }
    if constexpr (n > 0 && n % 8 == 0) {
      const int x = token(isq ? QBURST : PBURST);
      if constexpr (n > QFIRST) {
        if (!ok) {
          ok = q_there();
          if (ok) publish_q();
        }
      }
      if constexpr (n == OPOS + 16) {
        *reinterpret_cast<v4u*>(&s_old[w][lane * 4]) = (int)pa < 0 ? sent : eo;
        *reinterpret_cast<v4u*>(&s_dst[w][lane * 4]) = ~ed;
        LDS_FENCE();
        __hip_atomic_fetch_add(ctl, lane == C_OLD ? 1 : 0, __ATOMIC_RELAXED, __HIP_MEMORY_SCOPE_WORKGROUP);
      }
      SB();
      until(due_of(x));
      if constexpr (n == 8) {
        if (member) { qs0 = lowb(qv0); qs1 = lowb(qv1); }
      }
      if constexpr (n >= QFIRST) {
        if (!ok) load_q();
      }
      if constexpr (n == OPOS) {
        const int p = (w - 2) * 64 + lane;
        unsigned da = (isy && p < 112) ? my(p) : OOB;
        da = (w & 6) == 2 ? da : (w & 6) == 4 ? mo_ + (w - 4) * 4 * AREA : (w == 6 && isf) ? mf : OOB;
        eo = ld4<XB>(rws, pa, 0);
        ed = ld4<XA>(rws, da, 0);
      }
      SB();
    }
    if constexpr (n == QPOS) {
      STAMP(9);
      pv = ld4<16>(rws, (member || leader) ? poll_off : OOB, 0);
      SB();
    }
    if constexpr (n >= 8) {
      if constexpr (n >= PACE) WAITV(PACE + ((n >= QPOS && n < QPOS + PACE) ? 1 : 0) + ((n >= OPOS && n < OPOS + PACE) ? 2 : 0));
      if constexpr (n < 32) pre[n >> 3][n & 7] = ldk(ubase((n & 56) + w), n & 7); else ring[n & 7] = ldk(ubase(32 + w), n & 7);
    }
    SB();
    if constexpr (n - PACE == 7 || n - PACE == 15 || n - PACE == 23 || n - PACE == 31) STAMP(16 + (n - PACE) / 8);
  });
  inc = lane == C_TICK ? 1 : inc;
  int tok = token(KBURST);
  STAMP(10);
  bool svc = member || leader;
  unsigned seenm = 0, last_echo = 0;
  bool echoed = false;
  auto service = [&]() {
    if (leader) {
      seenm |= (pv[0] == e1[0] ? 1u : 0u) | (pv[1] == e1[1] ? 2u : 0u) | (pv[2] == e1[2] ? 4u : 0u) | (pv[3] == e1[3] ? 8u : 0u);
      svc = __builtin_amdgcn_ballot_w64(seenm != 15u) != 0;
      st1c(go_ref, rws, (lane == 0 && !svc) ? WS_GO : OOB);
    } else {
      const unsigned req = (unsigned)__builtin_amdgcn_readfirstlane((int)((b & 2) ? ((b & 1) ? pv[3] : pv[2]) : ((b & 1) ? pv[1] : pv[0])));
      const unsigned go = (unsigned)__builtin_amdgcn_readlane((int)pv[0], 1);
      go_ref = echoed ? go_ref : go;
      svc = go == (unsigned)__builtin_amdgcn_readfirstlane((int)go_ref);
      const bool rdy = ctl_get(C_OLD) == 8;
      st1c(req, rws, (lane == 0 && svc && rdy && !(echoed && req == last_echo)) ? WS_ECHO + b * 4 : OOB);
      echoed |= rdy; last_echo = req;
      if (!svc) {
        if (isf) {
          while (ctl_get(C_FC) != 4) __builtin_amdgcn_s_sleep(1);
          LDS_FENCE();
          const int j = (lane >> 3) & 3, pc = lane & 7, m = pc >> 2, col = j * 16 + (pc & 3) * 4;
          const f32x4 v = *reinterpret_cast<const f32x4*>(&s_mv[m][col]) + *reinterpret_cast<const f32x4*>(&s_mv[m + 2][col]);
          st4c(tag4(v, *reinterpret_cast<const v4u*>(&s_dst[6][lane * 4])), rws, mf);
        }
      }
    }
    pv = ld4<16>(rws, svc ? poll_off : OOB, 0);
    SB();
  };

  {
    if (!ok) {
      ok = q_there();
      if (ok) publish_q();
    }
#pragma unroll 1
    for (int it = 0; it < SPIN_CAP && !ctl_get(C_Q); ++it) {
      if (svc && it >= QTRIES) { service(); asm volatile("" :: "v"(pv)); }
      if (!ok) {
        load_q();
        ok = q_there() | !svc;
        if (ok) publish_q();
      } else {
        __builtin_amdgcn_s_sleep(1);
      }
    }
  }
  LDS_FENCE();
  v4u qb[8];
#pragma unroll
  for (int i = 0; i < 4; ++i) {
    const v4u z = {0u, 0u, 0u, 0u}, q = *reinterpret_cast<const v4u*>(&s_q[(lane & 8) * 16 + i * 32 + (lane >> 4) * 8 + (lane & 1) * 4]);
    qb[i] = (lane & 2) ? z : q; qb[4 + i] = (lane & 2) ? q : z;
  }
  v4u pm;
#pragma unroll
  for (int j = 0; j < 4; ++j) pm[j] = ((lane >> 1) & 3) == j ? ((lane & 1) ? 0xffff0000u : 0xffffu) : 0u;
  STAMP(1);
  SB();

  Att st;
  int sx = 20;
  {
    float* const sc = &s_sc[0][(lane >> 3) * 4 + (lane & 3)];
    rsrc_t nra, nrb;
    unsigned nva, nvb, nba, nbb, nsa, nsb;
    int kind = 0, nu = 32 + w, cu;
    auto next = [&]() {
      cu = nu;
      const int t = __builtin_amdgcn_readlane(tok, C_TICK);
      const bool fx = t < 2 * nk, tw = t >= nt;
      kind = fx ? (t >= nk ? 1 : 0) : tw ? 3 : 2;
      nu = t >= nk ? t - nk : t;
      nba = (unsigned)(fx ? first + 2 * nu : tw ? (TWP ? 0x80000 : 0) : first + t) * 4096u;
      nbb = fx ? nba + 4096 : nba;
      nra = selr(tw, rwo, selr(kind == 1, rv, rk));
      nrb = selr(tw, rwd, selr(kind == 0, rk, rv));
      nva = selu(tw, wt_off, selu(kind == 1, vvo, kvo)); nvb = selu(tw, wt_off, selu(kind == 0, kvo, vvo));
      nsa = selu(tw, 8 * D * 2, selu(kind == 1, 256, 128)); nsb = selu(tw, 8 * D * 2, selu(kind == 0, 128, 256));
      until(tw ? 0 : due_of(tok));
      inc = (lane == C_NEXT && t + PL >= 2 * nk) ? PP : inc;
      SB();
    };
    auto ldn = [&](int i) { ring[i] = i < 4 ? ld4<2>(nra, nva, nba + i * nsa) : ld4<2>(nrb, nvb, nbb + (i - 4) * nsb); };
    float mx;
    if (svc) service();
    {
      const float s0 = scores<8>(pre[0], qb, lane, [&](int) {});
      const float s1 = scores<8>(pre[1], qb, lane, [&](int) {});
      const float s2 = scores<8>(pre[2], qb, lane, [&](int) {});
      const float s3 = scores<8>(pre[3], qb, lane, [&](int) {});
      sc[w * 32] = s0; sc[(8 + w) * 32] = s1; sc[(16 + w) * 32] = s2; sc[(24 + w) * 32] = s3;
      mx = fmaxf(fmaxf(s0, s1), fmaxf(s2, s3));
      SB();
      STAMP(11);
    }
#pragma unroll 1
    while (kind == 0) {
      next();
      const float sv = scores<8>(ring, qb, lane, ldn);
      sc[cu * 32] = sv;
      tok = token(KBURST);
      mx = fmaxf(mx, sv);
      STAMP(sx); ++sx;
      if (svc) service();
      SB();
    }
    STAMP(7);
    st.m = red_max(mx); st.l = 0.f;
    st.a[0] = st.a[1] = (f32x4){0.f, 0.f, 0.f, 0.f};
#pragma unroll 1
    while (kind == 1) {
      float sv;
      for (;;) {
        sv = __hip_atomic_load(&sc[nu * 32], __ATOMIC_RELAXED, __HIP_MEMORY_SCOPE_WORKGROUP);
        if (__builtin_amdgcn_ballot_w64(asu(sv) == SENT) == 0) break;
        __builtin_amdgcn_s_sleep(1);
      }
      if (__builtin_amdgcn_ballot_w64(sv > st.m) != 0) {
        const float m2 = fmaxf(st.m, red_max(sv));
        const float al = __builtin_amdgcn_exp2f((st.m - m2) * EXP_C);
        st.m = m2; st.l *= al;
        st.a[0] *= al; st.a[1] *= al;
      }
      const float p = __builtin_amdgcn_exp2f((sv - st.m) * EXP_C);
      st.l += p;
      SB();
      next();
      phase_v<8>(ring, p, pm, st, ldn);
      tok = token(VBURST);
      STAMP(sx); ++sx;
      if (svc) service();
      SB();
    }
    STAMP(8);
#pragma unroll 1
    while (kind == 2) {
      next();
      const float p = phase_k4(ring, qb, st, lane, ldn);
      phase_v<4>(ring, p, pm, st, ldn);
      tok = token(VBURST);
      STAMP(sx); ++sx;
    }
#pragma unroll 1
    for (int it = 0; it < SPIN_CAP && svc; ++it) {
      service();
      asm volatile("" :: "v"(pv));
    }
    LDS_FENCE();
  }
  STAMP(12);
  auto tailw = [&]() {
#pragma unroll
    for (int i = 0; i < 6; ++i) ring[i] = i < 4 ? ld4<2>(rwo, wt_off, i * 8 * D * 2) : ld4<2>(rwd, wt_off, (i - 4) * 8 * D * 2);
    SB();
  };
  if (TWP == 1) tailw();
  auto send_o = [&](const f32x4& o, float M, float L, const v4u& t, int hf) {
    v4u msg;
#pragma unroll
    for (int j = 0; j < 3; ++j) msg[j] = tag<0x100>(__builtin_amdgcn_perm(asu(o[j]), asu(o[3]), 0x07060501u + j), t[j]);
    msg[3] = tag<1>(asu((lane & 1) ? L : M), t[3]);
    __builtin_amdgcn_raw_buffer_store_b128(msg, rws, (int)mo_, hf * 4 * AREA, 16);
  };
  {
    const lds4_t pw = (lds4_t)&s_part[w][lane * 4], pr = (lds4_t)&s_part[0][lane * 4], pmr = (lds4_t)&s_ml[(lane >> 3) & 1][0];
    const lds1_t pml = (lds1_t)&s_ml[(lane >> 3) & 1][w];
    pw[0] = st.a[0]; pw[64] = st.a[1];
    float l = st.l;
    l += dpp<XOR1>(l); l += dpp<XOR2>(l); l = x32_add(x16_add(l));
    pml[0] = st.m; pml[8] = l;
    LDS_FENCE(); bar(); LDS_FENCE();
    STAMP(2);
    if (w == 4 || w == 5) {
      f32x4 ml[4], pt[8];
#pragma unroll
      for (int k = 0; k < 4; ++k) ml[k] = pmr[k];
#pragma unroll
      for (int k = 0; k < 8; ++k) pt[k] = pr[k * 128 + (w - 4) * 64];
      const v4u dt = *reinterpret_cast<const v4u*>(&s_dst[w][lane * 4]);
      float M = -3.0e38f;
#pragma unroll
      for (int k = 0; k < 8; ++k) M = fmaxf(M, ml[k >> 2][k & 3]);
      f32x4 e[2] = {(ml[0] - M) * EXP_C, (ml[1] - M) * EXP_C};
      f32x4 o = {0.f, 0.f, 0.f, 0.f};
#pragma unroll
      for (int k = 0; k < 8; ++k) {
        e[k >> 2][k & 3] = __builtin_amdgcn_exp2f(e[k >> 2][k & 3]);
        o += pt[k] * e[k >> 2][k & 3];
      }
      const f32x4 l4 = ml[2] * e[0] + ml[3] * e[1];
      const float L = (l4[0] + l4[2]) + (l4[1] + l4[3]);
      send_o(o, M, L, dt, w - 4);
      SB();
      asm volatile("" :: "v"(ring[0]), "v"(ring[1]), "v"(ring[2]), "v"(ring[3]), "v"(ring[4]), "v"(ring[5]), "v"(ring[6]), "v"(ring[7]), "v"(pv));
    }
  }
  const v4u old = *reinterpret_cast<const v4u*>(&s_old[w][lane * 4]);
  if (isy) {
    if (TWP == 2) tailw();
    v4u d = ld4<16>(rws, pa, 0);
    SB();
    f32x2 wf[6][4];
#pragma unroll
    for (int i = 0; i < 6; ++i)
#pragma unroll
      for (int j = 0; j < 4; ++j) {
        wf[i][j] = unp(ring[i][j]);
        asm volatile("" : "+v"(wf[i][j]));
      }
    SB();
    STAMP(14);
    const v4u dt = *reinterpret_cast<const v4u*>(&s_dst[2][(w * 16 + (lane & 7) * 2 + (lane >> 5)) * 4]);
#pragma unroll 1
    for (int it = 0; it < SPIN_CAP; ++it) {
      if (arrived(d, old)) break;
      __builtin_amdgcn_s_sleep(1);
      LDS_FENCE();
      d = ld4<16>(rws, pa, 0);
    }
    STAMP(15);
    if (w < 4) {
      d &= (v4u){~0x100u, ~0x100u, ~0x100u, ~1u};
      const float ot = dpp<XOR1>(asf(d[3]));
      const float m = (lane & 1) ? ot : asf(d[3]), l = (lane & 1) ? asf(d[3]) : ot;
      float M = fmaxf(m, dpp<ROR8>(m)); M = x16_max(M); M = x32_max(M);
      const float e = __builtin_amdgcn_exp2f((m - M) * EXP_C);
      const float v3 = asf(__builtin_amdgcn_perm(__builtin_amdgcn_perm(d[2], d[1], 0x04000c0cu), d[0], 0x0706000cu));
      float y0 = tr32(asf(d[0]) * e, asf(d[2]) * e), y1 = tr32(asf(d[1]) * e, v3 * e), L = x32_add(l * e);
      y0 = tr16(y0, y1); L = x16_add(L);
      const bool s_ = lane & 8;
      y0 = (s_ ? L : y0) + dpp<ROR8>(s_ ? y0 : L);
      if (!s_) s_o[w][(lane & 7) * 4 + (lane >> 5) * 2 + ((lane >> 4) & 1)] = y0;
      if (lane == 8) *reinterpret_cast<f32x2*>(&s_ml2[w * 2]) = (f32x2){M, y0};
    } else if (w == 7) {
      float hv[4];
#pragma unroll
      for (int j = 0; j < 4; ++j) {
        float g = asf(d[j] & ~1u);
        g += dpp<ROR8>(g); g = rbf(x16_add(g));
        const float u = dpp<ROR4>(g);
        hv[j] = rbf(rbf(g / (1.0f + __expf(-g))) * u);
      }
      if (lane < 4) {
#pragma unroll
        for (int j = 0; j < 4; ++j) s_h[((lane & 1) * 4 + j) * 2 + (lane >> 1)] = hv[j];
      }
    }
    LDS_FENCE(); bar(); LDS_FENCE();
    STAMP(3);
    if (w < 7) {
      const f32x4 ma = *reinterpret_cast<const f32x4*>(&s_ml2[0]), mb = *reinterpret_cast<const f32x4*>(&s_ml2[4]);
      f32x4 pq[4];
#pragma unroll
      for (int k = 0; k < 4; ++k) pq[k] = *reinterpret_cast<const f32x4*>(&s_o[k][(lane >> 3) * 4]);
      const f32x2 hv = *reinterpret_cast<const f32x2*>(&s_h[(lane >> 3) * 2]);
      const float M = fmaxf(fmaxf(ma[0], ma[2]), fmaxf(mb[0], mb[2]));
      const float e0 = __builtin_amdgcn_exp2f((ma[0] - M) * EXP_C), e1_ = __builtin_amdgcn_exp2f((ma[2] - M) * EXP_C);
      const float e2 = __builtin_amdgcn_exp2f((mb[0] - M) * EXP_C), e3 = __builtin_amdgcn_exp2f((mb[2] - M) * EXP_C);
      const float inv = __builtin_amdgcn_rcpf((ma[1] * e0 + ma[3] * e1_) + (mb[1] * e2 + mb[3] * e3));
      const f32x4 av = ((pq[0] * e0 + pq[1] * e1_) + (pq[2] * e2 + pq[3] * e3)) * inv;
      f32x2 acc[4];
#pragma unroll
      for (int j = 0; j < 4; ++j) acc[j] = wf[4][j] * hv.x + wf[5][j] * hv.y;
#pragma unroll
      for (int i = 0; i < 4; ++i) {
        const float a = rbf(av[i]);
#pragma unroll
        for (int j = 0; j < 4; ++j) acc[j] += wf[i][j] * a;
      }
      float z[4] = {tr32(acc[0].x, acc[2].x), tr32(acc[0].y, acc[2].y), tr32(acc[1].x, acc[3].x), tr32(acc[1].y, acc[3].y)};
#pragma unroll
      for (int j = 0; j < 4; ++j) { z[j] = x16_add(z[j]); z[j] += dpp<ROR8>(z[j]); }
      const unsigned ra = (unsigned)(w * 32 + ((lane >> 1) & 3) * 4 + ((-w) & 3)) * AREA;
      st4c(tag4((f32x4){z[0], z[1], z[2], z[3]}, dt), rws, (ra + A_Y2 + y * 64 + (lane & 1) * 32 + (lane >> 5) * 16) | ((lane & 24) ? OOB : 0u));
    }
  } else if (isr) {
    const float xo = asf((unsigned)xg[ro * 16 + (lane & 15)] << 16);
    v4u d;
#pragma unroll 1
    for (int it = 0; it < SPIN_CAP; ++it) {
      d = ld4<16>(rws, pa, 0);
      if (arrived(d, old)) break;
      __builtin_amdgcn_s_sleep(1);
      LDS_FENCE();
    }
    d &= ~1u;
    float y0 = tr32(asf(d[0]), asf(d[2])), y1 = tr32(asf(d[1]), asf(d[3]));
    y0 = tr16(y0, y1);
    y0 += dpp<ROR8>(y0); y0 += dpp<ROR4>(y0);
    if (!(lane & 12)) s_r[w][(lane & 3) * 4 + (lane >> 5) * 2 + ((lane >> 4) & 1)] = y0;
    LDS_FENCE(); bar(); LDS_FENCE();
    if (w == 0) {
      float a = s_r[lane >> 4][lane & 15] + s_r[4 + (lane >> 4)][lane & 15];
      a = x16_add(a); a = x32_add(a);
      if (lane < 16) out[ro * 16 + lane] = (u16)f2bf(xo + a);
    }
  }
  STAMP(4);
  STAMP_OUT();
}

extern "C" void kernel_launch(void* const* d_in, const int* in_sizes, int n_in,
                              void* d_out, int out_size, void* d_ws, size_t ws_size,
                              hipStream_t stream) {
  fused_kernel<<<NWG, NT, 0, stream>>>((const u16*)d_in[0], (const u16*)d_in[1], (const u16*)d_in[2], (const u16*)d_in[3],
                                       (const u16*)d_in[4], (const u16*)d_in[6], (const u16*)d_in[7], (const u16*)d_in[8],
                                       (u16*)d_out, (char*)d_ws);
}
```

```cpp
#include <hip/hip_runtime.h>
#include <hip/hip_bf16.h>
#include <stdint.h>

#define QPOS 24
#define PACE 8
#define QTRIES 8
#define TQ 125
#define TF 130
#define TFY 126
#define TY 130
#define NP 4
#define TAU 3650
#define PP 3000
#define PL 8
#define TOFF 150
#define TOFFQ 180
#define QBURST 100000
#define PBURST 0
#define KBURST 100000
#define VBURST 100000
#define QFIRST 24
#define OPOS 8
#define TWK 6
#define TWD 0
#define WARM 0
#define HOT 0
#define HOTA 16
#define HOTT 24
#define OA 16
#define OB 16
#define PF 16
#define SF 16
#define P1 16
#define S1 16
#define P2 16
#define S2 16
#define G1 0
#define G2 0
#define G1N 0
#define PMASK 0
#define EO 3
#define XW 0
#define SND 0
#define DIAG 0
#define LITE 0

typedef unsigned v4u __attribute__((ext_vector_type(4)));
typedef unsigned v2u __attribute__((ext_vector_type(2)));
typedef float f32x4 __attribute__((ext_vector_type(4)));
typedef float f32x2 __attribute__((ext_vector_type(2)));
typedef __bf16 bf16x2 __attribute__((ext_vector_type(2)));
typedef __bf16 bf16x8 __attribute__((ext_vector_type(8)));
typedef unsigned short u16;
typedef __amdgpu_buffer_rsrc_t rsrc_t;
typedef __attribute__((address_space(3))) f32x4* lds4_t;
typedef __attribute__((address_space(3))) v4u* ldsu_t;
typedef __attribute__((address_space(3))) float* lds1_t;

constexpr int D = 448, SK = 32768, DH = 256, F = 1792, NQ = 4096;
constexpr int NWG = 256, NT = 512;
constexpr unsigned OOB = 0x80000000u;
constexpr unsigned SENT = 0xFFFFFFFFu;
constexpr unsigned AREA = 9216;
constexpr unsigned A_O1 = 1024;
constexpr unsigned A_F = 5120;
constexpr unsigned A_Y2 = 1024;
constexpr unsigned WS_REQ = NWG * AREA;
constexpr unsigned WS_ECHO = WS_REQ + 1024;
constexpr unsigned WS_GO = WS_ECHO + 1024;
constexpr unsigned WS_Q = WS_GO + 256;
constexpr unsigned WS_END = WS_Q + 8 * 2048;
constexpr int SPIN_CAP = 1 << 18;
constexpr int C_NEXT = 0, C_TICK = 1, C_GO = 2, C_Q = 3, C_T0 = 4, C_FC = 5, C_OLD = 6, C_DONE = 7;

__device__ __forceinline__ float asf(unsigned u) { return __builtin_bit_cast(float, u); }
__device__ __forceinline__ unsigned asu(float f) { return __builtin_bit_cast(unsigned, f); }
__device__ __forceinline__ float rbf(float f) { __bf16 b = (__bf16)f; return (float)b; }
__device__ __forceinline__ unsigned f2bf(float f) { __bf16 b = (__bf16)f; return (unsigned)__builtin_bit_cast(u16, b); }
__device__ __forceinline__ float dot2(unsigned k, unsigned q, float acc) {
  return __builtin_amdgcn_fdot2_f32_bf16(__builtin_bit_cast(bf16x2, k), __builtin_bit_cast(bf16x2, q), acc, false);
}
__device__ __forceinline__ f32x2 unp(unsigned u) { f32x2 r; r.x = asf(u << 16); r.y = asf(u & 0xffff0000u); return r; }
template <int CTRL> __device__ __forceinline__ float dpp(float x) {
  return asf((unsigned)__builtin_amdgcn_update_dpp(0, (int)asu(x), CTRL, 0xf, 0xf, true));
}
constexpr int XOR1 = 0xB1, XOR2 = 0x4E, XOR7 = 0x141, ROR8 = 0x128, ROR4 = 0x124, ROR2 = 0x122, BCAST = 0x150;
constexpr int QP_EVEN = 0xA0, QP_ODD = 0xF5;
__device__ __forceinline__ float x16_add(float x) { auto s = __builtin_amdgcn_permlane16_swap(asu(x), asu(x), false, false); return asf(s[0]) + asf(s[1]); }
__device__ __forceinline__ float x32_add(float x) { auto s = __builtin_amdgcn_permlane32_swap(asu(x), asu(x), false, false); return asf(s[0]) + asf(s[1]); }
__device__ __forceinline__ float x16_max(float x) { auto s = __builtin_amdgcn_permlane16_swap(asu(x), asu(x), false, false); return fmaxf(asf(s[0]), asf(s[1])); }
__device__ __forceinline__ float x32_max(float x) { auto s = __builtin_amdgcn_permlane32_swap(asu(x), asu(x), false, false); return fmaxf(asf(s[0]), asf(s[1])); }
__device__ __forceinline__ float tr32(float a, float b) { auto s = __builtin_amdgcn_permlane32_swap(asu(a), asu(b), false, false); return asf(s[0]) + asf(s[1]); }
__device__ __forceinline__ float tr16(float a, float b) { auto s = __builtin_amdgcn_permlane16_swap(asu(a), asu(b), false, false); return asf(s[0]) + asf(s[1]); }
__device__ __forceinline__ float wave_sum(float v) {
  v += dpp<XOR1>(v); v += dpp<XOR2>(v); v += dpp<XOR7>(v); v += dpp<ROR8>(v);
  v = x16_add(v); return x32_add(v);
}
__device__ __forceinline__ float wave_max(float v) {
  v = fmaxf(v, dpp<XOR1>(v)); v = fmaxf(v, dpp<XOR2>(v)); v = fmaxf(v, dpp<XOR7>(v)); v = fmaxf(v, dpp<ROR8>(v));
  v = x16_max(v); return x32_max(v);
}
__device__ __forceinline__ rsrc_t mkrs(const void* p, unsigned bytes) { return __builtin_amdgcn_make_buffer_rsrc((void*)p, 0, (int)bytes, 0x00020000); }
__device__ __forceinline__ rsrc_t selr(bool c, rsrc_t a, rsrc_t b) { return c ? a : b; }
__device__ __forceinline__ unsigned selu(bool c, unsigned a, unsigned b) { return c ? a : b; }
template <int AUX> __device__ __forceinline__ v4u ld4(rsrc_t r, unsigned vo, unsigned so) { return __builtin_amdgcn_raw_buffer_load_b128(r, (int)vo, (int)so, AUX); }
template <int AUX> __device__ __forceinline__ v2u ld2(rsrc_t r, unsigned vo, unsigned so) { return __builtin_amdgcn_raw_buffer_load_b64(r, (int)vo, (int)so, AUX); }
__device__ __forceinline__ void st4c(v4u v, rsrc_t r, unsigned vo) { __builtin_amdgcn_raw_buffer_store_b128(v, r, (int)vo, 0, 16); }
__device__ __forceinline__ void st1c(unsigned v, rsrc_t r, unsigned vo) { __builtin_amdgcn_raw_buffer_store_b32(v, r, (int)vo, 0, 16); }
template <unsigned M> __device__ __forceinline__ unsigned tag(unsigned u, unsigned t) { return (u & ~M) | (t & M); }
__device__ __forceinline__ v4u tag4(const f32x4& v, const v4u& t) { return (v4u){tag<1>(asu(v[0]), t[0]), tag<1>(asu(v[1]), t[1]), tag<1>(asu(v[2]), t[2]), tag<1>(asu(v[3]), t[3])}; }
__device__ __forceinline__ bool arrived(const v4u& v, const v4u& o) { return __builtin_amdgcn_ballot_w64(v[0] == o[0] || v[1] == o[1] || v[2] == o[2] || v[3] == o[3]) == 0; }
__device__ __forceinline__ unsigned lowb(const v4u& v) { return __builtin_amdgcn_perm(v[1], v[0], 0x0c0c0400u) | __builtin_amdgcn_perm(v[3], v[2], 0x04000c0cu); }
#define SB() __builtin_amdgcn_sched_barrier(0)
__device__ __forceinline__ void bar() { __builtin_amdgcn_s_waitcnt(0xC07F); __builtin_amdgcn_s_barrier(); }
#define WAITV(n) __builtin_amdgcn_s_waitcnt(((n) & 15) | (((n) >> 4) << 14) | 0x0F70)
template <int V> struct IC { static constexpr int value = V; constexpr operator int() const { return V; } };
template <int A, int B, class Fn> __device__ __forceinline__ void static_for(Fn&& f) { if constexpr (A < B) { f(IC<A>{}); static_for<A + 1, B>(f); } }
#define LDS_FENCE() asm volatile("" ::: "memory")

constexpr float EXP_C = 0.0625f * 1.4426950408889634f;

struct Att {
  float m, l;
  f32x4 a[2];
};
__device__ __forceinline__ f32x4 mfma(const v4u& a, const v4u& b, const f32x4& c) {
  return __builtin_amdgcn_mfma_f32_16x16x32_bf16(__builtin_bit_cast(bf16x8, a), __builtin_bit_cast(bf16x8, b), c, 0, 0, 0);
}
__device__ __forceinline__ float red_max(float v) { v = fmaxf(v, dpp<XOR1>(v)); v = fmaxf(v, dpp<XOR2>(v)); return x32_max(x16_max(v)); }

template <int NL, class ISSUE>
__device__ __forceinline__ float scores(v4u (&kb)[8], const v4u (&qb)[8], int lane, ISSUE issue) {
  f32x4 d = {0.f, 0.f, 0.f, 0.f};
#pragma unroll
  for (int i = 0; i < NL; ++i) {
    d = mfma(kb[i], qb[i], d);
    asm volatile("" : "+v"(d));
    issue(i);
    SB();
  }
  const bool odd = lane & 1;
  return rbf((odd ? d[3] : d[0]) + dpp<XOR1>(odd ? d[1] : d[2]));
}
template <class ISSUE>
__device__ __forceinline__ float phase_k4(v4u (&kb)[8], const v4u (&qb)[8], Att& st, int lane, ISSUE issue) {
  float s = scores<4>(kb, qb, lane, issue);
  s = (lane & 2) ? -__builtin_inff() : s;
  const float mn = fmaxf(st.m, red_max(s));
  const float al = __builtin_amdgcn_exp2f((st.m - mn) * EXP_C);
  const float p = __builtin_amdgcn_exp2f((s - mn) * EXP_C);
  st.m = mn; st.l = st.l * al + p;
  st.a[0] *= al; st.a[1] *= al;
  SB();
  return p;
}
template <int I, int O, class ISSUE>
__device__ __forceinline__ void pv_step(v4u (&vb)[8], unsigned pp, const v4u& pm, Att& st, ISSUE& issue) {
  const v4u pb = pm & (unsigned)__builtin_amdgcn_update_dpp(0, (int)pp, I * 0x55, 0xf, 0xf, true);
  st.a[0] = mfma(vb[O + 2 * I], pb, st.a[0]);
  asm volatile("" : "+v"(st.a[0]));
  issue(O + 2 * I);
  SB();
  st.a[1] = mfma(vb[O + 2 * I + 1], pb, st.a[1]);
  asm volatile("" : "+v"(st.a[1]));
  issue(O + 2 * I + 1);
  SB();
}
template <int NL, class ISSUE>
__device__ __forceinline__ void phase_v(v4u (&vb)[8], float p, const v4u& pm, Att& st, ISSUE issue) {
  constexpr int O = 8 - NL;
  const unsigned pp = __builtin_bit_cast(unsigned, (bf16x2){(__bf16)p, (__bf16)p});
  pv_step<0, O>(vb, pp, pm, st, issue); pv_step<1, O>(vb, pp, pm, st, issue);
  if constexpr (NL == 8) { pv_step<2, O>(vb, pp, pm, st, issue); pv_step<3, O>(vb, pp, pm, st, issue); }
}

#define STAMP(k) do {} while (0)
#define STAMP_OUT() do {} while (0)

__device__ __forceinline__ f32x2 ln_stats(const v4u& xa, int lane) {
  f32x2 xv[4];
  float sm = 0.f;
#pragma unroll
  for (int j = 0; j < 4; ++j) { xv[j] = unp(xa[j]); sm += xv[j].x + xv[j].y; }
  const float mean = wave_sum(sm) * (1.0f / D);
  float sq = 0.f;
#pragma unroll
  for (int j = 0; j < 4; ++j) { const f32x2 d = xv[j] - mean; sq += d.x * d.x + d.y * d.y; }
  sq = lane < 56 ? sq : 0.f;
  const float var = rbf(wave_sum(sq) * (1.0f / D));
  const float sd = rbf(sqrtf(rbf(var + 1e-5f)));
  return (f32x2){rbf(mean), 1.0f / sd};
}
__device__ __forceinline__ float ln_apply(float x, f32x2 st) { return rbf(rbf(x - st.x) * st.y); }

__global__ void __launch_bounds__(NT, 2)
fused_kernel(const u16* __restrict__ xg, const u16* __restrict__ kc, const u16* __restrict__ vc, const u16* __restrict__ wq,
             const u16* __restrict__ wo, const u16* __restrict__ wg, const u16* __restrict__ wu, const u16* __restrict__ wd,
             u16* __restrict__ out, char* ws) {
  __shared__ __attribute__((aligned(16))) float s_part[8][512];
  __shared__ __attribute__((aligned(16))) float s_mv[8][64];
  __shared__ __attribute__((aligned(16))) unsigned s_q[256];
  __shared__ __attribute__((aligned(16))) unsigned s_qs[64];
  __shared__ __attribute__((aligned(16))) unsigned s_old[8][256];
  __shared__ __attribute__((aligned(16))) unsigned s_dst[8][256];
  __shared__ __attribute__((aligned(16))) float s_ml[2][16];
  __shared__ __attribute__((aligned(16))) float s_o[4][32];
  __shared__ __attribute__((aligned(16))) float s_ml2[8];
  __shared__ __attribute__((aligned(16))) float s_h[16];
  __shared__ __attribute__((aligned(16))) float s_r[8][16];
  __shared__ __attribute__((aligned(16))) float s_sc[64][32];
  __shared__ int s_ctl[64];
  const int tid = threadIdx.x, lane = tid & 63, w = __builtin_amdgcn_readfirstlane(tid >> 6);
  const int bi = blockIdx.x, h = (((bi & 1) ? bi + 5 : bi) & 6) | (bi >> 7), s = ((bi >> 3) & 15) * 2 + (bi & 1);
  const int b = h * 32 + s;
  static_assert(8 * TQ + 8 * TF + 6 * TFY + 10 * TY == 4096 && TQ - NP > 80 && TF - NP > 80 && TFY - NP > 80 && TY - NP > 80, "tickets");
  static_assert(TQ - NP <= 128 && TF - NP <= 128 && TFY - NP <= 128 && TY - NP <= 128, "tickets");
  const int nt = s < 8 ? TQ : s < 16 ? TF : s < 22 ? TFY : TY, nk = (nt - NP) >> 1;
  const int first = s * TY - min(s, 22) * (TY - TFY) - min(s, 16) * (TFY - TF) - min(s, 8) * (TF - TQ);
  asm volatile("" :: "s"(xg), "s"(kc), "s"(vc), "s"(wq), "s"(wo), "s"(wg), "s"(wu), "s"(wd), "s"(out), "s"(ws));
  STAMP(0);
  const rsrc_t rx = mkrs(xg, D * 2);
  const rsrc_t rk = mkrs(kc + (size_t)h * SK * DH, SK * DH * 2);
  const rsrc_t rv = mkrs(vc + (size_t)h * SK * DH, SK * DH * 2);
  const rsrc_t rws = mkrs(ws, WS_END);
  const unsigned l16 = lane * 16;
  auto ubase = [&](int u) -> unsigned { return (unsigned)(first + 2 * u) * 4096u; };
  const unsigned kvo = ((lane >> 1) & 7) * 512 + (lane >> 4) * 32 + (lane & 1) * 16, vvo = (lane >> 4) * 1024 + (lane & 15) * 16;
  auto ldk = [&](unsigned base, int i) { return ld4<2>(rk, kvo + (i & 3) * 128, base + (i >> 2) * 4096); };
  const unsigned area = (unsigned)b * AREA;
  auto area_of = [&](int ss) -> unsigned { return (unsigned)(h * 32 + ss) * AREA; };
  const unsigned lmask = lane < 56 ? 0u : OOB;

  const bool isq = s < 8, isf = s >= 8 && s < 22, isy = s >= 16;
  const int f = h * 14 + s - 8, y = h * 16 + s - 16, ro = h * 4 + (s >> 2);
  const bool isr = s < 16 && ((s + h) & 3) == 0 && ro < 28;
  const bool tile = isq || (isf && w < 4);
  const bool leader = (b == NWG - 1 && w == 1), member = (w == 0);

  const unsigned pa = isy ? (w < 4 ? area + A_O1 + w * 1024 + l16 : (w == 7 && y < 112) ? area + A_F + (lane & 31) * 16 : OOB) : isr ? area + A_Y2 + w * 1024 + l16 : OOB;
  const unsigned mo_ = area_of(16 + (lane & 8) + (lane >> 4)) + A_O1 + s * 128 + (lane & 7) * 16;
  const int fd = (f & ~3) + ((lane >> 3) & 3);
  const unsigned mf = ((unsigned)((fd >> 4) * 32 + 16 + (fd & 15)) * AREA + A_F + (f & 3) * 128 + (lane & 7) * 16) | (lane < 32 ? 0u : OOB);
  auto my = [&](int p) -> unsigned { return (unsigned)((p >> 4) * 32 + (p & 12) + ((-(p >> 4)) & 3)) * AREA + A_Y2 + y * 64 + (p & 3) * 16; };
  v4u eo, ed;
  auto da_of = [&]() {
    const int p = (w - 2) * 64 + lane;
    unsigned da = (isy && p < 112) ? my(p) : OOB;
    return (w & 6) == 2 ? da : (w & 6) == 4 ? mo_ + (w - 4) * 4 * AREA : (w == 6 && isf) ? mf : OOB;
  };
  if (EO == 1) { eo = ld4<OA>(rws, pa, 0); SB(); }
  if (EO == 3) { eo = ld4<OA>(rws, pa, 0); ed = ld4<OB>(rws, da_of(), 0); SB(); }
  const v4u sent = {SENT, SENT, SENT, SENT};
  unsigned poll_off = lane == 1 ? WS_GO : OOB;
  poll_off = lane == 0 ? WS_REQ + (b >> 2) * 16 : poll_off;
  poll_off = leader ? WS_ECHO + l16 : poll_off;
  v4u pv = {0u, 0u, 0u, 0u}, e1 = {0u, 0u, 0u, 0u};
  unsigned go_ref = 0;
  int* const ctl = &s_ctl[lane];
  auto ctl_get = [&](int i) { return __builtin_amdgcn_readfirstlane(__hip_atomic_load(&s_ctl[i], __ATOMIC_RELAXED, __HIP_MEMORY_SCOPE_WORKGROUP)); };
  auto ctl_set = [&](int i) { __hip_atomic_store(&s_ctl[i], 1, __ATOMIC_RELAXED, __HIP_MEMORY_SCOPE_WORKGROUP); };
  const unsigned qoff = WS_Q + h * 2048 + l16;
  v4u qv0, qv1;
  if (member) {
    qv0 = ld4<16>(rws, qoff, 0);
    qv1 = ld4<16>(rws, qoff, 1024);
    SB();
    int c = lane == C_NEXT ? (isq ? TOFFQ : TOFF) * 128 : 0;
    c = lane == C_TICK ? 40 : c;
    c = lane == C_T0 ? (int)(__builtin_amdgcn_s_memrealtime() & 0x7fffffff) : c;
    __hip_atomic_store(ctl, c, __ATOMIC_RELAXED, __HIP_MEMORY_SCOPE_WORKGROUP);
  }
  *reinterpret_cast<v4u*>(&s_sc[w * 8][lane * 4]) = sent;
  int t0 = 0;
  auto now = [&]() { return ((int)__builtin_amdgcn_s_memrealtime() - t0) & 0x7fffff; };
  const int neg = lane == C_NEXT ? 0 : (int)0x80000000;
  int inc = lane == C_NEXT ? TAU : 0;
  auto token = [&](int burst) {
    const int t = max(now() * 128 - burst, 0);
    __hip_atomic_fetch_max(ctl, t | neg, __ATOMIC_RELAXED, __HIP_MEMORY_SCOPE_WORKGROUP);
    return __hip_atomic_fetch_add(ctl, inc, __ATOMIC_RELAXED, __HIP_MEMORY_SCOPE_WORKGROUP);
  };
  auto due_of = [&](int x) { return __builtin_amdgcn_readlane(x, C_NEXT) >> 7; };
  auto until = [&](int due) {
#pragma unroll 1
    for (int it = 0; it < 4096 && now() < due; ++it) __builtin_amdgcn_s_sleep(1);
    SB();
  };
  unsigned qs0, qs1;
  auto q_there = [&]() { return __builtin_amdgcn_ballot_w64(((lowb(qv0) ^ qs0) & (lowb(qv1) ^ qs1)) != 0xFFFFFFFFu) == 0; };
  auto load_q = [&]() { qv0 = ld4<16>(rws, qoff, 0); qv1 = ld4<16>(rws, qoff, 1024); };
  auto publish_q = [&]() {
    *reinterpret_cast<v2u*>(&s_q[lane * 2]) = (v2u){__builtin_amdgcn_perm(qv0[1], qv0[0], 0x07060302u), __builtin_amdgcn_perm(qv0[3], qv0[2], 0x07060302u)};
    *reinterpret_cast<v2u*>(&s_q[128 + lane * 2]) = (v2u){__builtin_amdgcn_perm(qv1[1], qv1[0], 0x07060302u), __builtin_amdgcn_perm(qv1[3], qv1[2], 0x07060302u)};
    LDS_FENCE();
    __builtin_amdgcn_s_waitcnt(0xC07F);
    ctl_set(C_Q);
  };
  v4u pre[4][8], ring[8];
  auto unit0 = [&]() {
#pragma unroll
    for (int i = 0; i < 8; ++i) { pre[0][i] = ldk(ubase(w), i); SB(); }
    if (EO == 2) { eo = ld4<OA>(rws, pa, 0); SB(); }
  };
  SB();
  if (tile) {
    v4u tw[8];
    const int oct = isq ? 7 * w : (f & 3) * 14 + (w >> 1) * 7;
    const int stride = isq ? NQ * 2 : F * 2;
    const rsrc_t rt = mkrs(isq ? wq : (w & 1) ? wu : wg, D * stride);
    const unsigned to = (unsigned)((oct + (lane >> 3)) * 8 * stride + (isq ? h * 8 + s : f >> 2) * 128 + (lane & 7) * 16) | lmask;
    const v4u xa = ld4<0>(rx, l16, 0);
    const v4u xb = ld4<0>(rx, (unsigned)(oct + (lane >> 3)) * 16 | lmask, 0);
    SB();
#pragma unroll
    for (int i = 0; i < 8; ++i) { tw[i] = ld4<2>(rt, to, i * stride); SB(); }
    LDS_FENCE(); bar(); LDS_FENCE();
    t0 = ctl_get(C_T0);
    SB();
    const f32x2 ls = ln_stats(xa, lane);
    f32x2 acc[4];
#pragma unroll
    for (int i = 0; i < 8; ++i) {
      const float xn = ln_apply(asf((i & 1) ? (xb[i >> 1] & 0xffff0000u) : (xb[i >> 1] << 16)), ls);
#pragma unroll
      for (int j = 0; j < 4; ++j) acc[j] = i ? acc[j] + unp(tw[i][j]) * xn : unp(tw[i][j]) * xn;
    }
    float y0 = tr32(acc[0].x, acc[2].x), y1 = tr32(acc[0].y, acc[2].y), y2 = tr32(acc[1].x, acc[3].x), y3 = tr32(acc[1].y, acc[3].y);
    y0 = tr16(y0, y2); y1 = tr16(y1, y3);
    const bool s_ = lane & 8;
    y0 = (s_ ? y1 : y0) + dpp<ROR8>(s_ ? y0 : y1);
    s_mv[w][(lane & 7) * 8 + (lane >> 3)] = y0;
    STAMP(6);
    token(isq ? QBURST : PBURST);
    unit0();
    if (isq) {
      LDS_FENCE(); bar(); LDS_FENCE();
      if (member) {
        float a = 0.f;
#pragma unroll
        for (int k = 0; k < 8; ++k) a += s_mv[k][lane];
        s_qs[lane] = f2bf(a) << 16;
        LDS_FENCE();
        const v4u qs = *reinterpret_cast<const v4u*>(&s_qs[(lane & 15) * 4]) | (~((s & 4) ? qv1 : qv0) & 0xffffu);
        st4c(qs, rws, (lane >> 4) == (s & 3) ? qoff + (s >> 2) * 1024 : OOB);
      }
    } else {
      LDS_FENCE();
      __builtin_amdgcn_s_waitcnt(0xC07F);
      __hip_atomic_fetch_add(ctl, lane == C_FC ? 1 : 0, __ATOMIC_RELAXED, __HIP_MEMORY_SCOPE_WORKGROUP);
    }
  } else if (leader) {
    e1 = ld4<16>(rws, poll_off, 0);
    go_ref = __builtin_amdgcn_raw_buffer_load_b32(rws, (int)WS_GO, 0, 16);
  }
  SB();
  STAMP(5);
  SB();
  bool ok = !member;
  static_for<0, 40>([&](auto N) {
    constexpr int n = N;
    if constexpr (n == 0) {
      if (!tile) unit0();
    }
    if constexpr (n == 8) {
      if (!tile) {
        LDS_FENCE(); bar(); LDS_FENCE();
        t0 = ctl_get(C_T0);
        if (leader) {
          e1 += 1u;
          go_ref += 1u;
          st4c(e1, rws, WS_REQ + l16);
        }
      }
    }
    if constexpr (n > 0 && n % 8 == 0) {
      const int x = token(isq ? QBURST : PBURST);
      if constexpr (n > QFIRST) {
        if (!ok) {
          ok = q_there();
          if (ok) publish_q();
        }
      }
      if constexpr (n == OPOS + 16) {
        *reinterpret_cast<v4u*>(&s_old[w][lane * 4]) = (int)pa < 0 ? sent : eo;
        *reinterpret_cast<v4u*>(&s_dst[w][lane * 4]) = ~ed;
        LDS_FENCE();
        __hip_atomic_fetch_add(ctl, lane == C_OLD ? 1 : 0, __ATOMIC_RELAXED, __HIP_MEMORY_SCOPE_WORKGROUP);
      }
      SB();
      until(due_of(x));
      if constexpr (n == 8) {
        if (member) { qs0 = lowb(qv0); qs1 = lowb(qv1); }
      }
      if constexpr (n >= QFIRST) {
        if (!ok) load_q();
      }
      if constexpr (n == OPOS) {
        if (EO == 0) eo = ld4<OA>(rws, pa, 0);
        if (EO != 3) ed = ld4<OB>(rws, da_of(), 0);
      }
      SB();
    }
    if constexpr (n == QPOS) {
      STAMP(9);
      pv = ld4<16>(rws, (member || leader) ? poll_off : OOB, 0);
      SB();
    }
    if constexpr (n >= 8) {
      if constexpr (n >= PACE) WAITV(PACE + ((n >= QPOS && n < QPOS + PACE) ? 1 : 0) + ((n >= OPOS && n < OPOS + PACE) ? (EO == 0 ? 2 : EO == 3 ? 0 : 1) : 0));
      if constexpr (n < 32) pre[n >> 3][n & 7] = ldk(ubase((n & 56) + w), n & 7); else ring[n & 7] = ldk(ubase(32 + w), n & 7);
    }
    SB();
    if constexpr (n - PACE == 7 || n - PACE == 15 || n - PACE == 23 || n - PACE == 31) STAMP(16 + (n - PACE) / 8);
  });
  inc = lane == C_TICK ? 1 : inc;
  int tok = token(KBURST);
  STAMP(10);
  bool svc = member || leader;
  unsigned seenm = 0, last_echo = 0;
  bool echoed = false;
  auto service = [&]() {
    if (leader) {
      seenm |= (pv[0] == e1[0] ? 1u : 0u) | (pv[1] == e1[1] ? 2u : 0u) | (pv[2] == e1[2] ? 4u : 0u) | (pv[3] == e1[3] ? 8u : 0u);
      svc = __builtin_amdgcn_ballot_w64(seenm != 15u) != 0;
      st1c(go_ref, rws, (lane == 0 && !svc) ? WS_GO : OOB);
    } else {
      const unsigned req = (unsigned)__builtin_amdgcn_readfirstlane((int)((b & 2) ? ((b & 1) ? pv[3] : pv[2]) : ((b & 1) ? pv[1] : pv[0])));
      const unsigned go = (unsigned)__builtin_amdgcn_readlane((int)pv[0], 1);
      go_ref = echoed ? go_ref : go;
      svc = go == (unsigned)__builtin_amdgcn_readfirstlane((int)go_ref);
      const bool rdy = ctl_get(C_OLD) == 8;
      st1c(req, rws, (lane == 0 && svc && rdy && !(echoed && req == last_echo)) ? WS_ECHO + b * 4 : OOB);
      echoed |= rdy; last_echo = req;
      if (!svc) {
        if (isf) {
          while (ctl_get(C_FC) != 4) __builtin_amdgcn_s_sleep(1);
          LDS_FENCE();
          const int j = (lane >> 3) & 3, pc = lane & 7, m = pc >> 2, col = j * 16 + (pc & 3) * 4;
          const f32x4 v = *reinterpret_cast<const f32x4*>(&s_mv[m][col]) + *reinterpret_cast<const f32x4*>(&s_mv[m + 2][col]);
          __builtin_amdgcn_raw_buffer_store_b128(tag4(v, *reinterpret_cast<const v4u*>(&s_dst[6][lane * 4])), rws, (int)mf, 0, SF);
        }
      }
    }
    pv = ld4<16>(rws, svc ? poll_off : OOB, 0);
    SB();
  };

  {
    if (!ok) {
      ok = q_there();
      if (ok) publish_q();
    }
#pragma unroll 1
    for (int it = 0; it < SPIN_CAP && !ctl_get(C_Q); ++it) {
      if (svc && it >= QTRIES) { service(); asm volatile("" :: "v"(pv)); }
      if (!ok) {
        load_q();
        ok = q_there() | !svc;
        if (ok) publish_q();
      } else {
        __builtin_amdgcn_s_sleep(1);
      }
    }
  }
  LDS_FENCE();
  v4u qb[8];
#pragma unroll
  for (int i = 0; i < 4; ++i) {
    const v4u z = {0u, 0u, 0u, 0u}, q = *reinterpret_cast<const v4u*>(&s_q[(lane & 8) * 16 + i * 32 + (lane >> 4) * 8 + (lane & 1) * 4]);
    qb[i] = (lane & 2) ? z : q; qb[4 + i] = (lane & 2) ? q : z;
  }
  v4u pm;
#pragma unroll
  for (int j = 0; j < 4; ++j) pm[j] = ((lane >> 1) & 3) == j ? ((lane & 1) ? 0xffff0000u : 0xffffu) : 0u;
  STAMP(1);
  SB();

  Att st;
  v4u wa = sent, wb = sent;
  int sx = 20;
  {
    float* const sc = &s_sc[0][(lane >> 3) * 4 + (lane & 3)];
    rsrc_t nra, nrb;
    unsigned nva, nvb, nba, nbb, nsa, nsb;
    int kind = 0, nu = 32 + w, cu;
    auto next = [&]() {
      cu = nu;
      const int t = __builtin_amdgcn_readlane(tok, C_TICK);
      const bool fx = t < 2 * nk, tw = t >= nt;
      kind = fx ? (t >= nk ? 1 : 0) : tw ? 3 : 2;
      nu = t >= nk ? t - nk : t;
      nba = (unsigned)(fx ? first + 2 * nu : tw ? 0x80000 : first + t) * 4096u;
      nbb = fx ? nba + 4096 : nba;
      nra = selr(kind == 1, rv, rk);
      nrb = selr(kind == 0, rk, rv);
      nva = selu(kind == 1, vvo, kvo); nvb = selu(kind == 0, kvo, vvo);
      nsa = selu(kind == 1, 256, 128); nsb = selu(kind == 0, 128, 256);
      until(tw ? 0 : due_of(tok));
      inc = (lane == C_NEXT && t + PL >= 2 * nk) ? PP : inc;
      SB();
    };
    auto ldn = [&](int i) { ring[i] = i < 4 ? ld4<2>(nra, nva, nba + i * nsa) : ld4<2>(nrb, nvb, nbb + (i - 4) * nsb); };
    float mx;
    if (svc) service();
    {
      const float s0 = scores<8>(pre[0], qb, lane, [&](int) {});
      const float s1 = scores<8>(pre[1], qb, lane, [&](int) {});
      const float s2 = scores<8>(pre[2], qb, lane, [&](int) {});
      const float s3 = scores<8>(pre[3], qb, lane, [&](int) {});
      sc[w * 32] = s0; sc[(8 + w) * 32] = s1; sc[(16 + w) * 32] = s2; sc[(24 + w) * 32] = s3;
      mx = fmaxf(fmaxf(s0, s1), fmaxf(s2, s3));
      SB();
      STAMP(11);
    }
#pragma unroll 1
    while (kind == 0) {
      next();
      const float sv = scores<8>(ring, qb, lane, ldn);
      sc[cu * 32] = sv;
      tok = token(KBURST);
      mx = fmaxf(mx, sv);
      STAMP(sx); ++sx;
      if (svc) service();
      SB();
    }
    STAMP(7);
    if (isy && w == 7) {
      const v4u old = *reinterpret_cast<const v4u*>(&s_old[7][lane * 4]);
      v4u d;
#pragma unroll 1
      for (int it = 0; it < SPIN_CAP; ++it) {
        d = ld4<PF>(rws, pa, 0);
        if (arrived(d, old)) break;
        __builtin_amdgcn_s_sleep(1);
        LDS_FENCE();
      }
      float hv[4];
#pragma unroll
      for (int j = 0; j < 4; ++j) {
        float g = asf(d[j] & ~1u);
        g += dpp<ROR8>(g); g = rbf(x16_add(g));
        const float u = dpp<ROR4>(g);
        hv[j] = rbf(rbf(g / (1.0f + __expf(-g))) * u);
      }
      if (lane < 4) {
#pragma unroll
        for (int j = 0; j < 4; ++j) s_h[((lane & 1) * 4 + j) * 2 + (lane >> 1)] = hv[j];
      }
      SB();
    }
    st.m = red_max(mx); st.l = 0.f;
    st.a[0] = st.a[1] = (f32x4){0.f, 0.f, 0.f, 0.f};
#pragma unroll 1
    while (kind == 1) {
      float sv;
      for (;;) {
        sv = __hip_atomic_load(&sc[nu * 32], __ATOMIC_RELAXED, __HIP_MEMORY_SCOPE_WORKGROUP);
        if (__builtin_amdgcn_ballot_w64(asu(sv) == SENT) == 0) break;
        __builtin_amdgcn_s_sleep(1);
      }
      if (__builtin_amdgcn_ballot_w64(sv > st.m) != 0) {
        const float m2 = fmaxf(st.m, red_max(sv));
        const float al = __builtin_amdgcn_exp2f((st.m - m2) * EXP_C);
        st.m = m2; st.l *= al;
        st.a[0] *= al; st.a[1] *= al;
      }
      const float p = __builtin_amdgcn_exp2f((sv - st.m) * EXP_C);
      st.l += p;
      SB();
      next();
      phase_v<8>(ring, p, pm, st, ldn);
      tok = token(VBURST);
      STAMP(sx); ++sx;
      if (svc) service();
      SB();
    }
    STAMP(8);
#pragma unroll 1
    while (kind == 2) {
      next();
      const float p = phase_k4(ring, qb, st, lane, ldn);
      phase_v<4>(ring, p, pm, st, ldn);
      tok = token(VBURST);
      STAMP(sx); ++sx;
    }
#pragma unroll 1
    for (int it = 0; it < SPIN_CAP && svc; ++it) {
      service();
      asm volatile("" :: "v"(pv));
    }
    LDS_FENCE();
  }
  STAMP(12);
  v4u wt[6][2], d;
  auto tailw = [&]() {
    const rsrc_t rwo = mkrs(wo + (size_t)y * 32 * D, XW ? 0 : 32 * D * 2);
    const rsrc_t rwd = mkrs(wd + (size_t)(y < 112 ? y : 0) * 16 * D, (y < 112 && !XW) ? 16 * D * 2 : 0);
    const unsigned o0 = (lane >> 3) * (D * 2) + (w - 4) * 256 + (lane & 7) * 16, o1 = w == 7 ? OOB : o0 + 128;
    SB();
#pragma unroll
    for (int k = 0; k < 6; ++k) {
      const int i = (k + 4) % 6;
      wt[i][0] = ld4<2>(i < 4 ? rwo : rwd, o0, (i & 3) * 8 * D * 2);
      wt[i][1] = ld4<2>(i < 4 ? rwo : rwd, o1, (i & 3) * 8 * D * 2);
      SB();
    }
  };
  {
    const lds4_t pw = (lds4_t)&s_part[w][lane * 4], pr = (lds4_t)&s_part[0][lane * 4], pmr = (lds4_t)&s_ml[(lane >> 3) & 1][0];
    const lds1_t pml = (lds1_t)&s_ml[(lane >> 3) & 1][w];
    pw[0] = st.a[0]; pw[64] = st.a[1];
    float l = st.l;
    l += dpp<XOR1>(l); l += dpp<XOR2>(l); l = x32_add(x16_add(l));
    pml[0] = st.m; pml[8] = l;
    if (TWK < 8) {
      if (TWK > 1) __hip_atomic_fetch_add(ctl, lane == C_DONE ? 1 : 0, __ATOMIC_RELAXED, __HIP_MEMORY_SCOPE_WORKGROUP);
      if (isy && w >= 4) {
        if (TWK > 1) while (ctl_get(C_DONE) < TWK) __builtin_amdgcn_s_sleep(1);
        tailw();
      }
    }
    LDS_FENCE(); bar(); LDS_FENCE();
    STAMP(2);
    if (TWK == 8 && isy && w >= 4) { if (TWD) __builtin_amdgcn_s_sleep(TWD); tailw(); }
    if (w == SND || w == SND + 1) {
      const int hf = w - SND;
      f32x4 ml[4], pt[8];
#pragma unroll
      for (int k = 0; k < 4; ++k) ml[k] = pmr[k];
#pragma unroll
      for (int k = 0; k < 8; ++k) pt[k] = pr[k * 128 + hf * 64];
      const v4u t = *reinterpret_cast<const v4u*>(&s_dst[4 + hf][lane * 4]);
      float M = -3.0e38f;
#pragma unroll
      for (int k = 0; k < 8; ++k) M = fmaxf(M, ml[k >> 2][k & 3]);
      f32x4 e[2] = {(ml[0] - M) * EXP_C, (ml[1] - M) * EXP_C};
      f32x4 o = {0.f, 0.f, 0.f, 0.f};
#pragma unroll
      for (int k = 0; k < 8; ++k) {
        e[k >> 2][k & 3] = __builtin_amdgcn_exp2f(e[k >> 2][k & 3]);
        o += pt[k] * e[k >> 2][k & 3];
      }
      const f32x4 l4 = ml[2] * e[0] + ml[3] * e[1];
      const float L = (l4[0] + l4[2]) + (l4[1] + l4[3]);
      v4u msg;
#pragma unroll
      for (int j = 0; j < 3; ++j) msg[j] = tag<0x100>(__builtin_amdgcn_perm(asu(o[j]), asu(o[3]), 0x07060501u + j), t[j]);
      msg[3] = tag<1>(asu((lane & 1) ? L : M), t[3]);
      __builtin_amdgcn_raw_buffer_store_b128(msg, rws, (int)mo_, hf * 4 * AREA, S1);
      SB();
    }
  }
  const v4u old = *reinterpret_cast<const v4u*>(&s_old[w][lane * 4]);
  if (isy) {
    if (w < 4) {
      if (G1) until(G1);
      STAMP(14);
#pragma unroll 1
      for (int it = 0; it < SPIN_CAP; ++it) {
        d = (G1N && now() < G1N) ? ld4<18>(rws, pa, 0) : ld4<P1>(rws, pa, 0);
        if (arrived(d, old)) break;
        __builtin_amdgcn_s_sleep(1);
        LDS_FENCE();
      }
      STAMP(15);
      d &= (v4u){~0x100u, ~0x100u, ~0x100u, ~1u};
      const float ot = dpp<XOR1>(asf(d[3]));
      const float m = (lane & 1) ? ot : asf(d[3]), l = (lane & 1) ? asf(d[3]) : ot;
      float M = fmaxf(m, dpp<ROR8>(m)); M = x16_max(M); M = x32_max(M);
      const float e = __builtin_amdgcn_exp2f((m - M) * EXP_C);
      const float v3 = asf(__builtin_amdgcn_perm(__builtin_amdgcn_perm(d[2], d[1], 0x04000c0cu), d[0], 0x0706000cu));
      float y0 = tr32(asf(d[0]) * e, asf(d[2]) * e), y1 = tr32(asf(d[1]) * e, v3 * e), L = x32_add(l * e);
      y0 = tr16(y0, y1); L = x16_add(L);
      const bool s_ = lane & 8;
      y0 = (s_ ? L : y0) + dpp<ROR8>(s_ ? y0 : L);
      if (!s_) s_o[w][(lane & 7) * 4 + (lane >> 5) * 2 + ((lane >> 4) & 1)] = y0;
      if (lane == 8) *reinterpret_cast<f32x2*>(&s_ml2[w * 2]) = (f32x2){M, y0};
      LDS_FENCE(); bar(); LDS_FENCE();
      STAMP(3);
    } else {
      const int p = (w - 4) * 32 + (lane >> 4) * 8 + (lane & 7);
      v4u dt = *reinterpret_cast<const v4u*>(&s_dst[2][p * 4]) & 1u;
      unsigned to = ((lane & 8) || p >= 112) ? OOB : my(p);
      const f32x2 hv = *reinterpret_cast<const f32x2*>(&s_h[(lane >> 3) * 2]);
      f32x4 acc[2] = {{0.f, 0.f, 0.f, 0.f}, {0.f, 0.f, 0.f, 0.f}};
      auto step = [&](int i, float a) {
        const v4u pb = pm & __builtin_bit_cast(unsigned, (bf16x2){(__bf16)a, (__bf16)a});
        acc[0] = mfma(wt[i][0], pb, acc[0]); acc[1] = mfma(wt[i][1], pb, acc[1]);
      };
      asm volatile("" : "+v"(dt), "+v"(to));
      step(4, hv.x); step(5, hv.y);
      asm volatile("" : "+v"(acc[0]), "+v"(acc[1]));
      STAMP(14);
      LDS_FENCE(); bar(); LDS_FENCE();
      STAMP(3);
      const f32x4 ma = *reinterpret_cast<const f32x4*>(&s_ml2[0]), mb = *reinterpret_cast<const f32x4*>(&s_ml2[4]);
      f32x4 pq[4];
#pragma unroll
      for (int k = 0; k < 4; ++k) pq[k] = *reinterpret_cast<const f32x4*>(&s_o[k][(lane >> 3) * 4]);
      SB();
      const float M = fmaxf(fmaxf(ma[0], ma[2]), fmaxf(mb[0], mb[2]));
      const float e0 = __builtin_amdgcn_exp2f((ma[0] - M) * EXP_C), e1_ = __builtin_amdgcn_exp2f((ma[2] - M) * EXP_C);
      const float e2 = __builtin_amdgcn_exp2f((mb[0] - M) * EXP_C), e3 = __builtin_amdgcn_exp2f((mb[2] - M) * EXP_C);
      const float inv = __builtin_amdgcn_rcpf((ma[1] * e0 + ma[3] * e1_) + (mb[1] * e2 + mb[3] * e3));
      const f32x4 av = ((pq[0] * e0 + pq[1] * e1_) + (pq[2] * e2 + pq[3] * e3)) * inv;
#pragma unroll
      for (int i = 0; i < 4; ++i) step(i, av[i]);
      f32x4 z;
#pragma unroll
      for (int v = 0; v < 4; ++v) {
        const float a0 = asf((unsigned)__builtin_amdgcn_update_dpp((int)asu(acc[0][v]), (int)asu(acc[0][v]), ROR8, 0xc, 0xf, false));
        const float a1 = asf((unsigned)__builtin_amdgcn_update_dpp((int)asu(acc[1][v]), (int)asu(acc[1][v]), ROR8, 0xc, 0xf, false));
        z[v] = tr32(a0, a1);
      }
      __builtin_amdgcn_raw_buffer_store_b128(tag4(z, dt), rws, (int)to, 0, S2);
    }
  } else if (isr) {
    const int col = (ro >> 1) * 32 + (ro & 1) * 4 + (lane & 3) * 8 + ((lane >> 2) & 3);
    const float xo = asf((unsigned)xg[col] << 16);
    if (G2) until(G2);
#pragma unroll 1
    for (int it = 0; it < SPIN_CAP; ++it) {
      d = ld4<P2>(rws, pa, 0);
      if (arrived(d, old)) break;
      __builtin_amdgcn_s_sleep(1);
      LDS_FENCE();
    }
    d &= ~1u;
    float y0 = tr32(asf(d[0]), asf(d[2])), y1 = tr32(asf(d[1]), asf(d[3]));
    y0 = tr16(y0, y1);
    y0 += dpp<ROR8>(y0); y0 += dpp<ROR4>(y0);
    if (!(lane & 12)) s_r[w][(lane & 3) * 4 + (lane >> 5) * 2 + ((lane >> 4) & 1)] = y0;
    LDS_FENCE(); bar(); LDS_FENCE();
    if (w == 0) {
      float a = s_r[lane >> 4][lane & 15] + s_r[4 + (lane >> 4)][lane & 15];
      a = x16_add(a); a = x32_add(a);
      if (lane < 16) out[col] = (u16)f2bf(xo + a);
    }
  }
  STAMP(4);
  asm volatile("" :: "v"(wa), "v"(wb));
  STAMP_OUT();
}

extern "C" void kernel_launch(void* const* d_in, const int* in_sizes, int n_in,
                              void* d_out, int out_size, void* d_ws, size_t ws_size,
                              hipStream_t stream) {
  fused_kernel<<<NWG, NT, 0, stream>>>((const u16*)d_in[0], (const u16*)d_in[1], (const u16*)d_in[2], (const u16*)d_in[3],
                                       (const u16*)d_in[4], (const u16*)d_in[6], (const u16*)d_in[7], (const u16*)d_in[8],
                                       (u16*)d_out, (char*)d_ws);
}
```

```cpp
#include <hip/hip_runtime.h>
#include <hip/hip_bf16.h>
#include <stdint.h>

#define QPOS 24
#define PACE 8
#define QTRIES 8
#define TQ 124
#define TF 129
#define TFY 127
#define TY 131
#define NP 4
#define TAU 3650
#define PP 3000
#define PL 8
#define TOFF 150
#define TOFFQ 180
#define QBURST 100000
#define PBURST 4000
#define KBURST 100000
#define VBURST 100000
#define QFIRST 24
#define OPOS 8
#define TWK 6
#define TWD 0
#define WARM 0
#define HOT 0
#define HOTA 16
#define HOTT 24
#define OA 16
#define OB 16
#define PF 16
#define SF 16
#define P1 16
#define S1 16
#define P2 16
#define S2 16
#define G1 0
#define G2 0
#define G1N 0
#define PMASK 0
#define EO 3
#define PIPE1 6
#define PIPE2 6
#define XW 0
#define SND 0
#define DIAG 0
#define LITE 0

typedef unsigned v4u __attribute__((ext_vector_type(4)));
typedef unsigned v2u __attribute__((ext_vector_type(2)));
typedef float f32x4 __attribute__((ext_vector_type(4)));
typedef float f32x2 __attribute__((ext_vector_type(2)));
typedef __bf16 bf16x2 __attribute__((ext_vector_type(2)));
typedef __bf16 bf16x8 __attribute__((ext_vector_type(8)));
typedef unsigned short u16;
typedef __amdgpu_buffer_rsrc_t rsrc_t;
typedef __attribute__((address_space(3))) f32x4* lds4_t;
typedef __attribute__((address_space(3))) v4u* ldsu_t;
typedef __attribute__((address_space(3))) float* lds1_t;

constexpr int D = 448, SK = 32768, DH = 256, F = 1792, NQ = 4096;
constexpr int NWG = 256, NT = 512;
constexpr unsigned OOB = 0x80000000u;
constexpr unsigned SENT = 0xFFFFFFFFu;
constexpr unsigned AREA = 9216;
constexpr unsigned A_O1 = 1024;
constexpr unsigned A_F = 5120;
constexpr unsigned A_Y2 = 1024;
constexpr unsigned WS_REQ = NWG * AREA;
constexpr unsigned WS_ECHO = WS_REQ + 1024;
constexpr unsigned WS_GO = WS_ECHO + 1024;
constexpr unsigned WS_Q = WS_GO + 256;
constexpr unsigned WS_END = WS_Q + 8 * 2048;
constexpr int SPIN_CAP = 1 << 18;
constexpr int C_NEXT = 0, C_TICK = 1, C_GO = 2, C_Q = 3, C_T0 = 4, C_FC = 5, C_OLD = 6, C_DONE = 7;

__device__ __forceinline__ float asf(unsigned u) { return __builtin_bit_cast(float, u); }
__device__ __forceinline__ unsigned asu(float f) { return __builtin_bit_cast(unsigned, f); }
__device__ __forceinline__ float rbf(float f) { __bf16 b = (__bf16)f; return (float)b; }
__device__ __forceinline__ unsigned f2bf(float f) { __bf16 b = (__bf16)f; return (unsigned)__builtin_bit_cast(u16, b); }
__device__ __forceinline__ float dot2(unsigned k, unsigned q, float acc) {
  return __builtin_amdgcn_fdot2_f32_bf16(__builtin_bit_cast(bf16x2, k), __builtin_bit_cast(bf16x2, q), acc, false);
}
__device__ __forceinline__ f32x2 unp(unsigned u) { f32x2 r; r.x = asf(u << 16); r.y = asf(u & 0xffff0000u); return r; }
template <int CTRL> __device__ __forceinline__ float dpp(float x) {
  return asf((unsigned)__builtin_amdgcn_update_dpp(0, (int)asu(x), CTRL, 0xf, 0xf, true));
}
constexpr int XOR1 = 0xB1, XOR2 = 0x4E, XOR7 = 0x141, ROR8 = 0x128, ROR4 = 0x124, ROR2 = 0x122, BCAST = 0x150;
constexpr int QP_EVEN = 0xA0, QP_ODD = 0xF5;
__device__ __forceinline__ float x16_add(float x) { auto s = __builtin_amdgcn_permlane16_swap(asu(x), asu(x), false, false); return asf(s[0]) + asf(s[1]); }
__device__ __forceinline__ float x32_add(float x) { auto s = __builtin_amdgcn_permlane32_swap(asu(x), asu(x), false, false); return asf(s[0]) + asf(s[1]); }
__device__ __forceinline__ float x16_max(float x) { auto s = __builtin_amdgcn_permlane16_swap(asu(x), asu(x), false, false); return fmaxf(asf(s[0]), asf(s[1])); }
__device__ __forceinline__ float x32_max(float x) { auto s = __builtin_amdgcn_permlane32_swap(asu(x), asu(x), false, false); return fmaxf(asf(s[0]), asf(s[1])); }
__device__ __forceinline__ float tr32(float a, float b) { auto s = __builtin_amdgcn_permlane32_swap(asu(a), asu(b), false, false); return asf(s[0]) + asf(s[1]); }
__device__ __forceinline__ float tr16(float a, float b) { auto s = __builtin_amdgcn_permlane16_swap(asu(a), asu(b), false, false); return asf(s[0]) + asf(s[1]); }
__device__ __forceinline__ float wave_sum(float v) {
  v += dpp<XOR1>(v); v += dpp<XOR2>(v); v += dpp<XOR7>(v); v += dpp<ROR8>(v);
  v = x16_add(v); return x32_add(v);
}
__device__ __forceinline__ float wave_max(float v) {
  v = fmaxf(v, dpp<XOR1>(v)); v = fmaxf(v, dpp<XOR2>(v)); v = fmaxf(v, dpp<XOR7>(v)); v = fmaxf(v, dpp<ROR8>(v));
  v = x16_max(v); return x32_max(v);
}
__device__ __forceinline__ rsrc_t mkrs(const void* p, unsigned bytes) { return __builtin_amdgcn_make_buffer_rsrc((void*)p, 0, (int)bytes, 0x00020000); }
__device__ __forceinline__ rsrc_t selr(bool c, rsrc_t a, rsrc_t b) { return c ? a : b; }
__device__ __forceinline__ unsigned selu(bool c, unsigned a, unsigned b) { return c ? a : b; }
template <int AUX> __device__ __forceinline__ v4u ld4(rsrc_t r, unsigned vo, unsigned so) { return __builtin_amdgcn_raw_buffer_load_b128(r, (int)vo, (int)so, AUX); }
template <int AUX> __device__ __forceinline__ v2u ld2(rsrc_t r, unsigned vo, unsigned so) { return __builtin_amdgcn_raw_buffer_load_b64(r, (int)vo, (int)so, AUX); }
__device__ __forceinline__ void st4c(v4u v, rsrc_t r, unsigned vo) { __builtin_amdgcn_raw_buffer_store_b128(v, r, (int)vo, 0, 16); }
__device__ __forceinline__ void st1c(unsigned v, rsrc_t r, unsigned vo) { __builtin_amdgcn_raw_buffer_store_b32(v, r, (int)vo, 0, 16); }
template <unsigned M> __device__ __forceinline__ unsigned tag(unsigned u, unsigned t) { return (u & ~M) | (t & M); }
__device__ __forceinline__ v4u tag4(const f32x4& v, const v4u& t) { return (v4u){tag<1>(asu(v[0]), t[0]), tag<1>(asu(v[1]), t[1]), tag<1>(asu(v[2]), t[2]), tag<1>(asu(v[3]), t[3])}; }
__device__ __forceinline__ bool arrived(const v4u& v, const v4u& o) { return __builtin_amdgcn_ballot_w64(v[0] == o[0] || v[1] == o[1] || v[2] == o[2] || v[3] == o[3]) == 0; }
__device__ __forceinline__ unsigned lowb(const v4u& v) { return __builtin_amdgcn_perm(v[1], v[0], 0x0c0c0400u) | __builtin_amdgcn_perm(v[3], v[2], 0x04000c0cu); }
#define SB() __builtin_amdgcn_sched_barrier(0)
__device__ __forceinline__ void bar() { __builtin_amdgcn_s_waitcnt(0xC07F); __builtin_amdgcn_s_barrier(); }
#define WAITV(n) __builtin_amdgcn_s_waitcnt(((n) & 15) | (((n) >> 4) << 14) | 0x0F70)
template <int V> struct IC { static constexpr int value = V; constexpr operator int() const { return V; } };
template <int A, int B, class Fn> __device__ __forceinline__ void static_for(Fn&& f) { if constexpr (A < B) { f(IC<A>{}); static_for<A + 1, B>(f); } }
#define LDS_FENCE() asm volatile("" ::: "memory")

constexpr float EXP_C = 0.0625f * 1.4426950408889634f;

struct Att {
  float m, l;
  f32x4 a[2];
};
__device__ __forceinline__ f32x4 mfma(const v4u& a, const v4u& b, const f32x4& c) {
  return __builtin_amdgcn_mfma_f32_16x16x32_bf16(__builtin_bit_cast(bf16x8, a), __builtin_bit_cast(bf16x8, b), c, 0, 0, 0);
}
__device__ __forceinline__ float red_max(float v) { v = fmaxf(v, dpp<XOR1>(v)); v = fmaxf(v, dpp<XOR2>(v)); return x32_max(x16_max(v)); }

template <int NL, class ISSUE>
__device__ __forceinline__ float scores(v4u (&kb)[8], const v4u (&qb)[8], int lane, ISSUE issue) {
  f32x4 d = {0.f, 0.f, 0.f, 0.f};
#pragma unroll
  for (int i = 0; i < NL; ++i) {
    d = mfma(kb[i], qb[i], d);
    asm volatile("" : "+v"(d));
    issue(i);
    SB();
  }
  const bool odd = lane & 1;
  return rbf((odd ? d[3] : d[0]) + dpp<XOR1>(odd ? d[1] : d[2]));
}
template <class ISSUE>
__device__ __forceinline__ float phase_k4(v4u (&kb)[8], const v4u (&qb)[8], Att& st, int lane, ISSUE issue) {
  float s = scores<4>(kb, qb, lane, issue);
  s = (lane & 2) ? -__builtin_inff() : s;
  const float mn = fmaxf(st.m, red_max(s));
  const float al = __builtin_amdgcn_exp2f((st.m - mn) * EXP_C);
  const float p = __builtin_amdgcn_exp2f((s - mn) * EXP_C);
  st.m = mn; st.l = st.l * al + p;
  st.a[0] *= al; st.a[1] *= al;
  SB();
  return p;
}
template <int I, int O, class ISSUE>
__device__ __forceinline__ void pv_step(v4u (&vb)[8], unsigned pp, const v4u& pm, Att& st, ISSUE& issue) {
  const v4u pb = pm & (unsigned)__builtin_amdgcn_update_dpp(0, (int)pp, I * 0x55, 0xf, 0xf, true);
  st.a[0] = mfma(vb[O + 2 * I], pb, st.a[0]);
  asm volatile("" : "+v"(st.a[0]));
  issue(O + 2 * I);
  SB();
  st.a[1] = mfma(vb[O + 2 * I + 1], pb, st.a[1]);
  asm volatile("" : "+v"(st.a[1]));
  issue(O + 2 * I + 1);
  SB();
}
template <int NL, class ISSUE>
__device__ __forceinline__ void phase_v(v4u (&vb)[8], float p, const v4u& pm, Att& st, ISSUE issue) {
  constexpr int O = 8 - NL;
  const unsigned pp = __builtin_bit_cast(unsigned, (bf16x2){(__bf16)p, (__bf16)p});
  pv_step<0, O>(vb, pp, pm, st, issue); pv_step<1, O>(vb, pp, pm, st, issue);
  if constexpr (NL == 8) { pv_step<2, O>(vb, pp, pm, st, issue); pv_step<3, O>(vb, pp, pm, st, issue); }
}

#define STAMP(k) do {} while (0)
#define STAMP_OUT() do {} while (0)

__device__ __forceinline__ f32x2 ln_stats(const v4u& xa, int lane) {
  f32x2 xv[4];
  float sm = 0.f;
#pragma unroll
  for (int j = 0; j < 4; ++j) { xv[j] = unp(xa[j]); sm += xv[j].x + xv[j].y; }
  const float mean = wave_sum(sm) * (1.0f / D);
  float sq = 0.f;
#pragma unroll
  for (int j = 0; j < 4; ++j) { const f32x2 d = xv[j] - mean; sq += d.x * d.x + d.y * d.y; }
  sq = lane < 56 ? sq : 0.f;
  const float var = rbf(wave_sum(sq) * (1.0f / D));
  const float sd = rbf(sqrtf(rbf(var + 1e-5f)));
  return (f32x2){rbf(mean), 1.0f / sd};
}
__device__ __forceinline__ float ln_apply(float x, f32x2 st) { return rbf(rbf(x - st.x) * st.y); }

__global__ void __launch_bounds__(NT, 2)
fused_kernel(const u16* __restrict__ xg, const u16* __restrict__ kc, const u16* __restrict__ vc, const u16* __restrict__ wq,
             const u16* __restrict__ wo, const u16* __restrict__ wg, const u16* __restrict__ wu, const u16* __restrict__ wd,
             u16* __restrict__ out, char* ws) {
  __shared__ __attribute__((aligned(16))) float s_part[8][512];
  __shared__ __attribute__((aligned(16))) float s_mv[8][64];
  __shared__ __attribute__((aligned(16))) unsigned s_q[256];
  __shared__ __attribute__((aligned(16))) unsigned s_qs[64];
  __shared__ __attribute__((aligned(16))) unsigned s_old[8][256];
  __shared__ __attribute__((aligned(16))) unsigned s_dst[8][256];
  __shared__ __attribute__((aligned(16))) float s_ml[2][16];
  __shared__ __attribute__((aligned(16))) float s_o[4][32];
  __shared__ __attribute__((aligned(16))) float s_ml2[8];
  __shared__ __attribute__((aligned(16))) float s_h[16];
  __shared__ __attribute__((aligned(16))) float s_r[8][16];
  __shared__ __attribute__((aligned(16))) float s_sc[64][32];
  __shared__ int s_ctl[64];
  const int tid = threadIdx.x, lane = tid & 63, w = __builtin_amdgcn_readfirstlane(tid >> 6);
  const int bi = blockIdx.x, h = (((bi & 1) ? bi + 5 : bi) & 6) | (bi >> 7), s = ((bi >> 3) & 15) * 2 + (bi & 1);
  const int b = h * 32 + s;
  static_assert(8 * TQ + 8 * TF + 6 * TFY + 10 * TY == 4096 && TQ - NP > 80 && TF - NP > 80 && TFY - NP > 80 && TY - NP > 80, "tickets");
  static_assert(TQ - NP <= 128 && TF - NP <= 128 && TFY - NP <= 128 && TY - NP <= 128, "tickets");
  const int nt = s < 8 ? TQ : s < 16 ? TF : s < 22 ? TFY : TY, nk = (nt - NP) >> 1;
  const int first = s * TY - min(s, 22) * (TY - TFY) - min(s, 16) * (TFY - TF) - min(s, 8) * (TF - TQ);
  asm volatile("" :: "s"(xg), "s"(kc), "s"(vc), "s"(wq), "s"(wo), "s"(wg), "s"(wu), "s"(wd), "s"(out), "s"(ws));
  STAMP(0);
  const rsrc_t rx = mkrs(xg, D * 2);
  const rsrc_t rk = mkrs(kc + (size_t)h * SK * DH, SK * DH * 2);
  const rsrc_t rv = mkrs(vc + (size_t)h * SK * DH, SK * DH * 2);
  const rsrc_t rws = mkrs(ws, WS_END);
  const unsigned l16 = lane * 16;
  auto ubase = [&](int u) -> unsigned { return (unsigned)(first + 2 * u) * 4096u; };
  const unsigned kvo = ((lane >> 1) & 7) * 512 + (lane >> 4) * 32 + (lane & 1) * 16, vvo = (lane >> 4) * 1024 + (lane & 15) * 16;
  auto ldk = [&](unsigned base, int i) { return ld4<2>(rk, kvo + (i & 3) * 128, base + (i >> 2) * 4096); };
  const unsigned area = (unsigned)b * AREA;
  auto area_of = [&](int ss) -> unsigned { return (unsigned)(h * 32 + ss) * AREA; };
  const unsigned lmask = lane < 56 ? 0u : OOB;

  const bool isq = s < 8, isf = s >= 8 && s < 22, isy = s >= 16;
  const int f = h * 14 + s - 8, y = h * 16 + s - 16, ro = h * 4 + (s >> 2);
  const bool isr = s < 16 && ((s + h) & 3) == 0 && ro < 28;
  const bool tile = isq || (isf && w < 4);
  const bool leader = (b == NWG - 1 && w == 1), member = (w == 0);

  const unsigned pa = isy ? (w < 4 ? area + A_O1 + w * 1024 + l16 : (w == 7 && y < 112) ? area + A_F + (lane & 31) * 16 : OOB) : isr ? area + A_Y2 + w * 1024 + l16 : OOB;
  const unsigned mo_ = area_of(16 + (lane & 8) + (lane >> 4)) + A_O1 + s * 128 + (lane & 7) * 16;
  const int fd = (f & ~3) + ((lane >> 3) & 3);
  const unsigned mf = ((unsigned)((fd >> 4) * 32 + 16 + (fd & 15)) * AREA + A_F + (f & 3) * 128 + (lane & 7) * 16) | (lane < 32 ? 0u : OOB);
  auto my = [&](int p) -> unsigned { return (unsigned)((p >> 4) * 32 + (p & 12) + ((-(p >> 4)) & 3)) * AREA + A_Y2 + y * 64 + (p & 3) * 16; };
  v4u eo, ed;
  auto da_of = [&]() {
    const int p = (w - 2) * 64 + lane;
    unsigned da = (isy && p < 112) ? my(p) : OOB;
    return (w & 6) == 2 ? da : (w & 6) == 4 ? mo_ + (w - 4) * 4 * AREA : (w == 6 && isf) ? mf : OOB;
  };
  if (EO == 1) { eo = ld4<OA>(rws, pa, 0); SB(); }
  if (EO == 3 || (EO == 4 && !tile)) { eo = ld4<OA>(rws, pa, 0); ed = ld4<OB>(rws, da_of(), 0); SB(); }
  if (EO == 5) { ed = ld4<OB>(rws, da_of(), 0); SB(); }
  const v4u sent = {SENT, SENT, SENT, SENT};
  unsigned poll_off = lane == 1 ? WS_GO : OOB;
  poll_off = lane == 0 ? WS_REQ + (b >> 2) * 16 : poll_off;
  poll_off = leader ? WS_ECHO + l16 : poll_off;
  v4u pv = {0u, 0u, 0u, 0u}, e1 = {0u, 0u, 0u, 0u};
  unsigned go_ref = 0;
  int* const ctl = &s_ctl[lane];
  auto ctl_get = [&](int i) { return __builtin_amdgcn_readfirstlane(__hip_atomic_load(&s_ctl[i], __ATOMIC_RELAXED, __HIP_MEMORY_SCOPE_WORKGROUP)); };
  auto ctl_set = [&](int i) { __hip_atomic_store(&s_ctl[i], 1, __ATOMIC_RELAXED, __HIP_MEMORY_SCOPE_WORKGROUP); };
  const unsigned qoff = WS_Q + h * 2048 + l16;
  v4u qv0, qv1;
  if (member) {
    qv0 = ld4<16>(rws, qoff, 0);
    qv1 = ld4<16>(rws, qoff, 1024);
    SB();
    int c = lane == C_NEXT ? (isq ? TOFFQ : TOFF) * 128 : 0;
    c = lane == C_TICK ? 40 : c;
    c = lane == C_T0 ? (int)(__builtin_amdgcn_s_memrealtime() & 0x7fffffff) : c;
    __hip_atomic_store(ctl, c, __ATOMIC_RELAXED, __HIP_MEMORY_SCOPE_WORKGROUP);
  }
  *reinterpret_cast<v4u*>(&s_sc[w * 8][lane * 4]) = sent;
  int t0 = 0;
  auto now = [&]() { return ((int)__builtin_amdgcn_s_memrealtime() - t0) & 0x7fffff; };
  const int neg = lane == C_NEXT ? 0 : (int)0x80000000;
  int inc = lane == C_NEXT ? TAU : 0;
  auto token = [&](int burst) {
    const int t = max(now() * 128 - burst, 0);
    __hip_atomic_fetch_max(ctl, t | neg, __ATOMIC_RELAXED, __HIP_MEMORY_SCOPE_WORKGROUP);
    return __hip_atomic_fetch_add(ctl, inc, __ATOMIC_RELAXED, __HIP_MEMORY_SCOPE_WORKGROUP);
  };
  auto due_of = [&](int x) { return __builtin_amdgcn_readlane(x, C_NEXT) >> 7; };
  auto until = [&](int due) {
#pragma unroll 1
    for (int it = 0; it < 4096 && now() < due; ++it) __builtin_amdgcn_s_sleep(1);
    SB();
  };
  unsigned qs0, qs1;
  auto q_there = [&]() { return __builtin_amdgcn_ballot_w64(((lowb(qv0) ^ qs0) & (lowb(qv1) ^ qs1)) != 0xFFFFFFFFu) == 0; };
  auto load_q = [&]() { qv0 = ld4<16>(rws, qoff, 0); qv1 = ld4<16>(rws, qoff, 1024); };
  auto publish_q = [&]() {
    *reinterpret_cast<v2u*>(&s_q[lane * 2]) = (v2u){__builtin_amdgcn_perm(qv0[1], qv0[0], 0x07060302u), __builtin_amdgcn_perm(qv0[3], qv0[2], 0x07060302u)};
    *reinterpret_cast<v2u*>(&s_q[128 + lane * 2]) = (v2u){__builtin_amdgcn_perm(qv1[1], qv1[0], 0x07060302u), __builtin_amdgcn_perm(qv1[3], qv1[2], 0x07060302u)};
    LDS_FENCE();
    __builtin_amdgcn_s_waitcnt(0xC07F);
    ctl_set(C_Q);
  };
  v4u pre[4][8], ring[8];
  auto unit0 = [&]() {
#pragma unroll
    for (int i = 0; i < 8; ++i) { pre[0][i] = ldk(ubase(w), i); SB(); }
    if (EO == 2) { eo = ld4<OA>(rws, pa, 0); SB(); }
  };
  SB();
  if (tile) {
    v4u tw[8];
    const int oct = isq ? 7 * w : (f & 3) * 14 + (w >> 1) * 7;
    const int stride = isq ? NQ * 2 : F * 2;
    const rsrc_t rt = mkrs(isq ? wq : (w & 1) ? wu : wg, D * stride);
    const unsigned to = (unsigned)((oct + (lane >> 3)) * 8 * stride + (isq ? h * 8 + s : f >> 2) * 128 + (lane & 7) * 16) | lmask;
    const v4u xa = ld4<0>(rx, l16, 0);
    const v4u xb = ld4<0>(rx, (unsigned)(oct + (lane >> 3)) * 16 | lmask, 0);
    SB();
#pragma unroll
    for (int i = 0; i < 8; ++i) { tw[i] = ld4<2>(rt, to, i * stride); SB(); }
    if (EO == 4) { eo = ld4<OA>(rws, pa, 0); ed = ld4<OB>(rws, da_of(), 0); SB(); }
    LDS_FENCE(); bar(); LDS_FENCE();
    t0 = ctl_get(C_T0);
    SB();
    const f32x2 ls = ln_stats(xa, lane);
    f32x2 acc[4];
#pragma unroll
    for (int i = 0; i < 8; ++i) {
      const float xn = ln_apply(asf((i & 1) ? (xb[i >> 1] & 0xffff0000u) : (xb[i >> 1] << 16)), ls);
#pragma unroll
      for (int j = 0; j < 4; ++j) acc[j] = i ? acc[j] + unp(tw[i][j]) * xn : unp(tw[i][j]) * xn;
    }
    float y0 = tr32(acc[0].x, acc[2].x), y1 = tr32(acc[0].y, acc[2].y), y2 = tr32(acc[1].x, acc[3].x), y3 = tr32(acc[1].y, acc[3].y);
    y0 = tr16(y0, y2); y1 = tr16(y1, y3);
    const bool s_ = lane & 8;
    y0 = (s_ ? y1 : y0) + dpp<ROR8>(s_ ? y0 : y1);
    s_mv[w][(lane & 7) * 8 + (lane >> 3)] = y0;
    STAMP(6);
    token(isq ? QBURST : PBURST);
    unit0();
    if (isq) {
      LDS_FENCE(); bar(); LDS_FENCE();
      if (member) {
        float a = 0.f;
#pragma unroll
        for (int k = 0; k < 8; ++k) a += s_mv[k][lane];
        s_qs[lane] = f2bf(a) << 16;
        LDS_FENCE();
        const v4u qs = *reinterpret_cast<const v4u*>(&s_qs[(lane & 15) * 4]) | (~((s & 4) ? qv1 : qv0) & 0xffffu);
        st4c(qs, rws, (lane >> 4) == (s & 3) ? qoff + (s >> 2) * 1024 : OOB);
      }
    } else {
      LDS_FENCE();
      __builtin_amdgcn_s_waitcnt(0xC07F);
      __hip_atomic_fetch_add(ctl, lane == C_FC ? 1 : 0, __ATOMIC_RELAXED, __HIP_MEMORY_SCOPE_WORKGROUP);
    }
  } else if (leader) {
    e1 = ld4<16>(rws, poll_off, 0);
    go_ref = __builtin_amdgcn_raw_buffer_load_b32(rws, (int)WS_GO, 0, 16);
  }
  SB();
  STAMP(5);
  SB();
  bool ok = !member;
  static_for<0, 40>([&](auto N) {
    constexpr int n = N;
    if constexpr (n == 0) {
      if (!tile) unit0();
    }
    if constexpr (n == 8) {
      if (!tile) {
        LDS_FENCE(); bar(); LDS_FENCE();
        t0 = ctl_get(C_T0);
        if (leader) {
          e1 += 1u;
          go_ref += 1u;
          st4c(e1, rws, WS_REQ + l16);
        }
      }
    }
    if constexpr (n > 0 && n % 8 == 0) {
      const int x = token(isq ? QBURST : PBURST);
      if constexpr (n > QFIRST) {
        if (!ok) {
          ok = q_there();
          if (ok) publish_q();
        }
      }
      if constexpr (n == OPOS + 16) {
        *reinterpret_cast<v4u*>(&s_old[w][lane * 4]) = (int)pa < 0 ? sent : eo;
        *reinterpret_cast<v4u*>(&s_dst[w][lane * 4]) = ~ed;
        LDS_FENCE();
        __hip_atomic_fetch_add(ctl, lane == C_OLD ? 1 : 0, __ATOMIC_RELAXED, __HIP_MEMORY_SCOPE_WORKGROUP);
      }
      SB();
      until(due_of(x));
      if constexpr (n == 8) {
        if (member) { qs0 = lowb(qv0); qs1 = lowb(qv1); }
      }
      if constexpr (n >= QFIRST) {
        if (!ok) load_q();
      }
      if constexpr (n == OPOS) {
        if (EO == 0 || EO == 5) eo = ld4<OA>(rws, pa, 0);
        if (EO < 3) ed = ld4<OB>(rws, da_of(), 0);
      }
      SB();
    }
    if constexpr (n == QPOS) {
      STAMP(9);
      pv = ld4<16>(rws, (member || leader) ? poll_off : OOB, 0);
      SB();
    }
    if constexpr (n >= 8) {
      if constexpr (n >= PACE) WAITV(PACE + ((n >= QPOS && n < QPOS + PACE) ? 1 : 0) + ((n >= OPOS && n < OPOS + PACE) ? (EO == 0 ? 2 : (EO == 3 || EO == 4) ? 0 : 1) : 0));
      if constexpr (n < 32) pre[n >> 3][n & 7] = ldk(ubase((n & 56) + w), n & 7); else ring[n & 7] = ldk(ubase(32 + w), n & 7);
    }
    SB();
    if constexpr (n - PACE == 7 || n - PACE == 15 || n - PACE == 23 || n - PACE == 31) STAMP(16 + (n - PACE) / 8);
  });
  inc = lane == C_TICK ? 1 : inc;
  int tok = token(KBURST);
  STAMP(10);
  bool svc = member || leader;
  unsigned seenm = 0, last_echo = 0;
  bool echoed = false;
  auto service = [&]() {
    if (leader) {
      seenm |= (pv[0] == e1[0] ? 1u : 0u) | (pv[1] == e1[1] ? 2u : 0u) | (pv[2] == e1[2] ? 4u : 0u) | (pv[3] == e1[3] ? 8u : 0u);
      svc = __builtin_amdgcn_ballot_w64(seenm != 15u) != 0;
      st1c(go_ref, rws, (lane == 0 && !svc) ? WS_GO : OOB);
    } else {
      const unsigned req = (unsigned)__builtin_amdgcn_readfirstlane((int)((b & 2) ? ((b & 1) ? pv[3] : pv[2]) : ((b & 1) ? pv[1] : pv[0])));
      const unsigned go = (unsigned)__builtin_amdgcn_readlane((int)pv[0], 1);
      go_ref = echoed ? go_ref : go;
      svc = go == (unsigned)__builtin_amdgcn_readfirstlane((int)go_ref);
      const bool rdy = ctl_get(C_OLD) == 8;
      st1c(req, rws, (lane == 0 && svc && rdy && !(echoed && req == last_echo)) ? WS_ECHO + b * 4 : OOB);
      echoed |= rdy; last_echo = req;
      if (!svc) {
        if (isf) {
          while (ctl_get(C_FC) != 4) __builtin_amdgcn_s_sleep(1);
          LDS_FENCE();
          const int j = (lane >> 3) & 3, pc = lane & 7, m = pc >> 2, col = j * 16 + (pc & 3) * 4;
          const f32x4 v = *reinterpret_cast<const f32x4*>(&s_mv[m][col]) + *reinterpret_cast<const f32x4*>(&s_mv[m + 2][col]);
          __builtin_amdgcn_raw_buffer_store_b128(tag4(v, *reinterpret_cast<const v4u*>(&s_dst[6][lane * 4])), rws, (int)mf, 0, SF);
        }
      }
    }
    pv = ld4<16>(rws, svc ? poll_off : OOB, 0);
    SB();
  };

  {
    if (!ok) {
      ok = q_there();
      if (ok) publish_q();
    }
#pragma unroll 1
    for (int it = 0; it < SPIN_CAP && !ctl_get(C_Q); ++it) {
      if (svc && it >= QTRIES) { service(); asm volatile("" :: "v"(pv)); }
      if (!ok) {
        load_q();
        ok = q_there() | !svc;
        if (ok) publish_q();
      } else {
        __builtin_amdgcn_s_sleep(1);
      }
    }
  }
  LDS_FENCE();
  v4u qb[8];
#pragma unroll
  for (int i = 0; i < 4; ++i) {
    const v4u z = {0u, 0u, 0u, 0u}, q = *reinterpret_cast<const v4u*>(&s_q[(lane & 8) * 16 + i * 32 + (lane >> 4) * 8 + (lane & 1) * 4]);
    qb[i] = (lane & 2) ? z : q; qb[4 + i] = (lane & 2) ? q : z;
  }
  v4u pm;
#pragma unroll
  for (int j = 0; j < 4; ++j) pm[j] = ((lane >> 1) & 3) == j ? ((lane & 1) ? 0xffff0000u : 0xffffu) : 0u;
  STAMP(1);
  SB();

  Att st;
  v4u wa = sent, wb = sent;
  int sx = 20;
  {
    float* const sc = &s_sc[0][(lane >> 3) * 4 + (lane & 3)];
    rsrc_t nra, nrb;
    unsigned nva, nvb, nba, nbb, nsa, nsb;
    int kind = 0, nu = 32 + w, cu;
    auto next = [&]() {
      cu = nu;
      const int t = __builtin_amdgcn_readlane(tok, C_TICK);
      const bool fx = t < 2 * nk, tw = t >= nt;
      kind = fx ? (t >= nk ? 1 : 0) : tw ? 3 : 2;
      nu = t >= nk ? t - nk : t;
      nba = (unsigned)(fx ? first + 2 * nu : tw ? 0x80000 : first + t) * 4096u;
      nbb = fx ? nba + 4096 : nba;
      nra = selr(kind == 1, rv, rk);
      nrb = selr(kind == 0, rk, rv);
      nva = selu(kind == 1, vvo, kvo); nvb = selu(kind == 0, kvo, vvo);
      nsa = selu(kind == 1, 256, 128); nsb = selu(kind == 0, 128, 256);
      until(tw ? 0 : due_of(tok));
      inc = (lane == C_NEXT && t + PL >= 2 * nk) ? PP : inc;
      SB();
    };
    auto ldn = [&](int i) { ring[i] = i < 4 ? ld4<2>(nra, nva, nba + i * nsa) : ld4<2>(nrb, nvb, nbb + (i - 4) * nsb); };
    float mx;
    if (svc) service();
    {
      const float s0 = scores<8>(pre[0], qb, lane, [&](int) {});
      const float s1 = scores<8>(pre[1], qb, lane, [&](int) {});
      const float s2 = scores<8>(pre[2], qb, lane, [&](int) {});
      const float s3 = scores<8>(pre[3], qb, lane, [&](int) {});
      sc[w * 32] = s0; sc[(8 + w) * 32] = s1; sc[(16 + w) * 32] = s2; sc[(24 + w) * 32] = s3;
      mx = fmaxf(fmaxf(s0, s1), fmaxf(s2, s3));
      SB();
      STAMP(11);
    }
#pragma unroll 1
    while (kind == 0) {
      next();
      const float sv = scores<8>(ring, qb, lane, ldn);
      sc[cu * 32] = sv;
      tok = token(KBURST);
      mx = fmaxf(mx, sv);
      STAMP(sx); ++sx;
      if (svc) service();
      SB();
    }
    STAMP(7);
    if (isy && w == 7) {
      const v4u old = *reinterpret_cast<const v4u*>(&s_old[7][lane * 4]);
      v4u d;
#pragma unroll 1
      for (int it = 0; it < SPIN_CAP; ++it) {
        d = ld4<PF>(rws, pa, 0);
        if (arrived(d, old)) break;
        __builtin_amdgcn_s_sleep(1);
        LDS_FENCE();
      }
      float hv[4];
#pragma unroll
      for (int j = 0; j < 4; ++j) {
        float g = asf(d[j] & ~1u);
        g += dpp<ROR8>(g); g = rbf(x16_add(g));
        const float u = dpp<ROR4>(g);
        hv[j] = rbf(rbf(g / (1.0f + __expf(-g))) * u);
      }
      if (lane < 4) {
#pragma unroll
        for (int j = 0; j < 4; ++j) s_h[((lane & 1) * 4 + j) * 2 + (lane >> 1)] = hv[j];
      }
      SB();
    }
    st.m = red_max(mx); st.l = 0.f;
    st.a[0] = st.a[1] = (f32x4){0.f, 0.f, 0.f, 0.f};
#pragma unroll 1
    while (kind == 1) {
      float sv;
      for (;;) {
        sv = __hip_atomic_load(&sc[nu * 32], __ATOMIC_RELAXED, __HIP_MEMORY_SCOPE_WORKGROUP);
        if (__builtin_amdgcn_ballot_w64(asu(sv) == SENT) == 0) break;
        __builtin_amdgcn_s_sleep(1);
      }
      if (__builtin_amdgcn_ballot_w64(sv > st.m) != 0) {
        const float m2 = fmaxf(st.m, red_max(sv));
        const float al = __builtin_amdgcn_exp2f((st.m - m2) * EXP_C);
        st.m = m2; st.l *= al;
        st.a[0] *= al; st.a[1] *= al;
      }
      const float p = __builtin_amdgcn_exp2f((sv - st.m) * EXP_C);
      st.l += p;
      SB();
      next();
      phase_v<8>(ring, p, pm, st, ldn);
      tok = token(VBURST);
      STAMP(sx); ++sx;
      if (svc) service();
      SB();
    }
    STAMP(8);
#pragma unroll 1
    while (kind == 2) {
      next();
      const float p = phase_k4(ring, qb, st, lane, ldn);
      phase_v<4>(ring, p, pm, st, ldn);
      tok = token(VBURST);
      STAMP(sx); ++sx;
    }
#pragma unroll 1
    for (int it = 0; it < SPIN_CAP && svc; ++it) {
      service();
      asm volatile("" :: "v"(pv));
    }
    LDS_FENCE();
  }
  STAMP(12);
  v4u wt[6][2], d;
  auto tailw = [&]() {
    const rsrc_t rwo = mkrs(wo + (size_t)y * 32 * D, XW ? 0 : 32 * D * 2);
    const rsrc_t rwd = mkrs(wd + (size_t)(y < 112 ? y : 0) * 16 * D, (y < 112 && !XW) ? 16 * D * 2 : 0);
    const unsigned o0 = (lane >> 3) * (D * 2) + (w - 4) * 256 + (lane & 7) * 16, o1 = w == 7 ? OOB : o0 + 128;
    SB();
#pragma unroll
    for (int k = 0; k < 6; ++k) {
      const int i = (k + 4) % 6;
      wt[i][0] = ld4<2>(i < 4 ? rwo : rwd, o0, (i & 3) * 8 * D * 2);
      wt[i][1] = ld4<2>(i < 4 ? rwo : rwd, o1, (i & 3) * 8 * D * 2);
      SB();
    }
  };
  {
    const lds4_t pw = (lds4_t)&s_part[w][lane * 4], pr = (lds4_t)&s_part[0][lane * 4], pmr = (lds4_t)&s_ml[(lane >> 3) & 1][0];
    const lds1_t pml = (lds1_t)&s_ml[(lane >> 3) & 1][w];
    pw[0] = st.a[0]; pw[64] = st.a[1];
    float l = st.l;
    l += dpp<XOR1>(l); l += dpp<XOR2>(l); l = x32_add(x16_add(l));
    pml[0] = st.m; pml[8] = l;
    if (TWK < 8) {
      if (TWK > 1) __hip_atomic_fetch_add(ctl, lane == C_DONE ? 1 : 0, __ATOMIC_RELAXED, __HIP_MEMORY_SCOPE_WORKGROUP);
      if (isy && w >= 4) {
        if (TWK > 1) while (ctl_get(C_DONE) < TWK) __builtin_amdgcn_s_sleep(1);
        tailw();
      }
    }
    LDS_FENCE(); bar(); LDS_FENCE();
    STAMP(2);
    if (TWK == 8 && isy && w >= 4) { if (TWD) __builtin_amdgcn_s_sleep(TWD); tailw(); }
    if (w == SND || w == SND + 1) {
      const int hf = w - SND;
      f32x4 ml[4], pt[8];
#pragma unroll
      for (int k = 0; k < 4; ++k) ml[k] = pmr[k];
#pragma unroll
      for (int k = 0; k < 8; ++k) pt[k] = pr[k * 128 + hf * 64];
      const v4u t = *reinterpret_cast<const v4u*>(&s_dst[4 + hf][lane * 4]);
      float M = -3.0e38f;
#pragma unroll
      for (int k = 0; k < 8; ++k) M = fmaxf(M, ml[k >> 2][k & 3]);
      f32x4 e[2] = {(ml[0] - M) * EXP_C, (ml[1] - M) * EXP_C};
      f32x4 o = {0.f, 0.f, 0.f, 0.f};
#pragma unroll
      for (int k = 0; k < 8; ++k) {
        e[k >> 2][k & 3] = __builtin_amdgcn_exp2f(e[k >> 2][k & 3]);
        o += pt[k] * e[k >> 2][k & 3];
      }
      const f32x4 l4 = ml[2] * e[0] + ml[3] * e[1];
      const float L = (l4[0] + l4[2]) + (l4[1] + l4[3]);
      v4u msg;
#pragma unroll
      for (int j = 0; j < 3; ++j) msg[j] = tag<0x100>(__builtin_amdgcn_perm(asu(o[j]), asu(o[3]), 0x07060501u + j), t[j]);
      msg[3] = tag<1>(asu((lane & 1) ? L : M), t[3]);
      __builtin_amdgcn_raw_buffer_store_b128(msg, rws, (int)mo_, hf * 4 * AREA, S1);
      SB();
    }
  }
  const v4u old = *reinterpret_cast<const v4u*>(&s_old[w][lane * 4]);
  if (isy) {
    if (w < 4) {
      if (G1) until(G1);
      STAMP(14);
      {
        wa = ld4<P1>(rws, pa, 0);
__builtin_amdgcn_s_sleep(PIPE1);
#pragma unroll 1
for (int it = 0; it < SPIN_CAP; ++it) {
  LDS_FENCE();
  wb = ld4<P1>(rws, pa, 0);
  SB();
  d = wa; asm volatile("" : "+v"(d));
  if (arrived(d, old)) break;
  LDS_FENCE();
  wa = ld4<P1>(rws, pa, 0);
  SB();
  d = wb; asm volatile("" : "+v"(d));
  if (arrived(d, old)) break;
        }
      }
      STAMP(15);
      d &= (v4u){~0x100u, ~0x100u, ~0x100u, ~1u};
      const float ot = dpp<XOR1>(asf(d[3]));
      const float m = (lane & 1) ? ot : asf(d[3]), l = (lane & 1) ? asf(d[3]) : ot;
      float M = fmaxf(m, dpp<ROR8>(m)); M = x16_max(M); M = x32_max(M);
      const float e = __builtin_amdgcn_exp2f((m - M) * EXP_C);
      const float v3 = asf(__builtin_amdgcn_perm(__builtin_amdgcn_perm(d[2], d[1], 0x04000c0cu), d[0], 0x0706000cu));
      float y0 = tr32(asf(d[0]) * e, asf(d[2]) * e), y1 = tr32(asf(d[1]) * e, v3 * e), L = x32_add(l * e);
      y0 = tr16(y0, y1); L = x16_add(L);
      const bool s_ = lane & 8;
      y0 = (s_ ? L : y0) + dpp<ROR8>(s_ ? y0 : L);
      if (!s_) s_o[w][(lane & 7) * 4 + (lane >> 5) * 2 + ((lane >> 4) & 1)] = y0;
      if (lane == 8) *reinterpret_cast<f32x2*>(&s_ml2[w * 2]) = (f32x2){M, y0};
      LDS_FENCE(); bar(); LDS_FENCE();
      STAMP(3);
    } else {
      const int p = (w - 4) * 32 + (lane >> 4) * 8 + (lane & 7);
      v4u dt = *reinterpret_cast<const v4u*>(&s_dst[2][p * 4]) & 1u;
      unsigned to = ((lane & 8) || p >= 112) ? OOB : my(p);
      const f32x2 hv = *reinterpret_cast<const f32x2*>(&s_h[(lane >> 3) * 2]);
      f32x4 acc[2] = {{0.f, 0.f, 0.f, 0.f}, {0.f, 0.f, 0.f, 0.f}};
      auto step = [&](int i, float a) {
        const v4u pb = pm & __builtin_bit_cast(unsigned, (bf16x2){(__bf16)a, (__bf16)a});
        acc[0] = mfma(wt[i][0], pb, acc[0]); acc[1] = mfma(wt[i][1], pb, acc[1]);
      };
      asm volatile("" : "+v"(dt), "+v"(to));
      step(4, hv.x); step(5, hv.y);
      asm volatile("" : "+v"(acc[0]), "+v"(acc[1]));
      STAMP(14);
      LDS_FENCE(); bar(); LDS_FENCE();
      STAMP(3);
      const f32x4 ma = *reinterpret_cast<const f32x4*>(&s_ml2[0]), mb = *reinterpret_cast<const f32x4*>(&s_ml2[4]);
      f32x4 pq[4];
#pragma unroll
      for (int k = 0; k < 4; ++k) pq[k] = *reinterpret_cast<const f32x4*>(&s_o[k][(lane >> 3) * 4]);
      SB();
      const float M = fmaxf(fmaxf(ma[0], ma[2]), fmaxf(mb[0], mb[2]));
      const float e0 = __builtin_amdgcn_exp2f((ma[0] - M) * EXP_C), e1_ = __builtin_amdgcn_exp2f((ma[2] - M) * EXP_C);
      const float e2 = __builtin_amdgcn_exp2f((mb[0] - M) * EXP_C), e3 = __builtin_amdgcn_exp2f((mb[2] - M) * EXP_C);
      const float inv = __builtin_amdgcn_rcpf((ma[1] * e0 + ma[3] * e1_) + (mb[1] * e2 + mb[3] * e3));
      const f32x4 av = ((pq[0] * e0 + pq[1] * e1_) + (pq[2] * e2 + pq[3] * e3)) * inv;
#pragma unroll
      for (int i = 0; i < 4; ++i) step(i, av[i]);
      f32x4 z;
#pragma unroll
      for (int v = 0; v < 4; ++v) {
        const float a0 = asf((unsigned)__builtin_amdgcn_update_dpp((int)asu(acc[0][v]), (int)asu(acc[0][v]), ROR8, 0xc, 0xf, false));
        const float a1 = asf((unsigned)__builtin_amdgcn_update_dpp((int)asu(acc[1][v]), (int)asu(acc[1][v]), ROR8, 0xc, 0xf, false));
        z[v] = tr32(a0, a1);
      }
      __builtin_amdgcn_raw_buffer_store_b128(tag4(z, dt), rws, (int)to, 0, S2);
    }
  } else if (isr) {
    const int col = (ro >> 1) * 32 + (ro & 1) * 4 + (lane & 3) * 8 + ((lane >> 2) & 3);
    const float xo = asf((unsigned)xg[col] << 16);
    if (G2) until(G2);
    {
      wa = ld4<P2>(rws, pa, 0);
__builtin_amdgcn_s_sleep(PIPE2);
#pragma unroll 1
for (int it = 0; it < SPIN_CAP; ++it) {
  LDS_FENCE();
  wb = ld4<P2>(rws, pa, 0);
  SB();
  d = wa; asm volatile("" : "+v"(d));
  if (arrived(d, old)) break;
  LDS_FENCE();
  wa = ld4<P2>(rws, pa, 0);
  SB();
  d = wb; asm volatile("" : "+v"(d));
  if (arrived(d, old)) break;
      }
    }
    d &= ~1u;
    float y0 = tr32(asf(d[0]), asf(d[2])), y1 = tr32(asf(d[1]), asf(d[3]));
    y0 = tr16(y0, y1);
    y0 += dpp<ROR8>(y0); y0 += dpp<ROR4>(y0);
    if (!(lane & 12)) s_r[w][(lane & 3) * 4 + (lane >> 5) * 2 + ((lane >> 4) & 1)] = y0;
    LDS_FENCE(); bar(); LDS_FENCE();
    if (w == 0) {
      float a = s_r[lane >> 4][lane & 15] + s_r[4 + (lane >> 4)][lane & 15];
      a = x16_add(a); a = x32_add(a);
      if (lane < 16) out[col] = (u16)f2bf(xo + a);
    }
  }
  STAMP(4);
  asm volatile("" :: "v"(wa), "v"(wb));
  STAMP_OUT();
}

extern "C" void kernel_launch(void* const* d_in, const int* in_sizes, int n_in,
                              void* d_out, int out_size, void* d_ws, size_t ws_size,
                              hipStream_t stream) {
  fused_kernel<<<NWG, NT, 0, stream>>>((const u16*)d_in[0], (const u16*)d_in[1], (const u16*)d_in[2], (const u16*)d_in[3],
                                       (const u16*)d_in[4], (const u16*)d_in[6], (const u16*)d_in[7], (const u16*)d_in[8],
                                       (u16*)d_out, (char*)d_ws);
}
```

```cpp
#define PADK 0
#include <hip/hip_runtime.h>
#include <hip/hip_bf16.h>
#include <stdint.h>

#define QPOS 24
#define PACE 8
#define QTRIES 8
#define TQ 124
#define TF 129
#define TFY 127
#define TY 131
#define NP 4
#define TAU 3650
#define PP 3000
#define PL 8
#define TOFF 150
#define TOFFQ 180
#define QBURST 100000
#define PBURST 4000
#define KBURST 100000
#define VBURST 100000
#define QFIRST 24
#define OPOS 8
#define TWK 8
#define TWD 0
#define WARM 0
#define HOT 0
#define HOTA 16
#define HOTT 24
#define OA 16
#define OB 16
#define PF 16
#define SF 16
#define P1 16
#define S1 16
#define P2 16
#define S2 16
#define G1 3900
#define G2 4000
#define G1N 0
#define PMASK 0
#define EO 3
#define PIPE1 6
#define PIPE2 6
#define PD1 2
#define PG1 6
#define PD2 2
#define PG2 6
#define PS1 0
#define PS2 0
#define WT 0
#define WX 2
#define WS_ 0
#define PA1 0
#define PA2 0
#define FB 1
#define XW 0
#define SND 0
#define DIAG 0
#define LITE 0

typedef unsigned v4u __attribute__((ext_vector_type(4)));
typedef unsigned v2u __attribute__((ext_vector_type(2)));
typedef float f32x4 __attribute__((ext_vector_type(4)));
typedef float f32x2 __attribute__((ext_vector_type(2)));
typedef __bf16 bf16x2 __attribute__((ext_vector_type(2)));
typedef __bf16 bf16x8 __attribute__((ext_vector_type(8)));
typedef unsigned short u16;
typedef __amdgpu_buffer_rsrc_t rsrc_t;
typedef __attribute__((address_space(3))) f32x4* lds4_t;
typedef __attribute__((address_space(3))) v4u* ldsu_t;
typedef __attribute__((address_space(3))) float* lds1_t;

constexpr int D = 448, SK = 32768, DH = 256, F = 1792, NQ = 4096;
constexpr int NWG = 256, NT = 512;
constexpr unsigned OOB = 0x80000000u;
constexpr unsigned SENT = 0xFFFFFFFFu;
constexpr unsigned AREA = 9216;
constexpr unsigned A_O1 = 1024;
constexpr unsigned A_F = 5120;
constexpr unsigned A_Y2 = 1024;
constexpr unsigned WS_REQ = NWG * AREA;
constexpr unsigned WS_ECHO = WS_REQ + 1024;
constexpr unsigned WS_GO = WS_ECHO + 1024;
constexpr unsigned WS_Q = WS_GO + 256;
constexpr unsigned WS_END = WS_Q + 8 * 2048;
constexpr int SPIN_CAP = 1 << 18;
constexpr int C_NEXT = 0, C_TICK = 1, C_GO = 2, C_Q = 3, C_T0 = 4, C_FC = 5, C_OLD = 6, C_DONE = 7;

__device__ __forceinline__ float asf(unsigned u) { return __builtin_bit_cast(float, u); }
__device__ __forceinline__ unsigned asu(float f) { return __builtin_bit_cast(unsigned, f); }
__device__ __forceinline__ float rbf(float f) { __bf16 b = (__bf16)f; return (float)b; }
__device__ __forceinline__ unsigned f2bf(float f) { __bf16 b = (__bf16)f; return (unsigned)__builtin_bit_cast(u16, b); }
__device__ __forceinline__ float dot2(unsigned k, unsigned q, float acc) {
  return __builtin_amdgcn_fdot2_f32_bf16(__builtin_bit_cast(bf16x2, k), __builtin_bit_cast(bf16x2, q), acc, false);
}
__device__ __forceinline__ f32x2 unp(unsigned u) { f32x2 r; r.x = asf(u << 16); r.y = asf(u & 0xffff0000u); return r; }
template <int CTRL> __device__ __forceinline__ float dpp(float x) {
  return asf((unsigned)__builtin_amdgcn_update_dpp(0, (int)asu(x), CTRL, 0xf, 0xf, true));
}
constexpr int XOR1 = 0xB1, XOR2 = 0x4E, XOR7 = 0x141, ROR8 = 0x128, ROR4 = 0x124, ROR2 = 0x122, BCAST = 0x150;
constexpr int QP_EVEN = 0xA0, QP_ODD = 0xF5;
__device__ __forceinline__ float x16_add(float x) { auto s = __builtin_amdgcn_permlane16_swap(asu(x), asu(x), false, false); return asf(s[0]) + asf(s[1]); }
__device__ __forceinline__ float x32_add(float x) { auto s = __builtin_amdgcn_permlane32_swap(asu(x), asu(x), false, false); return asf(s[0]) + asf(s[1]); }
__device__ __forceinline__ float x16_max(float x) { auto s = __builtin_amdgcn_permlane16_swap(asu(x), asu(x), false, false); return fmaxf(asf(s[0]), asf(s[1])); }
__device__ __forceinline__ float x32_max(float x) { auto s = __builtin_amdgcn_permlane32_swap(asu(x), asu(x), false, false); return fmaxf(asf(s[0]), asf(s[1])); }
__device__ __forceinline__ float tr32(float a, float b) { auto s = __builtin_amdgcn_permlane32_swap(asu(a), asu(b), false, false); return asf(s[0]) + asf(s[1]); }
__device__ __forceinline__ float tr16(float a, float b) { auto s = __builtin_amdgcn_permlane16_swap(asu(a), asu(b), false, false); return asf(s[0]) + asf(s[1]); }
__device__ __forceinline__ float wave_sum(float v) {
  v += dpp<XOR1>(v); v += dpp<XOR2>(v); v += dpp<XOR7>(v); v += dpp<ROR8>(v);
  v = x16_add(v); return x32_add(v);
}
__device__ __forceinline__ float wave_max(float v) {
  v = fmaxf(v, dpp<XOR1>(v)); v = fmaxf(v, dpp<XOR2>(v)); v = fmaxf(v, dpp<XOR7>(v)); v = fmaxf(v, dpp<ROR8>(v));
  v = x16_max(v); return x32_max(v);
}
__device__ __forceinline__ rsrc_t mkrs(const void* p, unsigned bytes) { return __builtin_amdgcn_make_buffer_rsrc((void*)p, 0, (int)bytes, 0x00020000); }
__device__ __forceinline__ rsrc_t selr(bool c, rsrc_t a, rsrc_t b) { return c ? a : b; }
__device__ __forceinline__ unsigned selu(bool c, unsigned a, unsigned b) { return c ? a : b; }
template <int AUX> __device__ __forceinline__ v4u ld4(rsrc_t r, unsigned vo, unsigned so) { return __builtin_amdgcn_raw_buffer_load_b128(r, (int)vo, (int)so, AUX); }
template <int AUX> __device__ __forceinline__ v2u ld2(rsrc_t r, unsigned vo, unsigned so) { return __builtin_amdgcn_raw_buffer_load_b64(r, (int)vo, (int)so, AUX); }
__device__ __forceinline__ void st4c(v4u v, rsrc_t r, unsigned vo) { __builtin_amdgcn_raw_buffer_store_b128(v, r, (int)vo, 0, 16); }
__device__ __forceinline__ void st1c(unsigned v, rsrc_t r, unsigned vo) { __builtin_amdgcn_raw_buffer_store_b32(v, r, (int)vo, 0, 16); }
template <unsigned M> __device__ __forceinline__ unsigned tag(unsigned u, unsigned t) { return (u & ~M) | (t & M); }
__device__ __forceinline__ v4u tag4(const f32x4& v, const v4u& t) { return (v4u){tag<1>(asu(v[0]), t[0]), tag<1>(asu(v[1]), t[1]), tag<1>(asu(v[2]), t[2]), tag<1>(asu(v[3]), t[3])}; }
__device__ __forceinline__ bool arrived(const v4u& v, const v4u& o) { return __builtin_amdgcn_ballot_w64(v[0] == o[0] || v[1] == o[1] || v[2] == o[2] || v[3] == o[3]) == 0; }
__device__ __forceinline__ unsigned lowb(const v4u& v) { return __builtin_amdgcn_perm(v[1], v[0], 0x0c0c0400u) | __builtin_amdgcn_perm(v[3], v[2], 0x04000c0cu); }
#define SB() __builtin_amdgcn_sched_barrier(0)
__device__ __forceinline__ void bar() { __builtin_amdgcn_s_waitcnt(0xC07F); __builtin_amdgcn_s_barrier(); }
#define WAITV(n) __builtin_amdgcn_s_waitcnt(((n) & 15) | (((n) >> 4) << 14) | 0x0F70)
template <int V> struct IC { static constexpr int value = V; constexpr operator int() const { return V; } };
template <int A, int B, class Fn> __device__ __forceinline__ void static_for(Fn&& f) { if constexpr (A < B) { f(IC<A>{}); static_for<A + 1, B>(f); } }
#define LDS_FENCE() asm volatile("" ::: "memory")

constexpr float EXP_C = 0.0625f * 1.4426950408889634f;

struct Att {
  float m, l;
  f32x4 a[2];
};
__device__ __forceinline__ f32x4 mfma(const v4u& a, const v4u& b, const f32x4& c) {
  return __builtin_amdgcn_mfma_f32_16x16x32_bf16(__builtin_bit_cast(bf16x8, a), __builtin_bit_cast(bf16x8, b), c, 0, 0, 0);
}
__device__ __forceinline__ float red_max(float v) { v = fmaxf(v, dpp<XOR1>(v)); v = fmaxf(v, dpp<XOR2>(v)); return x32_max(x16_max(v)); }

template <int NL, class ISSUE>
__device__ __forceinline__ float scores(v4u (&kb)[8], const v4u (&qb)[8], int lane, ISSUE issue) {
  f32x4 d = {0.f, 0.f, 0.f, 0.f};
#pragma unroll
  for (int i = 0; i < NL; ++i) {
    d = mfma(kb[i], qb[i], d);
    asm volatile("" : "+v"(d));
    issue(i);
    SB();
  }
  const bool odd = lane & 1;
  return rbf((odd ? d[3] : d[0]) + dpp<XOR1>(odd ? d[1] : d[2]));
}
template <class ISSUE>
__device__ __forceinline__ float phase_k4(v4u (&kb)[8], const v4u (&qb)[8], Att& st, int lane, ISSUE issue) {
  float s = scores<4>(kb, qb, lane, issue);
  s = (lane & 2) ? -__builtin_inff() : s;
  const float mn = fmaxf(st.m, red_max(s));
  const float al = __builtin_amdgcn_exp2f((st.m - mn) * EXP_C);
  const float p = __builtin_amdgcn_exp2f((s - mn) * EXP_C);
  st.m = mn; st.l = st.l * al + p;
  st.a[0] *= al; st.a[1] *= al;
  SB();
  return p;
}
template <int I, int O, class ISSUE>
__device__ __forceinline__ void pv_step(v4u (&vb)[8], unsigned pp, const v4u& pm, Att& st, ISSUE& issue) {
  const v4u pb = pm & (unsigned)__builtin_amdgcn_update_dpp(0, (int)pp, I * 0x55, 0xf, 0xf, true);
  st.a[0] = mfma(vb[O + 2 * I], pb, st.a[0]);
  asm volatile("" : "+v"(st.a[0]));
  issue(O + 2 * I);
  SB();
  st.a[1] = mfma(vb[O + 2 * I + 1], pb, st.a[1]);
  asm volatile("" : "+v"(st.a[1]));
  issue(O + 2 * I + 1);
  SB();
}
template <int NL, class ISSUE>
__device__ __forceinline__ void phase_v(v4u (&vb)[8], float p, const v4u& pm, Att& st, ISSUE issue) {
  constexpr int O = 8 - NL;
  const unsigned pp = __builtin_bit_cast(unsigned, (bf16x2){(__bf16)p, (__bf16)p});
  pv_step<0, O>(vb, pp, pm, st, issue); pv_step<1, O>(vb, pp, pm, st, issue);
  if constexpr (NL == 8) { pv_step<2, O>(vb, pp, pm, st, issue); pv_step<3, O>(vb, pp, pm, st, issue); }
}

template <int N, int GAP, int SL>
__device__ __forceinline__ v4u poll(const v4u& r, unsigned pa, const v4u& old) {
  v4u d;
  unsigned long long t0, t1, t2;
  int n = SPIN_CAP;
  asm volatile(
      "s_nop 4\n\t"
      "buffer_load_dwordx4 v[200:203], %5, %6, 0 offen sc1\n\t"
      ".rept %11\n\t"
      "s_sleep %12\n\t"
      "buffer_load_dwordx4 v[200:203], %5, %6, 0 offen sc1\n\t"
      ".endr\n"
      "1:\n\t"
      "s_waitcnt vmcnt(%11)\n\t"
      "v_cmp_eq_u32 vcc, v200, %7\n\t"
      "v_cmp_eq_u32 %1, v201, %8\n\t"
      "v_cmp_eq_u32 %2, v202, %9\n\t"
      "v_cmp_eq_u32 %3, v203, %10\n\t"
      "s_or_b64 vcc, vcc, %1\n\t"
      "s_or_b64 %2, %2, %3\n\t"
      "s_or_b64 exec, vcc, %2\n\t"
      "s_cbranch_scc0 2f\n\t"
      ".if %13\n\ts_sleep %13\n\t.endif\n\t"
      "buffer_load_dwordx4 v[200:203], %5, %6, 0 offen sc1\n\t"
      "s_sub_u32 %4, %4, 1\n\t"
      "s_cbranch_scc0 1b\n"
      "2:\n\t"
      "s_mov_b64 exec, -1"
      : "=&{v[200:203]}"(d), "=&s"(t0), "=&s"(t1), "=&s"(t2), "+s"(n)
      : "v"(pa), "s"(r), "v"(old[0]), "v"(old[1]), "v"(old[2]), "v"(old[3]), "n"(N - 1), "n"(GAP), "n"(SL)
      : "vcc", "scc", "memory");
  return d;
}
__device__ __forceinline__ void poll_end(const v4u& d) { asm volatile("s_waitcnt vmcnt(0)" :: "{v[200:203]}"(d)); }

#define STAMP(k) do { if ((k) == PADK) { SB(); tpad = __builtin_amdgcn_s_memrealtime(); SB(); } } while (0)
#define STAMP_OUT() do { while (__builtin_amdgcn_s_memrealtime() - tpad < 6000) __builtin_amdgcn_s_sleep(1); } while (0)

__device__ __forceinline__ f32x2 ln_stats(const v4u& xa, int lane) {
  f32x2 xv[4];
  float sm = 0.f;
#pragma unroll
  for (int j = 0; j < 4; ++j) { xv[j] = unp(xa[j]); sm += xv[j].x + xv[j].y; }
  const float mean = wave_sum(sm) * (1.0f / D);
  float sq = 0.f;
#pragma unroll
  for (int j = 0; j < 4; ++j) { const f32x2 d = xv[j] - mean; sq += d.x * d.x + d.y * d.y; }
  sq = lane < 56 ? sq : 0.f;
  const float var = rbf(wave_sum(sq) * (1.0f / D));
  const float sd = rbf(sqrtf(rbf(var + 1e-5f)));
  return (f32x2){rbf(mean), 1.0f / sd};
}
__device__ __forceinline__ float ln_apply(float x, f32x2 st) { return rbf(rbf(x - st.x) * st.y); }

__global__ void __launch_bounds__(NT, 2)
fused_kernel(const u16* __restrict__ xg, const u16* __restrict__ kc, const u16* __restrict__ vc, const u16* __restrict__ wq,
             const u16* __restrict__ wo, const u16* __restrict__ wg, const u16* __restrict__ wu, const u16* __restrict__ wd,
             u16* __restrict__ out, char* ws) {
  __shared__ __attribute__((aligned(16))) float s_part[8][512];
  __shared__ __attribute__((aligned(16))) float s_mv[8][64];
  __shared__ __attribute__((aligned(16))) unsigned s_q[256];
  __shared__ __attribute__((aligned(16))) unsigned s_qs[64];
  __shared__ __attribute__((aligned(16))) unsigned s_old[8][256];
  __shared__ __attribute__((aligned(16))) unsigned s_dst[8][256];
  __shared__ __attribute__((aligned(16))) float s_ml[2][16];
  __shared__ __attribute__((aligned(16))) float s_o[4][32];
  __shared__ __attribute__((aligned(16))) float s_ml2[16];
  __shared__ __attribute__((aligned(16))) float s_h[16];
  __shared__ __attribute__((aligned(16))) float s_r[9][16];
  __shared__ __attribute__((aligned(16))) float s_sc[64][32];
  __shared__ int s_ctl[64];
  const int tid = threadIdx.x, lane = tid & 63, w = __builtin_amdgcn_readfirstlane(tid >> 6);
  const int bi = blockIdx.x, h = (((bi & 1) ? bi + 5 : bi) & 6) | (bi >> 7), s = ((bi >> 3) & 15) * 2 + (bi & 1);
  const int b = h * 32 + s;
  static_assert(8 * TQ + 8 * TF + 6 * TFY + 10 * TY == 4096 && TQ - NP > 80 && TF - NP > 80 && TFY - NP > 80 && TY - NP > 80, "tickets");
  static_assert(TQ - NP <= 128 && TF - NP <= 128 && TFY - NP <= 128 && TY - NP <= 128, "tickets");
  const int nt = s < 8 ? TQ : s < 16 ? TF : s < 22 ? TFY : TY, nk = (nt - NP) >> 1;
  const int first = s * TY - min(s, 22) * (TY - TFY) - min(s, 16) * (TFY - TF) - min(s, 8) * (TF - TQ);
  unsigned long long tpad = 0;
  asm volatile("" :: "s"(xg), "s"(kc), "s"(vc), "s"(wq), "s"(wo), "s"(wg), "s"(wu), "s"(wd), "s"(out), "s"(ws));
  STAMP(0);
  const rsrc_t rx = mkrs(xg, D * 2);
  const rsrc_t rk = mkrs(kc + (size_t)h * SK * DH, SK * DH * 2);
  const rsrc_t rv = mkrs(vc + (size_t)h * SK * DH, SK * DH * 2);
  const rsrc_t rws = mkrs(ws, WS_END);
  const unsigned l16 = lane * 16;
  auto ubase = [&](int u) -> unsigned { return (unsigned)(first + 2 * u) * 4096u; };
  const unsigned kvo = ((lane >> 1) & 7) * 512 + (lane >> 4) * 32 + (lane & 1) * 16, vvo = (lane >> 4) * 1024 + (lane & 15) * 16;
  auto ldk = [&](unsigned base, int i) { return ld4<2>(rk, kvo + (i & 3) * 128, base + (i >> 2) * 4096); };
  const unsigned area = (unsigned)b * AREA;
  auto area_of = [&](int ss) -> unsigned { return (unsigned)(h * 32 + ss) * AREA; };
  const unsigned lmask = lane < 56 ? 0u : OOB;

  const bool isq = s < 8, isf = s >= 8 && s < 22, isy = s >= 16;
  const int f = h * 14 + s - 8, y = h * 16 + s - 16, ro = h * 4 + (s >> 2);
  const bool isr = s < 16 && ((s + h) & 3) == 0 && ro < 28;
  const bool tile = isq || (isf && w < 4);
  const bool leader = (b == NWG - 1 && w == 1), member = (w == 0);

  const unsigned pa = isy ? (w < 4 ? area + A_O1 + w * 1024 + l16 : (w == 7 && y < 112) ? area + A_F + (lane & 31) * 16 : OOB) : isr ? area + A_Y2 + w * 1024 + l16 : OOB;
  const unsigned mo_ = area_of(16 + (lane & 8) + (lane >> 4)) + A_O1 + s * 128 + (lane & 7) * 16;
  const int fd = (f & ~3) + ((lane >> 3) & 3);
  const unsigned mf = ((unsigned)((fd >> 4) * 32 + 16 + (fd & 15)) * AREA + A_F + (f & 3) * 128 + (lane & 7) * 16) | (lane < 32 ? 0u : OOB);
  auto my = [&](int p) -> unsigned { return (unsigned)((p >> 4) * 32 + (p & 12) + ((-(p >> 4)) & 3)) * AREA + A_Y2 + y * 64 + (p & 3) * 16; };
  v4u eo, ed;
  auto da_of = [&]() {
    const int p = (w - 2) * 64 + lane;
    unsigned da = (isy && p < 112) ? my(p) : OOB;
    return (w & 6) == 2 ? da : (w & 6) == 4 ? mo_ + (w - 4) * 4 * AREA : (w == 6 && isf) ? mf : OOB;
  };
  if (EO == 1) { eo = ld4<OA>(rws, pa, 0); SB(); }
  if (EO == 3 || (EO == 4 && !tile)) { eo = ld4<OA>(rws, pa, 0); ed = ld4<OB>(rws, da_of(), 0); SB(); }
  if (EO == 5) { ed = ld4<OB>(rws, da_of(), 0); SB(); }
  const v4u sent = {SENT, SENT, SENT, SENT};
  unsigned poll_off = lane == 1 ? WS_GO : OOB;
  poll_off = lane == 0 ? WS_REQ + (b >> 2) * 16 : poll_off;
  poll_off = leader ? WS_ECHO + l16 : poll_off;
  v4u pv = {0u, 0u, 0u, 0u}, e1 = {0u, 0u, 0u, 0u};
  unsigned go_ref = 0;
  int* const ctl = &s_ctl[lane];
  auto ctl_get = [&](int i) { return __builtin_amdgcn_readfirstlane(__hip_atomic_load(&s_ctl[i], __ATOMIC_RELAXED, __HIP_MEMORY_SCOPE_WORKGROUP)); };
  auto ctl_set = [&](int i) { __hip_atomic_store(&s_ctl[i], 1, __ATOMIC_RELAXED, __HIP_MEMORY_SCOPE_WORKGROUP); };
  const unsigned qoff = WS_Q + h * 2048 + l16;
  v4u qv0, qv1;
  if (member) {
    qv0 = ld4<16>(rws, qoff, 0);
    qv1 = ld4<16>(rws, qoff, 1024);
    SB();
    int c = lane == C_NEXT ? (isq ? TOFFQ : TOFF) * 128 : 0;
    c = lane == C_TICK ? 40 : c;
    c = lane == C_T0 ? (int)(__builtin_amdgcn_s_memrealtime() & 0x7fffffff) : c;
    __hip_atomic_store(ctl, c, __ATOMIC_RELAXED, __HIP_MEMORY_SCOPE_WORKGROUP);
  }
  *reinterpret_cast<v4u*>(&s_sc[w * 8][lane * 4]) = sent;
  int t0 = 0;
  auto now = [&]() { return ((int)__builtin_amdgcn_s_memrealtime() - t0) & 0x7fffff; };
  const int neg = lane == C_NEXT ? 0 : (int)0x80000000;
  int inc = lane == C_NEXT ? TAU : 0;
  auto token = [&](int burst) {
    const int t = max(now() * 128 - burst, 0);
    __hip_atomic_fetch_max(ctl, t | neg, __ATOMIC_RELAXED, __HIP_MEMORY_SCOPE_WORKGROUP);
    return __hip_atomic_fetch_add(ctl, inc, __ATOMIC_RELAXED, __HIP_MEMORY_SCOPE_WORKGROUP);
  };
  auto due_of = [&](int x) { return __builtin_amdgcn_readlane(x, C_NEXT) >> 7; };
  auto until = [&](int due) {
#pragma unroll 1
    for (int it = 0; it < 4096 && now() < due; ++it) __builtin_amdgcn_s_sleep(1);
    SB();
  };
  unsigned qs0, qs1;
  auto q_there = [&]() { return __builtin_amdgcn_ballot_w64(((lowb(qv0) ^ qs0) & (lowb(qv1) ^ qs1)) != 0xFFFFFFFFu) == 0; };
  auto load_q = [&]() { qv0 = ld4<16>(rws, qoff, 0); qv1 = ld4<16>(rws, qoff, 1024); };
  auto publish_q = [&]() {
    *reinterpret_cast<v2u*>(&s_q[lane * 2]) = (v2u){__builtin_amdgcn_perm(qv0[1], qv0[0], 0x07060302u), __builtin_amdgcn_perm(qv0[3], qv0[2], 0x07060302u)};
    *reinterpret_cast<v2u*>(&s_q[128 + lane * 2]) = (v2u){__builtin_amdgcn_perm(qv1[1], qv1[0], 0x07060302u), __builtin_amdgcn_perm(qv1[3], qv1[2], 0x07060302u)};
    LDS_FENCE();
    __builtin_amdgcn_s_waitcnt(0xC07F);
    ctl_set(C_Q);
  };
  v4u pre[4][8], ring[8];
  auto unit0 = [&]() {
#pragma unroll
    for (int i = 0; i < 8; ++i) { pre[0][i] = ldk(ubase(w), i); SB(); }
    if (EO == 2) { eo = ld4<OA>(rws, pa, 0); SB(); }
  };
  SB();
  if (tile) {
    v4u tw[8];
    const int oct = isq ? 7 * w : (f & 3) * 14 + (w >> 1) * 7;
    const int stride = isq ? NQ * 2 : F * 2;
    const rsrc_t rt = mkrs(isq ? wq : (w & 1) ? wu : wg, D * stride);
    const unsigned to = (unsigned)((oct + (lane >> 3)) * 8 * stride + (isq ? h * 8 + s : f >> 2) * 128 + (lane & 7) * 16) | lmask;
    const v4u xa = ld4<0>(rx, l16, 0);
    const v4u xb = ld4<0>(rx, (unsigned)(oct + (lane >> 3)) * 16 | lmask, 0);
    SB();
#pragma unroll
    for (int i = 0; i < 8; ++i) { tw[i] = ld4<2>(rt, to, i * stride); SB(); }
    if (EO == 4) { eo = ld4<OA>(rws, pa, 0); ed = ld4<OB>(rws, da_of(), 0); SB(); }
    LDS_FENCE(); bar(); LDS_FENCE();
    t0 = ctl_get(C_T0);
    SB();
    const f32x2 ls = ln_stats(xa, lane);
    f32x2 acc[4];
#pragma unroll
    for (int i = 0; i < 8; ++i) {
      const float xn = ln_apply(asf((i & 1) ? (xb[i >> 1] & 0xffff0000u) : (xb[i >> 1] << 16)), ls);
#pragma unroll
      for (int j = 0; j < 4; ++j) acc[j] = i ? acc[j] + unp(tw[i][j]) * xn : unp(tw[i][j]) * xn;
    }
    float y0 = tr32(acc[0].x, acc[2].x), y1 = tr32(acc[0].y, acc[2].y), y2 = tr32(acc[1].x, acc[3].x), y3 = tr32(acc[1].y, acc[3].y);
    y0 = tr16(y0, y2); y1 = tr16(y1, y3);
    const bool s_ = lane & 8;
    y0 = (s_ ? y1 : y0) + dpp<ROR8>(s_ ? y0 : y1);
    s_mv[w][(lane & 7) * 8 + (lane >> 3)] = y0;
    STAMP(6);
    token(isq ? QBURST : PBURST);
    unit0();
    if (isq) {
      LDS_FENCE(); bar(); LDS_FENCE();
      if (member) {
        float a = 0.f;
#pragma unroll
        for (int k = 0; k < 8; ++k) a += s_mv[k][lane];
        s_qs[lane] = f2bf(a) << 16;
        LDS_FENCE();
        const v4u qs = *reinterpret_cast<const v4u*>(&s_qs[(lane & 15) * 4]) | (~((s & 4) ? qv1 : qv0) & 0xffffu);
        st4c(qs, rws, (lane >> 4) == (s & 3) ? qoff + (s >> 2) * 1024 : OOB);
      }
    } else {
      LDS_FENCE();
      __builtin_amdgcn_s_waitcnt(0xC07F);
      __hip_atomic_fetch_add(ctl, lane == C_FC ? 1 : 0, __ATOMIC_RELAXED, __HIP_MEMORY_SCOPE_WORKGROUP);
    }
  } else if (leader) {
    e1 = ld4<16>(rws, poll_off, 0);
    go_ref = __builtin_amdgcn_raw_buffer_load_b32(rws, (int)WS_GO, 0, 16);
  }
  SB();
  STAMP(5);
  SB();
  bool ok = !member;
  static_for<0, 40>([&](auto N) {
    constexpr int n = N;
    if constexpr (n == 0) {
      if (!tile) unit0();
    }
    if constexpr (n == 8) {
      if (!tile) {
        LDS_FENCE(); bar(); LDS_FENCE();
        t0 = ctl_get(C_T0);
        if (leader) {
          e1 += 1u;
          go_ref += 1u;
          st4c(e1, rws, WS_REQ + l16);
        }
      }
    }
    if constexpr (n > 0 && n % 8 == 0) {
      const int x = token(isq ? QBURST : PBURST);
      if constexpr (n > QFIRST) {
        if (!ok) {
          ok = q_there();
          if (ok) publish_q();
        }
      }
      if constexpr (n == OPOS + 16) {
        *reinterpret_cast<v4u*>(&s_old[w][lane * 4]) = (int)pa < 0 ? sent : eo;
        *reinterpret_cast<v4u*>(&s_dst[w][lane * 4]) = ~ed;
        LDS_FENCE();
        __hip_atomic_fetch_add(ctl, lane == C_OLD ? 1 : 0, __ATOMIC_RELAXED, __HIP_MEMORY_SCOPE_WORKGROUP);
      }
      SB();
      until(due_of(x));
      if constexpr (n == 8) {
        if (member) { qs0 = lowb(qv0); qs1 = lowb(qv1); }
      }
      if constexpr (n >= QFIRST) {
        if (!ok) load_q();
      }
      if constexpr (n == OPOS) {
        if (EO == 0 || EO == 5) eo = ld4<OA>(rws, pa, 0);
        if (EO < 3) ed = ld4<OB>(rws, da_of(), 0);
      }
      SB();
    }
    if constexpr (n == QPOS) {
      STAMP(9);
      pv = ld4<16>(rws, (member || leader) ? poll_off : OOB, 0);
      SB();
    }
    if constexpr (n >= 8) {
      if constexpr (n >= PACE) WAITV(PACE + ((n >= QPOS && n < QPOS + PACE) ? 1 : 0) + ((n >= OPOS && n < OPOS + PACE) ? (EO == 0 ? 2 : (EO == 3 || EO == 4) ? 0 : 1) : 0));
      if constexpr (n < 32) pre[n >> 3][n & 7] = ldk(ubase((n & 56) + w), n & 7); else ring[n & 7] = ldk(ubase(32 + w), n & 7);
    }
    SB();
    if constexpr (n - PACE == 7 || n - PACE == 15 || n - PACE == 23 || n - PACE == 31) STAMP(16 + (n - PACE) / 8);
  });
  inc = lane == C_TICK ? 1 : inc;
  int tok = token(KBURST);
  STAMP(10);
  bool svc = member || leader;
  unsigned seenm = 0, last_echo = 0;
  bool echoed = false;
  auto service = [&]() {
    if (leader) {
      seenm |= (pv[0] == e1[0] ? 1u : 0u) | (pv[1] == e1[1] ? 2u : 0u) | (pv[2] == e1[2] ? 4u : 0u) | (pv[3] == e1[3] ? 8u : 0u);
      svc = __builtin_amdgcn_ballot_w64(seenm != 15u) != 0;
      st1c(go_ref, rws, (lane == 0 && !svc) ? WS_GO : OOB);
    } else {
      const unsigned req = (unsigned)__builtin_amdgcn_readfirstlane((int)((b & 2) ? ((b & 1) ? pv[3] : pv[2]) : ((b & 1) ? pv[1] : pv[0])));
      const unsigned go = (unsigned)__builtin_amdgcn_readlane((int)pv[0], 1);
      go_ref = echoed ? go_ref : go;
      svc = go == (unsigned)__builtin_amdgcn_readfirstlane((int)go_ref);
      const bool rdy = ctl_get(C_OLD) == 8;
      st1c(req, rws, (lane == 0 && svc && rdy && !(echoed && req == last_echo)) ? WS_ECHO + b * 4 : OOB);
      echoed |= rdy; last_echo = req;
      if (!svc) {
        if (isf) {
          while (ctl_get(C_FC) != 4) __builtin_amdgcn_s_sleep(1);
          LDS_FENCE();
          const int j = (lane >> 3) & 3, pc = lane & 7, m = pc >> 2, col = j * 16 + (pc & 3) * 4;
          const f32x4 v = *reinterpret_cast<const f32x4*>(&s_mv[m][col]) + *reinterpret_cast<const f32x4*>(&s_mv[m + 2][col]);
          __builtin_amdgcn_raw_buffer_store_b128(tag4(v, *reinterpret_cast<const v4u*>(&s_dst[6][lane * 4])), rws, (int)mf, 0, SF);
        }
      }
    }
    pv = ld4<16>(rws, svc ? poll_off : OOB, 0);
    SB();
  };

  {
    if (!ok) {
      ok = q_there();
      if (ok) publish_q();
    }
#pragma unroll 1
    for (int it = 0; it < SPIN_CAP && !ctl_get(C_Q); ++it) {
      if (svc && it >= QTRIES) { service(); asm volatile("" :: "v"(pv)); }
      if (!ok) {
        load_q();
        ok = q_there() | !svc;
        if (ok) publish_q();
      } else {
        __builtin_amdgcn_s_sleep(1);
      }
    }
  }
  LDS_FENCE();
  v4u qb[8];
#pragma unroll
  for (int i = 0; i < 4; ++i) {
    const v4u z = {0u, 0u, 0u, 0u}, q = *reinterpret_cast<const v4u*>(&s_q[(lane & 8) * 16 + i * 32 + (lane >> 4) * 8 + (lane & 1) * 4]);
    qb[i] = (lane & 2) ? z : q; qb[4 + i] = (lane & 2) ? q : z;
  }
  v4u pm;
#pragma unroll
  for (int j = 0; j < 4; ++j) pm[j] = ((lane >> 1) & 3) == j ? ((lane & 1) ? 0xffff0000u : 0xffffu) : 0u;
  STAMP(1);
  SB();

  Att st;
  v4u wa = sent, wb = sent;
  int sx = 20;
  {
    float* const sc = &s_sc[0][(lane >> 3) * 4 + (lane & 3)];
    rsrc_t nra, nrb;
    unsigned nva, nvb, nba, nbb, nsa, nsb;
    int kind = 0, nu = 32 + w, cu;
    auto next = [&]() {
      cu = nu;
      const int t = __builtin_amdgcn_readlane(tok, C_TICK);
      const bool fx = t < 2 * nk, tw = t >= nt;
      kind = fx ? (t >= nk ? 1 : 0) : tw ? 3 : 2;
      nu = t >= nk ? t - nk : t;
      nba = (unsigned)(fx ? first + 2 * nu : tw ? 0x80000 : first + t) * 4096u;
      nbb = fx ? nba + 4096 : nba;
      nra = selr(kind == 1, rv, rk);
      nrb = selr(kind == 0, rk, rv);
      nva = selu(kind == 1, vvo, kvo); nvb = selu(kind == 0, kvo, vvo);
      nsa = selu(kind == 1, 256, 128); nsb = selu(kind == 0, 128, 256);
      until(tw ? 0 : due_of(tok));
      inc = (lane == C_NEXT && t + PL >= 2 * nk) ? PP : inc;
      SB();
    };
    auto ldn = [&](int i) { ring[i] = i < 4 ? ld4<2>(nra, nva, nba + i * nsa) : ld4<2>(nrb, nvb, nbb + (i - 4) * nsb); };
    float mx;
    if (svc) service();
    {
      const float s0 = scores<8>(pre[0], qb, lane, [&](int) {});
      const float s1 = scores<8>(pre[1], qb, lane, [&](int) {});
      const float s2 = scores<8>(pre[2], qb, lane, [&](int) {});
      const float s3 = scores<8>(pre[3], qb, lane, [&](int) {});
      sc[w * 32] = s0; sc[(8 + w) * 32] = s1; sc[(16 + w) * 32] = s2; sc[(24 + w) * 32] = s3;
      mx = fmaxf(fmaxf(s0, s1), fmaxf(s2, s3));
      SB();
      STAMP(11);
    }
#pragma unroll 1
    while (kind == 0) {
      next();
      const float sv = scores<8>(ring, qb, lane, ldn);
      sc[cu * 32] = sv;
      tok = token(KBURST);
      mx = fmaxf(mx, sv);
      STAMP(sx); ++sx;
      if (svc) service();
      SB();
    }
    STAMP(7);
    if (isy && w == 7) {
      const v4u old = *reinterpret_cast<const v4u*>(&s_old[7][lane * 4]);
      v4u d;
#pragma unroll 1
      for (int it = 0; it < SPIN_CAP; ++it) {
        d = ld4<PF>(rws, pa, 0);
        if (arrived(d, old)) break;
        __builtin_amdgcn_s_sleep(1);
        LDS_FENCE();
      }
      float hv[4];
#pragma unroll
      for (int j = 0; j < 4; ++j) {
        float g = asf(d[j] & ~1u);
        g += dpp<ROR8>(g); g = rbf(x16_add(g));
        const float u = dpp<ROR4>(g);
        hv[j] = rbf(rbf(g / (1.0f + __expf(-g))) * u);
      }
      if (lane < 4) {
#pragma unroll
        for (int j = 0; j < 4; ++j) s_h[((lane & 1) * 4 + j) * 2 + (lane >> 1)] = hv[j];
      }
      SB();
    }
    st.m = red_max(mx); st.l = 0.f;
    st.a[0] = st.a[1] = (f32x4){0.f, 0.f, 0.f, 0.f};
#pragma unroll 1
    while (kind == 1) {
      float sv;
      for (;;) {
        sv = __hip_atomic_load(&sc[nu * 32], __ATOMIC_RELAXED, __HIP_MEMORY_SCOPE_WORKGROUP);
        if (__builtin_amdgcn_ballot_w64(asu(sv) == SENT) == 0) break;
        __builtin_amdgcn_s_sleep(1);
      }
      if (__builtin_amdgcn_ballot_w64(sv > st.m) != 0) {
        const float m2 = fmaxf(st.m, red_max(sv));
        const float al = __builtin_amdgcn_exp2f((st.m - m2) * EXP_C);
        st.m = m2; st.l *= al;
        st.a[0] *= al; st.a[1] *= al;
      }
      const float p = __builtin_amdgcn_exp2f((sv - st.m) * EXP_C);
      st.l += p;
      SB();
      next();
      phase_v<8>(ring, p, pm, st, ldn);
      tok = token(VBURST);
      STAMP(sx); ++sx;
      if (svc) service();
      SB();
    }
    STAMP(8);
#pragma unroll 1
    while (kind == 2) {
      next();
      const float p = phase_k4(ring, qb, st, lane, ldn);
      phase_v<4>(ring, p, pm, st, ldn);
      tok = token(VBURST);
      STAMP(sx); ++sx;
    }
#pragma unroll 1
    for (int it = 0; it < SPIN_CAP && svc; ++it) {
      service();
      asm volatile("" :: "v"(pv));
    }
    LDS_FENCE();
  }
  STAMP(12);
  asm volatile("" :: "v"(wa), "v"(wb));
  const lds4_t pr = (lds4_t)&s_part[0][lane * 4], pmr = (lds4_t)&s_ml[(lane >> 3) & 1][0];
  {
    const lds4_t pw = (lds4_t)&s_part[w][lane * 4];
    const lds1_t pml = (lds1_t)&s_ml[(lane >> 3) & 1][w];
    pw[0] = st.a[0]; pw[64] = st.a[1];
    float l = st.l;
    l += dpp<XOR1>(l); l += dpp<XOR2>(l); l = x32_add(x16_add(l));
    pml[0] = st.m; pml[8] = l;
  }
  if (isy && w >= 4) {
    v4u wt[6][2];
    const rsrc_t rwo = mkrs(wo + (size_t)y * 32 * D, XW ? 0 : 32 * D * 2);
    const rsrc_t rwd = mkrs(wd + (size_t)(y < 112 ? y : 0) * 16 * D, (y < 112 && !XW) ? 16 * D * 2 : 0);
    unsigned o0 = (lane >> 3) * (D * 2) + (w - 4) * 256 + (lane & 7) * 16, o1 = w == 7 ? OOB : o0 + 128;
    asm volatile("" : "+v"(o0), "+v"(o1));
    SB();
    LDS_FENCE(); bar(); LDS_FENCE();
    STAMP(2);
    if (TWD) __builtin_amdgcn_s_sleep(TWD);
    SB();
#pragma unroll
    for (int k = 0; k < 6; ++k) {
      const int i = (k + 4) % 6;
      wt[i][0] = ld4<2>(i < 4 ? rwo : rwd, o0, (i & 3) * 8 * D * 2);
      wt[i][1] = ld4<2>(i < 4 ? rwo : rwd, o1, (i & 3) * 8 * D * 2);
      SB();
    }
    const int p = (w - 4) * 32 + (lane >> 4) * 8 + (lane & 7);
    v4u dt = *reinterpret_cast<const v4u*>(&s_dst[2][p * 4]) & 1u;
    unsigned to = ((lane & 8) || p >= 112) ? OOB : my(p);
    const f32x2 hv = *reinterpret_cast<const f32x2*>(&s_h[(lane >> 3) * 2]);
    f32x4 acc[2] = {{0.f, 0.f, 0.f, 0.f}, {0.f, 0.f, 0.f, 0.f}};
    auto bop = [&](float a) -> v4u { return pm & __builtin_bit_cast(unsigned, (bf16x2){(__bf16)a, (__bf16)a}); };
    auto step = [&](int i, const v4u& pb) { acc[0] = mfma(wt[i][0], pb, acc[0]); acc[1] = mfma(wt[i][1], pb, acc[1]); };
    v4u h0 = bop(hv.x), h1 = bop(hv.y);
    lds4_t po = (lds4_t)&s_o[0][(lane >> 3) * 4];
    asm volatile("" : "+v"(dt), "+v"(to), "+v"(h0), "+v"(h1), "+v"(po));
    SB();
    if (FB == 0) {
      step(4, h0); step(5, h1);
      asm volatile("" : "+v"(acc[0]), "+v"(acc[1]));
    }
    STAMP(14);
    LDS_FENCE(); bar(); LDS_FENCE();
    STAMP(3);
    const f32x4 ma = *reinterpret_cast<const f32x4*>(&s_ml2[0]), mb = *reinterpret_cast<const f32x4*>(&s_ml2[4]);
    f32x4 pq[4];
#pragma unroll
    for (int k = 0; k < 4; ++k) pq[k] = po[k * 8];
    SB();
    if (FB == 1) {
      step(4, h0); step(5, h1);
      asm volatile("" : "+v"(acc[0]), "+v"(acc[1]));
      SB();
    }
    const float M = fmaxf(fmaxf(ma[0], ma[2]), fmaxf(mb[0], mb[2]));
    const float e0 = __builtin_amdgcn_exp2f((ma[0] - M) * EXP_C), e1_ = __builtin_amdgcn_exp2f((ma[2] - M) * EXP_C);
    const float e2 = __builtin_amdgcn_exp2f((mb[0] - M) * EXP_C), e3 = __builtin_amdgcn_exp2f((mb[2] - M) * EXP_C);
    const float inv = __builtin_amdgcn_rcpf((ma[1] * e0 + ma[3] * e1_) + (mb[1] * e2 + mb[3] * e3));
    const f32x4 av = ((pq[0] * e0 + pq[1] * e1_) + (pq[2] * e2 + pq[3] * e3)) * inv;
    if (FB) {
      v4u pb[4];
#pragma unroll
      for (int i = 0; i < 4; ++i) pb[i] = bop(av[i]);
      asm volatile("" : "+v"(pb[0]), "+v"(pb[1]), "+v"(pb[2]), "+v"(pb[3]));
      SB();
      if (FB == 2) { step(4, h0); step(5, h1); }
#pragma unroll
      for (int i = 0; i < 4; ++i) step(i, pb[i]);
    } else {
#pragma unroll
      for (int i = 0; i < 4; ++i) step(i, bop(av[i]));
    }
    f32x4 z;
#pragma unroll
    for (int v = 0; v < 4; ++v) {
      const float a0 = asf((unsigned)__builtin_amdgcn_update_dpp((int)asu(acc[0][v]), (int)asu(acc[0][v]), ROR8, 0xc, 0xf, false));
      const float a1 = asf((unsigned)__builtin_amdgcn_update_dpp((int)asu(acc[1][v]), (int)asu(acc[1][v]), ROR8, 0xc, 0xf, false));
      z[v] = tr32(a0, a1);
    }
    __builtin_amdgcn_raw_buffer_store_b128(tag4(z, dt), rws, (int)to, 0, S2);
    STAMP(4);
    STAMP_OUT();
    __builtin_amdgcn_endpgm();
  }
  v4u old = *reinterpret_cast<const v4u*>(&s_old[w][lane * 4]);
  if (w < 2) {
    ldsu_t tp = (ldsu_t)&s_dst[4 + w][lane * 4];
    unsigned to = mo_ + w * 4 * AREA;
    lds4_t pp = pr + w * 64;
    asm volatile("" : "+v"(tp), "+v"(to), "+v"(pp), "+v"(old));
    SB();
    LDS_FENCE(); bar(); LDS_FENCE();
    STAMP(2);
    f32x4 ml[4], pt[8];
#pragma unroll
    for (int k = 0; k < 4; ++k) ml[k] = pmr[k];
#pragma unroll
    for (int k = 0; k < 8; ++k) pt[k] = pp[k * 128];
    const v4u t = *tp;
    float M = -3.0e38f;
#pragma unroll
    for (int k = 0; k < 8; ++k) M = fmaxf(M, ml[k >> 2][k & 3]);
    f32x4 e[2] = {(ml[0] - M) * EXP_C, (ml[1] - M) * EXP_C};
    f32x4 o = {0.f, 0.f, 0.f, 0.f};
#pragma unroll
    for (int k = 0; k < 8; ++k) {
      e[k >> 2][k & 3] = __builtin_amdgcn_exp2f(e[k >> 2][k & 3]);
      o += pt[k] * e[k >> 2][k & 3];
    }
    const f32x4 l4 = ml[2] * e[0] + ml[3] * e[1];
    const float L = (l4[0] + l4[2]) + (l4[1] + l4[3]);
    v4u msg;
#pragma unroll
    for (int j = 0; j < 3; ++j) msg[j] = tag<0x100>(__builtin_amdgcn_perm(asu(o[j]), asu(o[3]), 0x07060501u + j), t[j]);
    msg[3] = tag<1>(asu((lane & 1) ? L : M), t[3]);
    __builtin_amdgcn_raw_buffer_store_b128(msg, rws, (int)to, 0, S1);
    SB();
  } else {
    LDS_FENCE(); bar(); LDS_FENCE();
    STAMP(2);
  }
  const v4u rw4 = {(unsigned)(size_t)ws, (unsigned)((size_t)ws >> 32), WS_END, 0x00020000u};
  if (isy) {
    lds1_t po = (lane & 8) ? (lds1_t)&s_ml2[w * 2 + 1 + (lane == 8 ? 0 : 8)] : (lds1_t)&s_o[w][(lane & 7) * 4 + (lane >> 5) * 2 + ((lane >> 4) & 1)];
    lds1_t pq = (lds1_t)&s_ml2[w * 2];
    asm volatile("" : "+v"(po), "+v"(pq));
    if (G1) until(G1);
    STAMP(14);
    const v4u dr = poll<PD1, PG1, PS1>(rw4, pa, old);
    STAMP(15);
    const v4u d = dr & (v4u){~0x100u, ~0x100u, ~0x100u, ~1u};
    const float ot = dpp<XOR1>(asf(d[3]));
    const float m = (lane & 1) ? ot : asf(d[3]), l = (lane & 1) ? asf(d[3]) : ot;
    float M = fmaxf(m, dpp<ROR8>(m)); M = x16_max(M); M = x32_max(M);
    *pq = M;
    const float e = __builtin_amdgcn_exp2f((m - M) * EXP_C);
    const float v3 = asf(__builtin_amdgcn_perm(__builtin_amdgcn_perm(d[2], d[1], 0x04000c0cu), d[0], 0x0706000cu));
    float y0 = tr32(asf(d[0]) * e, asf(d[2]) * e), y1 = tr32(asf(d[1]) * e, v3 * e), L = x32_add(l * e);
    y0 = tr16(y0, y1); L = x16_add(L);
    const bool s_ = lane & 8;
    *po = (s_ ? L : y0) + dpp<ROR8>(s_ ? y0 : L);
    LDS_FENCE(); bar(); LDS_FENCE();
    STAMP(3);
    poll_end(dr);
    STAMP(4);
    STAMP_OUT();
    __builtin_amdgcn_endpgm();
  }
  if (isr) {
    const int col = (ro >> 1) * 32 + (ro & 1) * 4 + (lane & 3) * 8 + ((lane >> 2) & 3);
    float xo = asf((unsigned)xg[col] << 16);
    lds1_t po = (lds1_t)&s_r[(lane & 12) ? 8 : w][(lane & 3) * 4 + (lane >> 5) * 2 + ((lane >> 4) & 1)], pq = (lds1_t)&s_r[lane >> 4][lane & 15];
    const rsrc_t ro_ = mkrs(out, D * 2);
    unsigned oo = lane < 16 ? col * 2 : OOB;
    asm volatile("" : "+v"(xo), "+v"(po), "+v"(pq), "+v"(oo));
    if (G2) until(G2);
    const v4u dr = poll<PD2, PG2, PS2>(rw4, pa, old);
    const v4u d = dr & ~1u;
    float y0 = tr32(asf(d[0]), asf(d[2])), y1 = tr32(asf(d[1]), asf(d[3]));
    y0 = tr16(y0, y1);
    y0 += dpp<ROR8>(y0); y0 += dpp<ROR4>(y0);
    *po = y0;
    LDS_FENCE(); bar(); LDS_FENCE();
    if (w == 0) {
      float a = pq[0] + pq[64];
      a = x16_add(a); a = x32_add(a);
      __builtin_amdgcn_raw_buffer_store_b16((short)f2bf(xo + a), ro_, (int)oo, 0, 0);
    }
    STAMP(4);
    poll_end(dr);
  }
  if (!isr) STAMP(4);
  STAMP_OUT();
}

extern "C" void kernel_launch(void* const* d_in, const int* in_sizes, int n_in,
                              void* d_out, int out_size, void* d_ws, size_t ws_size,
                              hipStream_t stream) {
  fused_kernel<<<NWG, NT, 0, stream>>>((const u16*)d_in[0], (const u16*)d_in[1], (const u16*)d_in[2], (const u16*)d_in[3],
                                       (const u16*)d_in[4], (const u16*)d_in[6], (const u16*)d_in[7], (const u16*)d_in[8],
                                       (u16*)d_out, (char*)d_ws);
}
```

```cpp
#define PADK 99
#define PADC (bi < 8)
#include <hip/hip_runtime.h>
#include <hip/hip_bf16.h>
#include <stdint.h>

#define QPOS 24
#define PACE 8
#define QTRIES 8
#define TQ 124
#define TF 129
#define TFY 127
#define TY 131
#define NP 4
#define TAU 3650
#define PP 3000
#define PL 8
#define TOFF 150
#define TOFFQ 180
#define QBURST 100000
#define PBURST 4000
#define KBURST 100000
#define VBURST 100000
#define QFIRST 24
#define OPOS 8
#define TWK 8
#define TWD 0
#define WARM 0
#define HOT 0
#define HOTA 16
#define HOTT 24
#define OA 16
#define OB 16
#define PF 16
#define SF 16
#define P1 16
#define S1 16
#define P2 16
#define S2 16
#define G1 3900
#define G2 4000
#define G1N 0
#define PMASK 0
#define EO 3
#define PIPE1 6
#define PIPE2 6
#define PD1 2
#define PG1 6
#define PD2 2
#define PG2 6
#define PS1 0
#define PS2 0
#define WT 0
#define WX 2
#define WS_ 0
#define PA1 0
#define PA2 0
#define FB 1
#define XW 0
#define SND 0
#define DIAG 0
#define LITE 0

typedef unsigned v4u __attribute__((ext_vector_type(4)));
typedef unsigned v2u __attribute__((ext_vector_type(2)));
typedef float f32x4 __attribute__((ext_vector_type(4)));
typedef float f32x2 __attribute__((ext_vector_type(2)));
typedef __bf16 bf16x2 __attribute__((ext_vector_type(2)));
typedef __bf16 bf16x8 __attribute__((ext_vector_type(8)));
typedef unsigned short u16;
typedef __amdgpu_buffer_rsrc_t rsrc_t;
typedef __attribute__((address_space(3))) f32x4* lds4_t;
typedef __attribute__((address_space(3))) v4u* ldsu_t;
typedef __attribute__((address_space(3))) float* lds1_t;

constexpr int D = 448, SK = 32768, DH = 256, F = 1792, NQ = 4096;
constexpr int NWG = 256, NT = 512;
constexpr unsigned OOB = 0x80000000u;
constexpr unsigned SENT = 0xFFFFFFFFu;
constexpr unsigned AREA = 9216;
constexpr unsigned A_O1 = 1024;
constexpr unsigned A_F = 5120;
constexpr unsigned A_Y2 = 1024;
constexpr unsigned WS_REQ = NWG * AREA;
constexpr unsigned WS_ECHO = WS_REQ + 1024;
constexpr unsigned WS_GO = WS_ECHO + 1024;
constexpr unsigned WS_Q = WS_GO + 256;
constexpr unsigned WS_END = WS_Q + 8 * 2048;
constexpr int SPIN_CAP = 1 << 18;
constexpr int C_NEXT = 0, C_TICK = 1, C_GO = 2, C_Q = 3, C_T0 = 4, C_FC = 5, C_OLD = 6, C_DONE = 7;

__device__ __forceinline__ float asf(unsigned u) { return __builtin_bit_cast(float, u); }
__device__ __forceinline__ unsigned asu(float f) { return __builtin_bit_cast(unsigned, f); }
__device__ __forceinline__ float rbf(float f) { __bf16 b = (__bf16)f; return (float)b; }
__device__ __forceinline__ unsigned f2bf(float f) { __bf16 b = (__bf16)f; return (unsigned)__builtin_bit_cast(u16, b); }
__device__ __forceinline__ float dot2(unsigned k, unsigned q, float acc) {
  return __builtin_amdgcn_fdot2_f32_bf16(__builtin_bit_cast(bf16x2, k), __builtin_bit_cast(bf16x2, q), acc, false);
}
__device__ __forceinline__ f32x2 unp(unsigned u) { f32x2 r; r.x = asf(u << 16); r.y = asf(u & 0xffff0000u); return r; }
template <int CTRL> __device__ __forceinline__ float dpp(float x) {
  return asf((unsigned)__builtin_amdgcn_update_dpp(0, (int)asu(x), CTRL, 0xf, 0xf, true));
}
constexpr int XOR1 = 0xB1, XOR2 = 0x4E, XOR7 = 0x141, ROR8 = 0x128, ROR4 = 0x124, ROR2 = 0x122, BCAST = 0x150;
constexpr int QP_EVEN = 0xA0, QP_ODD = 0xF5;
__device__ __forceinline__ float x16_add(float x) { auto s = __builtin_amdgcn_permlane16_swap(asu(x), asu(x), false, false); return asf(s[0]) + asf(s[1]); }
__device__ __forceinline__ float x32_add(float x) { auto s = __builtin_amdgcn_permlane32_swap(asu(x), asu(x), false, false); return asf(s[0]) + asf(s[1]); }
__device__ __forceinline__ float x16_max(float x) { auto s = __builtin_amdgcn_permlane16_swap(asu(x), asu(x), false, false); return fmaxf(asf(s[0]), asf(s[1])); }
__device__ __forceinline__ float x32_max(float x) { auto s = __builtin_amdgcn_permlane32_swap(asu(x), asu(x), false, false); return fmaxf(asf(s[0]), asf(s[1])); }
__device__ __forceinline__ float tr32(float a, float b) { auto s = __builtin_amdgcn_permlane32_swap(asu(a), asu(b), false, false); return asf(s[0]) + asf(s[1]); }
__device__ __forceinline__ float tr16(float a, float b) { auto s = __builtin_amdgcn_permlane16_swap(asu(a), asu(b), false, false); return asf(s[0]) + asf(s[1]); }
__device__ __forceinline__ float wave_sum(float v) {
  v += dpp<XOR1>(v); v += dpp<XOR2>(v); v += dpp<XOR7>(v); v += dpp<ROR8>(v);
  v = x16_add(v); return x32_add(v);
}
__device__ __forceinline__ float wave_max(float v) {
  v = fmaxf(v, dpp<XOR1>(v)); v = fmaxf(v, dpp<XOR2>(v)); v = fmaxf(v, dpp<XOR7>(v)); v = fmaxf(v, dpp<ROR8>(v));
  v = x16_max(v); return x32_max(v);
}
__device__ __forceinline__ rsrc_t mkrs(const void* p, unsigned bytes) { return __builtin_amdgcn_make_buffer_rsrc((void*)p, 0, (int)bytes, 0x00020000); }
__device__ __forceinline__ rsrc_t selr(bool c, rsrc_t a, rsrc_t b) { return c ? a : b; }
__device__ __forceinline__ unsigned selu(bool c, unsigned a, unsigned b) { return c ? a : b; }
template <int AUX> __device__ __forceinline__ v4u ld4(rsrc_t r, unsigned vo, unsigned so) { return __builtin_amdgcn_raw_buffer_load_b128(r, (int)vo, (int)so, AUX); }
template <int AUX> __device__ __forceinline__ v2u ld2(rsrc_t r, unsigned vo, unsigned so) { return __builtin_amdgcn_raw_buffer_load_b64(r, (int)vo, (int)so, AUX); }
__device__ __forceinline__ void st4c(v4u v, rsrc_t r, unsigned vo) { __builtin_amdgcn_raw_buffer_store_b128(v, r, (int)vo, 0, 16); }
__device__ __forceinline__ void st1c(unsigned v, rsrc_t r, unsigned vo) { __builtin_amdgcn_raw_buffer_store_b32(v, r, (int)vo, 0, 16); }
template <unsigned M> __device__ __forceinline__ unsigned tag(unsigned u, unsigned t) { return (u & ~M) | (t & M); }
__device__ __forceinline__ v4u tag4(const f32x4& v, const v4u& t) { return (v4u){tag<1>(asu(v[0]), t[0]), tag<1>(asu(v[1]), t[1]), tag<1>(asu(v[2]), t[2]), tag<1>(asu(v[3]), t[3])}; }
__device__ __forceinline__ bool arrived(const v4u& v, const v4u& o) { return __builtin_amdgcn_ballot_w64(v[0] == o[0] || v[1] == o[1] || v[2] == o[2] || v[3] == o[3]) == 0; }
__device__ __forceinline__ unsigned lowb(const v4u& v) { return __builtin_amdgcn_perm(v[1], v[0], 0x0c0c0400u) | __builtin_amdgcn_perm(v[3], v[2], 0x04000c0cu); }
#define SB() __builtin_amdgcn_sched_barrier(0)
__device__ __forceinline__ void bar() { __builtin_amdgcn_s_waitcnt(0xC07F); __builtin_amdgcn_s_barrier(); }
#define WAITV(n) __builtin_amdgcn_s_waitcnt(((n) & 15) | (((n) >> 4) << 14) | 0x0F70)
template <int V> struct IC { static constexpr int value = V; constexpr operator int() const { return V; } };
template <int A, int B, class Fn> __device__ __forceinline__ void static_for(Fn&& f) { if constexpr (A < B) { f(IC<A>{}); static_for<A + 1, B>(f); } }
#define LDS_FENCE() asm volatile("" ::: "memory")

constexpr float EXP_C = 0.0625f * 1.4426950408889634f;

struct Att {
  float m, l;
  f32x4 a[2];
};
__device__ __forceinline__ f32x4 mfma(const v4u& a, const v4u& b, const f32x4& c) {
  return __builtin_amdgcn_mfma_f32_16x16x32_bf16(__builtin_bit_cast(bf16x8, a), __builtin_bit_cast(bf16x8, b), c, 0, 0, 0);
}
__device__ __forceinline__ float red_max(float v) { v = fmaxf(v, dpp<XOR1>(v)); v = fmaxf(v, dpp<XOR2>(v)); return x32_max(x16_max(v)); }

template <int NL, class ISSUE>
__device__ __forceinline__ float scores(v4u (&kb)[8], const v4u (&qb)[8], int lane, ISSUE issue) {
  f32x4 d = {0.f, 0.f, 0.f, 0.f};
#pragma unroll
  for (int i = 0; i < NL; ++i) {
    d = mfma(kb[i], qb[i], d);
    asm volatile("" : "+v"(d));
    issue(i);
    SB();
  }
  const bool odd = lane & 1;
  return rbf((odd ? d[3] : d[0]) + dpp<XOR1>(odd ? d[1] : d[2]));
}
template <class ISSUE>
__device__ __forceinline__ float phase_k4(v4u (&kb)[8], const v4u (&qb)[8], Att& st, int lane, ISSUE issue) {
  float s = scores<4>(kb, qb, lane, issue);
  s = (lane & 2) ? -__builtin_inff() : s;
  const float mn = fmaxf(st.m, red_max(s));
  const float al = __builtin_amdgcn_exp2f((st.m - mn) * EXP_C);
  const float p = __builtin_amdgcn_exp2f((s - mn) * EXP_C);
  st.m = mn; st.l = st.l * al + p;
  st.a[0] *= al; st.a[1] *= al;
  SB();
  return p;
}
template <int I, int O, class ISSUE>
__device__ __forceinline__ void pv_step(v4u (&vb)[8], unsigned pp, const v4u& pm, Att& st, ISSUE& issue) {
  const v4u pb = pm & (unsigned)__builtin_amdgcn_update_dpp(0, (int)pp, I * 0x55, 0xf, 0xf, true);
  st.a[0] = mfma(vb[O + 2 * I], pb, st.a[0]);
  asm volatile("" : "+v"(st.a[0]));
  issue(O + 2 * I);
  SB();
  st.a[1] = mfma(vb[O + 2 * I + 1], pb, st.a[1]);
  asm volatile("" : "+v"(st.a[1]));
  issue(O + 2 * I + 1);
  SB();
}
template <int NL, class ISSUE>
__device__ __forceinline__ void phase_v(v4u (&vb)[8], float p, const v4u& pm, Att& st, ISSUE issue) {
  constexpr int O = 8 - NL;
  const unsigned pp = __builtin_bit_cast(unsigned, (bf16x2){(__bf16)p, (__bf16)p});
  pv_step<0, O>(vb, pp, pm, st, issue); pv_step<1, O>(vb, pp, pm, st, issue);
  if constexpr (NL == 8) { pv_step<2, O>(vb, pp, pm, st, issue); pv_step<3, O>(vb, pp, pm, st, issue); }
}

template <int N, int GAP, int SL>
__device__ __forceinline__ v4u poll(const v4u& r, unsigned pa, const v4u& old) {
  v4u d;
  unsigned long long t0, t1, t2;
  int n = SPIN_CAP;
  asm volatile(
      "s_nop 4\n\t"
      "buffer_load_dwordx4 v[200:203], %5, %6, 0 offen sc1\n\t"
      ".rept %11\n\t"
      "s_sleep %12\n\t"
      "buffer_load_dwordx4 v[200:203], %5, %6, 0 offen sc1\n\t"
      ".endr\n"
      "1:\n\t"
      "s_waitcnt vmcnt(%11)\n\t"
      "v_cmp_eq_u32 vcc, v200, %7\n\t"
      "v_cmp_eq_u32 %1, v201, %8\n\t"
      "v_cmp_eq_u32 %2, v202, %9\n\t"
      "v_cmp_eq_u32 %3, v203, %10\n\t"
      "s_or_b64 vcc, vcc, %1\n\t"
      "s_or_b64 %2, %2, %3\n\t"
      "s_or_b64 exec, vcc, %2\n\t"
      "s_cbranch_scc0 2f\n\t"
      ".if %13\n\ts_sleep %13\n\t.endif\n\t"
      "buffer_load_dwordx4 v[200:203], %5, %6, 0 offen sc1\n\t"
      "s_sub_u32 %4, %4, 1\n\t"
      "s_cbranch_scc0 1b\n"
      "2:\n\t"
      "s_mov_b64 exec, -1"
      : "=&{v[200:203]}"(d), "=&s"(t0), "=&s"(t1), "=&s"(t2), "+s"(n)
      : "v"(pa), "s"(r), "v"(old[0]), "v"(old[1]), "v"(old[2]), "v"(old[3]), "n"(N - 1), "n"(GAP), "n"(SL)
      : "vcc", "scc", "memory");
  return d;
}
__device__ __forceinline__ void poll_end(const v4u& d) { asm volatile("s_waitcnt vmcnt(0)" :: "{v[200:203]}"(d)); }

#define STAMP(k) do { if ((k) == PADK) { SB(); tpad = __builtin_amdgcn_s_memrealtime(); SB(); } } while (0)
#define STAMP_OUT() do { if (PADC) while (__builtin_amdgcn_s_memrealtime() - tpad < 6000) __builtin_amdgcn_s_sleep(1); } while (0)

__device__ __forceinline__ f32x2 ln_stats(const v4u& xa, int lane) {
  f32x2 xv[4];
  float sm = 0.f;
#pragma unroll
  for (int j = 0; j < 4; ++j) { xv[j] = unp(xa[j]); sm += xv[j].x + xv[j].y; }
  const float mean = wave_sum(sm) * (1.0f / D);
  float sq = 0.f;
#pragma unroll
  for (int j = 0; j < 4; ++j) { const f32x2 d = xv[j] - mean; sq += d.x * d.x + d.y * d.y; }
  sq = lane < 56 ? sq : 0.f;
  const float var = rbf(wave_sum(sq) * (1.0f / D));
  const float sd = rbf(sqrtf(rbf(var + 1e-5f)));
  return (f32x2){rbf(mean), 1.0f / sd};
}
__device__ __forceinline__ float ln_apply(float x, f32x2 st) { return rbf(rbf(x - st.x) * st.y); }

__global__ void __launch_bounds__(NT, 2)
fused_kernel(const u16* __restrict__ xg, const u16* __restrict__ kc, const u16* __restrict__ vc, const u16* __restrict__ wq,
             const u16* __restrict__ wo, const u16* __restrict__ wg, const u16* __restrict__ wu, const u16* __restrict__ wd,
             u16* __restrict__ out, char* ws) {
  __shared__ __attribute__((aligned(16))) float s_part[8][512];
  __shared__ __attribute__((aligned(16))) float s_mv[8][64];
  __shared__ __attribute__((aligned(16))) unsigned s_q[256];
  __shared__ __attribute__((aligned(16))) unsigned s_qs[64];
  __shared__ __attribute__((aligned(16))) unsigned s_old[8][256];
  __shared__ __attribute__((aligned(16))) unsigned s_dst[8][256];
  __shared__ __attribute__((aligned(16))) float s_ml[2][16];
  __shared__ __attribute__((aligned(16))) float s_o[4][32];
  __shared__ __attribute__((aligned(16))) float s_ml2[16];
  __shared__ __attribute__((aligned(16))) float s_h[16];
  __shared__ __attribute__((aligned(16))) float s_r[9][16];
  __shared__ __attribute__((aligned(16))) float s_sc[64][32];
  __shared__ int s_ctl[64];
  unsigned long long tpad = 0;
  int bi_ = blockIdx.x;
  if (PADK == 99) asm volatile("s_memrealtime %0" : "=s"(tpad), "+s"(bi_));
  const int tid = threadIdx.x, lane = tid & 63, w = __builtin_amdgcn_readfirstlane(tid >> 6);
  const int bi = bi_, h = (((bi & 1) ? bi + 5 : bi) & 6) | (bi >> 7), s = ((bi >> 3) & 15) * 2 + (bi & 1);
  const int b = h * 32 + s;
  static_assert(8 * TQ + 8 * TF + 6 * TFY + 10 * TY == 4096 && TQ - NP > 80 && TF - NP > 80 && TFY - NP > 80 && TY - NP > 80, "tickets");
  static_assert(TQ - NP <= 128 && TF - NP <= 128 && TFY - NP <= 128 && TY - NP <= 128, "tickets");
  const int nt = s < 8 ? TQ : s < 16 ? TF : s < 22 ? TFY : TY, nk = (nt - NP) >> 1;
  const int first = s * TY - min(s, 22) * (TY - TFY) - min(s, 16) * (TFY - TF) - min(s, 8) * (TF - TQ);
  asm volatile("" :: "s"(xg), "s"(kc), "s"(vc), "s"(wq), "s"(wo), "s"(wg), "s"(wu), "s"(wd), "s"(out), "s"(ws));
  STAMP(0);
  const rsrc_t rx = mkrs(xg, D * 2);
  const rsrc_t rk = mkrs(kc + (size_t)h * SK * DH, SK * DH * 2);
  const rsrc_t rv = mkrs(vc + (size_t)h * SK * DH, SK * DH * 2);
  const rsrc_t rws = mkrs(ws, WS_END);
  const unsigned l16 = lane * 16;
  auto ubase = [&](int u) -> unsigned { return (unsigned)(first + 2 * u) * 4096u; };
  const unsigned kvo = ((lane >> 1) & 7) * 512 + (lane >> 4) * 32 + (lane & 1) * 16, vvo = (lane >> 4) * 1024 + (lane & 15) * 16;
  auto ldk = [&](unsigned base, int i) { return ld4<2>(rk, kvo + (i & 3) * 128, base + (i >> 2) * 4096); };
  const unsigned area = (unsigned)b * AREA;
  auto area_of = [&](int ss) -> unsigned { return (unsigned)(h * 32 + ss) * AREA; };
  const unsigned lmask = lane < 56 ? 0u : OOB;

  const bool isq = s < 8, isf = s >= 8 && s < 22, isy = s >= 16;
  const int f = h * 14 + s - 8, y = h * 16 + s - 16, ro = h * 4 + (s >> 2);
  const bool isr = s < 16 && ((s + h) & 3) == 0 && ro < 28;
  const bool tile = isq || (isf && w < 4);
  const bool leader = (b == NWG - 1 && w == 1), member = (w == 0);

  const unsigned pa = isy ? (w < 4 ? area + A_O1 + w * 1024 + l16 : (w == 7 && y < 112) ? area + A_F + (lane & 31) * 16 : OOB) : isr ? area + A_Y2 + w * 1024 + l16 : OOB;
  const unsigned mo_ = area_of(16 + (lane & 8) + (lane >> 4)) + A_O1 + s * 128 + (lane & 7) * 16;
  const int fd = (f & ~3) + ((lane >> 3) & 3);
  const unsigned mf = ((unsigned)((fd >> 4) * 32 + 16 + (fd & 15)) * AREA + A_F + (f & 3) * 128 + (lane & 7) * 16) | (lane < 32 ? 0u : OOB);
  auto my = [&](int p) -> unsigned { return (unsigned)((p >> 4) * 32 + (p & 12) + ((-(p >> 4)) & 3)) * AREA + A_Y2 + y * 64 + (p & 3) * 16; };
  v4u eo, ed;
  auto da_of = [&]() {
    const int p = (w - 2) * 64 + lane;
    unsigned da = (isy && p < 112) ? my(p) : OOB;
    return (w & 6) == 2 ? da : (w & 6) == 4 ? mo_ + (w - 4) * 4 * AREA : (w == 6 && isf) ? mf : OOB;
  };
  if (EO == 1) { eo = ld4<OA>(rws, pa, 0); SB(); }
  if (EO == 3 || (EO == 4 && !tile)) { eo = ld4<OA>(rws, pa, 0); ed = ld4<OB>(rws, da_of(), 0); SB(); }
  if (EO == 5) { ed = ld4<OB>(rws, da_of(), 0); SB(); }
  const v4u sent = {SENT, SENT, SENT, SENT};
  unsigned poll_off = lane == 1 ? WS_GO : OOB;
  poll_off = lane == 0 ? WS_REQ + (b >> 2) * 16 : poll_off;
  poll_off = leader ? WS_ECHO + l16 : poll_off;
  v4u pv = {0u, 0u, 0u, 0u}, e1 = {0u, 0u, 0u, 0u};
  unsigned go_ref = 0;
  int* const ctl = &s_ctl[lane];
  auto ctl_get = [&](int i) { return __builtin_amdgcn_readfirstlane(__hip_atomic_load(&s_ctl[i], __ATOMIC_RELAXED, __HIP_MEMORY_SCOPE_WORKGROUP)); };
  auto ctl_set = [&](int i) { __hip_atomic_store(&s_ctl[i], 1, __ATOMIC_RELAXED, __HIP_MEMORY_SCOPE_WORKGROUP); };
  const unsigned qoff = WS_Q + h * 2048 + l16;
  v4u qv0, qv1;
  if (member) {
    qv0 = ld4<16>(rws, qoff, 0);
    qv1 = ld4<16>(rws, qoff, 1024);
    SB();
    int c = lane == C_NEXT ? (isq ? TOFFQ : TOFF) * 128 : 0;
    c = lane == C_TICK ? 40 : c;
    c = lane == C_T0 ? (int)(__builtin_amdgcn_s_memrealtime() & 0x7fffffff) : c;
    __hip_atomic_store(ctl, c, __ATOMIC_RELAXED, __HIP_MEMORY_SCOPE_WORKGROUP);
  }
  *reinterpret_cast<v4u*>(&s_sc[w * 8][lane * 4]) = sent;
  int t0 = 0;
  auto now = [&]() { return ((int)__builtin_amdgcn_s_memrealtime() - t0) & 0x7fffff; };
  const int neg = lane == C_NEXT ? 0 : (int)0x80000000;
  int inc = lane == C_NEXT ? TAU : 0;
  auto token = [&](int burst) {
    const int t = max(now() * 128 - burst, 0);
    __hip_atomic_fetch_max(ctl, t | neg, __ATOMIC_RELAXED, __HIP_MEMORY_SCOPE_WORKGROUP);
    return __hip_atomic_fetch_add(ctl, inc, __ATOMIC_RELAXED, __HIP_MEMORY_SCOPE_WORKGROUP);
  };
  auto due_of = [&](int x) { return __builtin_amdgcn_readlane(x, C_NEXT) >> 7; };
  auto until = [&](int due) {
#pragma unroll 1
    for (int it = 0; it < 4096 && now() < due; ++it) __builtin_amdgcn_s_sleep(1);
    SB();
  };
  unsigned qs0, qs1;
  auto q_there = [&]() { return __builtin_amdgcn_ballot_w64(((lowb(qv0) ^ qs0) & (lowb(qv1) ^ qs1)) != 0xFFFFFFFFu) == 0; };
  auto load_q = [&]() { qv0 = ld4<16>(rws, qoff, 0); qv1 = ld4<16>(rws, qoff, 1024); };
  auto publish_q = [&]() {
    *reinterpret_cast<v2u*>(&s_q[lane * 2]) = (v2u){__builtin_amdgcn_perm(qv0[1], qv0[0], 0x07060302u), __builtin_amdgcn_perm(qv0[3], qv0[2], 0x07060302u)};
    *reinterpret_cast<v2u*>(&s_q[128 + lane * 2]) = (v2u){__builtin_amdgcn_perm(qv1[1], qv1[0], 0x07060302u), __builtin_amdgcn_perm(qv1[3], qv1[2], 0x07060302u)};
    LDS_FENCE();
    __builtin_amdgcn_s_waitcnt(0xC07F);
    ctl_set(C_Q);
  };
  v4u pre[4][8], ring[8];
  auto unit0 = [&]() {
#pragma unroll
    for (int i = 0; i < 8; ++i) { pre[0][i] = ldk(ubase(w), i); SB(); }
    if (EO == 2) { eo = ld4<OA>(rws, pa, 0); SB(); }
  };
  SB();
  if (tile) {
    v4u tw[8];
    const int oct = isq ? 7 * w : (f & 3) * 14 + (w >> 1) * 7;
    const int stride = isq ? NQ * 2 : F * 2;
    const rsrc_t rt = mkrs(isq ? wq : (w & 1) ? wu : wg, D * stride);
    const unsigned to = (unsigned)((oct + (lane >> 3)) * 8 * stride + (isq ? h * 8 + s : f >> 2) * 128 + (lane & 7) * 16) | lmask;
    const v4u xa = ld4<0>(rx, l16, 0);
    const v4u xb = ld4<0>(rx, (unsigned)(oct + (lane >> 3)) * 16 | lmask, 0);
    SB();
#pragma unroll
    for (int i = 0; i < 8; ++i) { tw[i] = ld4<2>(rt, to, i * stride); SB(); }
    if (EO == 4) { eo = ld4<OA>(rws, pa, 0); ed = ld4<OB>(rws, da_of(), 0); SB(); }
    LDS_FENCE(); bar(); LDS_FENCE();
    t0 = ctl_get(C_T0);
    SB();
    const f32x2 ls = ln_stats(xa, lane);
    f32x2 acc[4];
#pragma unroll
    for (int i = 0; i < 8; ++i) {
      const float xn = ln_apply(asf((i & 1) ? (xb[i >> 1] & 0xffff0000u) : (xb[i >> 1] << 16)), ls);
#pragma unroll
      for (int j = 0; j < 4; ++j) acc[j] = i ? acc[j] + unp(tw[i][j]) * xn : unp(tw[i][j]) * xn;
    }
    float y0 = tr32(acc[0].x, acc[2].x), y1 = tr32(acc[0].y, acc[2].y), y2 = tr32(acc[1].x, acc[3].x), y3 = tr32(acc[1].y, acc[3].y);
    y0 = tr16(y0, y2); y1 = tr16(y1, y3);
    const bool s_ = lane & 8;
    y0 = (s_ ? y1 : y0) + dpp<ROR8>(s_ ? y0 : y1);
    s_mv[w][(lane & 7) * 8 + (lane >> 3)] = y0;
    STAMP(6);
    token(isq ? QBURST : PBURST);
    unit0();
    if (isq) {
      LDS_FENCE(); bar(); LDS_FENCE();
      if (member) {
        float a = 0.f;
#pragma unroll
        for (int k = 0; k < 8; ++k) a += s_mv[k][lane];
        s_qs[lane] = f2bf(a) << 16;
        LDS_FENCE();
        const v4u qs = *reinterpret_cast<const v4u*>(&s_qs[(lane & 15) * 4]) | (~((s & 4) ? qv1 : qv0) & 0xffffu);
        st4c(qs, rws, (lane >> 4) == (s & 3) ? qoff + (s >> 2) * 1024 : OOB);
      }
    } else {
      LDS_FENCE();
      __builtin_amdgcn_s_waitcnt(0xC07F);
      __hip_atomic_fetch_add(ctl, lane == C_FC ? 1 : 0, __ATOMIC_RELAXED, __HIP_MEMORY_SCOPE_WORKGROUP);
    }
  } else if (leader) {
    e1 = ld4<16>(rws, poll_off, 0);
    go_ref = __builtin_amdgcn_raw_buffer_load_b32(rws, (int)WS_GO, 0, 16);
  }
  SB();
  STAMP(5);
  SB();
  bool ok = !member;
  static_for<0, 40>([&](auto N) {
    constexpr int n = N;
    if constexpr (n == 0) {
      if (!tile) unit0();
    }
    if constexpr (n == 8) {
      if (!tile) {
        LDS_FENCE(); bar(); LDS_FENCE();
        t0 = ctl_get(C_T0);
        if (leader) {
          e1 += 1u;
          go_ref += 1u;
          st4c(e1, rws, WS_REQ + l16);
        }
      }
    }
    if constexpr (n > 0 && n % 8 == 0) {
      const int x = token(isq ? QBURST : PBURST);
      if constexpr (n > QFIRST) {
        if (!ok) {
          ok = q_there();
          if (ok) publish_q();
        }
      }
      if constexpr (n == OPOS + 16) {
        *reinterpret_cast<v4u*>(&s_old[w][lane * 4]) = (int)pa < 0 ? sent : eo;
        *reinterpret_cast<v4u*>(&s_dst[w][lane * 4]) = ~ed;
        LDS_FENCE();
        __hip_atomic_fetch_add(ctl, lane == C_OLD ? 1 : 0, __ATOMIC_RELAXED, __HIP_MEMORY_SCOPE_WORKGROUP);
      }
      SB();
      until(due_of(x));
      if constexpr (n == 8) {
        if (member) { qs0 = lowb(qv0); qs1 = lowb(qv1); }
      }
      if constexpr (n >= QFIRST) {
        if (!ok) load_q();
      }
      if constexpr (n == OPOS) {
        if (EO == 0 || EO == 5) eo = ld4<OA>(rws, pa, 0);
        if (EO < 3) ed = ld4<OB>(rws, da_of(), 0);
      }
      SB();
    }
    if constexpr (n == QPOS) {
      STAMP(9);
      pv = ld4<16>(rws, (member || leader) ? poll_off : OOB, 0);
      SB();
    }
    if constexpr (n >= 8) {
      if constexpr (n >= PACE) WAITV(PACE + ((n >= QPOS && n < QPOS + PACE) ? 1 : 0) + ((n >= OPOS && n < OPOS + PACE) ? (EO == 0 ? 2 : (EO == 3 || EO == 4) ? 0 : 1) : 0));
      if constexpr (n < 32) pre[n >> 3][n & 7] = ldk(ubase((n & 56) + w), n & 7); else ring[n & 7] = ldk(ubase(32 + w), n & 7);
    }
    SB();
    if constexpr (n - PACE == 7 || n - PACE == 15 || n - PACE == 23 || n - PACE == 31) STAMP(16 + (n - PACE) / 8);
  });
  inc = lane == C_TICK ? 1 : inc;
  int tok = token(KBURST);
  STAMP(10);
  bool svc = member || leader;
  unsigned seenm = 0, last_echo = 0;
  bool echoed = false;
  auto service = [&]() {
    if (leader) {
      seenm |= (pv[0] == e1[0] ? 1u : 0u) | (pv[1] == e1[1] ? 2u : 0u) | (pv[2] == e1[2] ? 4u : 0u) | (pv[3] == e1[3] ? 8u : 0u);
      svc = __builtin_amdgcn_ballot_w64(seenm != 15u) != 0;
      st1c(go_ref, rws, (lane == 0 && !svc) ? WS_GO : OOB);
    } else {
      const unsigned req = (unsigned)__builtin_amdgcn_readfirstlane((int)((b & 2) ? ((b & 1) ? pv[3] : pv[2]) : ((b & 1) ? pv[1] : pv[0])));
      const unsigned go = (unsigned)__builtin_amdgcn_readlane((int)pv[0], 1);
      go_ref = echoed ? go_ref : go;
      svc = go == (unsigned)__builtin_amdgcn_readfirstlane((int)go_ref);
      const bool rdy = ctl_get(C_OLD) == 8;
      st1c(req, rws, (lane == 0 && svc && rdy && !(echoed && req == last_echo)) ? WS_ECHO + b * 4 : OOB);
      echoed |= rdy; last_echo = req;
      if (!svc) {
        if (isf) {
          while (ctl_get(C_FC) != 4) __builtin_amdgcn_s_sleep(1);
          LDS_FENCE();
          const int j = (lane >> 3) & 3, pc = lane & 7, m = pc >> 2, col = j * 16 + (pc & 3) * 4;
          const f32x4 v = *reinterpret_cast<const f32x4*>(&s_mv[m][col]) + *reinterpret_cast<const f32x4*>(&s_mv[m + 2][col]);
          __builtin_amdgcn_raw_buffer_store_b128(tag4(v, *reinterpret_cast<const v4u*>(&s_dst[6][lane * 4])), rws, (int)mf, 0, SF);
        }
      }
    }
    pv = ld4<16>(rws, svc ? poll_off : OOB, 0);
    SB();
  };

  {
    if (!ok) {
      ok = q_there();
      if (ok) publish_q();
    }
#pragma unroll 1
    for (int it = 0; it < SPIN_CAP && !ctl_get(C_Q); ++it) {
      if (svc && it >= QTRIES) { service(); asm volatile("" :: "v"(pv)); }
      if (!ok) {
        load_q();
        ok = q_there() | !svc;
        if (ok) publish_q();
      } else {
        __builtin_amdgcn_s_sleep(1);
      }
    }
  }
  LDS_FENCE();
  v4u qb[8];
#pragma unroll
  for (int i = 0; i < 4; ++i) {
    const v4u z = {0u, 0u, 0u, 0u}, q = *reinterpret_cast<const v4u*>(&s_q[(lane & 8) * 16 + i * 32 + (lane >> 4) * 8 + (lane & 1) * 4]);
    qb[i] = (lane & 2) ? z : q; qb[4 + i] = (lane & 2) ? q : z;
  }
  v4u pm;
#pragma unroll
  for (int j = 0; j < 4; ++j) pm[j] = ((lane >> 1) & 3) == j ? ((lane & 1) ? 0xffff0000u : 0xffffu) : 0u;
  STAMP(1);
  SB();

  Att st;
  v4u wa = sent, wb = sent;
  int sx = 20;
  {
    float* const sc = &s_sc[0][(lane >> 3) * 4 + (lane & 3)];
    rsrc_t nra, nrb;
    unsigned nva, nvb, nba, nbb, nsa, nsb;
    int kind = 0, nu = 32 + w, cu;
    auto next = [&]() {
      cu = nu;
      const int t = __builtin_amdgcn_readlane(tok, C_TICK);
      const bool fx = t < 2 * nk, tw = t >= nt;
      kind = fx ? (t >= nk ? 1 : 0) : tw ? 3 : 2;
      nu = t >= nk ? t - nk : t;
      nba = (unsigned)(fx ? first + 2 * nu : tw ? 0x80000 : first + t) * 4096u;
      nbb = fx ? nba + 4096 : nba;
      nra = selr(kind == 1, rv, rk);
      nrb = selr(kind == 0, rk, rv);
      nva = selu(kind == 1, vvo, kvo); nvb = selu(kind == 0, kvo, vvo);
      nsa = selu(kind == 1, 256, 128); nsb = selu(kind == 0, 128, 256);
      until(tw ? 0 : due_of(tok));
      inc = (lane == C_NEXT && t + PL >= 2 * nk) ? PP : inc;
      SB();
    };
    auto ldn = [&](int i) { ring[i] = i < 4 ? ld4<2>(nra, nva, nba + i * nsa) : ld4<2>(nrb, nvb, nbb + (i - 4) * nsb); };
    float mx;
    if (svc) service();
    {
      const float s0 = scores<8>(pre[0], qb, lane, [&](int) {});
      const float s1 = scores<8>(pre[1], qb, lane, [&](int) {});
      const float s2 = scores<8>(pre[2], qb, lane, [&](int) {});
      const float s3 = scores<8>(pre[3], qb, lane, [&](int) {});
      sc[w * 32] = s0; sc[(8 + w) * 32] = s1; sc[(16 + w) * 32] = s2; sc[(24 + w) * 32] = s3;
      mx = fmaxf(fmaxf(s0, s1), fmaxf(s2, s3));
      SB();
      STAMP(11);
    }
#pragma unroll 1
    while (kind == 0) {
      next();
      const float sv = scores<8>(ring, qb, lane, ldn);
      sc[cu * 32] = sv;
      tok = token(KBURST);
      mx = fmaxf(mx, sv);
      STAMP(sx); ++sx;
      if (svc) service();
      SB();
    }
    STAMP(7);
    if (isy && w == 7) {
      const v4u old = *reinterpret_cast<const v4u*>(&s_old[7][lane * 4]);
      v4u d;
#pragma unroll 1
      for (int it = 0; it < SPIN_CAP; ++it) {
        d = ld4<PF>(rws, pa, 0);
        if (arrived(d, old)) break;
        __builtin_amdgcn_s_sleep(1);
        LDS_FENCE();
      }
      float hv[4];
#pragma unroll
      for (int j = 0; j < 4; ++j) {
        float g = asf(d[j] & ~1u);
        g += dpp<ROR8>(g); g = rbf(x16_add(g));
        const float u = dpp<ROR4>(g);
        hv[j] = rbf(rbf(g / (1.0f + __expf(-g))) * u);
      }
      if (lane < 4) {
#pragma unroll
        for (int j = 0; j < 4; ++j) s_h[((lane & 1) * 4 + j) * 2 + (lane >> 1)] = hv[j];
      }
      SB();
    }
    st.m = red_max(mx); st.l = 0.f;
    st.a[0] = st.a[1] = (f32x4){0.f, 0.f, 0.f, 0.f};
#pragma unroll 1
    while (kind == 1) {
      float sv;
      for (;;) {
        sv = __hip_atomic_load(&sc[nu * 32], __ATOMIC_RELAXED, __HIP_MEMORY_SCOPE_WORKGROUP);
        if (__builtin_amdgcn_ballot_w64(asu(sv) == SENT) == 0) break;
        __builtin_amdgcn_s_sleep(1);
      }
      if (__builtin_amdgcn_ballot_w64(sv > st.m) != 0) {
        const float m2 = fmaxf(st.m, red_max(sv));
        const float al = __builtin_amdgcn_exp2f((st.m - m2) * EXP_C);
        st.m = m2; st.l *= al;
        st.a[0] *= al; st.a[1] *= al;
      }
      const float p = __builtin_amdgcn_exp2f((sv - st.m) * EXP_C);
      st.l += p;
      SB();
      next();
      phase_v<8>(ring, p, pm, st, ldn);
      tok = token(VBURST);
      STAMP(sx); ++sx;
      if (svc) service();
      SB();
    }
    STAMP(8);
#pragma unroll 1
    while (kind == 2) {
      next();
      const float p = phase_k4(ring, qb, st, lane, ldn);
      phase_v<4>(ring, p, pm, st, ldn);
      tok = token(VBURST);
      STAMP(sx); ++sx;
    }
#pragma unroll 1
    for (int it = 0; it < SPIN_CAP && svc; ++it) {
      service();
      asm volatile("" :: "v"(pv));
    }
    LDS_FENCE();
  }
  STAMP(12);
  asm volatile("" :: "v"(wa), "v"(wb));
  const lds4_t pr = (lds4_t)&s_part[0][lane * 4], pmr = (lds4_t)&s_ml[(lane >> 3) & 1][0];
  {
    const lds4_t pw = (lds4_t)&s_part[w][lane * 4];
    const lds1_t pml = (lds1_t)&s_ml[(lane >> 3) & 1][w];
    pw[0] = st.a[0]; pw[64] = st.a[1];
    float l = st.l;
    l += dpp<XOR1>(l); l += dpp<XOR2>(l); l = x32_add(x16_add(l));
    pml[0] = st.m; pml[8] = l;
  }
  if (isy && w >= 4) {
    v4u wt[6][2];
    const rsrc_t rwo = mkrs(wo + (size_t)y * 32 * D, XW ? 0 : 32 * D * 2);
    const rsrc_t rwd = mkrs(wd + (size_t)(y < 112 ? y : 0) * 16 * D, (y < 112 && !XW) ? 16 * D * 2 : 0);
    unsigned o0 = (lane >> 3) * (D * 2) + (w - 4) * 256 + (lane & 7) * 16, o1 = w == 7 ? OOB : o0 + 128;
    asm volatile("" : "+v"(o0), "+v"(o1));
    SB();
    LDS_FENCE(); bar(); LDS_FENCE();
    STAMP(2);
    if (TWD) __builtin_amdgcn_s_sleep(TWD);
    SB();
#pragma unroll
    for (int k = 0; k < 6; ++k) {
      const int i = (k + 4) % 6;
      wt[i][0] = ld4<2>(i < 4 ? rwo : rwd, o0, (i & 3) * 8 * D * 2);
      wt[i][1] = ld4<2>(i < 4 ? rwo : rwd, o1, (i & 3) * 8 * D * 2);
      SB();
    }
    const int p = (w - 4) * 32 + (lane >> 4) * 8 + (lane & 7);
    v4u dt = *reinterpret_cast<const v4u*>(&s_dst[2][p * 4]) & 1u;
    unsigned to = ((lane & 8) || p >= 112) ? OOB : my(p);
    const f32x2 hv = *reinterpret_cast<const f32x2*>(&s_h[(lane >> 3) * 2]);
    f32x4 acc[2] = {{0.f, 0.f, 0.f, 0.f}, {0.f, 0.f, 0.f, 0.f}};
    auto bop = [&](float a) -> v4u { return pm & __builtin_bit_cast(unsigned, (bf16x2){(__bf16)a, (__bf16)a}); };
    auto step = [&](int i, const v4u& pb) { acc[0] = mfma(wt[i][0], pb, acc[0]); acc[1] = mfma(wt[i][1], pb, acc[1]); };
    v4u h0 = bop(hv.x), h1 = bop(hv.y);
    lds4_t po = (lds4_t)&s_o[0][(lane >> 3) * 4];
    asm volatile("" : "+v"(dt), "+v"(to), "+v"(h0), "+v"(h1), "+v"(po));
    SB();
    if (FB == 0) {
      step(4, h0); step(5, h1);
      asm volatile("" : "+v"(acc[0]), "+v"(acc[1]));
    }
    STAMP(14);
    LDS_FENCE(); bar(); LDS_FENCE();
    STAMP(3);
    const f32x4 ma = *reinterpret_cast<const f32x4*>(&s_ml2[0]), mb = *reinterpret_cast<const f32x4*>(&s_ml2[4]);
    f32x4 pq[4];
#pragma unroll
    for (int k = 0; k < 4; ++k) pq[k] = po[k * 8];
    SB();
    if (FB == 1) {
      step(4, h0); step(5, h1);
      asm volatile("" : "+v"(acc[0]), "+v"(acc[1]));
      SB();
    }
    const float M = fmaxf(fmaxf(ma[0], ma[2]), fmaxf(mb[0], mb[2]));
    const float e0 = __builtin_amdgcn_exp2f((ma[0] - M) * EXP_C), e1_ = __builtin_amdgcn_exp2f((ma[2] - M) * EXP_C);
    const float e2 = __builtin_amdgcn_exp2f((mb[0] - M) * EXP_C), e3 = __builtin_amdgcn_exp2f((mb[2] - M) * EXP_C);
    const float inv = __builtin_amdgcn_rcpf((ma[1] * e0 + ma[3] * e1_) + (mb[1] * e2 + mb[3] * e3));
    const f32x4 av = ((pq[0] * e0 + pq[1] * e1_) + (pq[2] * e2 + pq[3] * e3)) * inv;
    if (FB) {
      v4u pb[4];
#pragma unroll
      for (int i = 0; i < 4; ++i) pb[i] = bop(av[i]);
      asm volatile("" : "+v"(pb[0]), "+v"(pb[1]), "+v"(pb[2]), "+v"(pb[3]));
      SB();
      if (FB == 2) { step(4, h0); step(5, h1); }
#pragma unroll
      for (int i = 0; i < 4; ++i) step(i, pb[i]);
    } else {
#pragma unroll
      for (int i = 0; i < 4; ++i) step(i, bop(av[i]));
    }
    f32x4 z;
#pragma unroll
    for (int v = 0; v < 4; ++v) {
      const float a0 = asf((unsigned)__builtin_amdgcn_update_dpp((int)asu(acc[0][v]), (int)asu(acc[0][v]), ROR8, 0xc, 0xf, false));
      const float a1 = asf((unsigned)__builtin_amdgcn_update_dpp((int)asu(acc[1][v]), (int)asu(acc[1][v]), ROR8, 0xc, 0xf, false));
      z[v] = tr32(a0, a1);
    }
    __builtin_amdgcn_raw_buffer_store_b128(tag4(z, dt), rws, (int)to, 0, S2);
    STAMP(4);
    STAMP_OUT();
    __builtin_amdgcn_endpgm();
  }
  v4u old = *reinterpret_cast<const v4u*>(&s_old[w][lane * 4]);
  if (w < 2) {
    ldsu_t tp = (ldsu_t)&s_dst[4 + w][lane * 4];
    unsigned to = mo_ + w * 4 * AREA;
    lds4_t pp = pr + w * 64;
    asm volatile("" : "+v"(tp), "+v"(to), "+v"(pp), "+v"(old));
    SB();
    LDS_FENCE(); bar(); LDS_FENCE();
    STAMP(2);
    f32x4 ml[4], pt[8];
#pragma unroll
    for (int k = 0; k < 4; ++k) ml[k] = pmr[k];
#pragma unroll
    for (int k = 0; k < 8; ++k) pt[k] = pp[k * 128];
    const v4u t = *tp;
    float M = -3.0e38f;
#pragma unroll
    for (int k = 0; k < 8; ++k) M = fmaxf(M, ml[k >> 2][k & 3]);
    f32x4 e[2] = {(ml[0] - M) * EXP_C, (ml[1] - M) * EXP_C};
    f32x4 o = {0.f, 0.f, 0.f, 0.f};
#pragma unroll
    for (int k = 0; k < 8; ++k) {
      e[k >> 2][k & 3] = __builtin_amdgcn_exp2f(e[k >> 2][k & 3]);
      o += pt[k] * e[k >> 2][k & 3];
    }
    const f32x4 l4 = ml[2] * e[0] + ml[3] * e[1];
    const float L = (l4[0] + l4[2]) + (l4[1] + l4[3]);
    v4u msg;
#pragma unroll
    for (int j = 0; j < 3; ++j) msg[j] = tag<0x100>(__builtin_amdgcn_perm(asu(o[j]), asu(o[3]), 0x07060501u + j), t[j]);
    msg[3] = tag<1>(asu((lane & 1) ? L : M), t[3]);
    __builtin_amdgcn_raw_buffer_store_b128(msg, rws, (int)to, 0, S1);
    SB();
  } else {
    LDS_FENCE(); bar(); LDS_FENCE();
    STAMP(2);
  }
  const v4u rw4 = {(unsigned)(size_t)ws, (unsigned)((size_t)ws >> 32), WS_END, 0x00020000u};
  if (isy) {
    lds1_t po = (lane & 8) ? (lds1_t)&s_ml2[w * 2 + 1 + (lane == 8 ? 0 : 8)] : (lds1_t)&s_o[w][(lane & 7) * 4 + (lane >> 5) * 2 + ((lane >> 4) & 1)];
    lds1_t pq = (lds1_t)&s_ml2[w * 2];
    asm volatile("" : "+v"(po), "+v"(pq));
    if (G1) until(G1);
    STAMP(14);
    const v4u dr = poll<PD1, PG1, PS1>(rw4, pa, old);
    STAMP(15);
    const v4u d = dr & (v4u){~0x100u, ~0x100u, ~0x100u, ~1u};
    const float ot = dpp<XOR1>(asf(d[3]));
    const float m = (lane & 1) ? ot : asf(d[3]), l = (lane & 1) ? asf(d[3]) : ot;
    float M = fmaxf(m, dpp<ROR8>(m)); M = x16_max(M); M = x32_max(M);
    *pq = M;
    const float e = __builtin_amdgcn_exp2f((m - M) * EXP_C);
    const float v3 = asf(__builtin_amdgcn_perm(__builtin_amdgcn_perm(d[2], d[1], 0x04000c0cu), d[0], 0x0706000cu));
    float y0 = tr32(asf(d[0]) * e, asf(d[2]) * e), y1 = tr32(asf(d[1]) * e, v3 * e), L = x32_add(l * e);
    y0 = tr16(y0, y1); L = x16_add(L);
    const bool s_ = lane & 8;
    *po = (s_ ? L : y0) + dpp<ROR8>(s_ ? y0 : L);
    LDS_FENCE(); bar(); LDS_FENCE();
    STAMP(3);
    poll_end(dr);
    STAMP(4);
    STAMP_OUT();
    __builtin_amdgcn_endpgm();
  }
  if (isr) {
    const int col = (ro >> 1) * 32 + (ro & 1) * 4 + (lane & 3) * 8 + ((lane >> 2) & 3);
    float xo = asf((unsigned)xg[col] << 16);
    lds1_t po = (lds1_t)&s_r[(lane & 12) ? 8 : w][(lane & 3) * 4 + (lane >> 5) * 2 + ((lane >> 4) & 1)], pq = (lds1_t)&s_r[lane >> 4][lane & 15];
    const rsrc_t ro_ = mkrs(out, D * 2);
    unsigned oo = lane < 16 ? col * 2 : OOB;
    asm volatile("" : "+v"(xo), "+v"(po), "+v"(pq), "+v"(oo));
    if (G2) until(G2);
    const v4u dr = poll<PD2, PG2, PS2>(rw4, pa, old);
    const v4u d = dr & ~1u;
    float y0 = tr32(asf(d[0]), asf(d[2])), y1 = tr32(asf(d[1]), asf(d[3]));
    y0 = tr16(y0, y1);
    y0 += dpp<ROR8>(y0); y0 += dpp<ROR4>(y0);
    *po = y0;
    LDS_FENCE(); bar(); LDS_FENCE();
    if (w == 0) {
      float a = pq[0] + pq[64];
      a = x16_add(a); a = x32_add(a);
      __builtin_amdgcn_raw_buffer_store_b16((short)f2bf(xo + a), ro_, (int)oo, 0, 0);
    }
    STAMP(4);
    poll_end(dr);
  }
  if (!isr) STAMP(4);
  STAMP_OUT();
}

extern "C" void kernel_launch(void* const* d_in, const int* in_sizes, int n_in,
                              void* d_out, int out_size, void* d_ws, size_t ws_size,
                              hipStream_t stream) {
  fused_kernel<<<NWG, NT, 0, stream>>>((const u16*)d_in[0], (const u16*)d_in[1], (const u16*)d_in[2], (const u16*)d_in[3],
                                       (const u16*)d_in[4], (const u16*)d_in[6], (const u16*)d_in[7], (const u16*)d_in[8],
                                       (u16*)d_out, (char*)d_ws);
}
```

```cpp
#include <hip/hip_runtime.h>
#include <hip/hip_bf16.h>
#include <stdint.h>

#define QPOS 24
#define PACE 8
#define QTRIES 8
#define TQ 124
#define TF 129
#define TFY 127
#define TY 131
#define NP 4
#define TAU 3640
#define PP 3000
#define PL 8
#define TOFF 150
#define TOFFQ 180
#define QBURST 100000
#define PBURST 4000
#define KBURST 100000
#define VBURST 100000
#define QFIRST 24
#define OPOS 8
#define TWK 8
#define TWD 0
#define WARM 0
#define HOT 0
#define HOTA 16
#define HOTT 24
#define OA 16
#define OB 16
#define PF 16
#define SF 16
#define P1 16
#define S1 16
#define P2 16
#define S2 16
#define G1 3850
#define G2 3950
#define G1N 0
#define PMASK 0
#define EO 3
#define PIPE1 6
#define PIPE2 6
#define PD1 2
#define PG1 6
#define PD2 2
#define PG2 6
#define PS1 0
#define PS2 0
#define WT 0
#define WX 2
#define WS_ 0
#define PA1 0
#define PA2 0
#define SO 0
#define DK 57
#define DS 0
#define DG 4
#define DB 0
#define TD 0
#define TE 0
#define VT 0
#define VPOS 16
#define VX 2
#define SQ 0
#define SF_ 0
#define SFY 0
#define SY 0
#define XA 4
#define XB 4
#define TX 0
#define NOHS 0
#define ES 1
#define EL 8
#define FB 1
#define XW 0
#define SND 0
#define DIAG 0
#define LITE 0

typedef unsigned v4u __attribute__((ext_vector_type(4)));
typedef unsigned v2u __attribute__((ext_vector_type(2)));
typedef float f32x4 __attribute__((ext_vector_type(4)));
typedef float f32x2 __attribute__((ext_vector_type(2)));
typedef __bf16 bf16x2 __attribute__((ext_vector_type(2)));
typedef __bf16 bf16x8 __attribute__((ext_vector_type(8)));
typedef unsigned short u16;
typedef __amdgpu_buffer_rsrc_t rsrc_t;
typedef __attribute__((address_space(3))) f32x4* lds4_t;
typedef __attribute__((address_space(3))) v4u* ldsu_t;
typedef __attribute__((address_space(3))) float* lds1_t;

constexpr int D = 448, SK = 32768, DH = 256, F = 1792, NQ = 4096;
constexpr int NWG = 256, NT = 512;
constexpr unsigned OOB = 0x80000000u;
constexpr unsigned SENT = 0xFFFFFFFFu;
constexpr unsigned AREA = 9216;
constexpr unsigned A_O1 = 1024;
constexpr unsigned A_F = 5120;
constexpr unsigned A_Y2 = 1024;
constexpr unsigned WS_REQ = NWG * AREA;
constexpr unsigned WS_ECHO = WS_REQ + 1024;
constexpr unsigned WS_GO = WS_ECHO + 1024;
constexpr unsigned WS_T = WS_GO + 256;
constexpr unsigned WS_Q = WS_T + 256;
constexpr unsigned WS_END = WS_Q + 8 * 2048;
constexpr int SPIN_CAP = 1 << 18;
constexpr int C_NEXT = 0, C_TICK = 1, C_GO = 2, C_Q = 3, C_T0 = 4, C_FC = 5, C_OLD = 6, C_DONE = 7, C_LEAD = 8;

__device__ __forceinline__ float asf(unsigned u) { return __builtin_bit_cast(float, u); }
__device__ __forceinline__ unsigned asu(float f) { return __builtin_bit_cast(unsigned, f); }
__device__ __forceinline__ float rbf(float f) { __bf16 b = (__bf16)f; return (float)b; }
__device__ __forceinline__ unsigned f2bf(float f) { __bf16 b = (__bf16)f; return (unsigned)__builtin_bit_cast(u16, b); }
__device__ __forceinline__ float dot2(unsigned k, unsigned q, float acc) {
  return __builtin_amdgcn_fdot2_f32_bf16(__builtin_bit_cast(bf16x2, k), __builtin_bit_cast(bf16x2, q), acc, false);
}
__device__ __forceinline__ f32x2 unp(unsigned u) { f32x2 r; r.x = asf(u << 16); r.y = asf(u & 0xffff0000u); return r; }
template <int CTRL> __device__ __forceinline__ float dpp(float x) {
  return asf((unsigned)__builtin_amdgcn_update_dpp(0, (int)asu(x), CTRL, 0xf, 0xf, true));
}
constexpr int XOR1 = 0xB1, XOR2 = 0x4E, XOR7 = 0x141, ROR8 = 0x128, ROR4 = 0x124, ROR2 = 0x122, BCAST = 0x150;
constexpr int QP_EVEN = 0xA0, QP_ODD = 0xF5;
__device__ __forceinline__ float x16_add(float x) { auto s = __builtin_amdgcn_permlane16_swap(asu(x), asu(x), false, false); return asf(s[0]) + asf(s[1]); }
__device__ __forceinline__ float x32_add(float x) { auto s = __builtin_amdgcn_permlane32_swap(asu(x), asu(x), false, false); return asf(s[0]) + asf(s[1]); }
__device__ __forceinline__ float x16_max(float x) { auto s = __builtin_amdgcn_permlane16_swap(asu(x), asu(x), false, false); return fmaxf(asf(s[0]), asf(s[1])); }
__device__ __forceinline__ float x32_max(float x) { auto s = __builtin_amdgcn_permlane32_swap(asu(x), asu(x), false, false); return fmaxf(asf(s[0]), asf(s[1])); }
__device__ __forceinline__ float tr32(float a, float b) { auto s = __builtin_amdgcn_permlane32_swap(asu(a), asu(b), false, false); return asf(s[0]) + asf(s[1]); }
__device__ __forceinline__ float tr16(float a, float b) { auto s = __builtin_amdgcn_permlane16_swap(asu(a), asu(b), false, false); return asf(s[0]) + asf(s[1]); }
__device__ __forceinline__ float wave_sum(float v) {
  v += dpp<XOR1>(v); v += dpp<XOR2>(v); v += dpp<XOR7>(v); v += dpp<ROR8>(v);
  v = x16_add(v); return x32_add(v);
}
__device__ __forceinline__ float wave_max(float v) {
  v = fmaxf(v, dpp<XOR1>(v)); v = fmaxf(v, dpp<XOR2>(v)); v = fmaxf(v, dpp<XOR7>(v)); v = fmaxf(v, dpp<ROR8>(v));
  v = x16_max(v); return x32_max(v);
}
__device__ __forceinline__ rsrc_t mkrs(const void* p, unsigned bytes) { return __builtin_amdgcn_make_buffer_rsrc((void*)p, 0, (int)bytes, 0x00020000); }
__device__ __forceinline__ rsrc_t selr(bool c, rsrc_t a, rsrc_t b) { return c ? a : b; }
__device__ __forceinline__ unsigned selu(bool c, unsigned a, unsigned b) { return c ? a : b; }
template <int AUX> __device__ __forceinline__ v4u ld4(rsrc_t r, unsigned vo, unsigned so) { return __builtin_amdgcn_raw_buffer_load_b128(r, (int)vo, (int)so, AUX); }
template <int AUX> __device__ __forceinline__ v2u ld2(rsrc_t r, unsigned vo, unsigned so) { return __builtin_amdgcn_raw_buffer_load_b64(r, (int)vo, (int)so, AUX); }
__device__ __forceinline__ void st4c(v4u v, rsrc_t r, unsigned vo) { __builtin_amdgcn_raw_buffer_store_b128(v, r, (int)vo, 0, 16); }
__device__ __forceinline__ void st1c(unsigned v, rsrc_t r, unsigned vo) { __builtin_amdgcn_raw_buffer_store_b32(v, r, (int)vo, 0, 16); }
template <unsigned M> __device__ __forceinline__ unsigned tag(unsigned u, unsigned t) { return (u & ~M) | (t & M); }
__device__ __forceinline__ v4u tag4(const f32x4& v, const v4u& t) { return (v4u){tag<1>(asu(v[0]), t[0]), tag<1>(asu(v[1]), t[1]), tag<1>(asu(v[2]), t[2]), tag<1>(asu(v[3]), t[3])}; }
__device__ __forceinline__ bool arrived(const v4u& v, const v4u& o) { return __builtin_amdgcn_ballot_w64(v[0] == o[0] || v[1] == o[1] || v[2] == o[2] || v[3] == o[3]) == 0; }
__device__ __forceinline__ unsigned lowb(const v4u& v) { return __builtin_amdgcn_perm(v[1], v[0], 0x0c0c0400u) | __builtin_amdgcn_perm(v[3], v[2], 0x04000c0cu); }
#define SB() __builtin_amdgcn_sched_barrier(0)
__device__ __forceinline__ void bar() { __builtin_amdgcn_s_waitcnt(0xC07F); __builtin_amdgcn_s_barrier(); }
#define WAITV(n) __builtin_amdgcn_s_waitcnt(((n) & 15) | (((n) >> 4) << 14) | 0x0F70)
template <int V> struct IC { static constexpr int value = V; constexpr operator int() const { return V; } };
template <int A, int B, class Fn> __device__ __forceinline__ void static_for(Fn&& f) { if constexpr (A < B) { f(IC<A>{}); static_for<A + 1, B>(f); } }
#define LDS_FENCE() asm volatile("" ::: "memory")

constexpr float EXP_C = 0.0625f * 1.4426950408889634f;

struct Att {
  float m, l;
  f32x4 a[2];
};
__device__ __forceinline__ f32x4 mfma(const v4u& a, const v4u& b, const f32x4& c) {
  return __builtin_amdgcn_mfma_f32_16x16x32_bf16(__builtin_bit_cast(bf16x8, a), __builtin_bit_cast(bf16x8, b), c, 0, 0, 0);
}
__device__ __forceinline__ float red_max(float v) { v = fmaxf(v, dpp<XOR1>(v)); v = fmaxf(v, dpp<XOR2>(v)); return x32_max(x16_max(v)); }

template <int NL, class ISSUE>
__device__ __forceinline__ float scores(v4u (&kb)[8], const v4u (&qb)[8], int lane, ISSUE issue) {
  f32x4 d = {0.f, 0.f, 0.f, 0.f};
#pragma unroll
  for (int i = 0; i < NL; ++i) {
    d = mfma(kb[i], qb[i], d);
    asm volatile("" : "+v"(d));
    issue(i);
    SB();
  }
  const bool odd = lane & 1;
  return rbf((odd ? d[3] : d[0]) + dpp<XOR1>(odd ? d[1] : d[2]));
}
template <class ISSUE>
__device__ __forceinline__ float phase_k4(v4u (&kb)[8], const v4u (&qb)[8], Att& st, int lane, ISSUE issue) {
  float s = scores<4>(kb, qb, lane, issue);
  s = (lane & 2) ? -__builtin_inff() : s;
  const float mn = fmaxf(st.m, red_max(s));
  const float al = __builtin_amdgcn_exp2f((st.m - mn) * EXP_C);
  const float p = __builtin_amdgcn_exp2f((s - mn) * EXP_C);
  st.m = mn; st.l = st.l * al + p;
  st.a[0] *= al; st.a[1] *= al;
  SB();
  return p;
}
template <int I, int O, class ISSUE>
__device__ __forceinline__ void pv_step(v4u (&vb)[8], unsigned pp, const v4u& pm, Att& st, ISSUE& issue) {
  const v4u pb = pm & (unsigned)__builtin_amdgcn_update_dpp(0, (int)pp, I * 0x55, 0xf, 0xf, true);
  st.a[0] = mfma(vb[O + 2 * I], pb, st.a[0]);
  asm volatile("" : "+v"(st.a[0]));
  issue(O + 2 * I);
  SB();
  st.a[1] = mfma(vb[O + 2 * I + 1], pb, st.a[1]);
  asm volatile("" : "+v"(st.a[1]));
  issue(O + 2 * I + 1);
  SB();
}
template <int NL, class ISSUE>
__device__ __forceinline__ void phase_v(v4u (&vb)[8], float p, const v4u& pm, Att& st, ISSUE issue) {
  constexpr int O = 8 - NL;
  const unsigned pp = __builtin_bit_cast(unsigned, (bf16x2){(__bf16)p, (__bf16)p});
  pv_step<0, O>(vb, pp, pm, st, issue); pv_step<1, O>(vb, pp, pm, st, issue);
  if constexpr (NL == 8) { pv_step<2, O>(vb, pp, pm, st, issue); pv_step<3, O>(vb, pp, pm, st, issue); }
}

template <int N, int GAP, int SL>
__device__ __forceinline__ v4u poll(const v4u& r, unsigned pa, const v4u& old) {
  v4u d;
  unsigned long long t0, t1, t2;
  int n = SPIN_CAP;
  asm volatile(
      "s_nop 4\n\t"
      "buffer_load_dwordx4 v[200:203], %5, %6, 0 offen sc1\n\t"
      ".rept %11\n\t"
      "s_sleep %12\n\t"
      "buffer_load_dwordx4 v[200:203], %5, %6, 0 offen sc1\n\t"
      ".endr\n"
      "1:\n\t"
      "s_waitcnt vmcnt(%11)\n\t"
      "v_cmp_eq_u32 vcc, v200, %7\n\t"
      "v_cmp_eq_u32 %1, v201, %8\n\t"
      "v_cmp_eq_u32 %2, v202, %9\n\t"
      "v_cmp_eq_u32 %3, v203, %10\n\t"
      "s_or_b64 vcc, vcc, %1\n\t"
      "s_or_b64 %2, %2, %3\n\t"
      "s_or_b64 exec, vcc, %2\n\t"
      "s_cbranch_scc0 2f\n\t"
      ".if %13\n\ts_sleep %13\n\t.endif\n\t"
      "buffer_load_dwordx4 v[200:203], %5, %6, 0 offen sc1\n\t"
      "s_sub_u32 %4, %4, 1\n\t"
      "s_cbranch_scc0 1b\n"
      "2:\n\t"
      "s_mov_b64 exec, -1"
      : "=&{v[200:203]}"(d), "=&s"(t0), "=&s"(t1), "=&s"(t2), "+s"(n)
      : "v"(pa), "s"(r), "v"(old[0]), "v"(old[1]), "v"(old[2]), "v"(old[3]), "n"(N - 1), "n"(GAP), "n"(SL)
      : "vcc", "scc", "memory");
  return d;
}
__device__ __forceinline__ void poll_end(const v4u& d) { asm volatile("s_waitcnt vmcnt(0)" :: "{v[200:203]}"(d)); }

#define STAMP(k) do {} while (0)
#define STAMP_OUT() do {} while (0)

__device__ __forceinline__ f32x2 ln_stats(const v4u& xa, int lane) {
  f32x2 xv[4];
  float sm = 0.f;
#pragma unroll
  for (int j = 0; j < 4; ++j) { xv[j] = unp(xa[j]); sm += xv[j].x + xv[j].y; }
  const float mean = wave_sum(sm) * (1.0f / D);
  float sq = 0.f;
#pragma unroll
  for (int j = 0; j < 4; ++j) { const f32x2 d = xv[j] - mean; sq += d.x * d.x + d.y * d.y; }
  sq = lane < 56 ? sq : 0.f;
  const float var = rbf(wave_sum(sq) * (1.0f / D));
  const float sd = rbf(sqrtf(rbf(var + 1e-5f)));
  return (f32x2){rbf(mean), 1.0f / sd};
}
__device__ __forceinline__ float ln_apply(float x, f32x2 st) { return rbf(rbf(x - st.x) * st.y); }

__global__ void __launch_bounds__(NT, 2)
fused_kernel(const u16* __restrict__ xg, const u16* __restrict__ kc, const u16* __restrict__ vc, const u16* __restrict__ wq,
             const u16* __restrict__ wo, const u16* __restrict__ wg, const u16* __restrict__ wu, const u16* __restrict__ wd,
             u16* __restrict__ out, char* ws) {
  __shared__ __attribute__((aligned(16))) float s_part[8][512];
  __shared__ __attribute__((aligned(16))) float s_mv[8][64];
  __shared__ __attribute__((aligned(16))) unsigned s_q[256];
  __shared__ __attribute__((aligned(16))) unsigned s_qs[64];
  __shared__ __attribute__((aligned(16))) unsigned s_old[8][256];
  __shared__ __attribute__((aligned(16))) unsigned s_dst[8][256];
  __shared__ __attribute__((aligned(16))) float s_ml[2][16];
  __shared__ __attribute__((aligned(16))) float s_o[4][32];
  __shared__ __attribute__((aligned(16))) float s_ml2[16];
  __shared__ __attribute__((aligned(16))) float s_h[16];
  __shared__ __attribute__((aligned(16))) float s_r[9][16];
  __shared__ __attribute__((aligned(16))) float s_sc[64][32];
  __shared__ int s_ctl[64];
  const int tid = threadIdx.x, lane = tid & 63, w = __builtin_amdgcn_readfirstlane(tid >> 6);
  const int bi = blockIdx.x, h = (((bi & 1) ? bi + 5 : bi) & 6) | (bi >> 7), s = ((bi >> 3) & 15) * 2 + (bi & 1);
  const int b = h * 32 + s;
  static_assert(8 * TQ + 8 * TF + 6 * TFY + 10 * TY == 4096 && TQ - NP > 80 && TF - NP > 80 && TFY - NP > 80 && TY - NP > 80, "tickets");
  static_assert(TQ - NP <= 128 && TF - NP <= 128 && TFY - NP <= 128 && TY - NP <= 128, "tickets");
  const int nt = s < 8 ? TQ : s < 16 ? TF : s < 22 ? TFY : TY, nk = (nt - NP) >> 1;
  const int first = s * TY - min(s, 22) * (TY - TFY) - min(s, 16) * (TFY - TF) - min(s, 8) * (TF - TQ);
  asm volatile("" :: "s"(xg), "s"(kc), "s"(vc), "s"(wq), "s"(wo), "s"(wg), "s"(wu), "s"(wd), "s"(out), "s"(ws));
  STAMP(0);
  const rsrc_t rx = mkrs(xg, D * 2);
  const rsrc_t rk = mkrs(kc + (size_t)h * SK * DH, SK * DH * 2);
  const rsrc_t rv = mkrs(vc + (size_t)h * SK * DH, SK * DH * 2);
  const rsrc_t rws = mkrs(ws, WS_END);
  const unsigned l16 = lane * 16;
  auto ubase = [&](int u) -> unsigned { return (unsigned)(first + 2 * u) * 4096u; };
  const unsigned kvo = ((lane >> 1) & 7) * 512 + (lane >> 4) * 32 + (lane & 1) * 16, vvo = (lane >> 4) * 1024 + (lane & 15) * 16;
  auto ldk = [&](unsigned base, int i) { return ld4<2>(rk, kvo + (i & 3) * 128, base + (i >> 2) * 4096); };
  const unsigned area = (unsigned)b * AREA;
  auto area_of = [&](int ss) -> unsigned { return (unsigned)(h * 32 + ss) * AREA; };
  const unsigned lmask = lane < 56 ? 0u : OOB;

  const bool isq = s < 8, isf = s >= 8 && s < 22, isy = s >= 16;
  const int f = h * 14 + s - 8, y = h * 16 + s - 16, ro = h * 4 + (s >> 2);
  const bool isr = s < 16 && ((s + h) & 3) == 0 && ro < 28;
  const bool tile = isq || (isf && w < 4);
  const bool leader = (!(NOHS & 1) && b == NWG - 1 && w == 1), member = (w == 0);

  const unsigned pa = isy ? (w < 4 ? area + A_O1 + w * 1024 + l16 : (w == 7 && y < 112) ? area + A_F + (lane & 31) * 16 : OOB) : isr ? area + A_Y2 + w * 1024 + l16 : OOB;
  const unsigned mo_ = area_of(16 + (lane & 8) + (lane >> 4)) + A_O1 + s * 128 + (lane & 7) * 16;
  const int fd = (f & ~3) + ((lane >> 3) & 3);
  const unsigned mf = ((unsigned)((fd >> 4) * 32 + 16 + (fd & 15)) * AREA + A_F + (f & 3) * 128 + (lane & 7) * 16) | (lane < 32 ? 0u : OOB);
  auto my = [&](int p) -> unsigned { return (unsigned)((p >> 4) * 32 + (p & 12) + ((-(p >> 4)) & 3)) * AREA + A_Y2 + y * 64 + (p & 3) * 16; };
  v4u eo, ed;
  auto da_of = [&]() {
    const int p = (w - 2) * 64 + lane;
    unsigned da = (isy && p < 112) ? my(p) : OOB;
    return (w & 6) == 2 ? da : (w & 6) == 4 ? mo_ + (w - 4) * 4 * AREA : (w == 6 && isf) ? mf : OOB;
  };
  if (EO == 1) { eo = ld4<OA>(rws, pa, 0); SB(); }
  if (EO == 3 || (EO == 4 && !tile)) { eo = ld4<OA>(rws, pa, 0); ed = ld4<OB>(rws, da_of(), 0); SB(); }
  if (EO == 5) { ed = ld4<OB>(rws, da_of(), 0); SB(); }
  const v4u sent = {SENT, SENT, SENT, SENT};
  unsigned poll_off = lane == 1 ? WS_GO : lane < 4 ? WS_T - 32 + l16 : OOB;
  poll_off = lane == 0 ? WS_REQ + (b >> 2) * 16 : poll_off;
  poll_off = leader ? (ES ? lane * 4 * AREA + (lane & 3) * 256 : WS_ECHO + l16) : poll_off;
  auto echoes = [&](unsigned seen) {
    v4u r;
#pragma unroll
    for (int j = 0; j < 4; ++j) r[j] = __builtin_amdgcn_raw_buffer_load_b32(rws, (int)(poll_off | (seen >> j << 31)), j * AREA, 16);
    return r;
  };
  v4u pv = {0u, 0u, 0u, 0u}, e1 = {0u, 0u, 0u, 0u};
  unsigned go_ref = 0;
  int* const ctl = &s_ctl[lane];
  auto ctl_get = [&](int i) { return __builtin_amdgcn_readfirstlane(__hip_atomic_load(&s_ctl[i], __ATOMIC_RELAXED, __HIP_MEMORY_SCOPE_WORKGROUP)); };
  auto ctl_set = [&](int i) { __hip_atomic_store(&s_ctl[i], 1, __ATOMIC_RELAXED, __HIP_MEMORY_SCOPE_WORKGROUP); };
  const unsigned qoff = WS_Q + h * 2048 + l16;
  v4u qv0, qv1;
  if (member) {
    qv0 = ld4<16>(rws, qoff, 0);
    qv1 = ld4<16>(rws, qoff, 1024);
    SB();
    int c = lane == C_NEXT ? (isq ? TOFFQ : TOFF) * 128 : 0;
    c = lane == C_TICK ? 40 : c;
    c = lane == C_T0 ? (int)(__builtin_amdgcn_s_memrealtime() & 0x7fffffff) : c;
    __hip_atomic_store(ctl, c, __ATOMIC_RELAXED, __HIP_MEMORY_SCOPE_WORKGROUP);
    st1c(c, rws, (lane == C_T0 && (bi >> 3) == 12) ? WS_T + (bi & 7) * 4 : OOB);
  }
  *reinterpret_cast<v4u*>(&s_sc[w * 8][lane * 4]) = sent;
  int t0 = 0;
  auto now = [&]() { return ((int)__builtin_amdgcn_s_memrealtime() - t0) & 0x7fffff; };
  const int neg = lane == C_NEXT ? 0 : (int)0x80000000;
  const int xcc = __builtin_amdgcn_s_getreg(20 | (3 << 11));
  constexpr auto tkf = [](int t, int sh) { return TAU + sh * 128 / ((t - NP) / 2 - 8); };
  constexpr auto tvf = [](int t, int sh) { return TAU - sh * 128 / ((t - NP) / 2 + 8 - PL); };
  int tk = s < 8 ? tkf(TQ + 16, SQ) : s < 16 ? tkf(TF, SF_) : s < 22 ? tkf(TFY, SFY) : tkf(TY, SY);
  int tv = s < 8 ? tvf(TQ, SQ) : s < 16 ? tvf(TF, SF_) : s < 22 ? tvf(TFY, SFY) : tvf(TY, SY);
  int inc = lane == C_NEXT ? tk : 0;
  auto token = [&](int burst) {
    const int t = max(now() * 128 - burst, 0);
    __hip_atomic_fetch_max(ctl, t | neg, __ATOMIC_RELAXED, __HIP_MEMORY_SCOPE_WORKGROUP);
    return __hip_atomic_fetch_add(ctl, inc, __ATOMIC_RELAXED, __HIP_MEMORY_SCOPE_WORKGROUP);
  };
  auto due_of = [&](int x) { return __builtin_amdgcn_readlane(x, C_NEXT) >> 7; };
  auto until = [&](int due) {
#pragma unroll 1
    for (int it = 0; it < 4096 && now() < due; ++it) __builtin_amdgcn_s_sleep(1);
    SB();
  };
  unsigned qs0, qs1;
  auto q_there = [&]() { return __builtin_amdgcn_ballot_w64(((lowb(qv0) ^ qs0) & (lowb(qv1) ^ qs1)) != 0xFFFFFFFFu) == 0; };
  auto load_q = [&]() { qv0 = ld4<16>(rws, qoff, 0); qv1 = ld4<16>(rws, qoff, 1024); };
  auto publish_q = [&]() {
    *reinterpret_cast<v2u*>(&s_q[lane * 2]) = (v2u){__builtin_amdgcn_perm(qv0[1], qv0[0], 0x07060302u), __builtin_amdgcn_perm(qv0[3], qv0[2], 0x07060302u)};
    *reinterpret_cast<v2u*>(&s_q[128 + lane * 2]) = (v2u){__builtin_amdgcn_perm(qv1[1], qv1[0], 0x07060302u), __builtin_amdgcn_perm(qv1[3], qv1[2], 0x07060302u)};
    LDS_FENCE();
    __builtin_amdgcn_s_waitcnt(0xC07F);
    ctl_set(C_Q);
  };
  v4u pre[4][8], ring[8];
  auto unit0 = [&]() {
#pragma unroll
    for (int i = 0; i < 8; ++i) { pre[0][i] = ldk(ubase(w), i); SB(); }
    if (EO == 2) { eo = ld4<OA>(rws, pa, 0); SB(); }
  };
  SB();
  if (tile) {
    v4u tw[8];
    const int oct = isq ? 7 * w : (f & 3) * 14 + (w >> 1) * 7;
    const int stride = isq ? NQ * 2 : F * 2;
    const rsrc_t rt = mkrs(isq ? wq : (w & 1) ? wu : wg, D * stride);
    const unsigned to = (unsigned)((oct + (lane >> 3)) * 8 * stride + (isq ? h * 8 + s : f >> 2) * 128 + (lane & 7) * 16) | lmask;
    const v4u xa = ld4<0>(rx, l16, 0);
    const v4u xb = ld4<0>(rx, (unsigned)(oct + (lane >> 3)) * 16 | lmask, 0);
    SB();
#pragma unroll
    for (int i = 0; i < 8; ++i) { tw[i] = ld4<2>(rt, to, i * stride); SB(); }
    if (EO == 4) { eo = ld4<OA>(rws, pa, 0); ed = ld4<OB>(rws, da_of(), 0); SB(); }
    LDS_FENCE(); bar(); LDS_FENCE();
    t0 = ctl_get(C_T0);
    SB();
    const f32x2 ls = ln_stats(xa, lane);
    f32x2 acc[4];
#pragma unroll
    for (int i = 0; i < 8; ++i) {
      const float xn = ln_apply(asf((i & 1) ? (xb[i >> 1] & 0xffff0000u) : (xb[i >> 1] << 16)), ls);
#pragma unroll
      for (int j = 0; j < 4; ++j) acc[j] = i ? acc[j] + unp(tw[i][j]) * xn : unp(tw[i][j]) * xn;
    }
    float y0 = tr32(acc[0].x, acc[2].x), y1 = tr32(acc[0].y, acc[2].y), y2 = tr32(acc[1].x, acc[3].x), y3 = tr32(acc[1].y, acc[3].y);
    y0 = tr16(y0, y2); y1 = tr16(y1, y3);
    const bool s_ = lane & 8;
    y0 = (s_ ? y1 : y0) + dpp<ROR8>(s_ ? y0 : y1);
    s_mv[w][(lane & 7) * 8 + (lane >> 3)] = y0;
    STAMP(6);
    token(isq ? QBURST : PBURST);
    unit0();
    if (isq) {
      LDS_FENCE(); bar(); LDS_FENCE();
      if (member) {
        float a = 0.f;
#pragma unroll
        for (int k = 0; k < 8; ++k) a += s_mv[k][lane];
        s_qs[lane] = f2bf(a) << 16;
        LDS_FENCE();
        const v4u qs = *reinterpret_cast<const v4u*>(&s_qs[(lane & 15) * 4]) | (~((s & 4) ? qv1 : qv0) & 0xffffu);
        st4c(qs, rws, (lane >> 4) == (s & 3) ? qoff + (s >> 2) * 1024 : OOB);
      }
    } else {
      LDS_FENCE();
      __builtin_amdgcn_s_waitcnt(0xC07F);
      __hip_atomic_fetch_add(ctl, lane == C_FC ? 1 : 0, __ATOMIC_RELAXED, __HIP_MEMORY_SCOPE_WORKGROUP);
    }
  } else if (leader) {
    e1 = ES ? echoes(0) : ld4<16>(rws, poll_off, 0);
    go_ref = __builtin_amdgcn_raw_buffer_load_b32(rws, (int)WS_GO, 0, 16);
  }
  SB();
  STAMP(5);
  SB();
  bool ok = !member;
  unsigned vt = 0;
  static_for<0, 40>([&](auto N) {
    constexpr int n = N;
    if constexpr (n == 0) {
      if (!tile) unit0();
    }
    if constexpr (n == 8) {
      if (!tile) {
        LDS_FENCE(); bar(); LDS_FENCE();
        t0 = ctl_get(C_T0);
        if (leader) {
          e1 += 1u;
          go_ref += 1u;
          st4c(e1, rws, WS_REQ + l16);
        }
      }
    }
    if constexpr (n > 0 && n % 8 == 0) {
      const int x = token(isq ? QBURST : PBURST);
      if constexpr (n > QFIRST) {
        if (!ok) {
          ok = q_there();
          if (ok) publish_q();
        }
      }
      if constexpr (n == OPOS + 16) {
        *reinterpret_cast<v4u*>(&s_old[w][lane * 4]) = (int)pa < 0 ? sent : eo;
        *reinterpret_cast<v4u*>(&s_dst[w][lane * 4]) = ~ed;
        LDS_FENCE();
        __hip_atomic_fetch_add(ctl, lane == C_OLD ? 1 : 0, __ATOMIC_RELAXED, __HIP_MEMORY_SCOPE_WORKGROUP);
      }
      SB();
      until(due_of(x));
      if constexpr (n == 8) {
        if (member) { qs0 = lowb(qv0); qs1 = lowb(qv1); }
      }
      if constexpr (n >= QFIRST) {
        if (!ok) load_q();
      }
      if constexpr (n == OPOS) {
        if (EO == 0 || EO == 5) eo = ld4<OA>(rws, pa, 0);
        if (EO < 3) ed = ld4<OB>(rws, da_of(), 0);
      }
      SB();
    }
    if constexpr (VT && n == VPOS) {
      vt = __builtin_amdgcn_raw_buffer_load_b32(rv, lane < VT ? (int)((w * VT + lane) * ((nt * 4096 - 4) / (8 * VT - 1) & ~3)) : (int)OOB, first * 4096, VX);
      SB();
    }
    if constexpr (n == QPOS) {
      STAMP(9);
      if (ES && leader) pv = echoes(0);
      else pv = ld4<16>(rws, (member || leader) ? poll_off : OOB, 0);
      SB();
    }
    if constexpr (n >= 8) {
      if constexpr (n >= PACE) WAITV(PACE + ((n >= QPOS && n < QPOS + PACE) ? 1 : 0) + ((VT && n >= VPOS && n < VPOS + PACE) ? 1 : 0) + ((n >= OPOS && n < OPOS + PACE) ? (EO == 0 ? 2 : (EO == 3 || EO == 4) ? 0 : 1) : 0));
      if constexpr (n < 32) pre[n >> 3][n & 7] = ldk(ubase((n & 56) + w), n & 7); else ring[n & 7] = ldk(ubase(32 + w), n & 7);
    }
    SB();
    if constexpr (n - PACE == 7 || n - PACE == 15 || n - PACE == 23 || n - PACE == 31) STAMP(16 + (n - PACE) / 8);
  });
  asm volatile("" :: "v"(vt));
  inc = lane == C_TICK ? 1 : inc;
  int tok = token(KBURST);
  STAMP(10);
  bool svc = member || leader;
  unsigned seenm = 0, last_echo = 0;
  bool echoed = false;
  auto service = [&]() {
    if (leader) {
      seenm |= (pv[0] == e1[0] ? 1u : 0u) | (pv[1] == e1[1] ? 2u : 0u) | (pv[2] == e1[2] ? 4u : 0u) | (pv[3] == e1[3] ? 8u : 0u);
      svc = __builtin_amdgcn_ballot_w64(seenm != 15u) != 0;
      st1c(go_ref, rws, (lane == 0 && !svc) ? WS_GO : OOB);
    } else {
      const unsigned req = (unsigned)__builtin_amdgcn_readfirstlane((int)((b & 2) ? ((b & 1) ? pv[3] : pv[2]) : ((b & 1) ? pv[1] : pv[0])));
      const unsigned go = (unsigned)__builtin_amdgcn_readlane((int)pv[0], 1);
      go_ref = echoed ? go_ref : go;
      svc = go == (unsigned)__builtin_amdgcn_readfirstlane((int)go_ref);
      if (NOHS & 1) svc = false;
      const bool rdy = ctl_get(C_OLD) == 8;
      if (ES && EL) st4c((v4u){req, req, req, req}, rws, (lane < EL && svc && rdy && !(echoed && req == last_echo)) ? area + ((b >> 2) & 3) * 256 + l16 : OOB);
      else st1c(req, rws, (lane == 0 && svc && rdy && !(echoed && req == last_echo)) ? (ES ? area + ((b >> 2) & 3) * 256 : WS_ECHO + b * 4) : OOB);
      echoed |= rdy; last_echo = req;
      if (!svc) {
        if (isf) {
          while (ctl_get(C_FC) != 4) __builtin_amdgcn_s_sleep(1);
          LDS_FENCE();
          const int j = (lane >> 3) & 3, pc = lane & 7, m = pc >> 2, col = j * 16 + (pc & 3) * 4;
          const f32x4 v = *reinterpret_cast<const f32x4*>(&s_mv[m][col]) + *reinterpret_cast<const f32x4*>(&s_mv[m + 2][col]);
          __builtin_amdgcn_raw_buffer_store_b128(tag4(v, *reinterpret_cast<const v4u*>(&s_dst[6][lane * 4])), rws, (NOHS & 2) ? (int)OOB : (int)mf, 0, SF);
        }
      }
    }
    if (ES && leader) pv = echoes(seenm);
    else pv = ld4<16>(rws, svc ? poll_off : OOB, 0);
    SB();
  };

  {
    if (!ok) {
      ok = q_there();
      if (ok) publish_q();
    }
#pragma unroll 1
    for (int it = 0; it < SPIN_CAP && !ctl_get(C_Q); ++it) {
      if (svc && it >= QTRIES) { service(); asm volatile("" :: "v"(pv)); }
      if (!ok) {
        load_q();
        ok = q_there() | !svc;
        if (ok) publish_q();
      } else {
        __builtin_amdgcn_s_sleep(1);
      }
    }
  }
  LDS_FENCE();
  v4u qb[8];
#pragma unroll
  for (int i = 0; i < 4; ++i) {
    const v4u z = {0u, 0u, 0u, 0u}, q = *reinterpret_cast<const v4u*>(&s_q[(lane & 8) * 16 + i * 32 + (lane >> 4) * 8 + (lane & 1) * 4]);
    qb[i] = (lane & 2) ? z : q; qb[4 + i] = (lane & 2) ? q : z;
  }
  v4u pm;
#pragma unroll
  for (int j = 0; j < 4; ++j) pm[j] = ((lane >> 1) & 3) == j ? ((lane & 1) ? 0xffff0000u : 0xffffu) : 0u;
  STAMP(1);
  SB();

  Att st;
  v4u wa = sent, wb = sent;
  int sx = 20;
  {
    float* const sc = &s_sc[0][(lane >> 3) * 4 + (lane & 3)];
    rsrc_t nra, nrb;
    unsigned nva, nvb, nba, nbb, nsa, nsb;
    int kind = 0, nu = 32 + w, cu;
    auto next = [&]() {
      cu = nu;
      const int t = __builtin_amdgcn_readlane(tok, C_TICK);
      const bool fx = t < 2 * nk, tw = t >= nt;
      kind = fx ? (t >= nk ? 1 : 0) : tw ? 3 : 2;
      nu = t >= nk ? t - nk : t;
      nba = (unsigned)(fx ? first + 2 * nu : tw ? 0x80000 : first + t) * 4096u;
      nbb = fx ? nba + 4096 : nba;
      nra = selr(kind == 1, rv, rk);
      nrb = selr(kind == 0, rk, rv);
      nva = selu(kind == 1, vvo, kvo); nvb = selu(kind == 0, kvo, vvo);
      nsa = selu(kind == 1, 256, 128); nsb = selu(kind == 0, 128, 256);
      until(tw ? 0 : due_of(tok));
      const int u = t + 8 - nk;
      inc = lane == C_NEXT ? (t + PL >= 2 * nk ? PP : (int)selu(u >= 0, tv, tk) + ((unsigned)(u + XA) < XA + XB ? TX : 0)) : inc;
      SB();
    };
    auto ldn = [&](int i) { ring[i] = i < 4 ? ld4<2>(nra, nva, nba + i * nsa) : ld4<2>(nrb, nvb, nbb + (i - 4) * nsb); };
    float mx;
    if (member) {
      const v4u d = pv - (unsigned)t0;
      const bool bad = max(max(d[0] + 512u, d[1] + 512u), max(d[2] + 512u, d[3] + 512u)) > 1024u;
      const int sm = (int)((d[0] + d[1]) + (d[2] + d[3]));
      const int lead = (__builtin_amdgcn_ballot_w64(bad) & 12) ? 0 : __builtin_amdgcn_readlane(sm, 2) + __builtin_amdgcn_readlane(sm, 3) + DB;
      tk += (lead * DK) >> 5; tv += (lead * DK) >> 5;
      __hip_atomic_fetch_add(ctl, lane == C_LEAD ? (lead * DG) >> 5 : (lane == C_NEXT ? (lead * DS) >> 5 : 0), __ATOMIC_RELAXED, __HIP_MEMORY_SCOPE_WORKGROUP);
    }
    if (svc) service();
    {
      const float s0 = scores<8>(pre[0], qb, lane, [&](int) {});
      const float s1 = scores<8>(pre[1], qb, lane, [&](int) {});
      const float s2 = scores<8>(pre[2], qb, lane, [&](int) {});
      const float s3 = scores<8>(pre[3], qb, lane, [&](int) {});
      sc[w * 32] = s0; sc[(8 + w) * 32] = s1; sc[(16 + w) * 32] = s2; sc[(24 + w) * 32] = s3;
      mx = fmaxf(fmaxf(s0, s1), fmaxf(s2, s3));
      SB();
      STAMP(11);
    }
#pragma unroll 1
    while (kind == 0) {
      next();
      const float sv = scores<8>(ring, qb, lane, ldn);
      sc[cu * 32] = sv;
      tok = token(KBURST);
      mx = fmaxf(mx, sv);
      STAMP(sx); ++sx;
      if (svc) service();
      SB();
    }
    STAMP(7);
    if (isy && w == 7) {
      const v4u old = *reinterpret_cast<const v4u*>(&s_old[7][lane * 4]);
      v4u d;
#pragma unroll 1
      for (int it = 0; it < SPIN_CAP; ++it) {
        d = ld4<PF>(rws, (NOHS & 2) ? OOB : pa, 0);
        if ((NOHS & 2) || arrived(d, old)) break;
        __builtin_amdgcn_s_sleep(1);
        LDS_FENCE();
      }
      float hv[4];
#pragma unroll
      for (int j = 0; j < 4; ++j) {
        float g = asf(d[j] & ~1u);
        g += dpp<ROR8>(g); g = rbf(x16_add(g));
        const float u = dpp<ROR4>(g);
        hv[j] = rbf(rbf(g / (1.0f + __expf(-g))) * u);
      }
      if (lane < 4) {
#pragma unroll
        for (int j = 0; j < 4; ++j) s_h[((lane & 1) * 4 + j) * 2 + (lane >> 1)] = hv[j];
      }
      SB();
    }
    st.m = red_max(mx); st.l = 0.f;
    st.a[0] = st.a[1] = (f32x4){0.f, 0.f, 0.f, 0.f};
#pragma unroll 1
    while (kind == 1) {
      float sv;
      for (;;) {
        sv = __hip_atomic_load(&sc[nu * 32], __ATOMIC_RELAXED, __HIP_MEMORY_SCOPE_WORKGROUP);
        if (__builtin_amdgcn_ballot_w64(asu(sv) == SENT) == 0) break;
        __builtin_amdgcn_s_sleep(1);
      }
      if (__builtin_amdgcn_ballot_w64(sv > st.m) != 0) {
        const float m2 = fmaxf(st.m, red_max(sv));
        const float al = __builtin_amdgcn_exp2f((st.m - m2) * EXP_C);
        st.m = m2; st.l *= al;
        st.a[0] *= al; st.a[1] *= al;
      }
      const float p = __builtin_amdgcn_exp2f((sv - st.m) * EXP_C);
      st.l += p;
      SB();
      next();
      phase_v<8>(ring, p, pm, st, ldn);
      tok = token(VBURST);
      STAMP(sx); ++sx;
      if (svc) service();
      SB();
    }
    STAMP(8);
#pragma unroll 1
    while (kind == 2) {
      next();
      const float p = phase_k4(ring, qb, st, lane, ldn);
      phase_v<4>(ring, p, pm, st, ldn);
      tok = token(VBURST);
      STAMP(sx); ++sx;
    }
#pragma unroll 1
    for (int it = 0; it < SPIN_CAP && svc; ++it) {
      service();
      asm volatile("" :: "v"(pv));
    }
    LDS_FENCE();
  }
  STAMP(12);
  asm volatile("" :: "v"(wa), "v"(wb));
  const lds4_t pr = (lds4_t)&s_part[0][lane * 4], pmr = (lds4_t)&s_ml[(lane >> 3) & 1][0];
  {
    const lds4_t pw = (lds4_t)&s_part[w][lane * 4];
    const lds1_t pml = (lds1_t)&s_ml[(lane >> 3) & 1][w];
    pw[0] = st.a[0]; pw[64] = st.a[1];
    float l = st.l;
    l += dpp<XOR1>(l); l += dpp<XOR2>(l); l = x32_add(x16_add(l));
    pml[0] = st.m; pml[8] = l;
  }
  if (isy && w >= 4) {
    v4u wt[6][2];
    const rsrc_t rwo = mkrs(wo + (size_t)y * 32 * D, XW ? 0 : 32 * D * 2);
    const rsrc_t rwd = mkrs(wd + (size_t)(y < 112 ? y : 0) * 16 * D, (y < 112 && !XW) ? 16 * D * 2 : 0);
    unsigned o0 = (lane >> 3) * (D * 2) + (w - 4) * 256 + (lane & 7) * 16, o1 = w == 7 ? OOB : o0 + 128;
    asm volatile("" : "+v"(o0), "+v"(o1));
    SB();
    LDS_FENCE(); bar(); LDS_FENCE();
    STAMP(2);
    if (TWD) __builtin_amdgcn_s_sleep(TWD);
    SB();
#pragma unroll
    for (int k = 0; k < 6; ++k) {
      const int i = (k + 4) % 6;
      wt[i][0] = ld4<2>(i < 4 ? rwo : rwd, o0, (i & 3) * 8 * D * 2);
      wt[i][1] = ld4<2>(i < 4 ? rwo : rwd, o1, (i & 3) * 8 * D * 2);
      SB();
    }
    const int p = (w - 4) * 32 + (lane >> 4) * 8 + (lane & 7);
    v4u dt = *reinterpret_cast<const v4u*>(&s_dst[2][p * 4]) & 1u;
    unsigned to = ((lane & 8) || p >= 112) ? OOB : my(p);
    const f32x2 hv = *reinterpret_cast<const f32x2*>(&s_h[(lane >> 3) * 2]);
    f32x4 acc[2] = {{0.f, 0.f, 0.f, 0.f}, {0.f, 0.f, 0.f, 0.f}};
    auto bop = [&](float a) -> v4u { return pm & __builtin_bit_cast(unsigned, (bf16x2){(__bf16)a, (__bf16)a}); };
    auto step = [&](int i, const v4u& pb) { acc[0] = mfma(wt[i][0], pb, acc[0]); acc[1] = mfma(wt[i][1], pb, acc[1]); };
    v4u h0 = bop(hv.x), h1 = bop(hv.y);
    lds4_t po = (lds4_t)&s_o[0][(lane >> 3) * 4];
    asm volatile("" : "+v"(dt), "+v"(to), "+v"(h0), "+v"(h1), "+v"(po));
    SB();
    if (FB == 0) {
      step(4, h0); step(5, h1);
      asm volatile("" : "+v"(acc[0]), "+v"(acc[1]));
    }
    STAMP(14);
    LDS_FENCE(); bar(); LDS_FENCE();
    STAMP(3);
    const f32x4 ma = *reinterpret_cast<const f32x4*>(&s_ml2[0]), mb = *reinterpret_cast<const f32x4*>(&s_ml2[4]);
    f32x4 pq[4];
#pragma unroll
    for (int k = 0; k < 4; ++k) pq[k] = po[k * 8];
    SB();
    if (FB == 1) {
      step(4, h0); step(5, h1);
      asm volatile("" : "+v"(acc[0]), "+v"(acc[1]));
      SB();
    }
    const float M = fmaxf(fmaxf(ma[0], ma[2]), fmaxf(mb[0], mb[2]));
    const float e0 = __builtin_amdgcn_exp2f((ma[0] - M) * EXP_C), e1_ = __builtin_amdgcn_exp2f((ma[2] - M) * EXP_C);
    const float e2 = __builtin_amdgcn_exp2f((mb[0] - M) * EXP_C), e3 = __builtin_amdgcn_exp2f((mb[2] - M) * EXP_C);
    const float inv = __builtin_amdgcn_rcpf((ma[1] * e0 + ma[3] * e1_) + (mb[1] * e2 + mb[3] * e3));
    const f32x4 av = ((pq[0] * e0 + pq[1] * e1_) + (pq[2] * e2 + pq[3] * e3)) * inv;
    if (FB) {
      v4u pb[4];
#pragma unroll
      for (int i = 0; i < 4; ++i) pb[i] = bop(av[i]);
      asm volatile("" : "+v"(pb[0]), "+v"(pb[1]), "+v"(pb[2]), "+v"(pb[3]));
      SB();
      if (FB == 2) { step(4, h0); step(5, h1); }
#pragma unroll
      for (int i = 0; i < 4; ++i) step(i, pb[i]);
    } else {
#pragma unroll
      for (int i = 0; i < 4; ++i) step(i, bop(av[i]));
    }
    f32x4 z;
#pragma unroll
    for (int v = 0; v < 4; ++v) {
      const float a0 = asf((unsigned)__builtin_amdgcn_update_dpp((int)asu(acc[0][v]), (int)asu(acc[0][v]), ROR8, 0xc, 0xf, false));
      const float a1 = asf((unsigned)__builtin_amdgcn_update_dpp((int)asu(acc[1][v]), (int)asu(acc[1][v]), ROR8, 0xc, 0xf, false));
      z[v] = tr32(a0, a1);
    }
    __builtin_amdgcn_raw_buffer_store_b128(tag4(z, dt), rws, (int)to, 0, S2);
    STAMP(4);
    STAMP_OUT();
    __builtin_amdgcn_endpgm();
  }
  v4u old = *reinterpret_cast<const v4u*>(&s_old[w][lane * 4]);
  if (w < 2) {
    ldsu_t tp = (ldsu_t)&s_dst[4 + w][lane * 4];
    unsigned to = mo_ + w * 4 * AREA;
    lds4_t pp = pr + w * 64;
    asm volatile("" : "+v"(tp), "+v"(to), "+v"(pp), "+v"(old));
    SB();
    LDS_FENCE(); bar(); LDS_FENCE();
    STAMP(2);
    f32x4 ml[4], pt[8];
#pragma unroll
    for (int k = 0; k < 4; ++k) ml[k] = pmr[k];
#pragma unroll
    for (int k = 0; k < 8; ++k) pt[k] = pp[k * 128];
    const v4u t = *tp;
    float M = -3.0e38f;
#pragma unroll
    for (int k = 0; k < 8; ++k) M = fmaxf(M, ml[k >> 2][k & 3]);
    f32x4 e[2] = {(ml[0] - M) * EXP_C, (ml[1] - M) * EXP_C};
    f32x4 o = {0.f, 0.f, 0.f, 0.f};
#pragma unroll
    for (int k = 0; k < 8; ++k) {
      e[k >> 2][k & 3] = __builtin_amdgcn_exp2f(e[k >> 2][k & 3]);
      o += pt[k] * e[k >> 2][k & 3];
    }
    const f32x4 l4 = ml[2] * e[0] + ml[3] * e[1];
    const float L = (l4[0] + l4[2]) + (l4[1] + l4[3]);
    v4u msg;
#pragma unroll
    for (int j = 0; j < 3; ++j) msg[j] = tag<0x100>(__builtin_amdgcn_perm(asu(o[j]), asu(o[3]), 0x07060501u + j), t[j]);
    msg[3] = tag<1>(asu((lane & 1) ? L : M), t[3]);
    __builtin_amdgcn_raw_buffer_store_b128(msg, rws, (int)to, 0, S1);
    SB();
  } else {
    LDS_FENCE(); bar(); LDS_FENCE();
    STAMP(2);
  }
  const v4u rw4 = {(unsigned)(size_t)ws, (unsigned)((size_t)ws >> 32), WS_END, 0x00020000u};
  if (isy) {
    lds1_t po = (lane & 8) ? (lds1_t)&s_ml2[w * 2 + 1 + (lane == 8 ? 0 : 8)] : (lds1_t)&s_o[w][(lane & 7) * 4 + (lane >> 5) * 2 + ((lane >> 4) & 1)];
    lds1_t pq = (lds1_t)&s_ml2[w * 2];
    asm volatile("" : "+v"(po), "+v"(pq));
    if (G1) until(G1 + __builtin_amdgcn_readlane(tok, C_LEAD));
    STAMP(14);
    const v4u dr = poll<PD1, PG1, PS1>(rw4, pa, old);
    STAMP(15);
    const v4u d = dr & (v4u){~0x100u, ~0x100u, ~0x100u, ~1u};
    const float ot = dpp<XOR1>(asf(d[3]));
    const float m = (lane & 1) ? ot : asf(d[3]), l = (lane & 1) ? asf(d[3]) : ot;
    float M = fmaxf(m, dpp<ROR8>(m)); M = x16_max(M); M = x32_max(M);
    *pq = M;
    const float e = __builtin_amdgcn_exp2f((m - M) * EXP_C);
    const float v3 = asf(__builtin_amdgcn_perm(__builtin_amdgcn_perm(d[2], d[1], 0x04000c0cu), d[0], 0x0706000cu));
    float y0 = tr32(asf(d[0]) * e, asf(d[2]) * e), y1 = tr32(asf(d[1]) * e, v3 * e), L = x32_add(l * e);
    y0 = tr16(y0, y1); L = x16_add(L);
    const bool s_ = lane & 8;
    *po = (s_ ? L : y0) + dpp<ROR8>(s_ ? y0 : L);
    LDS_FENCE(); bar(); LDS_FENCE();
    STAMP(3);
    poll_end(dr);
    STAMP(4);
    STAMP_OUT();
    __builtin_amdgcn_endpgm();
  }
  if (isr) {
    const int col = (ro >> 1) * 32 + (ro & 1) * 4 + (lane & 3) * 8 + ((lane >> 2) & 3);
    float xo = asf((unsigned)xg[col] << 16);
    lds1_t po = (lds1_t)&s_r[(lane & 12) ? 8 : w][(lane & 3) * 4 + (lane >> 5) * 2 + ((lane >> 4) & 1)], pq = (lds1_t)&s_r[lane >> 4][lane & 15];
    const rsrc_t ro_ = mkrs(out, D * 2);
    unsigned oo = lane < 16 ? col * 2 : OOB;
    asm volatile("" : "+v"(xo), "+v"(po), "+v"(pq), "+v"(oo));
    if (G2) until(G2 + __builtin_amdgcn_readlane(tok, C_LEAD));
    const v4u dr = poll<PD2, PG2, PS2>(rw4, pa, old);
    const v4u d = dr & ~1u;
    float y0 = tr32(asf(d[0]), asf(d[2])), y1 = tr32(asf(d[1]), asf(d[3]));
    y0 = tr16(y0, y1);
    y0 += dpp<ROR8>(y0); y0 += dpp<ROR4>(y0);
    *po = y0;
    LDS_FENCE(); bar(); LDS_FENCE();
    if (w == 0) {
      float a = pq[0] + pq[64];
      a = x16_add(a); a = x32_add(a);
      __builtin_amdgcn_raw_buffer_store_b16((short)f2bf(xo + a), ro_, (int)oo, 0, SO);
    }
    STAMP(4);
    poll_end(dr);
  }
  if (!isr) STAMP(4);
  STAMP_OUT();
}

extern "C" void kernel_launch(void* const* d_in, const int* in_sizes, int n_in,
                              void* d_out, int out_size, void* d_ws, size_t ws_size,
                              hipStream_t stream) {
  fused_kernel<<<NWG, NT, 0, stream>>>((const u16*)d_in[0], (const u16*)d_in[1], (const u16*)d_in[2], (const u16*)d_in[3],
                                       (const u16*)d_in[4], (const u16*)d_in[6], (const u16*)d_in[7], (const u16*)d_in[8],
                                       (u16*)d_out, (char*)d_ws);
}
```

```cpp
#include <hip/hip_runtime.h>
#include <hip/hip_bf16.h>
#include <stdint.h>

#define QPOS 24
#define PACE 8
#define QTRIES 8
#define TQ 123
#define TF 131
#define TFY 129
#define TY 129
#define NP 4
#define TAU 3620
#define PP 3000
#define PL 8
#define TOFF 150
#define TOFFQ 180
#define QBURST 100000
#define PBURST 4000
#define KBURST 100000
#define VBURST 100000
#define QFIRST 24
#define OPOS 8
#define TWD 0
#define OA 16
#define OB 16
#define S1 16
#define S2 16
#define G1 3835
#define G2 3955
#define EO 3
#define PD1 2
#define PG1 6
#define PD2 2
#define PG2 6
#define PD3 2
#define PG3 6
#define G3 3835
#define SR 2
#define PS1 0
#define PS2 0
#define SO 0
#define DK 57
#define DS 0
#define DG 4
#define DB 0
#define SQ 0
#define SF_ 0
#define SFY 0
#define SY 0
#define XA 4
#define XB 4
#define TX 0
#define NOHS 0
#define ES 1
#define EL 8
#define SVM 0
#define SVL 0
#define FO 4
#define FX 0
#define FB 1
#define XW 0
#define DIAG 0
#define LITE 0

typedef unsigned v4u __attribute__((ext_vector_type(4)));
typedef unsigned v2u __attribute__((ext_vector_type(2)));
typedef float f32x4 __attribute__((ext_vector_type(4)));
typedef float f32x2 __attribute__((ext_vector_type(2)));
typedef __bf16 bf16x2 __attribute__((ext_vector_type(2)));
typedef __bf16 bf16x8 __attribute__((ext_vector_type(8)));
typedef unsigned short u16;
typedef __amdgpu_buffer_rsrc_t rsrc_t;
typedef __attribute__((address_space(3))) f32x4* lds4_t;
typedef __attribute__((address_space(3))) v4u* ldsu_t;
typedef __attribute__((address_space(3))) float* lds1_t;

constexpr int D = 448, SK = 32768, DH = 256, F = 1792, NQ = 4096;
constexpr int NWG = 256, NT = 512;
constexpr unsigned OOB = 0x80000000u;
constexpr unsigned SENT = 0xFFFFFFFFu;
constexpr unsigned AREA = 9216;
constexpr unsigned A_O1 = 1024;
constexpr unsigned A_F = 5120;
constexpr unsigned A_Y2 = 1024;
constexpr unsigned WS_REQ = NWG * AREA;
constexpr unsigned WS_ECHO = WS_REQ + 1024;
constexpr unsigned WS_GO = WS_ECHO + 1024;
constexpr unsigned WS_T = WS_GO + 256;
constexpr unsigned WS_Q = WS_T + 256;
constexpr unsigned WS_END = WS_Q + 8 * 2048;
constexpr int SPIN_CAP = 1 << 18;
constexpr int C_NEXT = 0, C_TICK = 1, C_GO = 2, C_Q = 3, C_T0 = 4, C_FC = 5, C_OLD = 6, C_DONE = 7, C_LEAD = 8;

__device__ __forceinline__ float asf(unsigned u) { return __builtin_bit_cast(float, u); }
__device__ __forceinline__ unsigned asu(float f) { return __builtin_bit_cast(unsigned, f); }
__device__ __forceinline__ float rbf(float f) { __bf16 b = (__bf16)f; return (float)b; }
__device__ __forceinline__ unsigned f2bf(float f) { __bf16 b = (__bf16)f; return (unsigned)__builtin_bit_cast(u16, b); }
__device__ __forceinline__ float dot2(unsigned k, unsigned q, float acc) {
  return __builtin_amdgcn_fdot2_f32_bf16(__builtin_bit_cast(bf16x2, k), __builtin_bit_cast(bf16x2, q), acc, false);
}
__device__ __forceinline__ f32x2 unp(unsigned u) { f32x2 r; r.x = asf(u << 16); r.y = asf(u & 0xffff0000u); return r; }
template <int CTRL> __device__ __forceinline__ float dpp(float x) {
  return asf((unsigned)__builtin_amdgcn_update_dpp(0, (int)asu(x), CTRL, 0xf, 0xf, true));
}
constexpr int XOR1 = 0xB1, XOR2 = 0x4E, XOR7 = 0x141, ROR8 = 0x128, ROR4 = 0x124, ROR2 = 0x122, BCAST = 0x150;
constexpr int QP_EVEN = 0xA0, QP_ODD = 0xF5;
__device__ __forceinline__ float x16_add(float x) { auto s = __builtin_amdgcn_permlane16_swap(asu(x), asu(x), false, false); return asf(s[0]) + asf(s[1]); }
__device__ __forceinline__ float x32_add(float x) { auto s = __builtin_amdgcn_permlane32_swap(asu(x), asu(x), false, false); return asf(s[0]) + asf(s[1]); }
__device__ __forceinline__ float x16_max(float x) { auto s = __builtin_amdgcn_permlane16_swap(asu(x), asu(x), false, false); return fmaxf(asf(s[0]), asf(s[1])); }
__device__ __forceinline__ float x32_max(float x) { auto s = __builtin_amdgcn_permlane32_swap(asu(x), asu(x), false, false); return fmaxf(asf(s[0]), asf(s[1])); }
__device__ __forceinline__ float tr32(float a, float b) { auto s = __builtin_amdgcn_permlane32_swap(asu(a), asu(b), false, false); return asf(s[0]) + asf(s[1]); }
__device__ __forceinline__ float tr16(float a, float b) { auto s = __builtin_amdgcn_permlane16_swap(asu(a), asu(b), false, false); return asf(s[0]) + asf(s[1]); }
__device__ __forceinline__ float wave_sum(float v) {
  v += dpp<XOR1>(v); v += dpp<XOR2>(v); v += dpp<XOR7>(v); v += dpp<ROR8>(v);
  v = x16_add(v); return x32_add(v);
}
__device__ __forceinline__ float wave_max(float v) {
  v = fmaxf(v, dpp<XOR1>(v)); v = fmaxf(v, dpp<XOR2>(v)); v = fmaxf(v, dpp<XOR7>(v)); v = fmaxf(v, dpp<ROR8>(v));
  v = x16_max(v); return x32_max(v);
}
__device__ __forceinline__ rsrc_t mkrs(const void* p, unsigned bytes) { return __builtin_amdgcn_make_buffer_rsrc((void*)p, 0, (int)bytes, 0x00020000); }
__device__ __forceinline__ rsrc_t selr(bool c, rsrc_t a, rsrc_t b) { return c ? a : b; }
__device__ __forceinline__ unsigned selu(bool c, unsigned a, unsigned b) { return c ? a : b; }
template <int AUX> __device__ __forceinline__ v4u ld4(rsrc_t r, unsigned vo, unsigned so) { return __builtin_amdgcn_raw_buffer_load_b128(r, (int)vo, (int)so, AUX); }
template <int AUX> __device__ __forceinline__ v2u ld2(rsrc_t r, unsigned vo, unsigned so) { return __builtin_amdgcn_raw_buffer_load_b64(r, (int)vo, (int)so, AUX); }
__device__ __forceinline__ void st4c(v4u v, rsrc_t r, unsigned vo) { __builtin_amdgcn_raw_buffer_store_b128(v, r, (int)vo, 0, 16); }
__device__ __forceinline__ void st1c(unsigned v, rsrc_t r, unsigned vo) { __builtin_amdgcn_raw_buffer_store_b32(v, r, (int)vo, 0, 16); }
template <unsigned M> __device__ __forceinline__ unsigned tag(unsigned u, unsigned t) { return (u & ~M) | (t & M); }
__device__ __forceinline__ v4u tag4(const f32x4& v, const v4u& t) { return (v4u){tag<1>(asu(v[0]), t[0]), tag<1>(asu(v[1]), t[1]), tag<1>(asu(v[2]), t[2]), tag<1>(asu(v[3]), t[3])}; }
__device__ __forceinline__ bool arrived(const v4u& v, const v4u& o) { return __builtin_amdgcn_ballot_w64(v[0] == o[0] || v[1] == o[1] || v[2] == o[2] || v[3] == o[3]) == 0; }
__device__ __forceinline__ unsigned lowb(const v4u& v) { return __builtin_amdgcn_perm(v[1], v[0], 0x0c0c0400u) | __builtin_amdgcn_perm(v[3], v[2], 0x04000c0cu); }
#define SB() __builtin_amdgcn_sched_barrier(0)
__device__ __forceinline__ void bar() { __builtin_amdgcn_s_waitcnt(0xC07F); __builtin_amdgcn_s_barrier(); }
#define WAITV(n) __builtin_amdgcn_s_waitcnt(((n) & 15) | (((n) >> 4) << 14) | 0x0F70)
template <int V> struct IC { static constexpr int value = V; constexpr operator int() const { return V; } };
template <int A, int B, class Fn> __device__ __forceinline__ void static_for(Fn&& f) { if constexpr (A < B) { f(IC<A>{}); static_for<A + 1, B>(f); } }
#define LDS_FENCE() asm volatile("" ::: "memory")

constexpr float EXP_C = 0.0625f * 1.4426950408889634f;

struct Att {
  float m, l;
  f32x4 a[2];
};
__device__ __forceinline__ f32x4 mfma(const v4u& a, const v4u& b, const f32x4& c) {
  return __builtin_amdgcn_mfma_f32_16x16x32_bf16(__builtin_bit_cast(bf16x8, a), __builtin_bit_cast(bf16x8, b), c, 0, 0, 0);
}
__device__ __forceinline__ float red_max(float v) { v = fmaxf(v, dpp<XOR1>(v)); v = fmaxf(v, dpp<XOR2>(v)); return x32_max(x16_max(v)); }

template <int NL, class ISSUE>
__device__ __forceinline__ float scores(v4u (&kb)[8], const v4u (&qb)[8], int lane, ISSUE issue) {
  f32x4 d = {0.f, 0.f, 0.f, 0.f};
#pragma unroll
  for (int i = 0; i < NL; ++i) {
    d = mfma(kb[i], qb[i], d);
    asm volatile("" : "+v"(d));
    issue(i);
    SB();
  }
  const bool odd = lane & 1;
  return rbf((odd ? d[3] : d[0]) + dpp<XOR1>(odd ? d[1] : d[2]));
}
template <class ISSUE>
__device__ __forceinline__ float phase_k4(v4u (&kb)[8], const v4u (&qb)[8], Att& st, int lane, ISSUE issue) {
  float s = scores<4>(kb, qb, lane, issue);
  s = (lane & 2) ? -__builtin_inff() : s;
  const float mn = fmaxf(st.m, red_max(s));
  const float al = __builtin_amdgcn_exp2f((st.m - mn) * EXP_C);
  const float p = __builtin_amdgcn_exp2f((s - mn) * EXP_C);
  st.m = mn; st.l = st.l * al + p;
  st.a[0] *= al; st.a[1] *= al;
  SB();
  return p;
}
template <int I, int O, class ISSUE>
__device__ __forceinline__ void pv_step(v4u (&vb)[8], unsigned pp, const v4u& pm, Att& st, ISSUE& issue) {
  const v4u pb = pm & (unsigned)__builtin_amdgcn_update_dpp(0, (int)pp, I * 0x55, 0xf, 0xf, true);
  st.a[0] = mfma(vb[O + 2 * I], pb, st.a[0]);
  asm volatile("" : "+v"(st.a[0]));
  issue(O + 2 * I);
  SB();
  st.a[1] = mfma(vb[O + 2 * I + 1], pb, st.a[1]);
  asm volatile("" : "+v"(st.a[1]));
  issue(O + 2 * I + 1);
  SB();
}
template <int NL, class ISSUE>
__device__ __forceinline__ void phase_v(v4u (&vb)[8], float p, const v4u& pm, Att& st, ISSUE issue) {
  constexpr int O = 8 - NL;
  const unsigned pp = __builtin_bit_cast(unsigned, (bf16x2){(__bf16)p, (__bf16)p});
  pv_step<0, O>(vb, pp, pm, st, issue); pv_step<1, O>(vb, pp, pm, st, issue);
  if constexpr (NL == 8) { pv_step<2, O>(vb, pp, pm, st, issue); pv_step<3, O>(vb, pp, pm, st, issue); }
}

template <int N, int GAP, int SL>
__device__ __forceinline__ v4u poll(const v4u& r, unsigned pa, const v4u& old) {
  v4u d;
  unsigned long long t0, t1, t2;
  int n = SPIN_CAP;
  asm volatile(
      "s_nop 4\n\t"
      "buffer_load_dwordx4 v[200:203], %5, %6, 0 offen sc1\n\t"
      ".rept %11\n\t"
      "s_sleep %12\n\t"
      "buffer_load_dwordx4 v[200:203], %5, %6, 0 offen sc1\n\t"
      ".endr\n"
      "1:\n\t"
      "s_waitcnt vmcnt(%11)\n\t"
      "v_cmp_eq_u32 vcc, v200, %7\n\t"
      "v_cmp_eq_u32 %1, v201, %8\n\t"
      "v_cmp_eq_u32 %2, v202, %9\n\t"
      "v_cmp_eq_u32 %3, v203, %10\n\t"
      "s_or_b64 vcc, vcc, %1\n\t"
      "s_or_b64 %2, %2, %3\n\t"
      "s_or_b64 exec, vcc, %2\n\t"
      "s_cbranch_scc0 2f\n\t"
      ".if %13\n\ts_sleep %13\n\t.endif\n\t"
      "buffer_load_dwordx4 v[200:203], %5, %6, 0 offen sc1\n\t"
      "s_sub_u32 %4, %4, 1\n\t"
      "s_cbranch_scc0 1b\n"
      "2:\n\t"
      "s_mov_b64 exec, -1"
      : "=&{v[200:203]}"(d), "=&s"(t0), "=&s"(t1), "=&s"(t2), "+s"(n)
      : "v"(pa), "s"(r), "v"(old[0]), "v"(old[1]), "v"(old[2]), "v"(old[3]), "n"(N - 1), "n"(GAP), "n"(SL)
      : "vcc", "scc", "memory");
  return d;
}
__device__ __forceinline__ void poll_end(const v4u& d) { asm volatile("s_waitcnt vmcnt(0)" :: "{v[200:203]}"(d)); }

#define STAMP(k) do {} while (0)
#define STAMP_OUT() do {} while (0)

__device__ __forceinline__ f32x2 ln_stats(const v4u& xa, int lane) {
  f32x2 xv[4];
  float sm = 0.f;
#pragma unroll
  for (int j = 0; j < 4; ++j) { xv[j] = unp(xa[j]); sm += xv[j].x + xv[j].y; }
  const float mean = wave_sum(sm) * (1.0f / D);
  float sq = 0.f;
#pragma unroll
  for (int j = 0; j < 4; ++j) { const f32x2 d = xv[j] - mean; sq += d.x * d.x + d.y * d.y; }
  sq = lane < 56 ? sq : 0.f;
  const float var = rbf(wave_sum(sq) * (1.0f / D));
  const float sd = rbf(sqrtf(rbf(var + 1e-5f)));
  return (f32x2){rbf(mean), 1.0f / sd};
}
__device__ __forceinline__ float ln_apply(float x, f32x2 st) { return rbf(rbf(x - st.x) * st.y); }

__global__ void __launch_bounds__(NT, 2)
fused_kernel(const u16* __restrict__ xg, const u16* __restrict__ kc, const u16* __restrict__ vc, const u16* __restrict__ wq,
             const u16* __restrict__ wo, const u16* __restrict__ wg, const u16* __restrict__ wu, const u16* __restrict__ wd,
             u16* __restrict__ out, char* ws) {
  __shared__ __attribute__((aligned(16))) float s_part[8][512];
  __shared__ __attribute__((aligned(16))) float s_mv[8][64];
  __shared__ __attribute__((aligned(16))) unsigned s_q[256];
  __shared__ __attribute__((aligned(16))) unsigned s_qs[64];
  __shared__ __attribute__((aligned(16))) unsigned s_old[8][256];
  __shared__ __attribute__((aligned(16))) unsigned s_dst[8][256];
  __shared__ __attribute__((aligned(16))) float s_ml[2][16];
  __shared__ __attribute__((aligned(16))) float s_o[4][32];
  __shared__ __attribute__((aligned(16))) float s_ml2[16];
  __shared__ __attribute__((aligned(16))) unsigned s_xn[256];
  __shared__ __attribute__((aligned(16))) float s_h[32];
  __shared__ __attribute__((aligned(16))) float s_r[9][16];
  __shared__ __attribute__((aligned(16))) float s_sc[64][32];
  __shared__ int s_ctl[64];
  const int tid = threadIdx.x, lane = tid & 63, w = __builtin_amdgcn_readfirstlane(tid >> 6);
  const int bi = blockIdx.x, h = (((bi & 1) ? bi + 5 : bi) & 6) | (bi >> 7), s = ((bi >> 3) & 15) * 2 + (bi & 1);
  const int b = h * 32 + s;
  static_assert(8 * TQ + 8 * TF + 6 * TFY + 10 * TY == 4096 && TQ - NP > 80 && TF - NP > 80 && TFY - NP > 80 && TY - NP > 80, "tickets");
  static_assert(TQ - NP <= 128 && TF - NP <= 128 && TFY - NP <= 128 && TY - NP <= 128, "tickets");
  const int nt = s < 8 ? TQ : s < 16 ? TF : s < 22 ? TFY : TY, nk = (nt - NP) >> 1;
  const int first = s * TY - min(s, 22) * (TY - TFY) - min(s, 16) * (TFY - TF) - min(s, 8) * (TF - TQ);
  asm volatile("" :: "s"(xg), "s"(kc), "s"(vc), "s"(wq), "s"(wo), "s"(wg), "s"(wu), "s"(wd), "s"(out), "s"(ws));
  STAMP(0);
  const rsrc_t rx = mkrs(xg, D * 2);
  const rsrc_t rk = mkrs(kc + (size_t)h * SK * DH, SK * DH * 2);
  const rsrc_t rv = mkrs(vc + (size_t)h * SK * DH, SK * DH * 2);
  const rsrc_t rws = mkrs(ws, WS_END);
  const unsigned l16 = lane * 16;
  auto ubase = [&](int u) -> unsigned { return (unsigned)(first + 2 * u) * 4096u; };
  const unsigned kvo = ((lane >> 1) & 7) * 512 + (lane >> 4) * 32 + (lane & 1) * 16, vvo = (lane >> 4) * 1024 + (lane & 15) * 16;
  auto ldk = [&](unsigned base, int i) { return ld4<2>(rk, kvo + (i & 3) * 128, base + (i >> 2) * 4096); };
  const unsigned area = (unsigned)b * AREA;
  auto area_of = [&](int ss) -> unsigned { return (unsigned)(h * 32 + ss) * AREA; };
  const unsigned lmask = lane < 56 ? 0u : OOB;

  const bool isq = s < 8, isf = s < 14, isy = s >= 16;
  const int f = h * 14 + s, y = h * 16 + s - 16, ro = h * 4 + (s >> 2);
  const bool isr = s < 16 && ((s + h) & 3) == 0 && ro < 28;
  const bool tile = isq;
  const bool leader = (!(NOHS & 1) && b == NWG - 1 && w == 1), member = (w == 0);

  const unsigned pa = isy ? (w < 4 ? area + A_O1 + w * 1024 + l16 : (w == 7 && y < 112) ? area + A_F + l16 : OOB) : isr ? area + A_Y2 + w * 1024 + l16 : OOB;
  const unsigned mo_ = area_of(16 + (lane & 8) + (lane >> 4)) + A_O1 + s * 128 + (lane & 7) * 16;
  auto mf = [&](int p) -> unsigned {
    const int fd = (f & ~3) + ((p >> 2) & 3);
    return (unsigned)((fd >> 4) * 32 + 16 + (fd & 15)) * AREA + A_F + (f & 3) * 256 + (p >> 4) * 64 + (p & 3) * 16;
  };
  auto my = [&](int p) -> unsigned { return (unsigned)((p >> 4) * 32 + (p & 12) + ((-(p >> 4)) & 3)) * AREA + A_Y2 + y * 64 + (p & 3) * 16; };
  v4u eo, ed;
  auto da_of = [&]() {
    const int p = (w - 2) * 64 + lane;
    unsigned da = (isy && p < 112) ? my(p) : OOB;
    return (w & 6) == 2 ? da : (w & 6) == 4 ? mo_ + (w - 4) * 4 * AREA : (w == 6 && isf) ? mf(lane) : OOB;
  };
  if (EO == 1) { eo = ld4<OA>(rws, pa, 0); SB(); }
  if (EO == 3 || (EO == 4 && !tile)) { eo = ld4<OA>(rws, pa, 0); ed = ld4<OB>(rws, da_of(), 0); SB(); }
  if (EO == 5) { ed = ld4<OB>(rws, da_of(), 0); SB(); }
  const v4u sent = {SENT, SENT, SENT, SENT};
  unsigned poll_off = lane == 1 ? WS_GO : lane < 4 ? WS_T - 32 + l16 : OOB;
  poll_off = lane == 0 ? WS_REQ + (b >> 2) * 16 : poll_off;
  poll_off = leader ? (ES ? lane * 4 * AREA + (lane & 3) * 256 : WS_ECHO + l16) : poll_off;
  auto echoes = [&](unsigned seen) {
    v4u r;
#pragma unroll
    for (int j = 0; j < 4; ++j) r[j] = __builtin_amdgcn_raw_buffer_load_b32(rws, (int)(poll_off | (seen >> j << 31)), j * AREA, 16);
    return r;
  };
  v4u pv = {0u, 0u, 0u, 0u}, e1 = {0u, 0u, 0u, 0u};
  unsigned go_ref = 0;
  int* const ctl = &s_ctl[lane];
  auto ctl_get = [&](int i) { return __builtin_amdgcn_readfirstlane(__hip_atomic_load(&s_ctl[i], __ATOMIC_RELAXED, __HIP_MEMORY_SCOPE_WORKGROUP)); };
  auto ctl_set = [&](int i) { __hip_atomic_store(&s_ctl[i], 1, __ATOMIC_RELAXED, __HIP_MEMORY_SCOPE_WORKGROUP); };
  const unsigned qoff = WS_Q + h * 2048 + l16;
  v4u qv0, qv1;
  if (member) {
    qv0 = ld4<16>(rws, qoff, 0);
    qv1 = ld4<16>(rws, qoff, 1024);
    SB();
    int c = lane == C_NEXT ? (isq ? TOFFQ : TOFF) * 128 : 0;
    c = lane == C_TICK ? 40 : c;
    c = lane == C_T0 ? (int)(__builtin_amdgcn_s_memrealtime() & 0x7fffffff) : c;
    __hip_atomic_store(ctl, c, __ATOMIC_RELAXED, __HIP_MEMORY_SCOPE_WORKGROUP);
    st1c(c, rws, (lane == C_T0 && (bi >> 3) == 12) ? WS_T + (bi & 7) * 4 : OOB);
  }
  *reinterpret_cast<v4u*>(&s_sc[w * 8][lane * 4]) = sent;
  int t0 = 0;
  auto now = [&]() { return ((int)__builtin_amdgcn_s_memrealtime() - t0) & 0x7fffff; };
  const int neg = lane == C_NEXT ? 0 : (int)0x80000000;
  const int xcc = __builtin_amdgcn_s_getreg(20 | (3 << 11));
  constexpr auto tkf = [](int t, int sh) { return TAU + sh * 128 / ((t - NP) / 2 - 8); };
  constexpr auto tvf = [](int t, int sh) { return TAU - sh * 128 / ((t - NP) / 2 + 8 - PL); };
  int tk = s < 8 ? tkf(TQ + 16, SQ) : s < 16 ? tkf(TF, SF_) : s < 22 ? tkf(TFY, SFY) : tkf(TY, SY);
  int tv = s < 8 ? tvf(TQ, SQ) : s < 16 ? tvf(TF, SF_) : s < 22 ? tvf(TFY, SFY) : tvf(TY, SY);
  int inc = lane == C_NEXT ? tk : 0;
  auto token = [&](int burst) {
    const int t = max(now() * 128 - burst, 0);
    __hip_atomic_fetch_max(ctl, t | neg, __ATOMIC_RELAXED, __HIP_MEMORY_SCOPE_WORKGROUP);
    return __hip_atomic_fetch_add(ctl, inc, __ATOMIC_RELAXED, __HIP_MEMORY_SCOPE_WORKGROUP);
  };
  auto due_of = [&](int x) { return __builtin_amdgcn_readlane(x, C_NEXT) >> 7; };
  auto until = [&](int due) {
#pragma unroll 1
    for (int it = 0; it < 4096 && now() < due; ++it) __builtin_amdgcn_s_sleep(1);
    SB();
  };
  unsigned qs0, qs1;
  auto q_there = [&]() { return __builtin_amdgcn_ballot_w64(((lowb(qv0) ^ qs0) & (lowb(qv1) ^ qs1)) != 0xFFFFFFFFu) == 0; };
  auto load_q = [&]() { qv0 = ld4<16>(rws, qoff, 0); qv1 = ld4<16>(rws, qoff, 1024); };
  auto publish_q = [&]() {
    *reinterpret_cast<v2u*>(&s_q[lane * 2]) = (v2u){__builtin_amdgcn_perm(qv0[1], qv0[0], 0x07060302u), __builtin_amdgcn_perm(qv0[3], qv0[2], 0x07060302u)};
    *reinterpret_cast<v2u*>(&s_q[128 + lane * 2]) = (v2u){__builtin_amdgcn_perm(qv1[1], qv1[0], 0x07060302u), __builtin_amdgcn_perm(qv1[3], qv1[2], 0x07060302u)};
    LDS_FENCE();
    __builtin_amdgcn_s_waitcnt(0xC07F);
    ctl_set(C_Q);
  };
  v4u pre[4][8], ring[8];
  auto unit0 = [&]() {
#pragma unroll
    for (int i = 0; i < 8; ++i) { pre[0][i] = ldk(ubase(w), i); SB(); }
    if (EO == 2) { eo = ld4<OA>(rws, pa, 0); SB(); }
  };
  SB();
  if (tile) {
    v4u tw[8];
    const int oct = 7 * w;
    constexpr int stride = NQ * 2;
    const rsrc_t rt = mkrs(wq, D * stride);
    const unsigned to = (unsigned)((oct + (lane >> 3)) * 8 * stride + (h * 8 + s) * 128 + (lane & 7) * 16) | lmask;
    const v4u xa = ld4<0>(rx, l16, 0);
    const v4u xb = ld4<0>(rx, (unsigned)(oct + (lane >> 3)) * 16 | lmask, 0);
    SB();
#pragma unroll
    for (int i = 0; i < 8; ++i) { tw[i] = ld4<2>(rt, to, i * stride); SB(); }
    if (EO == 4) { eo = ld4<OA>(rws, pa, 0); ed = ld4<OB>(rws, da_of(), 0); SB(); }
    LDS_FENCE(); bar(); LDS_FENCE();
    t0 = ctl_get(C_T0);
    SB();
    const f32x2 ls = ln_stats(xa, lane);
    f32x2 acc[4];
#pragma unroll
    for (int i = 0; i < 8; ++i) {
      const float xn = ln_apply(asf((i & 1) ? (xb[i >> 1] & 0xffff0000u) : (xb[i >> 1] << 16)), ls);
#pragma unroll
      for (int j = 0; j < 4; ++j) acc[j] = i ? acc[j] + unp(tw[i][j]) * xn : unp(tw[i][j]) * xn;
    }
    float y0 = tr32(acc[0].x, acc[2].x), y1 = tr32(acc[0].y, acc[2].y), y2 = tr32(acc[1].x, acc[3].x), y3 = tr32(acc[1].y, acc[3].y);
    y0 = tr16(y0, y2); y1 = tr16(y1, y3);
    const bool s_ = lane & 8;
    y0 = (s_ ? y1 : y0) + dpp<ROR8>(s_ ? y0 : y1);
    s_mv[w][(lane & 7) * 8 + (lane >> 3)] = y0;
    STAMP(6);
    token(QBURST);
    unit0();
    LDS_FENCE(); bar(); LDS_FENCE();
    if (member) {
      float a = 0.f;
#pragma unroll
      for (int k = 0; k < 8; ++k) a += s_mv[k][lane];
      s_qs[lane] = f2bf(a) << 16;
      LDS_FENCE();
      const v4u qs = *reinterpret_cast<const v4u*>(&s_qs[(lane & 15) * 4]) | (~((s & 4) ? qv1 : qv0) & 0xffffu);
      st4c(qs, rws, (lane >> 4) == (s & 3) ? qoff + (s >> 2) * 1024 : OOB);
    }
  } else if (leader) {
    e1 = ES ? echoes(0) : ld4<16>(rws, poll_off, 0);
    go_ref = __builtin_amdgcn_raw_buffer_load_b32(rws, (int)WS_GO, 0, 16);
  }
  SB();
  STAMP(5);
  SB();
  const u16* wa = isy ? wo + (size_t)y * 32 * D : wg;
  const u16* wb = isy ? wd + (size_t)(y < 112 ? (y >> 1) * 32 + (y & 1) * 4 : 0) * D : wu;
  asm volatile("" : "+s"(wa), "+s"(wb));
  bool ok = !member;
  static_for<0, 40>([&](auto N) {
    constexpr int n = N;
    if constexpr (n == 0) {
      if (!tile) unit0();
    }
    if constexpr (n == 8) {
      if (!tile) {
        LDS_FENCE(); bar(); LDS_FENCE();
        t0 = ctl_get(C_T0);
        if (leader) {
          e1 += 1u;
          go_ref += 1u;
          st4c(e1, rws, WS_REQ + l16);
        }
      }
    }
    if constexpr (n > 0 && n % 8 == 0) {
      const int x = token(isq ? QBURST : PBURST);
      if constexpr (n > QFIRST) {
        if (!ok) {
          ok = q_there();
          if (ok) publish_q();
        }
      }
      if constexpr (n == OPOS + 16) {
        *reinterpret_cast<v4u*>(&s_old[w][lane * 4]) = (int)pa < 0 ? sent : eo;
        *reinterpret_cast<v4u*>(&s_dst[w][lane * 4]) = ~ed;
        LDS_FENCE();
        __hip_atomic_fetch_add(ctl, lane == C_OLD ? 1 : 0, __ATOMIC_RELAXED, __HIP_MEMORY_SCOPE_WORKGROUP);
      }
      SB();
      until(due_of(x));
      if constexpr (n == 8) {
        if (member) { qs0 = lowb(qv0); qs1 = lowb(qv1); }
      }
      if constexpr (n >= QFIRST) {
        if (!ok) load_q();
      }
      if constexpr (n == OPOS) {
        if (EO == 0 || EO == 5) eo = ld4<OA>(rws, pa, 0);
        if (EO < 3) ed = ld4<OB>(rws, da_of(), 0);
      }
      SB();
    }
    if constexpr (n == QPOS) {
      STAMP(9);
      if (ES && leader) pv = echoes(0);
      else pv = ld4<16>(rws, (member || leader) ? poll_off : OOB, 0);
      SB();
    }
    if constexpr (n >= 8) {
      if constexpr (n >= PACE) WAITV(PACE + ((n >= QPOS && n < QPOS + PACE) ? 1 : 0) + ((n >= OPOS && n < OPOS + PACE) ? (EO == 0 ? 2 : (EO == 3 || EO == 4) ? 0 : 1) : 0));
      if constexpr (n < 32) pre[n >> 3][n & 7] = ldk(ubase((n & 56) + w), n & 7); else ring[n & 7] = ldk(ubase(32 + w), n & 7);
    }
    SB();
    if constexpr (n - PACE == 7 || n - PACE == 15 || n - PACE == 23 || n - PACE == 31) STAMP(16 + (n - PACE) / 8);
  });
  inc = lane == C_TICK ? 1 : inc;
  int tok = token(KBURST);
  STAMP(10);
  bool svc = member || leader;
  unsigned seenm = 0, last_echo = 0;
  bool echoed = false;
  auto service = [&]() {
    if (leader) {
      seenm |= (pv[0] == e1[0] ? 1u : 0u) | (pv[1] == e1[1] ? 2u : 0u) | (pv[2] == e1[2] ? 4u : 0u) | (pv[3] == e1[3] ? 8u : 0u);
      svc = __builtin_amdgcn_ballot_w64(seenm != 15u) != 0;
      st1c(go_ref, rws, (lane == 0 && !svc) ? WS_GO : OOB);
    } else {
      const unsigned req = (unsigned)__builtin_amdgcn_readfirstlane((int)((b & 2) ? ((b & 1) ? pv[3] : pv[2]) : ((b & 1) ? pv[1] : pv[0])));
      const unsigned go = (unsigned)__builtin_amdgcn_readlane((int)pv[0], 1);
      go_ref = echoed ? go_ref : go;
      svc = go == (unsigned)__builtin_amdgcn_readfirstlane((int)go_ref);
      if (NOHS & 1) svc = false;
      const bool rdy = ctl_get(C_OLD) == 8;
      if (ES && EL > 0) st4c((v4u){req, req, req, req}, rws, (lane < EL && svc && rdy && !(echoed && req == last_echo)) ? area + ((b >> 2) & 3) * 256 + l16 : OOB);
      else st1c(req, rws, (lane == 0 && svc && rdy && !(echoed && req == last_echo)) ? (ES ? area + ((b >> 2) & 3) * 256 : WS_ECHO + b * 4) : OOB);
      echoed |= rdy; last_echo = req;
    }
    if (ES && leader) pv = echoes(seenm);
    else pv = ld4<16>(rws, svc ? poll_off : OOB, 0);
    SB();
  };

  {
    if (!ok) {
      ok = q_there();
      if (ok) publish_q();
    }
#pragma unroll 1
    for (int it = 0; it < SPIN_CAP && !ctl_get(C_Q); ++it) {
      if (svc && it >= QTRIES) { service(); asm volatile("" :: "v"(pv)); }
      if (!ok) {
        load_q();
        ok = q_there() | !svc;
        if (ok) publish_q();
      } else {
        __builtin_amdgcn_s_sleep(1);
      }
    }
  }
  LDS_FENCE();
  v4u qb[8];
#pragma unroll
  for (int i = 0; i < 4; ++i) {
    const v4u z = {0u, 0u, 0u, 0u}, q = *reinterpret_cast<const v4u*>(&s_q[(lane & 8) * 16 + i * 32 + (lane >> 4) * 8 + (lane & 1) * 4]);
    qb[i] = (lane & 2) ? z : q; qb[4 + i] = (lane & 2) ? q : z;
  }
  v4u pm;
#pragma unroll
  for (int j = 0; j < 4; ++j) pm[j] = ((lane >> 1) & 3) == j ? ((lane & 1) ? 0xffff0000u : 0xffffu) : 0u;
  STAMP(1);
  SB();

  Att st;
  int sx = 20;
  unsigned fj, rank;
  {
    float* const sc = &s_sc[0][(lane >> 3) * 4 + (lane & 3)];
    const unsigned fvo = ((lane >> 3) * (8 * F * 2) + (lane & 7) * 16) | lmask;
    rsrc_t nra, nrb;
    unsigned nva, nvb, nba, nbb, nsa, nsb;
    int kind = 0, nu = 32 + w, cu;
    auto next = [&]() {
      cu = nu;
      const int t = __builtin_amdgcn_readlane(tok, C_TICK);
      const bool fx = t < 2 * nk, tw = t >= nt;
      kind = fx ? (t >= nk ? 1 : 0) : tw ? 3 : 2;
      nu = t >= nk ? t - nk : t;
      nba = (unsigned)(fx ? first + 2 * nu : first + t) * 4096u;
      nbb = fx ? nba + 4096 : nba;
      nra = selr(kind == 1, rv, rk);
      nrb = selr(kind == 0, rk, rv);
      nva = selu(kind == 1, vvo, kvo); nvb = selu(kind == 0, kvo, vvo);
      nsa = selu(kind == 1, 256, 128); nsb = selu(kind == 0, 128, 256);
      const int u = t + 8 - nk;
      inc = lane == C_NEXT ? (t + PL >= 2 * nk ? PP : (int)selu(u >= 0, tv, tk) + ((unsigned)(u + XA) < XA + XB ? TX : 0)) : inc;
      if (tw) {
        rank = t - nt;
        fj = isf ? rank - FO : 4;
        nba = ((f & 3) * 2 + ((fj >> 1) & 1)) * (56 * F * 2) + (f >> 2) * 128;
        nbb = nba + 4 * F * 2;
        nra = nrb = mkrs((fj & 1) ? wb : wa, (fj < 4 && !FX) ? D * F * 2 : 0);
        nva = nvb = fvo;
        nsa = nsb = F * 2;
        inc = 0;
      } else {
        until(due_of(tok));
      }
      SB();
    };
    auto ldn = [&](int i) { ring[i] = i < 4 ? ld4<2>(nra, nva, nba + i * nsa) : ld4<2>(nrb, nvb, nbb + (i - 4) * nsb); };
    float mx;
    if (member) {
      const v4u d = pv - (unsigned)t0;
      const bool bad = max(max(d[0] + 512u, d[1] + 512u), max(d[2] + 512u, d[3] + 512u)) > 1024u;
      const int sm = (int)((d[0] + d[1]) + (d[2] + d[3]));
      const int lead = (__builtin_amdgcn_ballot_w64(bad) & 12) ? 0 : __builtin_amdgcn_readlane(sm, 2) + __builtin_amdgcn_readlane(sm, 3) + DB;
      tk += (lead * DK) >> 5; tv += (lead * DK) >> 5;
      __hip_atomic_fetch_add(ctl, lane == C_LEAD ? (lead * DG) >> 5 : (lane == C_NEXT ? (lead * DS) >> 5 : 0), __ATOMIC_RELAXED, __HIP_MEMORY_SCOPE_WORKGROUP);
    }
    if (svc) service();
    {
      const float s0 = scores<8>(pre[0], qb, lane, [&](int) {});
      const float s1 = scores<8>(pre[1], qb, lane, [&](int) {});
      const float s2 = scores<8>(pre[2], qb, lane, [&](int) {});
      const float s3 = scores<8>(pre[3], qb, lane, [&](int) {});
      sc[w * 32] = s0; sc[(8 + w) * 32] = s1; sc[(16 + w) * 32] = s2; sc[(24 + w) * 32] = s3;
      mx = fmaxf(fmaxf(s0, s1), fmaxf(s2, s3));
      SB();
      STAMP(11);
    }
    const v4u xa2 = ld4<0>(rx, (isf && w == 7) ? l16 : OOB, 0);
    SB();
#pragma unroll 1
    while (kind == 0) {
      next();
      const float sv = scores<8>(ring, qb, lane, ldn);
      sc[cu * 32] = sv;
      tok = token(KBURST);
      mx = fmaxf(mx, sv);
      STAMP(sx); ++sx;
      if (svc) service();
      SB();
    }
    STAMP(7);
    if (isf && w == 7) {
      const f32x2 ls = ln_stats(xa2, lane);
      v4u xn;
#pragma unroll
      for (int j = 0; j < 4; ++j) {
        const f32x2 v = unp(xa2[j]);
        xn[j] = __builtin_amdgcn_perm(asu(ln_apply(v.y, ls)), asu(ln_apply(v.x, ls)), 0x07060302u);
      }
      *reinterpret_cast<v4u*>(&s_xn[lane * 4]) = xn;
      LDS_FENCE();
      ctl_set(C_FC);
      SB();
    }
    st.m = red_max(mx); st.l = 0.f;
    st.a[0] = st.a[1] = (f32x4){0.f, 0.f, 0.f, 0.f};
#pragma unroll 1
    while (kind == 1) {
      float sv;
      for (;;) {
        sv = __hip_atomic_load(&sc[nu * 32], __ATOMIC_RELAXED, __HIP_MEMORY_SCOPE_WORKGROUP);
        if (__builtin_amdgcn_ballot_w64(asu(sv) == SENT) == 0) break;
        __builtin_amdgcn_s_sleep(1);
      }
      if (__builtin_amdgcn_ballot_w64(sv > st.m) != 0) {
        const float m2 = fmaxf(st.m, red_max(sv));
        const float al = __builtin_amdgcn_exp2f((st.m - m2) * EXP_C);
        st.m = m2; st.l *= al;
        st.a[0] *= al; st.a[1] *= al;
      }
      const float p = __builtin_amdgcn_exp2f((sv - st.m) * EXP_C);
      st.l += p;
      SB();
      next();
      phase_v<8>(ring, p, pm, st, ldn);
      tok = token(VBURST);
      STAMP(sx); ++sx;
      if (svc) service();
      SB();
    }
    STAMP(8);
#pragma unroll 1
    while (kind == 2) {
      next();
      const float p = phase_k4(ring, qb, st, lane, ldn);
      phase_v<4>(ring, p, pm, st, ldn);
      tok = token(VBURST);
      STAMP(sx); ++sx;
    }
#pragma unroll 1
    for (int it = 0; it < SPIN_CAP && svc; ++it) {
      service();
      asm volatile("" :: "v"(pv));
    }
    LDS_FENCE();
  }
  STAMP(12);
  const lds4_t pr = (lds4_t)&s_part[0][lane * 4], pmr = (lds4_t)&s_ml[(lane >> 3) & 1][0];
  {
    const lds4_t pw = (lds4_t)&s_part[w][lane * 4];
    const lds1_t pml = (lds1_t)&s_ml[(lane >> 3) & 1][w];
    pw[0] = st.a[0]; pw[64] = st.a[1];
    float l = st.l;
    l += dpp<XOR1>(l); l += dpp<XOR2>(l); l = x32_add(x16_add(l));
    pml[0] = st.m; pml[8] = l;
  }
  const v4u rw4 = {(unsigned)(size_t)ws, (unsigned)((size_t)ws >> 32), WS_END, 0x00020000u};
  v4u old = *reinterpret_cast<const v4u*>(&s_old[w][lane * 4]);
  if (isy && w >= 4) {
    v4u wt[6][2];
    const rsrc_t rwo = mkrs(wa, XW ? 0 : 32 * D * 2);
    const rsrc_t rwd = mkrs(wb, (y < 112 && !XW) ? 28 * D * 2 : 0);
    unsigned o0 = (lane >> 3) * (D * 2) + (w - 4) * 256 + (lane & 7) * 16, o1 = w == 7 ? OOB : o0 + 128;
    unsigned o2 = o0 + (lane >> 5) * (4 * D * 2), o3 = o1 + (lane >> 5) * (4 * D * 2);
    asm volatile("" : "+v"(o0), "+v"(o1), "+v"(o2), "+v"(o3), "+v"(old));
    SB();
    LDS_FENCE(); bar(); LDS_FENCE();
    STAMP(2);
    if (TWD) __builtin_amdgcn_s_sleep(TWD);
    SB();
#pragma unroll
    for (int k = 0; k < 6; ++k) {
      const int i = (k + 4) % 6;
      wt[i][0] = i < 4 ? ld4<2>(rwo, o0, i * 8 * D * 2) : ld4<2>(rwd, o2, (i & 3) * 16 * D * 2);
      wt[i][1] = i < 4 ? ld4<2>(rwo, o1, i * 8 * D * 2) : ld4<2>(rwd, o3, (i & 3) * 16 * D * 2);
      SB();
    }
    const int p = (w - 4) * 32 + (lane >> 4) * 8 + (lane & 7);
    v4u dt = *reinterpret_cast<const v4u*>(&s_dst[2][p * 4]) & 1u;
    unsigned to = ((lane & 8) || p >= 112) ? OOB : my(p);
    f32x4 acc[2] = {{0.f, 0.f, 0.f, 0.f}, {0.f, 0.f, 0.f, 0.f}};
    auto bop = [&](float a) -> v4u { return pm & __builtin_bit_cast(unsigned, (bf16x2){(__bf16)a, (__bf16)a}); };
    auto step = [&](int i, const v4u& pb) { acc[0] = mfma(wt[i][0], pb, acc[0]); acc[1] = mfma(wt[i][1], pb, acc[1]); };
    lds4_t po = (lds4_t)&s_o[0][(lane >> 3) * 4];
    lds1_t ph = (lds1_t)&s_h[(lane & 4) * 4 + ((lane >> 4) & 1) * 8 + (lane & 3) * 2 + (lane >> 5)];
    asm volatile("" : "+v"(dt), "+v"(to), "+v"(po), "+v"(ph));
    SB();
    v4u dr = old;
    if (w == 7) {
      if (G3) until(G3 + __builtin_amdgcn_readlane(tok, C_LEAD));
      dr = poll<PD3, PG3, 0>(rw4, pa, old);
      const v4u d = dr & ~1u;
      float g = tr16(tr32(asf(d[0]), asf(d[2])), tr32(asf(d[1]), asf(d[3])));
      g = rbf(g + dpp<ROR8>(g));
      const float u = dpp<ROR4>(g);
      *ph = rbf(g / (1.0f + __expf(-g))) * u;
      SB();
    }
    STAMP(14);
    LDS_FENCE(); bar(); LDS_FENCE();
    STAMP(3);
    const f32x2 hv = *reinterpret_cast<const f32x2*>(&s_h[(lane >> 3) * 2]);
    const f32x4 ma = *reinterpret_cast<const f32x4*>(&s_ml2[0]), mb = *reinterpret_cast<const f32x4*>(&s_ml2[4]);
    f32x4 pq[4];
#pragma unroll
    for (int k = 0; k < 4; ++k) pq[k] = po[k * 8];
    SB();
    const v4u h0 = bop(hv.x), h1 = bop(hv.y);
    if (FB == 1) {
      step(4, h0); step(5, h1);
      asm volatile("" : "+v"(acc[0]), "+v"(acc[1]));
      SB();
    }
    const float M = fmaxf(fmaxf(ma[0], ma[2]), fmaxf(mb[0], mb[2]));
    const float e0 = __builtin_amdgcn_exp2f((ma[0] - M) * EXP_C), e1_ = __builtin_amdgcn_exp2f((ma[2] - M) * EXP_C);
    const float e2 = __builtin_amdgcn_exp2f((mb[0] - M) * EXP_C), e3 = __builtin_amdgcn_exp2f((mb[2] - M) * EXP_C);
    const float inv = __builtin_amdgcn_rcpf((ma[1] * e0 + ma[3] * e1_) + (mb[1] * e2 + mb[3] * e3));
    const f32x4 av = ((pq[0] * e0 + pq[1] * e1_) + (pq[2] * e2 + pq[3] * e3)) * inv;
    if (FB) {
      v4u pb[4];
#pragma unroll
      for (int i = 0; i < 4; ++i) pb[i] = bop(av[i]);
      asm volatile("" : "+v"(pb[0]), "+v"(pb[1]), "+v"(pb[2]), "+v"(pb[3]));
      SB();
      if (FB == 2) { step(4, h0); step(5, h1); }
#pragma unroll
      for (int i = 0; i < 4; ++i) step(i, pb[i]);
    } else {
#pragma unroll
      for (int i = 0; i < 4; ++i) step(i, bop(av[i]));
    }
    f32x4 z;
#pragma unroll
    for (int v = 0; v < 4; ++v) {
      const float a0 = asf((unsigned)__builtin_amdgcn_update_dpp((int)asu(acc[0][v]), (int)asu(acc[0][v]), ROR8, 0xc, 0xf, false));
      const float a1 = asf((unsigned)__builtin_amdgcn_update_dpp((int)asu(acc[1][v]), (int)asu(acc[1][v]), ROR8, 0xc, 0xf, false));
      z[v] = tr32(a0, a1);
    }
    __builtin_amdgcn_raw_buffer_store_b128(tag4(z, dt), rws, (int)to, 0, S2);
    STAMP(4);
    poll_end(dr);
    STAMP_OUT();
    __builtin_amdgcn_endpgm();
  }
  const unsigned half = isy ? w : rank - SR;
  if (half < 2) {
    WAITV(0);
    ldsu_t tp = (ldsu_t)&s_dst[4 + half][lane * 4];
    unsigned to = mo_ + half * 4 * AREA;
    lds4_t pp = pr + half * 64;
    asm volatile("" : "+v"(tp), "+v"(to), "+v"(pp), "+v"(old));
    SB();
    LDS_FENCE(); bar(); LDS_FENCE();
    STAMP(2);
    f32x4 ml[4], pt[8];
#pragma unroll
    for (int k = 0; k < 4; ++k) ml[k] = pmr[k];
#pragma unroll
    for (int k = 0; k < 8; ++k) pt[k] = pp[k * 128];
    const v4u t = *tp;
    float M = -3.0e38f;
#pragma unroll
    for (int k = 0; k < 8; ++k) M = fmaxf(M, ml[k >> 2][k & 3]);
    f32x4 e[2] = {(ml[0] - M) * EXP_C, (ml[1] - M) * EXP_C};
    f32x4 o = {0.f, 0.f, 0.f, 0.f};
#pragma unroll
    for (int k = 0; k < 8; ++k) {
      e[k >> 2][k & 3] = __builtin_amdgcn_exp2f(e[k >> 2][k & 3]);
      o += pt[k] * e[k >> 2][k & 3];
    }
    const f32x4 l4 = ml[2] * e[0] + ml[3] * e[1];
    const float L = (l4[0] + l4[2]) + (l4[1] + l4[3]);
    v4u msg;
#pragma unroll
    for (int j = 0; j < 3; ++j) msg[j] = tag<0x100>(__builtin_amdgcn_perm(asu(o[j]), asu(o[3]), 0x07060501u + j), t[j]);
    msg[3] = tag<1>(asu((lane & 1) ? L : M), t[3]);
    __builtin_amdgcn_raw_buffer_store_b128(msg, rws, (int)to, 0, S1);
    SB();
  } else if (fj < 4) {
#pragma unroll 1
    for (int it = 0; it < SPIN_CAP && !ctl_get(C_FC); ++it) __builtin_amdgcn_s_sleep(1);
    LDS_FENCE();
    const v4u xn = *reinterpret_cast<const v4u*>(&s_xn[(((f & 3) * 2 + (fj >> 1)) * 7 + (lane >> 3)) * 4]);
    const int p = fj * 16 + ((lane >> 4) & 1) * 8 + (lane & 7);
    ldsu_t tp = (ldsu_t)&s_dst[6][p * 4];
    unsigned to = (lane & 40) ? OOB : mf(p);
    v4u pb[8];
#pragma unroll
    for (int i = 0; i < 8; ++i) {
      pb[i] = pm & __builtin_amdgcn_perm(xn[i >> 1], xn[i >> 1], (i & 1) ? 0x03020302u : 0x01000100u);
      asm volatile("" : "+v"(pb[i]));
    }
    asm volatile("" : "+v"(tp), "+v"(to), "+v"(old));
    SB();
    LDS_FENCE(); bar(); LDS_FENCE();
    SB();
    STAMP(2);
    const v4u t = *tp;
    f32x4 acc[2] = {{0.f, 0.f, 0.f, 0.f}, {0.f, 0.f, 0.f, 0.f}};
#pragma unroll
    for (int i = 0; i < 8; ++i) acc[i & 1] = mfma(ring[i], pb[i], acc[i & 1]);
    asm volatile("" :: "v"(pv));
    acc[0] += acc[1];
    f32x4 z;
#pragma unroll
    for (int v = 0; v < 4; ++v)
      z[v] = x32_add(asf((unsigned)__builtin_amdgcn_update_dpp((int)asu(acc[0][v]), (int)asu(acc[0][v]), ROR8, 0xc, 0xf, false)));
    __builtin_amdgcn_raw_buffer_store_b128(tag4(z, t), rws, (int)to, 0, S1);
    SB();
  } else {
    WAITV(0);
    LDS_FENCE(); bar(); LDS_FENCE();
    STAMP(2);
  }
  if (isy) {
    lds1_t po = (lane & 8) ? (lds1_t)&s_ml2[w * 2 + 1 + (lane == 8 ? 0 : 8)] : (lds1_t)&s_o[w][(lane & 7) * 4 + (lane >> 5) * 2 + ((lane >> 4) & 1)];
    lds1_t pq = (lds1_t)&s_ml2[w * 2];
    asm volatile("" : "+v"(po), "+v"(pq));
    if (G1) until(G1 + __builtin_amdgcn_readlane(tok, C_LEAD));
    STAMP(14);
    const v4u dr = poll<PD1, PG1, PS1>(rw4, pa, old);
    STAMP(15);
    const v4u d = dr & (v4u){~0x100u, ~0x100u, ~0x100u, ~1u};
    const float ot = dpp<XOR1>(asf(d[3]));
    const float m = (lane & 1) ? ot : asf(d[3]), l = (lane & 1) ? asf(d[3]) : ot;
    float M = fmaxf(m, dpp<ROR8>(m)); M = x16_max(M); M = x32_max(M);
    *pq = M;
    const float e = __builtin_amdgcn_exp2f((m - M) * EXP_C);
    const float v3 = asf(__builtin_amdgcn_perm(__builtin_amdgcn_perm(d[2], d[1], 0x04000c0cu), d[0], 0x0706000cu));
    float y0 = tr32(asf(d[0]) * e, asf(d[2]) * e), y1 = tr32(asf(d[1]) * e, v3 * e), L = x32_add(l * e);
    y0 = tr16(y0, y1); L = x16_add(L);
    const bool s_ = lane & 8;
    *po = (s_ ? L : y0) + dpp<ROR8>(s_ ? y0 : L);
    LDS_FENCE(); bar(); LDS_FENCE();
    STAMP(3);
    poll_end(dr);
    STAMP(4);
    STAMP_OUT();
    __builtin_amdgcn_endpgm();
  }
  if (isr) {
    const int col = (ro >> 1) * 32 + (ro & 1) * 4 + (lane & 3) * 8 + ((lane >> 2) & 3);
    float xo = asf((unsigned)xg[col] << 16);
    lds1_t po = (lds1_t)&s_r[(lane & 12) ? 8 : w][(lane & 3) * 4 + (lane >> 5) * 2 + ((lane >> 4) & 1)], pq = (lds1_t)&s_r[lane >> 4][lane & 15];
    const rsrc_t ro_ = mkrs(out, D * 2);
    unsigned oo = lane < 16 ? col * 2 : OOB;
    asm volatile("" : "+v"(xo), "+v"(po), "+v"(pq), "+v"(oo));
    if (G2) until(G2 + __builtin_amdgcn_readlane(tok, C_LEAD));
    const v4u dr = poll<PD2, PG2, PS2>(rw4, pa, old);
    const v4u d = dr & ~1u;
    float y0 = tr32(asf(d[0]), asf(d[2])), y1 = tr32(asf(d[1]), asf(d[3]));
    y0 = tr16(y0, y1);
    y0 += dpp<ROR8>(y0); y0 += dpp<ROR4>(y0);
    *po = y0;
    LDS_FENCE(); bar(); LDS_FENCE();
    if (w == 0) {
      float a = pq[0] + pq[64];
      a = x16_add(a); a = x32_add(a);
      __builtin_amdgcn_raw_buffer_store_b16((short)f2bf(xo + a), ro_, (int)oo, 0, SO);
    }
    STAMP(4);
    poll_end(dr);
  }
  if (!isr) STAMP(4);
  STAMP_OUT();
}

extern "C" void kernel_launch(void* const* d_in, const int* in_sizes, int n_in,
                              void* d_out, int out_size, void* d_ws, size_t ws_size,
                              hipStream_t stream) {
  fused_kernel<<<NWG, NT, 0, stream>>>((const u16*)d_in[0], (const u16*)d_in[1], (const u16*)d_in[2], (const u16*)d_in[3],
                                       (const u16*)d_in[4], (const u16*)d_in[6], (const u16*)d_in[7], (const u16*)d_in[8],
                                       (u16*)d_out, (char*)d_ws);
}
```

```cpp
#include <hip/hip_runtime.h>
#include <hip/hip_bf16.h>
#include <stdint.h>

#define QPOS 24
#define PACE 8
#define QTRIES 8
#define TQ 123
#define TF 131
#define TFY 129
#define TY 129
#define NP 4
#define TAU 3640
#define PP 3000
#define PL 8
#define TOFF 150
#define TOFFQ 180
#define QBURST 100000
#define PBURST 4000
#define KBURST 100000
#define VBURST 100000
#define QFIRST 24
#define OPOS 8
#define TWD 0
#define OA 16
#define OB 16
#define S1 16
#define S2 16
#define G1 3855
#define G2 3975
#define EO 3
#define PD1 2
#define PG1 6
#define PD2 2
#define PG2 6
#define PD3 2
#define PG3 6
#define G3 3855
#define SR 2
#define PS1 0
#define PS2 0
#define SO 0
#define DK 57
#define DS 0
#define DG 4
#define DB 0
#define SQ 0
#define SF_ 0
#define SFY 0
#define SY 0
#define XA 4
#define XB 4
#define TX 0
#define NOHS 0
#define ES 1
#define EL 8
#define SVM 0
#define SVL 0
#define FO 4
#define FX 0
#define FB 1
#define XW 0
#define DIAG 0
#define LITE 0

typedef unsigned v4u __attribute__((ext_vector_type(4)));
typedef unsigned v2u __attribute__((ext_vector_type(2)));
typedef float f32x4 __attribute__((ext_vector_type(4)));
typedef float f32x2 __attribute__((ext_vector_type(2)));
typedef __bf16 bf16x2 __attribute__((ext_vector_type(2)));
typedef __bf16 bf16x8 __attribute__((ext_vector_type(8)));
typedef unsigned short u16;
typedef __amdgpu_buffer_rsrc_t rsrc_t;
typedef __attribute__((address_space(3))) f32x4* lds4_t;
typedef __attribute__((address_space(3))) v4u* ldsu_t;
typedef __attribute__((address_space(3))) float* lds1_t;

constexpr int D = 448, SK = 32768, DH = 256, F = 1792, NQ = 4096;
constexpr int NWG = 256, NT = 512;
constexpr unsigned OOB = 0x80000000u;
constexpr unsigned SENT = 0xFFFFFFFFu;
constexpr unsigned AREA = 9216;
constexpr unsigned A_O1 = 1024;
constexpr unsigned A_F = 5120;
constexpr unsigned A_Y2 = 1024;
constexpr unsigned WS_REQ = NWG * AREA;
constexpr unsigned WS_ECHO = WS_REQ + 1024;
constexpr unsigned WS_GO = WS_ECHO + 1024;
constexpr unsigned WS_T = WS_GO + 256;
constexpr unsigned WS_Q = WS_T + 256;
constexpr unsigned WS_END = WS_Q + 8 * 2048;
constexpr int SPIN_CAP = 1 << 18;
constexpr int C_NEXT = 0, C_TICK = 1, C_GO = 2, C_Q = 3, C_T0 = 4, C_FC = 5, C_OLD = 6, C_DONE = 7, C_LEAD = 8;

__device__ __forceinline__ float asf(unsigned u) { return __builtin_bit_cast(float, u); }
__device__ __forceinline__ unsigned asu(float f) { return __builtin_bit_cast(unsigned, f); }
__device__ __forceinline__ float rbf(float f) { __bf16 b = (__bf16)f; return (float)b; }
__device__ __forceinline__ unsigned f2bf(float f) { __bf16 b = (__bf16)f; return (unsigned)__builtin_bit_cast(u16, b); }
__device__ __forceinline__ float dot2(unsigned k, unsigned q, float acc) {
  return __builtin_amdgcn_fdot2_f32_bf16(__builtin_bit_cast(bf16x2, k), __builtin_bit_cast(bf16x2, q), acc, false);
}
__device__ __forceinline__ f32x2 unp(unsigned u) { f32x2 r; r.x = asf(u << 16); r.y = asf(u & 0xffff0000u); return r; }
template <int CTRL> __device__ __forceinline__ float dpp(float x) {
  return asf((unsigned)__builtin_amdgcn_update_dpp(0, (int)asu(x), CTRL, 0xf, 0xf, true));
}
constexpr int XOR1 = 0xB1, XOR2 = 0x4E, XOR7 = 0x141, ROR8 = 0x128, ROR4 = 0x124, ROR2 = 0x122, BCAST = 0x150;
constexpr int QP_EVEN = 0xA0, QP_ODD = 0xF5;
__device__ __forceinline__ float x16_add(float x) { auto s = __builtin_amdgcn_permlane16_swap(asu(x), asu(x), false, false); return asf(s[0]) + asf(s[1]); }
__device__ __forceinline__ float x32_add(float x) { auto s = __builtin_amdgcn_permlane32_swap(asu(x), asu(x), false, false); return asf(s[0]) + asf(s[1]); }
__device__ __forceinline__ float x16_max(float x) { auto s = __builtin_amdgcn_permlane16_swap(asu(x), asu(x), false, false); return fmaxf(asf(s[0]), asf(s[1])); }
__device__ __forceinline__ float x32_max(float x) { auto s = __builtin_amdgcn_permlane32_swap(asu(x), asu(x), false, false); return fmaxf(asf(s[0]), asf(s[1])); }
__device__ __forceinline__ float tr32(float a, float b) { auto s = __builtin_amdgcn_permlane32_swap(asu(a), asu(b), false, false); return asf(s[0]) + asf(s[1]); }
__device__ __forceinline__ float tr16(float a, float b) { auto s = __builtin_amdgcn_permlane16_swap(asu(a), asu(b), false, false); return asf(s[0]) + asf(s[1]); }
__device__ __forceinline__ float wave_sum(float v) {
  v += dpp<XOR1>(v); v += dpp<XOR2>(v); v += dpp<XOR7>(v); v += dpp<ROR8>(v);
  v = x16_add(v); return x32_add(v);
}
__device__ __forceinline__ float wave_max(float v) {
  v = fmaxf(v, dpp<XOR1>(v)); v = fmaxf(v, dpp<XOR2>(v)); v = fmaxf(v, dpp<XOR7>(v)); v = fmaxf(v, dpp<ROR8>(v));
  v = x16_max(v); return x32_max(v);
}
__device__ __forceinline__ rsrc_t mkrs(const void* p, unsigned bytes) { return __builtin_amdgcn_make_buffer_rsrc((void*)p, 0, (int)bytes, 0x00020000); }
__device__ __forceinline__ rsrc_t selr(bool c, rsrc_t a, rsrc_t b) { return c ? a : b; }
__device__ __forceinline__ unsigned selu(bool c, unsigned a, unsigned b) { return c ? a : b; }
template <int AUX> __device__ __forceinline__ v4u ld4(rsrc_t r, unsigned vo, unsigned so) { return __builtin_amdgcn_raw_buffer_load_b128(r, (int)vo, (int)so, AUX); }
template <int AUX> __device__ __forceinline__ v2u ld2(rsrc_t r, unsigned vo, unsigned so) { return __builtin_amdgcn_raw_buffer_load_b64(r, (int)vo, (int)so, AUX); }
__device__ __forceinline__ void st4c(v4u v, rsrc_t r, unsigned vo) { __builtin_amdgcn_raw_buffer_store_b128(v, r, (int)vo, 0, 16); }
__device__ __forceinline__ void st1c(unsigned v, rsrc_t r, unsigned vo) { __builtin_amdgcn_raw_buffer_store_b32(v, r, (int)vo, 0, 16); }
template <unsigned M> __device__ __forceinline__ unsigned tag(unsigned u, unsigned t) { return (u & ~M) | (t & M); }
__device__ __forceinline__ v4u tag4(const f32x4& v, const v4u& t) { return (v4u){tag<1>(asu(v[0]), t[0]), tag<1>(asu(v[1]), t[1]), tag<1>(asu(v[2]), t[2]), tag<1>(asu(v[3]), t[3])}; }
__device__ __forceinline__ bool arrived(const v4u& v, const v4u& o) { return __builtin_amdgcn_ballot_w64(v[0] == o[0] || v[1] == o[1] || v[2] == o[2] || v[3] == o[3]) == 0; }
__device__ __forceinline__ unsigned lowb(const v4u& v) { return __builtin_amdgcn_perm(v[1], v[0], 0x0c0c0400u) | __builtin_amdgcn_perm(v[3], v[2], 0x04000c0cu); }
#define SB() __builtin_amdgcn_sched_barrier(0)
__device__ __forceinline__ void bar() { __builtin_amdgcn_s_waitcnt(0xC07F); __builtin_amdgcn_s_barrier(); }
#define WAITV(n) __builtin_amdgcn_s_waitcnt(((n) & 15) | (((n) >> 4) << 14) | 0x0F70)
template <int V> struct IC { static constexpr int value = V; constexpr operator int() const { return V; } };
template <int A, int B, class Fn> __device__ __forceinline__ void static_for(Fn&& f) { if constexpr (A < B) { f(IC<A>{}); static_for<A + 1, B>(f); } }
#define LDS_FENCE() asm volatile("" ::: "memory")

constexpr float EXP_C = 0.0625f * 1.4426950408889634f;

struct Att {
  float m, l;
  f32x4 a[2];
};
__device__ __forceinline__ f32x4 mfma(const v4u& a, const v4u& b, const f32x4& c) {
  return __builtin_amdgcn_mfma_f32_16x16x32_bf16(__builtin_bit_cast(bf16x8, a), __builtin_bit_cast(bf16x8, b), c, 0, 0, 0);
}
__device__ __forceinline__ float red_max(float v) { v = fmaxf(v, dpp<XOR1>(v)); v = fmaxf(v, dpp<XOR2>(v)); return x32_max(x16_max(v)); }

template <int NL, class ISSUE>
__device__ __forceinline__ float scores(v4u (&kb)[8], const v4u (&qb)[8], int lane, ISSUE issue) {
  f32x4 d = {0.f, 0.f, 0.f, 0.f};
#pragma unroll
  for (int i = 0; i < NL; ++i) {
    d = mfma(kb[i], qb[i], d);
    asm volatile("" : "+v"(d));
    issue(i);
    SB();
  }
  const bool odd = lane & 1;
  return rbf((odd ? d[3] : d[0]) + dpp<XOR1>(odd ? d[1] : d[2]));
}
template <class ISSUE>
__device__ __forceinline__ float phase_k4(v4u (&kb)[8], const v4u (&qb)[8], Att& st, int lane, ISSUE issue) {
  float s = scores<4>(kb, qb, lane, issue);
  s = (lane & 2) ? -__builtin_inff() : s;
  const float mn = fmaxf(st.m, red_max(s));
  const float al = __builtin_amdgcn_exp2f((st.m - mn) * EXP_C);
  const float p = __builtin_amdgcn_exp2f((s - mn) * EXP_C);
  st.m = mn; st.l = st.l * al + p;
  st.a[0] *= al; st.a[1] *= al;
  SB();
  return p;
}
template <int I, int O, class ISSUE>
__device__ __forceinline__ void pv_step(v4u (&vb)[8], unsigned pp, const v4u& pm, Att& st, ISSUE& issue) {
  const v4u pb = pm & (unsigned)__builtin_amdgcn_update_dpp(0, (int)pp, I * 0x55, 0xf, 0xf, true);
  st.a[0] = mfma(vb[O + 2 * I], pb, st.a[0]);
  asm volatile("" : "+v"(st.a[0]));
  issue(O + 2 * I);
  SB();
  st.a[1] = mfma(vb[O + 2 * I + 1], pb, st.a[1]);
  asm volatile("" : "+v"(st.a[1]));
  issue(O + 2 * I + 1);
  SB();
}
template <int NL, class ISSUE>
__device__ __forceinline__ void phase_v(v4u (&vb)[8], float p, const v4u& pm, Att& st, ISSUE issue) {
  constexpr int O = 8 - NL;
  const unsigned pp = __builtin_bit_cast(unsigned, (bf16x2){(__bf16)p, (__bf16)p});
  pv_step<0, O>(vb, pp, pm, st, issue); pv_step<1, O>(vb, pp, pm, st, issue);
  if constexpr (NL == 8) { pv_step<2, O>(vb, pp, pm, st, issue); pv_step<3, O>(vb, pp, pm, st, issue); }
}

template <int N, int GAP, int SL>
__device__ __forceinline__ v4u poll(const v4u& r, unsigned pa, const v4u& old) {
  v4u d;
  unsigned long long t0, t1, t2;
  int n = SPIN_CAP;
  asm volatile(
      "s_nop 4\n\t"
      "buffer_load_dwordx4 v[200:203], %5, %6, 0 offen sc1\n\t"
      ".rept %11\n\t"
      "s_sleep %12\n\t"
      "buffer_load_dwordx4 v[200:203], %5, %6, 0 offen sc1\n\t"
      ".endr\n"
      "1:\n\t"
      "s_waitcnt vmcnt(%11)\n\t"
      "v_cmp_eq_u32 vcc, v200, %7\n\t"
      "v_cmp_eq_u32 %1, v201, %8\n\t"
      "v_cmp_eq_u32 %2, v202, %9\n\t"
      "v_cmp_eq_u32 %3, v203, %10\n\t"
      "s_or_b64 vcc, vcc, %1\n\t"
      "s_or_b64 %2, %2, %3\n\t"
      "s_or_b64 exec, vcc, %2\n\t"
      "s_cbranch_scc0 2f\n\t"
      ".if %13\n\ts_sleep %13\n\t.endif\n\t"
      "buffer_load_dwordx4 v[200:203], %5, %6, 0 offen sc1\n\t"
      "s_sub_u32 %4, %4, 1\n\t"
      "s_cbranch_scc0 1b\n"
      "2:\n\t"
      "s_mov_b64 exec, -1"
      : "=&{v[200:203]}"(d), "=&s"(t0), "=&s"(t1), "=&s"(t2), "+s"(n)
      : "v"(pa), "s"(r), "v"(old[0]), "v"(old[1]), "v"(old[2]), "v"(old[3]), "n"(N - 1), "n"(GAP), "n"(SL)
      : "vcc", "scc", "memory");
  return d;
}
__device__ __forceinline__ void poll_end(const v4u& d) { asm volatile("s_waitcnt vmcnt(0)" :: "{v[200:203]}"(d)); }

#define STAMP(k) do {} while (0)
#define STAMP_OUT() do {} while (0)

__device__ __forceinline__ f32x2 ln_stats(const v4u& xa, int lane) {
  f32x2 xv[4];
  float sm = 0.f;
#pragma unroll
  for (int j = 0; j < 4; ++j) { xv[j] = unp(xa[j]); sm += xv[j].x + xv[j].y; }
  const float mean = wave_sum(sm) * (1.0f / D);
  float sq = 0.f;
#pragma unroll
  for (int j = 0; j < 4; ++j) { const f32x2 d = xv[j] - mean; sq += d.x * d.x + d.y * d.y; }
  sq = lane < 56 ? sq : 0.f;
  const float var = rbf(wave_sum(sq) * (1.0f / D));
  const float sd = rbf(sqrtf(rbf(var + 1e-5f)));
  return (f32x2){rbf(mean), 1.0f / sd};
}
__device__ __forceinline__ float ln_apply(float x, f32x2 st) { return rbf(rbf(x - st.x) * st.y); }

__global__ void __launch_bounds__(NT, 2)
fused_kernel(const u16* __restrict__ xg, const u16* __restrict__ kc, const u16* __restrict__ vc, const u16* __restrict__ wq,
             const u16* __restrict__ wo, const u16* __restrict__ wg, const u16* __restrict__ wu, const u16* __restrict__ wd,
             u16* __restrict__ out, char* ws) {
  __shared__ __attribute__((aligned(16))) float s_part[8][512];
  __shared__ __attribute__((aligned(16))) float s_mv[8][64];
  __shared__ __attribute__((aligned(16))) unsigned s_q[256];
  __shared__ __attribute__((aligned(16))) unsigned s_qs[64];
  __shared__ __attribute__((aligned(16))) unsigned s_old[8][256];
  __shared__ __attribute__((aligned(16))) unsigned s_dst[8][256];
  __shared__ __attribute__((aligned(16))) float s_ml[2][16];
  __shared__ __attribute__((aligned(16))) float s_o[4][32];
  __shared__ __attribute__((aligned(16))) float s_ml2[16];
  __shared__ __attribute__((aligned(16))) unsigned s_xn[256];
  __shared__ __attribute__((aligned(16))) float s_h[32];
  __shared__ __attribute__((aligned(16))) float s_r[9][16];
  __shared__ __attribute__((aligned(16))) float s_sc[64][32];
  __shared__ int s_ctl[64];
  const int tid = threadIdx.x, lane = tid & 63, w = __builtin_amdgcn_readfirstlane(tid >> 6);
  const int bi = blockIdx.x, h = (((bi & 1) ? bi + 5 : bi) & 6) | (bi >> 7), s = ((bi >> 3) & 15) * 2 + (bi & 1);
  const int b = h * 32 + s;
  static_assert(8 * TQ + 8 * TF + 6 * TFY + 10 * TY == 4096 && TQ - NP > 80 && TF - NP > 80 && TFY - NP > 80 && TY - NP > 80, "tickets");
  static_assert(TQ - NP <= 128 && TF - NP <= 128 && TFY - NP <= 128 && TY - NP <= 128, "tickets");
  const int nt = s < 8 ? TQ : s < 16 ? TF : s < 22 ? TFY : TY, nk = (nt - NP) >> 1;
  const int first = s * TY - min(s, 22) * (TY - TFY) - min(s, 16) * (TFY - TF) - min(s, 8) * (TF - TQ);
  asm volatile("" :: "s"(xg), "s"(kc), "s"(vc), "s"(wq), "s"(wo), "s"(wg), "s"(wu), "s"(wd), "s"(out), "s"(ws));
  STAMP(0);
  const rsrc_t rx = mkrs(xg, D * 2);
  const rsrc_t rk = mkrs(kc + (size_t)h * SK * DH, SK * DH * 2);
  const rsrc_t rv = mkrs(vc + (size_t)h * SK * DH, SK * DH * 2);
  const rsrc_t rws = mkrs(ws, WS_END);
  const unsigned l16 = lane * 16;
  auto ubase = [&](int u) -> unsigned { return (unsigned)(first + 2 * u) * 4096u; };
  const unsigned kvo = ((lane >> 1) & 7) * 512 + (lane >> 4) * 32 + (lane & 1) * 16, vvo = (lane >> 4) * 1024 + (lane & 15) * 16;
  auto ldk = [&](unsigned base, int i) { return ld4<2>(rk, kvo + (i & 3) * 128, base + (i >> 2) * 4096); };
  const unsigned area = (unsigned)b * AREA;
  auto area_of = [&](int ss) -> unsigned { return (unsigned)(h * 32 + ss) * AREA; };
  const unsigned lmask = lane < 56 ? 0u : OOB;

  const bool isq = s < 8, isf = s < 14, isy = s >= 16;
  const int f = h * 14 + s, y = h * 16 + s - 16, ro = h * 4 + (s >> 2);
  const bool isr = s < 16 && ((s + h) & 3) == 0 && ro < 28;
  const bool tile = isq;
  const bool leader = (!(NOHS & 1) && b == NWG - 1 && w == 1), member = (w == 0);

  const unsigned pa = isy ? (w < 4 ? area + A_O1 + w * 1024 + l16 : (w == 7 && y < 112) ? area + A_F + l16 : OOB) : isr ? area + A_Y2 + w * 1024 + l16 : OOB;
  const unsigned mo_ = area_of(16 + (lane & 8) + (lane >> 4)) + A_O1 + s * 128 + (lane & 7) * 16;
  auto mf = [&](int p) -> unsigned {
    const int fd = (f & ~3) + ((p >> 2) & 3);
    return (unsigned)((fd >> 4) * 32 + 16 + (fd & 15)) * AREA + A_F + (f & 3) * 256 + (p >> 4) * 64 + (p & 3) * 16;
  };
  auto my = [&](int p) -> unsigned { return (unsigned)((p >> 4) * 32 + (p & 12) + ((-(p >> 4)) & 3)) * AREA + A_Y2 + y * 64 + (p & 3) * 16; };
  v4u eo, ed;
  auto da_of = [&]() {
    const int p = (w - 2) * 64 + lane;
    unsigned da = (isy && p < 112) ? my(p) : OOB;
    return (w & 6) == 2 ? da : (w & 6) == 4 ? mo_ + (w - 4) * 4 * AREA : (w == 6 && isf) ? mf(lane) : OOB;
  };
  if (EO == 1) { eo = ld4<OA>(rws, pa, 0); SB(); }
  if (EO == 3 || (EO == 4 && !tile)) { eo = ld4<OA>(rws, pa, 0); ed = ld4<OB>(rws, da_of(), 0); SB(); }
  if (EO == 5) { ed = ld4<OB>(rws, da_of(), 0); SB(); }
  const v4u sent = {SENT, SENT, SENT, SENT};
  unsigned poll_off = lane == 1 ? WS_GO : lane < 4 ? WS_T - 32 + l16 : OOB;
  poll_off = lane == 0 ? WS_REQ + (b >> 2) * 16 : poll_off;
  poll_off = leader ? (ES ? lane * 4 * AREA + (lane & 3) * 256 : WS_ECHO + l16) : poll_off;
  auto echoes = [&](unsigned seen) {
    v4u r;
#pragma unroll
    for (int j = 0; j < 4; ++j) r[j] = __builtin_amdgcn_raw_buffer_load_b32(rws, (int)(poll_off | (seen >> j << 31)), j * AREA, 16);
    return r;
  };
  v4u pv = {0u, 0u, 0u, 0u}, e1 = {0u, 0u, 0u, 0u};
  unsigned go_ref = 0;
  int* const ctl = &s_ctl[lane];
  auto ctl_get = [&](int i) { return __builtin_amdgcn_readfirstlane(__hip_atomic_load(&s_ctl[i], __ATOMIC_RELAXED, __HIP_MEMORY_SCOPE_WORKGROUP)); };
  auto ctl_set = [&](int i) { __hip_atomic_store(&s_ctl[i], 1, __ATOMIC_RELAXED, __HIP_MEMORY_SCOPE_WORKGROUP); };
  const unsigned qoff = WS_Q + h * 2048 + l16;
  v4u qv0, qv1;
  if (member) {
    qv0 = ld4<16>(rws, qoff, 0);
    qv1 = ld4<16>(rws, qoff, 1024);
    SB();
    int c = lane == C_NEXT ? (isq ? TOFFQ : TOFF) * 128 : 0;
    c = lane == C_TICK ? 40 : c;
    c = lane == C_T0 ? (int)(__builtin_amdgcn_s_memrealtime() & 0x7fffffff) : c;
    __hip_atomic_store(ctl, c, __ATOMIC_RELAXED, __HIP_MEMORY_SCOPE_WORKGROUP);
    st1c(c, rws, (lane == C_T0 && (bi >> 3) == 12) ? WS_T + (bi & 7) * 4 : OOB);
  }
  *reinterpret_cast<v4u*>(&s_sc[w * 8][lane * 4]) = sent;
  int t0 = 0;
  auto now = [&]() { return ((int)__builtin_amdgcn_s_memrealtime() - t0) & 0x7fffff; };
  const int neg = lane == C_NEXT ? 0 : (int)0x80000000;
  const int xcc = __builtin_amdgcn_s_getreg(20 | (3 << 11));
  constexpr auto tkf = [](int t, int sh) { return TAU + sh * 128 / ((t - NP) / 2 - 8); };
  constexpr auto tvf = [](int t, int sh) { return TAU - sh * 128 / ((t - NP) / 2 + 8 - PL); };
  int tk = s < 8 ? tkf(TQ + 16, SQ) : s < 16 ? tkf(TF, SF_) : s < 22 ? tkf(TFY, SFY) : tkf(TY, SY);
  int tv = s < 8 ? tvf(TQ, SQ) : s < 16 ? tvf(TF, SF_) : s < 22 ? tvf(TFY, SFY) : tvf(TY, SY);
  int inc = lane == C_NEXT ? tk : 0;
  auto token = [&](int burst) {
    const int t = max(now() * 128 - burst, 0);
    __hip_atomic_fetch_max(ctl, t | neg, __ATOMIC_RELAXED, __HIP_MEMORY_SCOPE_WORKGROUP);
    return __hip_atomic_fetch_add(ctl, inc, __ATOMIC_RELAXED, __HIP_MEMORY_SCOPE_WORKGROUP);
  };
  auto due_of = [&](int x) { return __builtin_amdgcn_readlane(x, C_NEXT) >> 7; };
  auto until = [&](int due) {
#pragma unroll 1
    for (int it = 0; it < 4096 && now() < due; ++it) __builtin_amdgcn_s_sleep(1);
    SB();
  };
  unsigned qs0, qs1;
  auto q_there = [&]() { return __builtin_amdgcn_ballot_w64(((lowb(qv0) ^ qs0) & (lowb(qv1) ^ qs1)) != 0xFFFFFFFFu) == 0; };
  auto load_q = [&]() { qv0 = ld4<16>(rws, qoff, 0); qv1 = ld4<16>(rws, qoff, 1024); };
  auto publish_q = [&]() {
    *reinterpret_cast<v2u*>(&s_q[lane * 2]) = (v2u){__builtin_amdgcn_perm(qv0[1], qv0[0], 0x07060302u), __builtin_amdgcn_perm(qv0[3], qv0[2], 0x07060302u)};
    *reinterpret_cast<v2u*>(&s_q[128 + lane * 2]) = (v2u){__builtin_amdgcn_perm(qv1[1], qv1[0], 0x07060302u), __builtin_amdgcn_perm(qv1[3], qv1[2], 0x07060302u)};
    LDS_FENCE();
    __builtin_amdgcn_s_waitcnt(0xC07F);
    ctl_set(C_Q);
  };
  v4u pre[4][8], ring[8];
  auto unit0 = [&]() {
#pragma unroll
    for (int i = 0; i < 8; ++i) { pre[0][i] = ldk(ubase(w), i); SB(); }
    if (EO == 2) { eo = ld4<OA>(rws, pa, 0); SB(); }
  };
  SB();
  if (tile) {
    v4u tw[8];
    const int oct = 7 * w;
    constexpr int stride = NQ * 2;
    const rsrc_t rt = mkrs(wq, D * stride);
    const unsigned to = (unsigned)((oct + (lane >> 3)) * 8 * stride + (h * 8 + s) * 128 + (lane & 7) * 16) | lmask;
    const v4u xa = ld4<0>(rx, l16, 0);
    const v4u xb = ld4<0>(rx, (unsigned)(oct + (lane >> 3)) * 16 | lmask, 0);
    SB();
#pragma unroll
    for (int i = 0; i < 8; ++i) { tw[i] = ld4<2>(rt, to, i * stride); SB(); }
    if (EO == 4) { eo = ld4<OA>(rws, pa, 0); ed = ld4<OB>(rws, da_of(), 0); SB(); }
    LDS_FENCE(); bar(); LDS_FENCE();
    t0 = ctl_get(C_T0);
    SB();
    const f32x2 ls = ln_stats(xa, lane);
    f32x2 acc[4];
#pragma unroll
    for (int i = 0; i < 8; ++i) {
      const float xn = ln_apply(asf((i & 1) ? (xb[i >> 1] & 0xffff0000u) : (xb[i >> 1] << 16)), ls);
#pragma unroll
      for (int j = 0; j < 4; ++j) acc[j] = i ? acc[j] + unp(tw[i][j]) * xn : unp(tw[i][j]) * xn;
    }
    float y0 = tr32(acc[0].x, acc[2].x), y1 = tr32(acc[0].y, acc[2].y), y2 = tr32(acc[1].x, acc[3].x), y3 = tr32(acc[1].y, acc[3].y);
    y0 = tr16(y0, y2); y1 = tr16(y1, y3);
    const bool s_ = lane & 8;
    y0 = (s_ ? y1 : y0) + dpp<ROR8>(s_ ? y0 : y1);
    s_mv[w][(lane & 7) * 8 + (lane >> 3)] = y0;
    STAMP(6);
    token(QBURST);
    unit0();
    LDS_FENCE(); bar(); LDS_FENCE();
    if (member) {
      float a = 0.f;
#pragma unroll
      for (int k = 0; k < 8; ++k) a += s_mv[k][lane];
      s_qs[lane] = f2bf(a) << 16;
      LDS_FENCE();
      const v4u qs = *reinterpret_cast<const v4u*>(&s_qs[(lane & 15) * 4]) | (~((s & 4) ? qv1 : qv0) & 0xffffu);
      st4c(qs, rws, (lane >> 4) == (s & 3) ? qoff + (s >> 2) * 1024 : OOB);
    }
  } else if (leader) {
    e1 = ES ? echoes(0) : ld4<16>(rws, poll_off, 0);
    go_ref = __builtin_amdgcn_raw_buffer_load_b32(rws, (int)WS_GO, 0, 16);
  }
  SB();
  STAMP(5);
  SB();
  const u16* wa = isy ? wo + (size_t)y * 32 * D : wg;
  const u16* wb = isy ? wd + (size_t)(y < 112 ? (y >> 1) * 32 + (y & 1) * 4 : 0) * D : wu;
  asm volatile("" : "+s"(wa), "+s"(wb));
  bool ok = !member;
  static_for<0, 40>([&](auto N) {
    constexpr int n = N;
    if constexpr (n == 0) {
      if (!tile) unit0();
    }
    if constexpr (n == 8) {
      if (!tile) {
        LDS_FENCE(); bar(); LDS_FENCE();
        t0 = ctl_get(C_T0);
        if (leader) {
          e1 += 1u;
          go_ref += 1u;
          st4c(e1, rws, WS_REQ + l16);
        }
      }
    }
    if constexpr (n > 0 && n % 8 == 0) {
      const int x = token(isq ? QBURST : PBURST);
      if constexpr (n > QFIRST) {
        if (!ok) {
          ok = q_there();
          if (ok) publish_q();
        }
      }
      if constexpr (n == OPOS + 16) {
        *reinterpret_cast<v4u*>(&s_old[w][lane * 4]) = (int)pa < 0 ? sent : eo;
        *reinterpret_cast<v4u*>(&s_dst[w][lane * 4]) = ~ed;
        LDS_FENCE();
        __hip_atomic_fetch_add(ctl, lane == C_OLD ? 1 : 0, __ATOMIC_RELAXED, __HIP_MEMORY_SCOPE_WORKGROUP);
      }
      SB();
      until(due_of(x));
      if constexpr (n == 8) {
        if (member) { qs0 = lowb(qv0); qs1 = lowb(qv1); }
      }
      if constexpr (n >= QFIRST) {
        if (!ok) load_q();
      }
      if constexpr (n == OPOS) {
        if (EO == 0 || EO == 5) eo = ld4<OA>(rws, pa, 0);
        if (EO < 3) ed = ld4<OB>(rws, da_of(), 0);
      }
      SB();
    }
    if constexpr (n == QPOS) {
      STAMP(9);
      if (ES && leader) pv = echoes(0);
      else pv = ld4<16>(rws, (member || leader) ? poll_off : OOB, 0);
      SB();
    }
    if constexpr (n >= 8) {
      if constexpr (n >= PACE) WAITV(PACE + ((n >= QPOS && n < QPOS + PACE) ? 1 : 0) + ((n >= OPOS && n < OPOS + PACE) ? (EO == 0 ? 2 : (EO == 3 || EO == 4) ? 0 : 1) : 0));
      if constexpr (n < 32) pre[n >> 3][n & 7] = ldk(ubase((n & 56) + w), n & 7); else ring[n & 7] = ldk(ubase(32 + w), n & 7);
    }
    SB();
    if constexpr (n - PACE == 7 || n - PACE == 15 || n - PACE == 23 || n - PACE == 31) STAMP(16 + (n - PACE) / 8);
  });
  inc = lane == C_TICK ? 1 : inc;
  int tok = token(KBURST);
  STAMP(10);
  bool svc = member || leader;
  unsigned seenm = 0, last_echo = 0;
  bool echoed = false;
  auto service = [&]() {
    if (leader) {
      seenm |= (pv[0] == e1[0] ? 1u : 0u) | (pv[1] == e1[1] ? 2u : 0u) | (pv[2] == e1[2] ? 4u : 0u) | (pv[3] == e1[3] ? 8u : 0u);
      svc = __builtin_amdgcn_ballot_w64(seenm != 15u) != 0;
      st1c(go_ref, rws, (lane == 0 && !svc) ? WS_GO : OOB);
    } else {
      const unsigned req = (unsigned)__builtin_amdgcn_readfirstlane((int)((b & 2) ? ((b & 1) ? pv[3] : pv[2]) : ((b & 1) ? pv[1] : pv[0])));
      const unsigned go = (unsigned)__builtin_amdgcn_readlane((int)pv[0], 1);
      go_ref = echoed ? go_ref : go;
      svc = go == (unsigned)__builtin_amdgcn_readfirstlane((int)go_ref);
      if (NOHS & 1) svc = false;
      const bool rdy = ctl_get(C_OLD) == 8;
      if (ES && EL > 0) st4c((v4u){req, req, req, req}, rws, (lane < EL && svc && rdy && !(echoed && req == last_echo)) ? area + ((b >> 2) & 3) * 256 + l16 : OOB);
      else st1c(req, rws, (lane == 0 && svc && rdy && !(echoed && req == last_echo)) ? (ES ? area + ((b >> 2) & 3) * 256 : WS_ECHO + b * 4) : OOB);
      echoed |= rdy; last_echo = req;
    }
    if (ES && leader) pv = echoes(seenm);
    else pv = ld4<16>(rws, svc ? poll_off : OOB, 0);
    SB();
  };

  {
    if (!ok) {
      ok = q_there();
      if (ok) publish_q();
    }
#pragma unroll 1
    for (int it = 0; it < SPIN_CAP && !ctl_get(C_Q); ++it) {
      if (svc && it >= QTRIES) { service(); asm volatile("" :: "v"(pv)); }
      if (!ok) {
        load_q();
        ok = q_there() | !svc;
        if (ok) publish_q();
      } else {
        __builtin_amdgcn_s_sleep(1);
      }
    }
  }
  LDS_FENCE();
  v4u qb[8];
#pragma unroll
  for (int i = 0; i < 4; ++i) {
    const v4u z = {0u, 0u, 0u, 0u}, q = *reinterpret_cast<const v4u*>(&s_q[(lane & 8) * 16 + i * 32 + (lane >> 4) * 8 + (lane & 1) * 4]);
    qb[i] = (lane & 2) ? z : q; qb[4 + i] = (lane & 2) ? q : z;
  }
  v4u pm;
#pragma unroll
  for (int j = 0; j < 4; ++j) pm[j] = ((lane >> 1) & 3) == j ? ((lane & 1) ? 0xffff0000u : 0xffffu) : 0u;
  STAMP(1);
  SB();

  Att st;
  int sx = 20;
  unsigned fj, rank;
  {
    float* const sc = &s_sc[0][(lane >> 3) * 4 + (lane & 3)];
    const unsigned fvo = ((lane >> 3) * (8 * F * 2) + (lane & 7) * 16) | lmask;
    rsrc_t nra, nrb;
    unsigned nva, nvb, nba, nbb, nsa, nsb;
    int kind = 0, nu = 32 + w, cu;
    auto next = [&]() {
      cu = nu;
      const int t = __builtin_amdgcn_readlane(tok, C_TICK);
      const bool fx = t < 2 * nk, tw = t >= nt;
      kind = fx ? (t >= nk ? 1 : 0) : tw ? 3 : 2;
      nu = t >= nk ? t - nk : t;
      nba = (unsigned)(fx ? first + 2 * nu : first + t) * 4096u;
      nbb = fx ? nba + 4096 : nba;
      nra = selr(kind == 1, rv, rk);
      nrb = selr(kind == 0, rk, rv);
      nva = selu(kind == 1, vvo, kvo); nvb = selu(kind == 0, kvo, vvo);
      nsa = selu(kind == 1, 256, 128); nsb = selu(kind == 0, 128, 256);
      const int u = t + 8 - nk;
      inc = lane == C_NEXT ? (t + PL >= 2 * nk ? PP : (int)selu(u >= 0, tv, tk) + ((unsigned)(u + XA) < XA + XB ? TX : 0)) : inc;
      if (tw) {
        rank = t - nt;
        fj = isf ? rank - FO : 4;
        nba = ((f & 3) * 2 + ((fj >> 1) & 1)) * (56 * F * 2) + (f >> 2) * 128;
        nbb = nba + 4 * F * 2;
        nra = nrb = mkrs((fj & 1) ? wb : wa, (fj < 4 && !FX) ? D * F * 2 : 0);
        nva = nvb = fvo;
        nsa = nsb = F * 2;
        inc = 0;
      } else {
        until(due_of(tok));
      }
      SB();
    };
    auto ldn = [&](int i) { ring[i] = i < 4 ? ld4<2>(nra, nva, nba + i * nsa) : ld4<2>(nrb, nvb, nbb + (i - 4) * nsb); };
    float mx;
    if (member) {
      const v4u d = pv - (unsigned)t0;
      const bool bad = max(max(d[0] + 512u, d[1] + 512u), max(d[2] + 512u, d[3] + 512u)) > 1024u;
      const int sm = (int)((d[0] + d[1]) + (d[2] + d[3]));
      const int lead = (__builtin_amdgcn_ballot_w64(bad) & 12) ? 0 : __builtin_amdgcn_readlane(sm, 2) + __builtin_amdgcn_readlane(sm, 3) + DB;
      tk += (lead * DK) >> 5; tv += (lead * DK) >> 5;
      __hip_atomic_fetch_add(ctl, lane == C_LEAD ? (lead * DG) >> 5 : (lane == C_NEXT ? (lead * DS) >> 5 : 0), __ATOMIC_RELAXED, __HIP_MEMORY_SCOPE_WORKGROUP);
    }
    if (svc) service();
    {
      const float s0 = scores<8>(pre[0], qb, lane, [&](int) {});
      const float s1 = scores<8>(pre[1], qb, lane, [&](int) {});
      const float s2 = scores<8>(pre[2], qb, lane, [&](int) {});
      const float s3 = scores<8>(pre[3], qb, lane, [&](int) {});
      sc[w * 32] = s0; sc[(8 + w) * 32] = s1; sc[(16 + w) * 32] = s2; sc[(24 + w) * 32] = s3;
      mx = fmaxf(fmaxf(s0, s1), fmaxf(s2, s3));
      SB();
      STAMP(11);
    }
    const v4u xa2 = ld4<0>(rx, (isf && w == 7) ? l16 : OOB, 0);
    SB();
#pragma unroll 1
    while (kind == 0) {
      next();
      const float sv = scores<8>(ring, qb, lane, ldn);
      sc[cu * 32] = sv;
      tok = token(KBURST);
      mx = fmaxf(mx, sv);
      STAMP(sx); ++sx;
      if (svc) service();
      SB();
    }
    STAMP(7);
    if (isf && w == 7) {
      const f32x2 ls = ln_stats(xa2, lane);
      v4u xn;
#pragma unroll
      for (int j = 0; j < 4; ++j) {
        const f32x2 v = unp(xa2[j]);
        xn[j] = __builtin_amdgcn_perm(asu(ln_apply(v.y, ls)), asu(ln_apply(v.x, ls)), 0x07060302u);
      }
      *reinterpret_cast<v4u*>(&s_xn[lane * 4]) = xn;
      LDS_FENCE();
      ctl_set(C_FC);
      SB();
    }
    st.m = red_max(mx); st.l = 0.f;
    st.a[0] = st.a[1] = (f32x4){0.f, 0.f, 0.f, 0.f};
#pragma unroll 1
    while (kind == 1) {
      float sv;
      for (;;) {
        sv = __hip_atomic_load(&sc[nu * 32], __ATOMIC_RELAXED, __HIP_MEMORY_SCOPE_WORKGROUP);
        if (__builtin_amdgcn_ballot_w64(asu(sv) == SENT) == 0) break;
        __builtin_amdgcn_s_sleep(1);
      }
      if (__builtin_amdgcn_ballot_w64(sv > st.m) != 0) {
        const float m2 = fmaxf(st.m, red_max(sv));
        const float al = __builtin_amdgcn_exp2f((st.m - m2) * EXP_C);
        st.m = m2; st.l *= al;
        st.a[0] *= al; st.a[1] *= al;
      }
      const float p = __builtin_amdgcn_exp2f((sv - st.m) * EXP_C);
      st.l += p;
      SB();
      next();
      phase_v<8>(ring, p, pm, st, ldn);
      tok = token(VBURST);
      STAMP(sx); ++sx;
      if (svc) service();
      SB();
    }
    STAMP(8);
#pragma unroll 1
    while (kind == 2) {
      next();
      const float p = phase_k4(ring, qb, st, lane, ldn);
      phase_v<4>(ring, p, pm, st, ldn);
      tok = token(VBURST);
      STAMP(sx); ++sx;
    }
#pragma unroll 1
    for (int it = 0; it < SPIN_CAP && svc; ++it) {
      service();
      asm volatile("" :: "v"(pv));
    }
    LDS_FENCE();
  }
  STAMP(12);
  const lds4_t pr = (lds4_t)&s_part[0][lane * 4], pmr = (lds4_t)&s_ml[(lane >> 3) & 1][0];
  {
    const lds4_t pw = (lds4_t)&s_part[w][lane * 4];
    const lds1_t pml = (lds1_t)&s_ml[(lane >> 3) & 1][w];
    pw[0] = st.a[0]; pw[64] = st.a[1];
    float l = st.l;
    l += dpp<XOR1>(l); l += dpp<XOR2>(l); l = x32_add(x16_add(l));
    pml[0] = st.m; pml[8] = l;
  }
  const v4u rw4 = {(unsigned)(size_t)ws, (unsigned)((size_t)ws >> 32), WS_END, 0x00020000u};
  v4u old = *reinterpret_cast<const v4u*>(&s_old[w][lane * 4]);
  if (isy && w >= 4) {
    v4u wt[6][2];
    const rsrc_t rwo = mkrs(wa, XW ? 0 : 32 * D * 2);
    const rsrc_t rwd = mkrs(wb, (y < 112 && !XW) ? 28 * D * 2 : 0);
    unsigned o0 = (lane >> 3) * (D * 2) + (w - 4) * 256 + (lane & 7) * 16, o1 = w == 7 ? OOB : o0 + 128;
    unsigned o2 = o0 + (lane >> 5) * (4 * D * 2), o3 = o1 + (lane >> 5) * (4 * D * 2);
    asm volatile("" : "+v"(o0), "+v"(o1), "+v"(o2), "+v"(o3), "+v"(old));
    SB();
    LDS_FENCE(); bar(); LDS_FENCE();
    STAMP(2);
    if (TWD) __builtin_amdgcn_s_sleep(TWD);
    SB();
#pragma unroll
    for (int k = 0; k < 6; ++k) {
      const int i = (k + 4) % 6;
      wt[i][0] = i < 4 ? ld4<2>(rwo, o0, i * 8 * D * 2) : ld4<2>(rwd, o2, (i & 3) * 16 * D * 2);
      wt[i][1] = i < 4 ? ld4<2>(rwo, o1, i * 8 * D * 2) : ld4<2>(rwd, o3, (i & 3) * 16 * D * 2);
      SB();
    }
    const int p = (w - 4) * 32 + (lane >> 4) * 8 + (lane & 7);
    v4u dt = *reinterpret_cast<const v4u*>(&s_dst[2][p * 4]) & 1u;
    unsigned to = ((lane & 8) || p >= 112) ? OOB : my(p);
    f32x4 acc[2] = {{0.f, 0.f, 0.f, 0.f}, {0.f, 0.f, 0.f, 0.f}};
    auto bop = [&](float a) -> v4u { return pm & __builtin_bit_cast(unsigned, (bf16x2){(__bf16)a, (__bf16)a}); };
    auto step = [&](int i, const v4u& pb) { acc[0] = mfma(wt[i][0], pb, acc[0]); acc[1] = mfma(wt[i][1], pb, acc[1]); };
    lds4_t po = (lds4_t)&s_o[0][(lane >> 3) * 4];
    lds1_t ph = (lds1_t)&s_h[(lane & 4) * 4 + ((lane >> 4) & 1) * 8 + (lane & 3) * 2 + (lane >> 5)];
    asm volatile("" : "+v"(dt), "+v"(to), "+v"(po), "+v"(ph));
    SB();
    v4u dr = old;
    if (w == 7) {
      if (G3) until(G3 + __builtin_amdgcn_readlane(tok, C_LEAD));
      dr = poll<PD3, PG3, 0>(rw4, pa, old);
      const v4u d = dr & ~1u;
      float g = tr16(tr32(asf(d[0]), asf(d[2])), tr32(asf(d[1]), asf(d[3])));
      g = rbf(g + dpp<ROR8>(g));
      const float u = dpp<ROR4>(g);
      *ph = rbf(g / (1.0f + __expf(-g))) * u;
      SB();
    }
    STAMP(14);
    LDS_FENCE(); bar(); LDS_FENCE();
    STAMP(3);
    const f32x2 hv = *reinterpret_cast<const f32x2*>(&s_h[(lane >> 3) * 2]);
    const f32x4 ma = *reinterpret_cast<const f32x4*>(&s_ml2[0]), mb = *reinterpret_cast<const f32x4*>(&s_ml2[4]);
    f32x4 pq[4];
#pragma unroll
    for (int k = 0; k < 4; ++k) pq[k] = po[k * 8];
    SB();
    const v4u h0 = bop(hv.x), h1 = bop(hv.y);
    if (FB == 1) {
      step(4, h0); step(5, h1);
      asm volatile("" : "+v"(acc[0]), "+v"(acc[1]));
      SB();
    }
    const float M = fmaxf(fmaxf(ma[0], ma[2]), fmaxf(mb[0], mb[2]));
    const float e0 = __builtin_amdgcn_exp2f((ma[0] - M) * EXP_C), e1_ = __builtin_amdgcn_exp2f((ma[2] - M) * EXP_C);
    const float e2 = __builtin_amdgcn_exp2f((mb[0] - M) * EXP_C), e3 = __builtin_amdgcn_exp2f((mb[2] - M) * EXP_C);
    const float inv = __builtin_amdgcn_rcpf((ma[1] * e0 + ma[3] * e1_) + (mb[1] * e2 + mb[3] * e3));
    const f32x4 av = ((pq[0] * e0 + pq[1] * e1_) + (pq[2] * e2 + pq[3] * e3)) * inv;
    if (FB) {
      v4u pb[4];
#pragma unroll
      for (int i = 0; i < 4; ++i) pb[i] = bop(av[i]);
      asm volatile("" : "+v"(pb[0]), "+v"(pb[1]), "+v"(pb[2]), "+v"(pb[3]));
      SB();
      if (FB == 2) { step(4, h0); step(5, h1); }
#pragma unroll
      for (int i = 0; i < 4; ++i) step(i, pb[i]);
    } else {
#pragma unroll
      for (int i = 0; i < 4; ++i) step(i, bop(av[i]));
    }
    f32x4 z;
#pragma unroll
    for (int v = 0; v < 4; ++v) {
      const float a0 = asf((unsigned)__builtin_amdgcn_update_dpp((int)asu(acc[0][v]), (int)asu(acc[0][v]), ROR8, 0xc, 0xf, false));
      const float a1 = asf((unsigned)__builtin_amdgcn_update_dpp((int)asu(acc[1][v]), (int)asu(acc[1][v]), ROR8, 0xc, 0xf, false));
      z[v] = tr32(a0, a1);
    }
    __builtin_amdgcn_raw_buffer_store_b128(tag4(z, dt), rws, (int)to, 0, S2);
    STAMP(4);
    poll_end(dr);
    STAMP_OUT();
    __builtin_amdgcn_endpgm();
  }
  const unsigned half = isy ? w : rank - SR;
  if (half < 2) {
    WAITV(0);
    ldsu_t tp = (ldsu_t)&s_dst[4 + half][lane * 4];
    unsigned to = mo_ + half * 4 * AREA;
    lds4_t pp = pr + half * 64;
    asm volatile("" : "+v"(tp), "+v"(to), "+v"(pp), "+v"(old));
    SB();
    LDS_FENCE(); bar(); LDS_FENCE();
    STAMP(2);
    f32x4 ml[4], pt[8];
#pragma unroll
    for (int k = 0; k < 4; ++k) ml[k] = pmr[k];
#pragma unroll
    for (int k = 0; k < 8; ++k) pt[k] = pp[k * 128];
    const v4u t = *tp;
    float M = -3.0e38f;
#pragma unroll
    for (int k = 0; k < 8; ++k) M = fmaxf(M, ml[k >> 2][k & 3]);
    f32x4 e[2] = {(ml[0] - M) * EXP_C, (ml[1] - M) * EXP_C};
    f32x4 o = {0.f, 0.f, 0.f, 0.f};
#pragma unroll
    for (int k = 0; k < 8; ++k) {
      e[k >> 2][k & 3] = __builtin_amdgcn_exp2f(e[k >> 2][k & 3]);
      o += pt[k] * e[k >> 2][k & 3];
    }
    const f32x4 l4 = ml[2] * e[0] + ml[3] * e[1];
    const float L = (l4[0] + l4[2]) + (l4[1] + l4[3]);
    v4u msg;
#pragma unroll
    for (int j = 0; j < 3; ++j) msg[j] = tag<0x100>(__builtin_amdgcn_perm(asu(o[j]), asu(o[3]), 0x07060501u + j), t[j]);
    msg[3] = tag<1>(asu((lane & 1) ? L : M), t[3]);
    __builtin_amdgcn_raw_buffer_store_b128(msg, rws, (int)to, 0, S1);
    SB();
  } else if (fj < 4) {
#pragma unroll 1
    for (int it = 0; it < SPIN_CAP && !ctl_get(C_FC); ++it) __builtin_amdgcn_s_sleep(1);
    LDS_FENCE();
    const v4u xn = *reinterpret_cast<const v4u*>(&s_xn[(((f & 3) * 2 + (fj >> 1)) * 7 + (lane >> 3)) * 4]);
    const int p = fj * 16 + ((lane >> 4) & 1) * 8 + (lane & 7);
    ldsu_t tp = (ldsu_t)&s_dst[6][p * 4];
    unsigned to = (lane & 40) ? OOB : mf(p);
    v4u pb[8];
#pragma unroll
    for (int i = 0; i < 8; ++i) {
      pb[i] = pm & __builtin_amdgcn_perm(xn[i >> 1], xn[i >> 1], (i & 1) ? 0x03020302u : 0x01000100u);
      asm volatile("" : "+v"(pb[i]));
    }
    asm volatile("" : "+v"(tp), "+v"(to), "+v"(old));
    SB();
    LDS_FENCE(); bar(); LDS_FENCE();
    SB();
    STAMP(2);
    const v4u t = *tp;
    f32x4 acc[2] = {{0.f, 0.f, 0.f, 0.f}, {0.f, 0.f, 0.f, 0.f}};
#pragma unroll
    for (int i = 0; i < 8; ++i) acc[i & 1] = mfma(ring[i], pb[i], acc[i & 1]);
    asm volatile("" :: "v"(pv));
    acc[0] += acc[1];
    f32x4 z;
#pragma unroll
    for (int v = 0; v < 4; ++v)
      z[v] = x32_add(asf((unsigned)__builtin_amdgcn_update_dpp((int)asu(acc[0][v]), (int)asu(acc[0][v]), ROR8, 0xc, 0xf, false)));
    __builtin_amdgcn_raw_buffer_store_b128(tag4(z, t), rws, (int)to, 0, S1);
    SB();
  } else {
    WAITV(0);
    LDS_FENCE(); bar(); LDS_FENCE();
    STAMP(2);
  }
  if (isy) {
    lds1_t po = (lane & 8) ? (lds1_t)&s_ml2[w * 2 + 1 + (lane == 8 ? 0 : 8)] : (lds1_t)&s_o[w][(lane & 7) * 4 + (lane >> 5) * 2 + ((lane >> 4) & 1)];
    lds1_t pq = (lds1_t)&s_ml2[w * 2];
    asm volatile("" : "+v"(po), "+v"(pq));
    if (G1) until(G1 + __builtin_amdgcn_readlane(tok, C_LEAD));
    STAMP(14);
    const v4u dr = poll<PD1, PG1, PS1>(rw4, pa, old);
    STAMP(15);
    const v4u d = dr & (v4u){~0x100u, ~0x100u, ~0x100u, ~1u};
    const float ot = dpp<XOR1>(asf(d[3]));
    const float m = (lane & 1) ? ot : asf(d[3]), l = (lane & 1) ? asf(d[3]) : ot;
    float M = fmaxf(m, dpp<ROR8>(m)); M = x16_max(M); M = x32_max(M);
    *pq = M;
    const float e = __builtin_amdgcn_exp2f((m - M) * EXP_C);
    const float v3 = asf(__builtin_amdgcn_perm(__builtin_amdgcn_perm(d[2], d[1], 0x04000c0cu), d[0], 0x0706000cu));
    float y0 = tr32(asf(d[0]) * e, asf(d[2]) * e), y1 = tr32(asf(d[1]) * e, v3 * e), L = x32_add(l * e);
    y0 = tr16(y0, y1); L = x16_add(L);
    const bool s_ = lane & 8;
    *po = (s_ ? L : y0) + dpp<ROR8>(s_ ? y0 : L);
    LDS_FENCE(); bar(); LDS_FENCE();
    STAMP(3);
    poll_end(dr);
    STAMP(4);
    STAMP_OUT();
    __builtin_amdgcn_endpgm();
  }
  if (isr) {
    const int col = (ro >> 1) * 32 + (ro & 1) * 4 + (lane & 3) * 8 + ((lane >> 2) & 3);
    float xo = asf((unsigned)xg[col] << 16);
    lds1_t po = (lds1_t)&s_r[(lane & 12) ? 8 : w][(lane & 3) * 4 + (lane >> 5) * 2 + ((lane >> 4) & 1)], pq = (lds1_t)&s_r[lane >> 4][lane & 15];
    const rsrc_t ro_ = mkrs(out, D * 2);
    unsigned oo = lane < 16 ? col * 2 : OOB;
    asm volatile("" : "+v"(xo), "+v"(po), "+v"(pq), "+v"(oo));
    if (G2) until(G2 + __builtin_amdgcn_readlane(tok, C_LEAD));
    const v4u dr = poll<PD2, PG2, PS2>(rw4, pa, old);
    const v4u d = dr & ~1u;
    float y0 = tr32(asf(d[0]), asf(d[2])), y1 = tr32(asf(d[1]), asf(d[3]));
    y0 = tr16(y0, y1);
    y0 += dpp<ROR8>(y0); y0 += dpp<ROR4>(y0);
    *po = y0;
    LDS_FENCE(); bar(); LDS_FENCE();
    if (w == 0) {
      float a = pq[0] + pq[64];
      a = x16_add(a); a = x32_add(a);
      __builtin_amdgcn_raw_buffer_store_b16((short)f2bf(xo + a), ro_, (int)oo, 0, SO);
    }
    STAMP(4);
    poll_end(dr);
  }
  if (!isr) STAMP(4);
  STAMP_OUT();
}

extern "C" void kernel_launch(void* const* d_in, const int* in_sizes, int n_in,
                              void* d_out, int out_size, void* d_ws, size_t ws_size,
                              hipStream_t stream) {
  fused_kernel<<<NWG, NT, 0, stream>>>((const u16*)d_in[0], (const u16*)d_in[1], (const u16*)d_in[2], (const u16*)d_in[3],
                                       (const u16*)d_in[4], (const u16*)d_in[6], (const u16*)d_in[7], (const u16*)d_in[8],
                                       (u16*)d_out, (char*)d_ws);
}
```

```cpp
#include <hip/hip_runtime.h>
#include <hip/hip_bf16.h>
#include <stdint.h>

#define QPOS 24
#define PACE 8
#define QTRIES 8
#define TQ 123
#define TF 131
#define TFY 129
#define TY 129
#define NP 4
#define TAU 3640
#define PP 3000
#define PL 8
#define TOFF 150
#define TOFFQ 180
#define QBURST 100000
#define PBURST 4000
#define KBURST 100000
#define VBURST 100000
#define QFIRST 24
#define OPOS 8
#define TWD 0
#define OA 16
#define OB 16
#define S1 16
#define S2 16
#define G1 3835
#define G2 3955
#define EO 3
#define PD1 2
#define PG1 6
#define PD2 2
#define PG2 6
#define PD3 2
#define PG3 6
#define G3 3845
#define SR 2
#define PS1 0
#define PS2 0
#define SO 0
#define DK 57
#define DS 0
#define DG 4
#define DB 0
#define SQ 0
#define SF_ 0
#define SFY 0
#define SY 0
#define XA 4
#define XB 4
#define TX 0
#define NOHS 0
#define ES 1
#define EL 8
#define SVM 0
#define SVL 0
#define FO 4
#define FP 1
#define NT3 1
#define UF 1
#define WV 1
#define TT 0
#define TTS 8
#define FX 0
#define ST 0
#define GW 0
#define GD 0
#define STN 8
#define STP 0
#define FB 1
#define XW 0
#define DIAG 0
#define LITE 0

typedef unsigned v4u __attribute__((ext_vector_type(4)));
typedef unsigned v2u __attribute__((ext_vector_type(2)));
typedef float f32x4 __attribute__((ext_vector_type(4)));
typedef float f32x2 __attribute__((ext_vector_type(2)));
typedef __bf16 bf16x2 __attribute__((ext_vector_type(2)));
typedef __bf16 bf16x8 __attribute__((ext_vector_type(8)));
typedef unsigned short u16;
typedef __amdgpu_buffer_rsrc_t rsrc_t;
typedef __attribute__((address_space(3))) f32x4* lds4_t;
typedef __attribute__((address_space(3))) v4u* ldsu_t;
typedef __attribute__((address_space(3))) float* lds1_t;

constexpr int D = 448, SK = 32768, DH = 256, F = 1792, NQ = 4096;
constexpr int NWG = 256, NT = 512;
constexpr unsigned OOB = 0x80000000u;
constexpr unsigned SENT = 0xFFFFFFFFu;
constexpr unsigned AREA = 9216;
constexpr unsigned A_O1 = 1024;
constexpr unsigned A_F = 5120;
constexpr unsigned A_Y2 = 1024;
constexpr unsigned WS_REQ = NWG * AREA;
constexpr unsigned WS_ECHO = WS_REQ + 1024;
constexpr unsigned WS_GO = WS_ECHO + 1024;
constexpr unsigned WS_T = WS_GO + 256;
constexpr unsigned WS_Q = WS_T + 256;
constexpr unsigned WS_END = WS_Q + 8 * 2048;
constexpr int SPIN_CAP = 1 << 18;
constexpr int C_NEXT = 0, C_TICK = 1, C_GO = 2, C_Q = 3, C_T0 = 4, C_FC = 5, C_OLD = 6, C_DONE = 7, C_LEAD = 8;

__device__ __forceinline__ float asf(unsigned u) { return __builtin_bit_cast(float, u); }
__device__ __forceinline__ unsigned asu(float f) { return __builtin_bit_cast(unsigned, f); }
__device__ __forceinline__ float rbf(float f) { __bf16 b = (__bf16)f; return (float)b; }
__device__ __forceinline__ unsigned f2bf(float f) { __bf16 b = (__bf16)f; return (unsigned)__builtin_bit_cast(u16, b); }
__device__ __forceinline__ float dot2(unsigned k, unsigned q, float acc) {
  return __builtin_amdgcn_fdot2_f32_bf16(__builtin_bit_cast(bf16x2, k), __builtin_bit_cast(bf16x2, q), acc, false);
}
__device__ __forceinline__ f32x2 unp(unsigned u) { f32x2 r; r.x = asf(u << 16); r.y = asf(u & 0xffff0000u); return r; }
template <int CTRL> __device__ __forceinline__ float dpp(float x) {
  return asf((unsigned)__builtin_amdgcn_update_dpp(0, (int)asu(x), CTRL, 0xf, 0xf, true));
}
constexpr int XOR1 = 0xB1, XOR2 = 0x4E, XOR7 = 0x141, ROR8 = 0x128, ROR4 = 0x124, ROR2 = 0x122, BCAST = 0x150;
constexpr int QP_EVEN = 0xA0, QP_ODD = 0xF5;
__device__ __forceinline__ float x16_add(float x) { auto s = __builtin_amdgcn_permlane16_swap(asu(x), asu(x), false, false); return asf(s[0]) + asf(s[1]); }
__device__ __forceinline__ float x32_add(float x) { auto s = __builtin_amdgcn_permlane32_swap(asu(x), asu(x), false, false); return asf(s[0]) + asf(s[1]); }
__device__ __forceinline__ float x16_max(float x) { auto s = __builtin_amdgcn_permlane16_swap(asu(x), asu(x), false, false); return fmaxf(asf(s[0]), asf(s[1])); }
__device__ __forceinline__ float x32_max(float x) { auto s = __builtin_amdgcn_permlane32_swap(asu(x), asu(x), false, false); return fmaxf(asf(s[0]), asf(s[1])); }
__device__ __forceinline__ float tr32(float a, float b) { auto s = __builtin_amdgcn_permlane32_swap(asu(a), asu(b), false, false); return asf(s[0]) + asf(s[1]); }
__device__ __forceinline__ float tr16(float a, float b) { auto s = __builtin_amdgcn_permlane16_swap(asu(a), asu(b), false, false); return asf(s[0]) + asf(s[1]); }
__device__ __forceinline__ float wave_sum(float v) {
  v += dpp<XOR1>(v); v += dpp<XOR2>(v); v += dpp<XOR7>(v); v += dpp<ROR8>(v);
  v = x16_add(v); return x32_add(v);
}
__device__ __forceinline__ float wave_max(float v) {
  v = fmaxf(v, dpp<XOR1>(v)); v = fmaxf(v, dpp<XOR2>(v)); v = fmaxf(v, dpp<XOR7>(v)); v = fmaxf(v, dpp<ROR8>(v));
  v = x16_max(v); return x32_max(v);
}
__device__ __forceinline__ rsrc_t mkrs(const void* p, unsigned bytes) { return __builtin_amdgcn_make_buffer_rsrc((void*)p, 0, (int)bytes, 0x00020000); }
__device__ __forceinline__ rsrc_t selr(bool c, rsrc_t a, rsrc_t b) { return c ? a : b; }
__device__ __forceinline__ unsigned selu(bool c, unsigned a, unsigned b) { return c ? a : b; }
template <int AUX> __device__ __forceinline__ v4u ld4(rsrc_t r, unsigned vo, unsigned so) { return __builtin_amdgcn_raw_buffer_load_b128(r, (int)vo, (int)so, AUX); }
template <int AUX> __device__ __forceinline__ v2u ld2(rsrc_t r, unsigned vo, unsigned so) { return __builtin_amdgcn_raw_buffer_load_b64(r, (int)vo, (int)so, AUX); }
__device__ __forceinline__ void st4c(v4u v, rsrc_t r, unsigned vo) { __builtin_amdgcn_raw_buffer_store_b128(v, r, (int)vo, 0, 16); }
__device__ __forceinline__ void st1c(unsigned v, rsrc_t r, unsigned vo) { __builtin_amdgcn_raw_buffer_store_b32(v, r, (int)vo, 0, 16); }
template <unsigned M> __device__ __forceinline__ unsigned tag(unsigned u, unsigned t) { return (u & ~M) | (t & M); }
__device__ __forceinline__ v4u tag4(const f32x4& v, const v4u& t) { return (v4u){tag<1>(asu(v[0]), t[0]), tag<1>(asu(v[1]), t[1]), tag<1>(asu(v[2]), t[2]), tag<1>(asu(v[3]), t[3])}; }
__device__ __forceinline__ bool arrived(const v4u& v, const v4u& o) { return __builtin_amdgcn_ballot_w64(v[0] == o[0] || v[1] == o[1] || v[2] == o[2] || v[3] == o[3]) == 0; }
__device__ __forceinline__ unsigned lowb(const v4u& v) { return __builtin_amdgcn_perm(v[1], v[0], 0x0c0c0400u) | __builtin_amdgcn_perm(v[3], v[2], 0x04000c0cu); }
#define SB() __builtin_amdgcn_sched_barrier(0)
__device__ __forceinline__ void bar() { __builtin_amdgcn_s_waitcnt(0xC07F); __builtin_amdgcn_s_barrier(); }
#define WAITV(n) __builtin_amdgcn_s_waitcnt(((n) & 15) | (((n) >> 4) << 14) | 0x0F70)
template <int V> struct IC { static constexpr int value = V; constexpr operator int() const { return V; } };
template <int A, int B, class Fn> __device__ __forceinline__ void static_for(Fn&& f) { if constexpr (A < B) { f(IC<A>{}); static_for<A + 1, B>(f); } }
#define LDS_FENCE() asm volatile("" ::: "memory")

constexpr float EXP_C = 0.0625f * 1.4426950408889634f;

struct Att {
  float m, l;
  f32x4 a[2];
};
__device__ __forceinline__ f32x4 mfma(const v4u& a, const v4u& b, const f32x4& c) {
  return __builtin_amdgcn_mfma_f32_16x16x32_bf16(__builtin_bit_cast(bf16x8, a), __builtin_bit_cast(bf16x8, b), c, 0, 0, 0);
}
__device__ __forceinline__ float red_max(float v) { v = fmaxf(v, dpp<XOR1>(v)); v = fmaxf(v, dpp<XOR2>(v)); return x32_max(x16_max(v)); }

template <int NL, class ISSUE>
__device__ __forceinline__ float scores(v4u (&kb)[8], const v4u (&qb)[8], int lane, ISSUE issue) {
  f32x4 d = {0.f, 0.f, 0.f, 0.f};
#pragma unroll
  for (int i = 0; i < NL; ++i) {
    d = mfma(kb[i], qb[i], d);
    asm volatile("" : "+v"(d));
    issue(i);
    SB();
  }
  const bool odd = lane & 1;
  return rbf((odd ? d[3] : d[0]) + dpp<XOR1>(odd ? d[1] : d[2]));
}
template <class ISSUE>
__device__ __forceinline__ float phase_k4(v4u (&kb)[8], const v4u (&qb)[8], Att& st, int lane, ISSUE issue) {
  float s = scores<4>(kb, qb, lane, issue);
  s = (lane & 2) ? -__builtin_inff() : s;
  const float mn = fmaxf(st.m, red_max(s));
  const float al = __builtin_amdgcn_exp2f((st.m - mn) * EXP_C);
  const float p = __builtin_amdgcn_exp2f((s - mn) * EXP_C);
  st.m = mn; st.l = st.l * al + p;
  st.a[0] *= al; st.a[1] *= al;
  SB();
  return p;
}
template <int I, int O, class ISSUE>
__device__ __forceinline__ void pv_step(v4u (&vb)[8], unsigned pp, const v4u& pm, Att& st, ISSUE& issue) {
  const v4u pb = pm & (unsigned)__builtin_amdgcn_update_dpp(0, (int)pp, I * 0x55, 0xf, 0xf, true);
  st.a[0] = mfma(vb[O + 2 * I], pb, st.a[0]);
  asm volatile("" : "+v"(st.a[0]));
  issue(O + 2 * I);
  SB();
  st.a[1] = mfma(vb[O + 2 * I + 1], pb, st.a[1]);
  asm volatile("" : "+v"(st.a[1]));
  issue(O + 2 * I + 1);
  SB();
}
template <int NL, class ISSUE>
__device__ __forceinline__ void phase_v(v4u (&vb)[8], float p, const v4u& pm, Att& st, ISSUE issue) {
  constexpr int O = 8 - NL;
  const unsigned pp = __builtin_bit_cast(unsigned, (bf16x2){(__bf16)p, (__bf16)p});
  pv_step<0, O>(vb, pp, pm, st, issue); pv_step<1, O>(vb, pp, pm, st, issue);
  if constexpr (NL == 8) { pv_step<2, O>(vb, pp, pm, st, issue); pv_step<3, O>(vb, pp, pm, st, issue); }
}

template <int N, int GAP, int SL>
__device__ __forceinline__ v4u poll(const v4u& r, unsigned pa, const v4u& old) {
  v4u d;
  unsigned long long t0, t1, t2;
  int n = SPIN_CAP;
  asm volatile(
      "s_nop 4\n\t"
      "buffer_load_dwordx4 v[200:203], %5, %6, 0 offen sc1\n\t"
      ".rept %11\n\t"
      "s_sleep %12\n\t"
      "buffer_load_dwordx4 v[200:203], %5, %6, 0 offen sc1\n\t"
      ".endr\n"
      "1:\n\t"
      "s_waitcnt vmcnt(%11)\n\t"
      "v_cmp_eq_u32 vcc, v200, %7\n\t"
      "v_cmp_eq_u32 %1, v201, %8\n\t"
      "v_cmp_eq_u32 %2, v202, %9\n\t"
      "v_cmp_eq_u32 %3, v203, %10\n\t"
      "s_or_b64 vcc, vcc, %1\n\t"
      "s_or_b64 %2, %2, %3\n\t"
      "s_or_b64 exec, vcc, %2\n\t"
      "s_cbranch_scc0 2f\n\t"
      ".if %13\n\ts_sleep %13\n\t.endif\n\t"
      "buffer_load_dwordx4 v[200:203], %5, %6, 0 offen sc1\n\t"
      "s_sub_u32 %4, %4, 1\n\t"
      "s_cbranch_scc0 1b\n"
      "2:\n\t"
      "s_mov_b64 exec, -1"
      : "=&{v[200:203]}"(d), "=&s"(t0), "=&s"(t1), "=&s"(t2), "+s"(n)
      : "v"(pa), "s"(r), "v"(old[0]), "v"(old[1]), "v"(old[2]), "v"(old[3]), "n"(N - 1), "n"(GAP), "n"(SL)
      : "vcc", "scc", "memory");
  return d;
}
__device__ __forceinline__ void poll_end(const v4u& d) { asm volatile("s_waitcnt vmcnt(0)" :: "{v[200:203]}"(d)); }

#define STAMP(k) do {} while (0)
#define STAMP_OUT() do {} while (0)

__device__ __forceinline__ f32x2 ln_stats(const v4u& xa, int lane) {
  f32x2 xv[4];
  float sm = 0.f;
#pragma unroll
  for (int j = 0; j < 4; ++j) { xv[j] = unp(xa[j]); sm += xv[j].x + xv[j].y; }
  const float mean = wave_sum(sm) * (1.0f / D);
  float sq = 0.f;
#pragma unroll
  for (int j = 0; j < 4; ++j) { const f32x2 d = xv[j] - mean; sq += d.x * d.x + d.y * d.y; }
  sq = lane < 56 ? sq : 0.f;
  const float var = rbf(wave_sum(sq) * (1.0f / D));
  const float sd = rbf(sqrtf(rbf(var + 1e-5f)));
  return (f32x2){rbf(mean), 1.0f / sd};
}
__device__ __forceinline__ float ln_apply(float x, f32x2 st) { return rbf(rbf(x - st.x) * st.y); }

__global__ void __launch_bounds__(NT, 2)
fused_kernel(const u16* __restrict__ xg, const u16* __restrict__ kc, const u16* __restrict__ vc, const u16* __restrict__ wq,
             const u16* __restrict__ wo, const u16* __restrict__ wg, const u16* __restrict__ wu, const u16* __restrict__ wd,
             u16* __restrict__ out, char* ws) {
  __shared__ __attribute__((aligned(16))) float s_part[8][512];
  __shared__ __attribute__((aligned(16))) float s_mv[8][64];
  __shared__ __attribute__((aligned(16))) unsigned s_q[256];
  __shared__ __attribute__((aligned(16))) unsigned s_qs[64];
  __shared__ __attribute__((aligned(16))) unsigned s_old[8][256];
  __shared__ __attribute__((aligned(16))) unsigned s_dst[8][256];
  __shared__ __attribute__((aligned(16))) float s_ml[2][16];
  __shared__ __attribute__((aligned(16))) float s_o[4][32];
  __shared__ __attribute__((aligned(16))) float s_ml2[16];
  __shared__ __attribute__((aligned(16))) unsigned s_xn[256];
  __shared__ __attribute__((aligned(16))) float s_h[32];
  __shared__ __attribute__((aligned(16))) float s_r[9][16];
  __shared__ __attribute__((aligned(16))) float s_sc[64][32];
  __shared__ int s_ctl[64];
  const int tid = threadIdx.x, lane = tid & 63, w = __builtin_amdgcn_readfirstlane(tid >> 6);
  const int bi = blockIdx.x, h = (((bi & 1) ? bi + 5 : bi) & 6) | (bi >> 7), s = ((bi >> 3) & 15) * 2 + (bi & 1);
  const int b = h * 32 + s;
  static_assert(8 * TQ + 8 * TF + 6 * TFY + 10 * TY == 4096 && TQ - NP > 80 && TF - NP > 80 && TFY - NP > 80 && TY - NP > 80, "tickets");
  static_assert(TQ - NP <= 128 && TF - NP <= 128 && TFY - NP <= 128 && TY - NP <= 128, "tickets");
  const int nt = s < 8 ? TQ : s < 16 ? TF : s < 22 ? TFY : TY, nk = (nt - NP) >> 1;
  const int first = s * TY - min(s, 22) * (TY - TFY) - min(s, 16) * (TFY - TF) - min(s, 8) * (TF - TQ);
  asm volatile("" :: "s"(xg), "s"(kc), "s"(vc), "s"(wq), "s"(wo), "s"(wg), "s"(wu), "s"(wd), "s"(out), "s"(ws));
  STAMP(0);
  const rsrc_t rx = mkrs(xg, D * 2);
  const rsrc_t rk = mkrs(kc + (size_t)h * SK * DH, SK * DH * 2);
  const rsrc_t rv = mkrs(vc + (size_t)h * SK * DH, SK * DH * 2);
  const rsrc_t rws = mkrs(ws, WS_END);
  const unsigned l16 = lane * 16;
  auto ubase = [&](int u) -> unsigned { return (unsigned)(first + 2 * u) * 4096u; };
  const unsigned kvo = ((lane >> 1) & 7) * 512 + (lane >> 4) * 32 + (lane & 1) * 16, vvo = (lane >> 4) * 1024 + (lane & 15) * 16;
  auto ldk = [&](unsigned base, int i) { return ld4<2>(rk, kvo + (i & 3) * 128, base + (i >> 2) * 4096); };
  const unsigned area = (unsigned)b * AREA;
  auto area_of = [&](int ss) -> unsigned { return (unsigned)(h * 32 + ss) * AREA; };
  const unsigned lmask = lane < 56 ? 0u : OOB;

  const bool isq = s < 8, isf = s < 14, isy = s >= 16;
  const int f = h * 14 + s, y = h * 16 + s - 16, ro = h * 4 + (s >> 2);
  const bool isr = s < 16 && ((s + h) & 3) == 0 && ro < 28;
  const bool tile = isq;
  const bool leader = (!(NOHS & 1) && b == NWG - 1 && w == 1), member = (w == 0);

  const unsigned pa = isy ? (w < 4 ? area + A_O1 + w * 1024 + l16 : (w == 7 && y < 112) ? area + A_F + l16 : OOB) : isr ? area + A_Y2 + w * 1024 + l16 : OOB;
  const unsigned mo_ = area_of(16 + (lane & 8) + (lane >> 4)) + A_O1 + s * 128 + (lane & 7) * 16;
  auto mf = [&](int p) -> unsigned {
    const int fd = (f & ~3) + ((p >> 2) & 3);
    return (unsigned)((fd >> 4) * 32 + 16 + (fd & 15)) * AREA + A_F + (f & 3) * 256 + (p >> 4) * 64 + (p & 3) * 16;
  };
  auto my = [&](int p) -> unsigned { return (unsigned)((p >> 4) * 32 + (p & 12) + ((-(p >> 4)) & 3)) * AREA + A_Y2 + y * 64 + (p & 3) * 16; };
  v4u eo, ed;
  auto da_of = [&]() {
    const int p = (w - 2) * 64 + lane;
    unsigned da = (isy && p < 112) ? my(p) : OOB;
    return (w & 6) == 2 ? da : (w & 6) == 4 ? mo_ + (w - 4) * 4 * AREA : (w == 6 && isf) ? mf(lane) : OOB;
  };
  if (EO == 1) { eo = ld4<OA>(rws, pa, 0); SB(); }
  if (EO == 3 || (EO == 4 && !tile)) { eo = ld4<OA>(rws, pa, 0); ed = ld4<OB>(rws, da_of(), 0); SB(); }
  if (EO == 5) { ed = ld4<OB>(rws, da_of(), 0); SB(); }
  const v4u sent = {SENT, SENT, SENT, SENT};
  unsigned poll_off = lane == 1 ? WS_GO : lane < 4 ? WS_T - 32 + l16 : OOB;
  poll_off = lane == 0 ? WS_REQ + (b >> 2) * 16 : poll_off;
  poll_off = leader ? (ES ? lane * 4 * AREA + (lane & 3) * 256 : WS_ECHO + l16) : poll_off;
  auto echoes = [&](unsigned seen) {
    v4u r;
#pragma unroll
    for (int j = 0; j < 4; ++j) r[j] = __builtin_amdgcn_raw_buffer_load_b32(rws, (int)(poll_off | (seen >> j << 31)), j * AREA, 16);
    return r;
  };
  v4u pv = {0u, 0u, 0u, 0u}, e1 = {0u, 0u, 0u, 0u};
  unsigned go_ref = 0;
  int* const ctl = &s_ctl[lane];
  auto ctl_get = [&](int i) { return __builtin_amdgcn_readfirstlane(__hip_atomic_load(&s_ctl[i], __ATOMIC_RELAXED, __HIP_MEMORY_SCOPE_WORKGROUP)); };
  auto ctl_set = [&](int i) { __hip_atomic_store(&s_ctl[i], 1, __ATOMIC_RELAXED, __HIP_MEMORY_SCOPE_WORKGROUP); };
  const unsigned qoff = WS_Q + h * 2048 + l16;
  v4u qv0, qv1;
  if (member) {
    qv0 = ld4<16>(rws, qoff, 0);
    qv1 = ld4<16>(rws, qoff, 1024);
    SB();
    int c = lane == C_NEXT ? (isq ? TOFFQ : TOFF) * 128 : 0;
    c = lane == C_TICK ? 40 : c;
    c = lane == C_T0 ? (int)(__builtin_amdgcn_s_memrealtime() & 0x7fffffff) : c;
    __hip_atomic_store(ctl, c, __ATOMIC_RELAXED, __HIP_MEMORY_SCOPE_WORKGROUP);
    st1c(c, rws, (lane == C_T0 && (bi >> 3) == 12) ? WS_T + (bi & 7) * 4 : OOB);
  }
  *reinterpret_cast<v4u*>(&s_sc[w * 8][lane * 4]) = sent;
  int t0 = 0;
  auto now = [&]() { return ((int)__builtin_amdgcn_s_memrealtime() - t0) & 0x7fffff; };
  const int neg = lane == C_NEXT ? 0 : (int)0x80000000;
  const int xcc = __builtin_amdgcn_s_getreg(20 | (3 << 11));
  constexpr auto tkf = [](int t, int sh) { return TAU + sh * 128 / ((t - NP) / 2 - 8); };
  constexpr auto tvf = [](int t, int sh) { return TAU - sh * 128 / ((t - NP) / 2 + 8 - PL); };
  int tk = s < 8 ? tkf(TQ + 16, SQ) : s < 16 ? tkf(TF, SF_) : s < 22 ? tkf(TFY, SFY) : tkf(TY, SY);
  int tv = s < 8 ? tvf(TQ, SQ) : s < 16 ? tvf(TF, SF_) : s < 22 ? tvf(TFY, SFY) : tvf(TY, SY);
  int inc = lane == C_NEXT ? tk : 0;
  auto token = [&](int burst) {
    const int t = max(now() * 128 - burst, 0);
    __hip_atomic_fetch_max(ctl, t | neg, __ATOMIC_RELAXED, __HIP_MEMORY_SCOPE_WORKGROUP);
    return __hip_atomic_fetch_add(ctl, inc, __ATOMIC_RELAXED, __HIP_MEMORY_SCOPE_WORKGROUP);
  };
  auto due_of = [&](int x) { return __builtin_amdgcn_readlane(x, C_NEXT) >> 7; };
  auto until = [&](int due) {
#pragma unroll 1
    for (int it = 0; it < 4096 && now() < due; ++it) __builtin_amdgcn_s_sleep(1);
    SB();
  };
  unsigned qs0, qs1;
  auto q_there = [&]() { return __builtin_amdgcn_ballot_w64(((lowb(qv0) ^ qs0) & (lowb(qv1) ^ qs1)) != 0xFFFFFFFFu) == 0; };
  auto load_q = [&]() { qv0 = ld4<16>(rws, qoff, 0); qv1 = ld4<16>(rws, qoff, 1024); };
  auto publish_q = [&]() {
    *reinterpret_cast<v2u*>(&s_q[lane * 2]) = (v2u){__builtin_amdgcn_perm(qv0[1], qv0[0], 0x07060302u), __builtin_amdgcn_perm(qv0[3], qv0[2], 0x07060302u)};
    *reinterpret_cast<v2u*>(&s_q[128 + lane * 2]) = (v2u){__builtin_amdgcn_perm(qv1[1], qv1[0], 0x07060302u), __builtin_amdgcn_perm(qv1[3], qv1[2], 0x07060302u)};
    LDS_FENCE();
    __builtin_amdgcn_s_waitcnt(0xC07F);
    ctl_set(C_Q);
  };
  v4u pre[4][8], ring[8];
  auto unit0 = [&]() {
#pragma unroll
    for (int i = 0; i < 8; ++i) { pre[0][i] = ldk(ubase(w), i); SB(); }
    if (EO == 2) { eo = ld4<OA>(rws, pa, 0); SB(); }
  };
  SB();
  if (tile) {
    v4u tw[8];
    const int oct = 7 * w;
    constexpr int stride = NQ * 2;
    const rsrc_t rt = mkrs(wq, D * stride);
    const unsigned to = (unsigned)((oct + (lane >> 3)) * 8 * stride + (h * 8 + s) * 128 + (lane & 7) * 16) | lmask;
    const v4u xa = ld4<0>(rx, l16, 0);
    const v4u xb = ld4<0>(rx, (unsigned)(oct + (lane >> 3)) * 16 | lmask, 0);
    SB();
#pragma unroll
    for (int i = 0; i < 8; ++i) { tw[i] = ld4<2>(rt, to, i * stride); SB(); }
    if (EO == 4) { eo = ld4<OA>(rws, pa, 0); ed = ld4<OB>(rws, da_of(), 0); SB(); }
    LDS_FENCE(); bar(); LDS_FENCE();
    t0 = ctl_get(C_T0);
    SB();
    const f32x2 ls = ln_stats(xa, lane);
    f32x2 acc[4];
#pragma unroll
    for (int i = 0; i < 8; ++i) {
      const float xn = ln_apply(asf((i & 1) ? (xb[i >> 1] & 0xffff0000u) : (xb[i >> 1] << 16)), ls);
#pragma unroll
      for (int j = 0; j < 4; ++j) acc[j] = i ? acc[j] + unp(tw[i][j]) * xn : unp(tw[i][j]) * xn;
    }
    float y0 = tr32(acc[0].x, acc[2].x), y1 = tr32(acc[0].y, acc[2].y), y2 = tr32(acc[1].x, acc[3].x), y3 = tr32(acc[1].y, acc[3].y);
    y0 = tr16(y0, y2); y1 = tr16(y1, y3);
    const bool s_ = lane & 8;
    y0 = (s_ ? y1 : y0) + dpp<ROR8>(s_ ? y0 : y1);
    s_mv[w][(lane & 7) * 8 + (lane >> 3)] = y0;
    STAMP(6);
    token(QBURST);
    unit0();
    LDS_FENCE(); bar(); LDS_FENCE();
    if (member) {
      float a = 0.f;
#pragma unroll
      for (int k = 0; k < 8; ++k) a += s_mv[k][lane];
      s_qs[lane] = f2bf(a) << 16;
      LDS_FENCE();
      const v4u qs = *reinterpret_cast<const v4u*>(&s_qs[(lane & 15) * 4]) | (~((s & 4) ? qv1 : qv0) & 0xffffu);
      st4c(qs, rws, (lane >> 4) == (s & 3) ? qoff + (s >> 2) * 1024 : OOB);
    }
  } else if (leader) {
    e1 = ES ? echoes(0) : ld4<16>(rws, poll_off, 0);
    go_ref = __builtin_amdgcn_raw_buffer_load_b32(rws, (int)WS_GO, 0, 16);
  }
  SB();
  STAMP(5);
  SB();
  const u16* wa = isy ? wo + (size_t)y * 32 * D : wg;
  const u16* wb = isy ? wd + (size_t)(y < 112 ? (y >> 1) * 32 + (y & 1) * 4 : 0) * D : wu;
  asm volatile("" : "+s"(wa), "+s"(wb));
  bool ok = !member;
  static_for<0, 40>([&](auto N) {
    constexpr int n = N;
    if constexpr (n == 0) {
      if (!tile) unit0();
    }
    if constexpr (n == 8) {
      if (!tile) {
        LDS_FENCE(); bar(); LDS_FENCE();
        t0 = ctl_get(C_T0);
        if (leader) {
          e1 += 1u;
          go_ref += 1u;
          st4c(e1, rws, WS_REQ + l16);
        }
      }
    }
    if constexpr (n > 0 && n % 8 == 0) {
      const int x = token(isq ? QBURST : PBURST);
      if constexpr (n > QFIRST) {
        if (!ok) {
          ok = q_there();
          if (ok) publish_q();
        }
      }
      if constexpr (n == OPOS + 16) {
        *reinterpret_cast<v4u*>(&s_old[w][lane * 4]) = (int)pa < 0 ? sent : eo;
        *reinterpret_cast<v4u*>(&s_dst[w][lane * 4]) = ~ed;
        LDS_FENCE();
        __hip_atomic_fetch_add(ctl, lane == C_OLD ? 1 : 0, __ATOMIC_RELAXED, __HIP_MEMORY_SCOPE_WORKGROUP);
      }
      SB();
      until(due_of(x));
      if constexpr (n == 8) {
        if (member) { qs0 = lowb(qv0); qs1 = lowb(qv1); }
      }
      if constexpr (n >= QFIRST) {
        if (!ok) load_q();
      }
      if constexpr (n == OPOS) {
        if (EO == 0 || EO == 5) eo = ld4<OA>(rws, pa, 0);
        if (EO < 3) ed = ld4<OB>(rws, da_of(), 0);
      }
      SB();
    }
    if constexpr (n == QPOS) {
      STAMP(9);
      if (ES && leader) pv = echoes(0);
      else pv = ld4<16>(rws, (member || leader) ? poll_off : OOB, 0);
      SB();
    }
    if constexpr (n >= 8) {
      if constexpr (n >= PACE) WAITV(PACE + ((n >= QPOS && n < QPOS + PACE) ? 1 : 0) + ((n >= OPOS && n < OPOS + PACE) ? (EO == 0 ? 2 : (EO == 3 || EO == 4) ? 0 : 1) : 0));
      if constexpr (n < 32) pre[n >> 3][n & 7] = ldk(ubase((n & 56) + w), n & 7); else ring[n & 7] = ldk(ubase(32 + w), n & 7);
    }
    SB();
    if constexpr (n - PACE == 7 || n - PACE == 15 || n - PACE == 23 || n - PACE == 31) STAMP(16 + (n - PACE) / 8);
  });
  inc = lane == C_TICK ? 1 : inc;
  int tok = token(KBURST);
  STAMP(10);
  bool svc = member || leader;
  unsigned seenm = 0, last_echo = 0;
  bool echoed = false;
  auto service = [&]() {
    if (leader) {
      seenm |= (pv[0] == e1[0] ? 1u : 0u) | (pv[1] == e1[1] ? 2u : 0u) | (pv[2] == e1[2] ? 4u : 0u) | (pv[3] == e1[3] ? 8u : 0u);
      svc = __builtin_amdgcn_ballot_w64(seenm != 15u) != 0;
      st1c(go_ref, rws, (lane == 0 && !svc) ? WS_GO : OOB);
    } else {
      const unsigned req = (unsigned)__builtin_amdgcn_readfirstlane((int)((b & 2) ? ((b & 1) ? pv[3] : pv[2]) : ((b & 1) ? pv[1] : pv[0])));
      const unsigned go = (unsigned)__builtin_amdgcn_readlane((int)pv[0], 1);
      go_ref = echoed ? go_ref : go;
      svc = go == (unsigned)__builtin_amdgcn_readfirstlane((int)go_ref);
      if (NOHS & 1) svc = false;
      const bool rdy = ctl_get(C_OLD) == 8;
      if (ES && EL > 0) st4c((v4u){req, req, req, req}, rws, (lane < EL && svc && rdy && !(echoed && req == last_echo)) ? area + ((b >> 2) & 3) * 256 + l16 : OOB);
      else st1c(req, rws, (lane == 0 && svc && rdy && !(echoed && req == last_echo)) ? (ES ? area + ((b >> 2) & 3) * 256 : WS_ECHO + b * 4) : OOB);
      echoed |= rdy; last_echo = req;
    }
    if (ES && leader) pv = echoes(seenm);
    else pv = ld4<16>(rws, svc ? poll_off : OOB, 0);
    SB();
  };

  {
    if (!ok) {
      ok = q_there();
      if (ok) publish_q();
    }
#pragma unroll 1
    for (int it = 0; it < SPIN_CAP && !ctl_get(C_Q); ++it) {
      if (svc && it >= QTRIES) { service(); asm volatile("" :: "v"(pv)); }
      if (!ok) {
        load_q();
        ok = q_there() | !svc;
        if (ok) publish_q();
      } else {
        __builtin_amdgcn_s_sleep(1);
      }
    }
  }
  LDS_FENCE();
  v4u qb[8];
#pragma unroll
  for (int i = 0; i < 4; ++i) {
    const v4u z = {0u, 0u, 0u, 0u}, q = *reinterpret_cast<const v4u*>(&s_q[(lane & 8) * 16 + i * 32 + (lane >> 4) * 8 + (lane & 1) * 4]);
    qb[i] = (lane & 2) ? z : q; qb[4 + i] = (lane & 2) ? q : z;
  }
  v4u pm;
#pragma unroll
  for (int j = 0; j < 4; ++j) pm[j] = ((lane >> 1) & 3) == j ? ((lane & 1) ? 0xffff0000u : 0xffffu) : 0u;
  STAMP(1);
  SB();

  Att st;
  int sx = 20;
  unsigned fj, rank;
  {
    float* const sc = &s_sc[0][(lane >> 3) * 4 + (lane & 3)];
    const unsigned fvo = ((lane >> 3) * (8 * F * 2) + (lane & 7) * 16) | lmask;
    rsrc_t nra, nrb;
    unsigned nva, nvb, nba, nbb, nsa, nsb;
    int kind = 0, nu = 32 + w, cu;
    auto next = [&]() {
      cu = nu;
      const int t = __builtin_amdgcn_readlane(tok, C_TICK);
      const bool fx = t < 2 * nk, tw = t >= nt;
      kind = fx ? (t >= nk ? 1 : 0) : tw ? 3 : 2;
      nu = t >= nk ? t - nk : t;
      nba = (unsigned)(fx ? first + 2 * nu : first + t) * 4096u;
      nbb = fx ? nba + 4096 : nba;
      nra = selr(kind == 1, rv, rk);
      nrb = selr(kind == 0, rk, rv);
      nva = selu(kind == 1, vvo, kvo); nvb = selu(kind == 0, kvo, vvo);
      nsa = selu(kind == 1, 256, 128); nsb = selu(kind == 0, 128, 256);
      const int u = t + 8 - nk;
      inc = lane == C_NEXT ? (t + PL >= 2 * nk ? PP : (int)selu(u >= 0, tv, tk) + ((unsigned)(u + XA) < XA + XB ? TX : 0)) : inc;
      if (tw) {
        rank = t - nt;
        fj = isf ? rank - FO : 4;
        nba = ((f & 3) * 2 + ((fj >> 1) & 1)) * (56 * F * 2) + (f >> 2) * 128;
        nbb = nba + 4 * F * 2;
        nra = nrb = mkrs((fj & 1) ? wb : wa, (fj < 4 && !FX) ? D * F * 2 : 0);
        nva = nvb = fvo;
        nsa = nsb = F * 2;
        inc = 0;
      } else {
        until(due_of(tok));
      }
      SB();
    };
    auto ldn = [&](int i) { ring[i] = i < 4 ? ld4<2>(nra, nva, nba + i * nsa) : ld4<2>(nrb, nvb, nbb + (i - 4) * nsb); };
    float mx;
    if (member) {
      const v4u d = pv - (unsigned)t0;
      const bool bad = max(max(d[0] + 512u, d[1] + 512u), max(d[2] + 512u, d[3] + 512u)) > 1024u;
      const int sm = (int)((d[0] + d[1]) + (d[2] + d[3]));
      const int lead = (__builtin_amdgcn_ballot_w64(bad) & 12) ? 0 : __builtin_amdgcn_readlane(sm, 2) + __builtin_amdgcn_readlane(sm, 3) + DB;
      tk += (lead * DK) >> 5; tv += (lead * DK) >> 5;
      __hip_atomic_fetch_add(ctl, lane == C_LEAD ? (lead * DG) >> 5 : (lane == C_NEXT ? (lead * DS) >> 5 : 0), __ATOMIC_RELAXED, __HIP_MEMORY_SCOPE_WORKGROUP);
    }
    if (svc) service();
    {
      const float s0 = scores<8>(pre[0], qb, lane, [&](int) {});
      const float s1 = scores<8>(pre[1], qb, lane, [&](int) {});
      const float s2 = scores<8>(pre[2], qb, lane, [&](int) {});
      const float s3 = scores<8>(pre[3], qb, lane, [&](int) {});
      sc[w * 32] = s0; sc[(8 + w) * 32] = s1; sc[(16 + w) * 32] = s2; sc[(24 + w) * 32] = s3;
      mx = fmaxf(fmaxf(s0, s1), fmaxf(s2, s3));
      SB();
      STAMP(11);
    }
    const unsigned tto = isf ? (unsigned)((f & 3) * (112 * F * 2) + (f >> 2) * 128 + lane * (TTS * F * 2)) | (lane * TTS < 112 ? 0u : OOB) : lane < 2 ? lane * (27 * D * 2) : OOB;
    const rsrc_t rtt = mkrs(w == 7 ? xg : w == 6 ? wb : wa, w == 7 ? (isf ? D * 2 : 0) : ((w == 6 || w == 5) && ((TT & 1) && isf || (TT & 2) && isy)) ? (isf ? D * F * 2 : 28 * D * 2) : 0);
    const v4u xa2 = TT ? ld4<0>(rtt, w == 7 ? l16 : tto, 0) : ld4<0>(rx, (isf && w == 7) ? l16 : OOB, 0);
    SB();
#pragma unroll 1
    while (kind == 0) {
      next();
      const float sv = scores<8>(ring, qb, lane, ldn);
      sc[cu * 32] = sv;
      tok = token(KBURST);
      mx = fmaxf(mx, sv);
      STAMP(sx); ++sx;
      if (svc) service();
      SB();
    }
    STAMP(7);
    auto touch = [&]() {
      if (w == 6 && (((ST & 1) && isf) || ((ST & 2) && isy))) {
        const unsigned o = isf ? (f & 3) * (112 * F * 2) + (f >> 2) * 128 : 0, sp = isf ? (112 / STN) * F * 2 : 27 * D * 2 / (STN - 1) / 4 * 4;
        const unsigned* pa_ = (const unsigned*)((const char*)wa + o);
        const unsigned* pb_ = (const unsigned*)((const char*)wb + o);
        unsigned a = 0;
#pragma unroll
        for (int i = 0; i < STN; ++i) a ^= __builtin_nontemporal_load((const __attribute__((address_space(4))) unsigned*)(pa_ + i * (sp / 4))) ^ __builtin_nontemporal_load((const __attribute__((address_space(4))) unsigned*)(pb_ + i * (sp / 4)));
        asm volatile("" :: "s"(a));
      }
      SB();
    };
    if (STP == 0) touch();
    if (isf && w == 7) {
      const f32x2 ls = ln_stats(xa2, lane);
      v4u xn;
#pragma unroll
      for (int j = 0; j < 4; ++j) {
        const f32x2 v = unp(xa2[j]);
        xn[j] = __builtin_amdgcn_perm(asu(ln_apply(v.y, ls)), asu(ln_apply(v.x, ls)), 0x07060302u);
      }
      *reinterpret_cast<v4u*>(&s_xn[lane * 4]) = xn;
      LDS_FENCE();
      if (!FP) ctl_set(C_FC);
      SB();
    }
    st.m = red_max(mx); st.l = 0.f;
    st.a[0] = st.a[1] = (f32x4){0.f, 0.f, 0.f, 0.f};
#pragma unroll 1
    while (kind == 1) {
      float sv;
      for (;;) {
        sv = __hip_atomic_load(&sc[nu * 32], __ATOMIC_RELAXED, __HIP_MEMORY_SCOPE_WORKGROUP);
        if (__builtin_amdgcn_ballot_w64(asu(sv) == SENT) == 0) break;
        __builtin_amdgcn_s_sleep(1);
      }
      if (__builtin_amdgcn_ballot_w64(sv > st.m) != 0) {
        const float m2 = fmaxf(st.m, red_max(sv));
        const float al = __builtin_amdgcn_exp2f((st.m - m2) * EXP_C);
        st.m = m2; st.l *= al;
        st.a[0] *= al; st.a[1] *= al;
      }
      const float p = __builtin_amdgcn_exp2f((sv - st.m) * EXP_C);
      st.l += p;
      SB();
      next();
      phase_v<8>(ring, p, pm, st, ldn);
      if (NT3 && kind == 3) break;
      tok = token(VBURST);
      STAMP(sx); ++sx;
      if (svc) service();
      if (STP && (unsigned)(nk - STP - nu) < 8u) touch();
      SB();
    }
    STAMP(8);
#pragma unroll 1
    while (kind == 2) {
      next();
      if (UF) { nba = __builtin_amdgcn_readfirstlane(nba); nbb = __builtin_amdgcn_readfirstlane(nbb); nsa = __builtin_amdgcn_readfirstlane(nsa); nsb = __builtin_amdgcn_readfirstlane(nsb); }
      const float p = phase_k4(ring, qb, st, lane, ldn);
      phase_v<4>(ring, p, pm, st, ldn);
      if (NT3 && kind == 3) break;
      tok = token(VBURST);
      STAMP(sx); ++sx;
    }
#pragma unroll 1
    for (int it = 0; it < SPIN_CAP && svc; ++it) {
      service();
      asm volatile("" :: "v"(pv));
    }
    LDS_FENCE();
  }
  STAMP(12);
  const lds4_t pr = (lds4_t)&s_part[0][lane * 4], pmr = (lds4_t)&s_ml[(lane >> 3) & 1][0];
  {
    const lds4_t pw = (lds4_t)&s_part[w][lane * 4];
    const lds1_t pml = (lds1_t)&s_ml[(lane >> 3) & 1][w];
    pw[0] = st.a[0]; pw[64] = st.a[1];
    float l = st.l;
    l += dpp<XOR1>(l); l += dpp<XOR2>(l); l = x32_add(x16_add(l));
    pml[0] = st.m; pml[8] = l;
  }
  const v4u rw4 = {(unsigned)(size_t)ws, (unsigned)((size_t)ws >> 32), WS_END, 0x00020000u};
  v4u old = *reinterpret_cast<const v4u*>(&s_old[w][lane * 4]);
  if (isy && w >= 4) {
    v4u wt[6][2];
    const rsrc_t rwo = mkrs(wa, XW ? 0 : 32 * D * 2);
    const rsrc_t rwd = mkrs(wb, (y < 112 && !XW) ? 28 * D * 2 : 0);
    unsigned o0 = (lane >> 3) * (D * 2) + (w - 4) * 256 + (lane & 7) * 16, o1 = w == 7 ? OOB : o0 + 128;
    unsigned o2 = o0 + (lane >> 5) * (4 * D * 2), o3 = o1 + (lane >> 5) * (4 * D * 2);
    asm volatile("" : "+v"(o0), "+v"(o1), "+v"(o2), "+v"(o3), "+v"(old));
    SB();
    LDS_FENCE(); bar(); LDS_FENCE();
    STAMP(2);
    if (TWD) __builtin_amdgcn_s_sleep(TWD);
    if (GD) until(GD + __builtin_amdgcn_readlane(tok, C_LEAD));
    SB();
#pragma unroll
    for (int k = 0; k < 6; ++k) {
      const int i = (k + 4) % 6;
      if (GW && k == 2) until(GW + __builtin_amdgcn_readlane(tok, C_LEAD));
      wt[i][0] = i < 4 ? ld4<2>(rwo, o0, i * 8 * D * 2) : ld4<2>(rwd, o2, (i & 3) * 16 * D * 2);
      wt[i][1] = i < 4 ? ld4<2>(rwo, o1, i * 8 * D * 2) : ld4<2>(rwd, o3, (i & 3) * 16 * D * 2);
      SB();
    }
    const int p = (w - 4) * 32 + (lane >> 4) * 8 + (lane & 7);
    v4u dt = *reinterpret_cast<const v4u*>(&s_dst[2][p * 4]) & 1u;
    unsigned to = ((lane & 8) || p >= 112) ? OOB : my(p);
    f32x4 acc[2] = {{0.f, 0.f, 0.f, 0.f}, {0.f, 0.f, 0.f, 0.f}};
    auto bop = [&](float a) -> v4u { return pm & __builtin_bit_cast(unsigned, (bf16x2){(__bf16)a, (__bf16)a}); };
    auto step = [&](int i, const v4u& pb) { acc[0] = mfma(wt[i][0], pb, acc[0]); acc[1] = mfma(wt[i][1], pb, acc[1]); };
    lds4_t po = (lds4_t)&s_o[0][(lane >> 3) * 4];
    lds1_t ph = (lds1_t)&s_h[(lane & 4) * 4 + ((lane >> 4) & 1) * 8 + (lane & 3) * 2 + (lane >> 5)];
    asm volatile("" : "+v"(dt), "+v"(to), "+v"(po), "+v"(ph));
    SB();
    v4u dr = old;
    if (w == 7) {
      if (G3) until(G3 + __builtin_amdgcn_readlane(tok, C_LEAD));
      dr = poll<PD3, PG3, 0>(rw4, pa, old);
      const v4u d = dr & ~1u;
      float g = tr16(tr32(asf(d[0]), asf(d[2])), tr32(asf(d[1]), asf(d[3])));
      g = rbf(g + dpp<ROR8>(g));
      const float u = dpp<ROR4>(g);
      *ph = rbf(g / (1.0f + __expf(-g))) * u;
      SB();
    }
    STAMP(14);
    LDS_FENCE(); bar(); LDS_FENCE();
    STAMP(3);
    const f32x2 hv = *reinterpret_cast<const f32x2*>(&s_h[(lane >> 3) * 2]);
    const f32x4 ma = *reinterpret_cast<const f32x4*>(&s_ml2[0]), mb = *reinterpret_cast<const f32x4*>(&s_ml2[4]);
    f32x4 pq[4];
#pragma unroll
    for (int k = 0; k < 4; ++k) pq[k] = po[k * 8];
    SB();
    const v4u h0 = bop(hv.x), h1 = bop(hv.y);
    if (FB == 1) {
      step(4, h0); step(5, h1);
      asm volatile("" : "+v"(acc[0]), "+v"(acc[1]));
      SB();
    }
    const float M = fmaxf(fmaxf(ma[0], ma[2]), fmaxf(mb[0], mb[2]));
    const float e0 = __builtin_amdgcn_exp2f((ma[0] - M) * EXP_C), e1_ = __builtin_amdgcn_exp2f((ma[2] - M) * EXP_C);
    const float e2 = __builtin_amdgcn_exp2f((mb[0] - M) * EXP_C), e3 = __builtin_amdgcn_exp2f((mb[2] - M) * EXP_C);
    const float inv = __builtin_amdgcn_rcpf((ma[1] * e0 + ma[3] * e1_) + (mb[1] * e2 + mb[3] * e3));
    const f32x4 av = ((pq[0] * e0 + pq[1] * e1_) + (pq[2] * e2 + pq[3] * e3)) * inv;
    if (FB) {
      v4u pb[4];
#pragma unroll
      for (int i = 0; i < 4; ++i) pb[i] = bop(av[i]);
      asm volatile("" : "+v"(pb[0]), "+v"(pb[1]), "+v"(pb[2]), "+v"(pb[3]));
      SB();
      if (FB == 2) { step(4, h0); step(5, h1); }
#pragma unroll
      for (int i = 0; i < 4; ++i) step(i, pb[i]);
    } else {
#pragma unroll
      for (int i = 0; i < 4; ++i) step(i, bop(av[i]));
    }
    f32x4 z;
#pragma unroll
    for (int v = 0; v < 4; ++v) {
      const float a0 = asf((unsigned)__builtin_amdgcn_update_dpp((int)asu(acc[0][v]), (int)asu(acc[0][v]), ROR8, 0xc, 0xf, false));
      const float a1 = asf((unsigned)__builtin_amdgcn_update_dpp((int)asu(acc[1][v]), (int)asu(acc[1][v]), ROR8, 0xc, 0xf, false));
      z[v] = tr32(a0, a1);
    }
    __builtin_amdgcn_raw_buffer_store_b128(tag4(z, dt), rws, (int)to, 0, S2);
    STAMP(4);
    poll_end(dr);
    STAMP_OUT();
    __builtin_amdgcn_endpgm();
  }
  const unsigned half = isy ? w : rank - SR;
  if (half < 2) {
    WAITV(0);
    ldsu_t tp = (ldsu_t)&s_dst[4 + half][lane * 4];
    unsigned to = mo_ + half * 4 * AREA;
    lds4_t pp = pr + half * 64;
    asm volatile("" : "+v"(tp), "+v"(to), "+v"(pp), "+v"(old));
    SB();
    LDS_FENCE(); bar(); LDS_FENCE();
    STAMP(2);
    f32x4 ml[4], pt[8];
#pragma unroll
    for (int k = 0; k < 4; ++k) ml[k] = pmr[k];
#pragma unroll
    for (int k = 0; k < 8; ++k) pt[k] = pp[k * 128];
    const v4u t = *tp;
    float M = -3.0e38f;
#pragma unroll
    for (int k = 0; k < 8; ++k) M = fmaxf(M, ml[k >> 2][k & 3]);
    f32x4 e[2] = {(ml[0] - M) * EXP_C, (ml[1] - M) * EXP_C};
    f32x4 o = {0.f, 0.f, 0.f, 0.f};
#pragma unroll
    for (int k = 0; k < 8; ++k) {
      e[k >> 2][k & 3] = __builtin_amdgcn_exp2f(e[k >> 2][k & 3]);
      o += pt[k] * e[k >> 2][k & 3];
    }
    const f32x4 l4 = ml[2] * e[0] + ml[3] * e[1];
    const float L = (l4[0] + l4[2]) + (l4[1] + l4[3]);
    v4u msg;
#pragma unroll
    for (int j = 0; j < 3; ++j) msg[j] = tag<0x100>(__builtin_amdgcn_perm(asu(o[j]), asu(o[3]), 0x07060501u + j), t[j]);
    msg[3] = tag<1>(asu((lane & 1) ? L : M), t[3]);
    __builtin_amdgcn_raw_buffer_store_b128(msg, rws, (int)to, 0, S1);
    SB();
    if (!isy) STAMP(15);
  } else if (fj < 4) {
    if (FP) {
      SB();
      LDS_FENCE(); bar(); LDS_FENCE();
      SB();
      STAMP(2);
    } else {
#pragma unroll 1
      for (int it = 0; it < SPIN_CAP && !ctl_get(C_FC); ++it) __builtin_amdgcn_s_sleep(1);
    }
    LDS_FENCE();
    const v4u xn = *reinterpret_cast<const v4u*>(&s_xn[(((f & 3) * 2 + (fj >> 1)) * 7 + (lane >> 3)) * 4]);
    const int p = fj * 16 + ((lane >> 4) & 1) * 8 + (lane & 7);
    ldsu_t tp = (ldsu_t)&s_dst[6][p * 4];
    unsigned to = (lane & 40) ? OOB : mf(p);
    v4u pb[8];
#pragma unroll
    for (int i = 0; i < 8; ++i) {
      pb[i] = pm & __builtin_amdgcn_perm(xn[i >> 1], xn[i >> 1], (i & 1) ? 0x03020302u : 0x01000100u);
      asm volatile("" : "+v"(pb[i]));
    }
    asm volatile("" : "+v"(tp), "+v"(to), "+v"(old));
    SB();
    if (!FP) {
      LDS_FENCE(); bar(); LDS_FENCE();
      SB();
      STAMP(2);
    }
    const v4u t = *tp;
    f32x4 acc[2] = {{0.f, 0.f, 0.f, 0.f}, {0.f, 0.f, 0.f, 0.f}};
#pragma unroll
    for (int i = 0; i < 8; ++i) acc[i & 1] = mfma(ring[i], pb[i], acc[i & 1]);
    asm volatile("" :: "v"(pv));
    acc[0] += acc[1];
    f32x4 z;
#pragma unroll
    for (int v = 0; v < 4; ++v)
      z[v] = x32_add(asf((unsigned)__builtin_amdgcn_update_dpp((int)asu(acc[0][v]), (int)asu(acc[0][v]), ROR8, 0xc, 0xf, false)));
    __builtin_amdgcn_raw_buffer_store_b128(tag4(z, t), rws, (int)to, 0, S1);
    SB();
    STAMP(14);
  } else {
    if (!WV) WAITV(0);
    LDS_FENCE(); bar(); LDS_FENCE();
    SB();
    if (WV) WAITV(0);
    STAMP(2);
  }
  if (isy) {
    lds1_t po = (lane & 8) ? (lds1_t)&s_ml2[w * 2 + 1 + (lane == 8 ? 0 : 8)] : (lds1_t)&s_o[w][(lane & 7) * 4 + (lane >> 5) * 2 + ((lane >> 4) & 1)];
    lds1_t pq = (lds1_t)&s_ml2[w * 2];
    asm volatile("" : "+v"(po), "+v"(pq));
    if (G1) until(G1 + __builtin_amdgcn_readlane(tok, C_LEAD));
    STAMP(14);
    const v4u dr = poll<PD1, PG1, PS1>(rw4, pa, old);
    STAMP(15);
    const v4u d = dr & (v4u){~0x100u, ~0x100u, ~0x100u, ~1u};
    const float ot = dpp<XOR1>(asf(d[3]));
    const float m = (lane & 1) ? ot : asf(d[3]), l = (lane & 1) ? asf(d[3]) : ot;
    float M = fmaxf(m, dpp<ROR8>(m)); M = x16_max(M); M = x32_max(M);
    *pq = M;
    const float e = __builtin_amdgcn_exp2f((m - M) * EXP_C);
    const float v3 = asf(__builtin_amdgcn_perm(__builtin_amdgcn_perm(d[2], d[1], 0x04000c0cu), d[0], 0x0706000cu));
    float y0 = tr32(asf(d[0]) * e, asf(d[2]) * e), y1 = tr32(asf(d[1]) * e, v3 * e), L = x32_add(l * e);
    y0 = tr16(y0, y1); L = x16_add(L);
    const bool s_ = lane & 8;
    *po = (s_ ? L : y0) + dpp<ROR8>(s_ ? y0 : L);
    LDS_FENCE(); bar(); LDS_FENCE();
    STAMP(3);
    poll_end(dr);
    STAMP(4);
    STAMP_OUT();
    __builtin_amdgcn_endpgm();
  }
  if (isr) {
    const int col = (ro >> 1) * 32 + (ro & 1) * 4 + (lane & 3) * 8 + ((lane >> 2) & 3);
    float xo = asf((unsigned)xg[col] << 16);
    lds1_t po = (lds1_t)&s_r[(lane & 12) ? 8 : w][(lane & 3) * 4 + (lane >> 5) * 2 + ((lane >> 4) & 1)], pq = (lds1_t)&s_r[lane >> 4][lane & 15];
    const rsrc_t ro_ = mkrs(out, D * 2);
    unsigned oo = lane < 16 ? col * 2 : OOB;
    asm volatile("" : "+v"(xo), "+v"(po), "+v"(pq), "+v"(oo));
    if (G2) until(G2 + __builtin_amdgcn_readlane(tok, C_LEAD));
    const v4u dr = poll<PD2, PG2, PS2>(rw4, pa, old);
    const v4u d = dr & ~1u;
    float y0 = tr32(asf(d[0]), asf(d[2])), y1 = tr32(asf(d[1]), asf(d[3]));
    y0 = tr16(y0, y1);
    y0 += dpp<ROR8>(y0); y0 += dpp<ROR4>(y0);
    *po = y0;
    LDS_FENCE(); bar(); LDS_FENCE();
    if (w == 0) {
      float a = pq[0] + pq[64];
      a = x16_add(a); a = x32_add(a);
      __builtin_amdgcn_raw_buffer_store_b16((short)f2bf(xo + a), ro_, (int)oo, 0, SO);
    }
    STAMP(4);
    poll_end(dr);
  }
  if (!isr) STAMP(4);
  STAMP_OUT();
}

extern "C" void kernel_launch(void* const* d_in, const int* in_sizes, int n_in,
                              void* d_out, int out_size, void* d_ws, size_t ws_size,
                              hipStream_t stream) {
  fused_kernel<<<NWG, NT, 0, stream>>>((const u16*)d_in[0], (const u16*)d_in[1], (const u16*)d_in[2], (const u16*)d_in[3],
                                       (const u16*)d_in[4], (const u16*)d_in[6], (const u16*)d_in[7], (const u16*)d_in[8],
                                       (u16*)d_out, (char*)d_ws);
}
```

```cpp
#include <hip/hip_runtime.h>
#include <hip/hip_bf16.h>
#include <stdint.h>

#define QPOS 24
#define PACE 8
#define QTRIES 8
#define TQ 123
#define TF 131
#define TFY 129
#define TY 129
#define NP 4
#define TAU 3640
#define PP 3000
#define PL 8
#define TOFF 150
#define TOFFQ 180
#define QBURST 100000
#define PBURST 4000
#define KBURST 100000
#define VBURST 100000
#define QFIRST 24
#define OPOS 8
#define TWD 0
#define OA 16
#define OB 16
#define S1 16
#define S2 16
#define G1 3835
#define G2 3955
#define EO 3
#define PD1 2
#define PG1 6
#define PD2 2
#define PG2 6
#define PD3 2
#define PG3 6
#define G3 3845
#define SR 4
#define PS1 0
#define PS2 0
#define SO 0
#define DK 57
#define DS 0
#define DG 4
#define DB 0
#define SQ 0
#define SF_ 0
#define SFY 0
#define SY 0
#define XA 4
#define XB 4
#define TX 0
#define NOHS 0
#define ES 1
#define EL 8
#define SVM 0
#define SVL 0
#define FO 0
#define FP 1
#define NT3 1
#define UF 1
#define WV 1
#define FX 0
#define PM 5
#define FK 0
#define YB 1
#define FD 0
#define YD 0
#define FB 1
#define XW 0
#define DIAG 0
#define LITE 0

typedef unsigned v4u __attribute__((ext_vector_type(4)));
typedef unsigned v2u __attribute__((ext_vector_type(2)));
typedef float f32x4 __attribute__((ext_vector_type(4)));
typedef float f32x2 __attribute__((ext_vector_type(2)));
typedef __bf16 bf16x2 __attribute__((ext_vector_type(2)));
typedef __bf16 bf16x8 __attribute__((ext_vector_type(8)));
typedef unsigned short u16;
typedef __amdgpu_buffer_rsrc_t rsrc_t;
typedef __attribute__((address_space(3))) f32x4* lds4_t;
typedef __attribute__((address_space(3))) v4u* ldsu_t;
typedef __attribute__((address_space(3))) float* lds1_t;

constexpr int D = 448, SK = 32768, DH = 256, F = 1792, NQ = 4096;
constexpr int NWG = 256, NT = 512;
constexpr unsigned OOB = 0x80000000u;
constexpr unsigned SENT = 0xFFFFFFFFu;
constexpr unsigned AREA = 9216;
constexpr unsigned A_O1 = 1024;
constexpr unsigned A_F = 5120;
constexpr unsigned A_Y2 = 1024;
constexpr unsigned WS_REQ = NWG * AREA;
constexpr unsigned WS_ECHO = WS_REQ + 1024;
constexpr unsigned WS_GO = WS_ECHO + 1024;
constexpr unsigned WS_T = WS_GO + 256;
constexpr unsigned WS_Q = WS_T + 256;
constexpr unsigned WS_END = WS_Q + 8 * 2048;
constexpr int SPIN_CAP = 1 << 18;
constexpr int C_NEXT = 0, C_TICK = 1, C_GO = 2, C_Q = 3, C_T0 = 4, C_FC = 5, C_OLD = 6, C_DONE = 7, C_LEAD = 8;

__device__ __forceinline__ float asf(unsigned u) { return __builtin_bit_cast(float, u); }
__device__ __forceinline__ unsigned asu(float f) { return __builtin_bit_cast(unsigned, f); }
__device__ __forceinline__ float rbf(float f) { __bf16 b = (__bf16)f; return (float)b; }
__device__ __forceinline__ unsigned f2bf(float f) { __bf16 b = (__bf16)f; return (unsigned)__builtin_bit_cast(u16, b); }
__device__ __forceinline__ float dot2(unsigned k, unsigned q, float acc) {
  return __builtin_amdgcn_fdot2_f32_bf16(__builtin_bit_cast(bf16x2, k), __builtin_bit_cast(bf16x2, q), acc, false);
}
__device__ __forceinline__ f32x2 unp(unsigned u) { f32x2 r; r.x = asf(u << 16); r.y = asf(u & 0xffff0000u); return r; }
template <int CTRL> __device__ __forceinline__ float dpp(float x) {
  return asf((unsigned)__builtin_amdgcn_update_dpp(0, (int)asu(x), CTRL, 0xf, 0xf, true));
}
constexpr int XOR1 = 0xB1, XOR2 = 0x4E, XOR7 = 0x141, ROR8 = 0x128, ROR4 = 0x124, ROR2 = 0x122, BCAST = 0x150;
constexpr int QP_EVEN = 0xA0, QP_ODD = 0xF5;
__device__ __forceinline__ float x16_add(float x) { auto s = __builtin_amdgcn_permlane16_swap(asu(x), asu(x), false, false); return asf(s[0]) + asf(s[1]); }
__device__ __forceinline__ float x32_add(float x) { auto s = __builtin_amdgcn_permlane32_swap(asu(x), asu(x), false, false); return asf(s[0]) + asf(s[1]); }
__device__ __forceinline__ float x16_max(float x) { auto s = __builtin_amdgcn_permlane16_swap(asu(x), asu(x), false, false); return fmaxf(asf(s[0]), asf(s[1])); }
__device__ __forceinline__ float x32_max(float x) { auto s = __builtin_amdgcn_permlane32_swap(asu(x), asu(x), false, false); return fmaxf(asf(s[0]), asf(s[1])); }
__device__ __forceinline__ float tr32(float a, float b) { auto s = __builtin_amdgcn_permlane32_swap(asu(a), asu(b), false, false); return asf(s[0]) + asf(s[1]); }
__device__ __forceinline__ float tr16(float a, float b) { auto s = __builtin_amdgcn_permlane16_swap(asu(a), asu(b), false, false); return asf(s[0]) + asf(s[1]); }
__device__ __forceinline__ float fmx(float a, float b) { return __builtin_elementwise_maximum(a, b); }
__device__ __forceinline__ float x16_mx(float x) { auto s = __builtin_amdgcn_permlane16_swap(asu(x), asu(x), false, false); return fmx(asf(s[0]), asf(s[1])); }
__device__ __forceinline__ float x32_mx(float x) { auto s = __builtin_amdgcn_permlane32_swap(asu(x), asu(x), false, false); return fmx(asf(s[0]), asf(s[1])); }
__device__ __forceinline__ float wave_sum(float v) {
  v += dpp<XOR1>(v); v += dpp<XOR2>(v); v += dpp<XOR7>(v); v += dpp<ROR8>(v);
  v = x16_add(v); return x32_add(v);
}
__device__ __forceinline__ float wave_max(float v) {
  v = fmaxf(v, dpp<XOR1>(v)); v = fmaxf(v, dpp<XOR2>(v)); v = fmaxf(v, dpp<XOR7>(v)); v = fmaxf(v, dpp<ROR8>(v));
  v = x16_max(v); return x32_max(v);
}
__device__ __forceinline__ rsrc_t mkrs(const void* p, unsigned bytes) { return __builtin_amdgcn_make_buffer_rsrc((void*)p, 0, (int)bytes, 0x00020000); }
__device__ __forceinline__ rsrc_t selr(bool c, rsrc_t a, rsrc_t b) { return c ? a : b; }
__device__ __forceinline__ unsigned selu(bool c, unsigned a, unsigned b) { return c ? a : b; }
template <int AUX> __device__ __forceinline__ v4u ld4(rsrc_t r, unsigned vo, unsigned so) { return __builtin_amdgcn_raw_buffer_load_b128(r, (int)vo, (int)so, AUX); }
template <int AUX> __device__ __forceinline__ v2u ld2(rsrc_t r, unsigned vo, unsigned so) { return __builtin_amdgcn_raw_buffer_load_b64(r, (int)vo, (int)so, AUX); }
__device__ __forceinline__ void st4c(v4u v, rsrc_t r, unsigned vo) { __builtin_amdgcn_raw_buffer_store_b128(v, r, (int)vo, 0, 16); }
__device__ __forceinline__ void st1c(unsigned v, rsrc_t r, unsigned vo) { __builtin_amdgcn_raw_buffer_store_b32(v, r, (int)vo, 0, 16); }
template <unsigned M> __device__ __forceinline__ unsigned tag(unsigned u, unsigned t) { return (u & ~M) | (t & M); }
__device__ __forceinline__ v4u tag4(const f32x4& v, const v4u& t) { return (v4u){tag<1>(asu(v[0]), t[0]), tag<1>(asu(v[1]), t[1]), tag<1>(asu(v[2]), t[2]), tag<1>(asu(v[3]), t[3])}; }
__device__ __forceinline__ bool arrived(const v4u& v, const v4u& o) { return __builtin_amdgcn_ballot_w64(v[0] == o[0] || v[1] == o[1] || v[2] == o[2] || v[3] == o[3]) == 0; }
__device__ __forceinline__ unsigned lowb(const v4u& v) { return __builtin_amdgcn_perm(v[1], v[0], 0x0c0c0400u) | __builtin_amdgcn_perm(v[3], v[2], 0x04000c0cu); }
#define SB() __builtin_amdgcn_sched_barrier(0)
__device__ __forceinline__ void bar() { __builtin_amdgcn_s_waitcnt(0xC07F); __builtin_amdgcn_s_barrier(); }
#define WAITV(n) __builtin_amdgcn_s_waitcnt(((n) & 15) | (((n) >> 4) << 14) | 0x0F70)
template <int V> struct IC { static constexpr int value = V; constexpr operator int() const { return V; } };
template <int A, int B, class Fn> __device__ __forceinline__ void static_for(Fn&& f) { if constexpr (A < B) { f(IC<A>{}); static_for<A + 1, B>(f); } }
#define LDS_FENCE() asm volatile("" ::: "memory")

constexpr float EXP_C = 0.0625f * 1.4426950408889634f;

struct Att {
  float m, l;
  f32x4 a[2];
};
__device__ __forceinline__ f32x4 mfma(const v4u& a, const v4u& b, const f32x4& c) {
  return __builtin_amdgcn_mfma_f32_16x16x32_bf16(__builtin_bit_cast(bf16x8, a), __builtin_bit_cast(bf16x8, b), c, 0, 0, 0);
}
__device__ __forceinline__ float red_max(float v) { v = fmaxf(v, dpp<XOR1>(v)); v = fmaxf(v, dpp<XOR2>(v)); return x32_max(x16_max(v)); }

template <int NL, class ISSUE>
__device__ __forceinline__ float scores(v4u (&kb)[8], const v4u (&qb)[8], int lane, ISSUE issue) {
  f32x4 d = {0.f, 0.f, 0.f, 0.f};
#pragma unroll
  for (int i = 0; i < NL; ++i) {
    d = mfma(kb[i], qb[i], d);
    asm volatile("" : "+v"(d));
    issue(i);
    SB();
  }
  const bool odd = lane & 1;
  return rbf((odd ? d[3] : d[0]) + dpp<XOR1>(odd ? d[1] : d[2]));
}
template <class ISSUE>
__device__ __forceinline__ float phase_k4(v4u (&kb)[8], const v4u (&qb)[8], Att& st, int lane, ISSUE issue) {
  float s = scores<4>(kb, qb, lane, issue);
  s = (lane & 2) ? -__builtin_inff() : s;
  const float mn = fmaxf(st.m, red_max(s));
  const float al = __builtin_amdgcn_exp2f((st.m - mn) * EXP_C);
  const float p = __builtin_amdgcn_exp2f((s - mn) * EXP_C);
  st.m = mn; st.l = st.l * al + p;
  st.a[0] *= al; st.a[1] *= al;
  SB();
  return p;
}
template <int I, int O, class ISSUE>
__device__ __forceinline__ void pv_step(v4u (&vb)[8], unsigned pp, const v4u& pm, Att& st, ISSUE& issue) {
  const v4u pb = pm & (unsigned)__builtin_amdgcn_update_dpp(0, (int)pp, I * 0x55, 0xf, 0xf, true);
  st.a[0] = mfma(vb[O + 2 * I], pb, st.a[0]);
  asm volatile("" : "+v"(st.a[0]));
  issue(O + 2 * I);
  SB();
  st.a[1] = mfma(vb[O + 2 * I + 1], pb, st.a[1]);
  asm volatile("" : "+v"(st.a[1]));
  issue(O + 2 * I + 1);
  SB();
}
template <int NL, class ISSUE>
__device__ __forceinline__ void phase_v(v4u (&vb)[8], float p, const v4u& pm, Att& st, ISSUE issue) {
  constexpr int O = 8 - NL;
  const unsigned pp = __builtin_bit_cast(unsigned, (bf16x2){(__bf16)p, (__bf16)p});
  pv_step<0, O>(vb, pp, pm, st, issue); pv_step<1, O>(vb, pp, pm, st, issue);
  if constexpr (NL == 8) { pv_step<2, O>(vb, pp, pm, st, issue); pv_step<3, O>(vb, pp, pm, st, issue); }
}

template <int N, int GAP, int SL>
__device__ __forceinline__ v4u poll(const v4u& r, unsigned pa, const v4u& old) {
  v4u d;
  unsigned long long t0, t1, t2;
  int n = SPIN_CAP;
  asm volatile(
      "s_nop 4\n\t"
      "buffer_load_dwordx4 v[200:203], %5, %6, 0 offen sc1\n\t"
      ".rept %11\n\t"
      "s_sleep %12\n\t"
      "buffer_load_dwordx4 v[200:203], %5, %6, 0 offen sc1\n\t"
      ".endr\n"
      "1:\n\t"
      "s_waitcnt vmcnt(%11)\n\t"
      "v_cmp_eq_u32 vcc, v200, %7\n\t"
      "v_cmp_eq_u32 %1, v201, %8\n\t"
      "v_cmp_eq_u32 %2, v202, %9\n\t"
      "v_cmp_eq_u32 %3, v203, %10\n\t"
      "s_or_b64 vcc, vcc, %1\n\t"
      "s_or_b64 %2, %2, %3\n\t"
      "s_or_b64 exec, vcc, %2\n\t"
      "s_cbranch_scc0 2f\n\t"
      ".if %13\n\ts_sleep %13\n\t.endif\n\t"
      "buffer_load_dwordx4 v[200:203], %5, %6, 0 offen sc1\n\t"
      "s_sub_u32 %4, %4, 1\n\t"
      "s_cbranch_scc0 1b\n"
      "2:\n\t"
      "s_mov_b64 exec, -1"
      : "=&{v[200:203]}"(d), "=&s"(t0), "=&s"(t1), "=&s"(t2), "+s"(n)
      : "v"(pa), "s"(r), "v"(old[0]), "v"(old[1]), "v"(old[2]), "v"(old[3]), "n"(N - 1), "n"(GAP), "n"(SL)
      : "vcc", "scc", "memory");
  return d;
}
__device__ __forceinline__ void poll_end(const v4u& d) { asm volatile("s_waitcnt vmcnt(0)" :: "{v[200:203]}"(d)); }

#define STAMP(k) do {} while (0)
#define STAMP_OUT() do {} while (0)

__device__ __forceinline__ f32x2 ln_stats(const v4u& xa, int lane) {
  f32x2 xv[4];
  float sm = 0.f;
#pragma unroll
  for (int j = 0; j < 4; ++j) { xv[j] = unp(xa[j]); sm += xv[j].x + xv[j].y; }
  const float mean = wave_sum(sm) * (1.0f / D);
  float sq = 0.f;
#pragma unroll
  for (int j = 0; j < 4; ++j) { const f32x2 d = xv[j] - mean; sq += d.x * d.x + d.y * d.y; }
  sq = lane < 56 ? sq : 0.f;
  const float var = rbf(wave_sum(sq) * (1.0f / D));
  const float sd = rbf(sqrtf(rbf(var + 1e-5f)));
  return (f32x2){rbf(mean), 1.0f / sd};
}
__device__ __forceinline__ float ln_apply(float x, f32x2 st) { return rbf(rbf(x - st.x) * st.y); }

__global__ void __launch_bounds__(NT, 2)
fused_kernel(const u16* __restrict__ xg, const u16* __restrict__ kc, const u16* __restrict__ vc, const u16* __restrict__ wq,
             const u16* __restrict__ wo, const u16* __restrict__ wg, const u16* __restrict__ wu, const u16* __restrict__ wd,
             u16* __restrict__ out, char* ws) {
  __shared__ __attribute__((aligned(16))) float s_part[8][512];
  __shared__ __attribute__((aligned(16))) float s_mv[8][64];
  __shared__ __attribute__((aligned(16))) unsigned s_q[256];
  __shared__ __attribute__((aligned(16))) unsigned s_qs[64];
  __shared__ __attribute__((aligned(16))) unsigned s_old[8][256];
  __shared__ __attribute__((aligned(16))) unsigned s_dst[8][256];
  __shared__ __attribute__((aligned(16))) float s_ml[2][16];
  __shared__ __attribute__((aligned(16))) float s_o[4][32];
  __shared__ __attribute__((aligned(16))) float s_ml2[16];
  __shared__ __attribute__((aligned(16))) unsigned s_xn[256];
  __shared__ __attribute__((aligned(16))) float s_h[32];
  __shared__ __attribute__((aligned(16))) float s_r[9][16];
  __shared__ __attribute__((aligned(16))) float s_sc[64][32];
  __shared__ int s_ctl[64];
  const int tid = threadIdx.x, lane = tid & 63, w = __builtin_amdgcn_readfirstlane(tid >> 6);
  const int bi = blockIdx.x, h = (((bi & 1) ? bi + 5 : bi) & 6) | (bi >> 7), s = ((bi >> 3) & 15) * 2 + (bi & 1);
  const int b = h * 32 + s;
  static_assert(8 * TQ + 8 * TF + 6 * TFY + 10 * TY == 4096 && TQ - NP > 80 && TF - NP > 80 && TFY - NP > 80 && TY - NP > 80, "tickets");
  static_assert(TQ - NP <= 128 && TF - NP <= 128 && TFY - NP <= 128 && TY - NP <= 128, "tickets");
  const int nt = s < 8 ? TQ : s < 16 ? TF : s < 22 ? TFY : TY, nk = (nt - NP) >> 1;
  const int first = s * TY - min(s, 22) * (TY - TFY) - min(s, 16) * (TFY - TF) - min(s, 8) * (TF - TQ);
  asm volatile("" :: "s"(xg), "s"(kc), "s"(vc), "s"(wq), "s"(wo), "s"(wg), "s"(wu), "s"(wd), "s"(out), "s"(ws));
  STAMP(0);
  const rsrc_t rx = mkrs(xg, D * 2);
  const rsrc_t rk = mkrs(kc + (size_t)h * SK * DH, SK * DH * 2);
  const rsrc_t rv = mkrs(vc + (size_t)h * SK * DH, SK * DH * 2);
  const rsrc_t rws = mkrs(ws, WS_END);
  const unsigned l16 = lane * 16;
  auto ubase = [&](int u) -> unsigned { return (unsigned)(first + 2 * u) * 4096u; };
  const unsigned kvo = ((lane >> 1) & 7) * 512 + (lane >> 4) * 32 + (lane & 1) * 16, vvo = (lane >> 4) * 1024 + (lane & 15) * 16;
  auto ldk = [&](unsigned base, int i) { return ld4<2>(rk, kvo + (i & 3) * 128, base + (i >> 2) * 4096); };
  const unsigned area = (unsigned)b * AREA;
  auto area_of = [&](int ss) -> unsigned { return (unsigned)(h * 32 + ss) * AREA; };
  const unsigned lmask = lane < 56 ? 0u : OOB;

  const bool isq = s < 8, isf = s < 14, isy = s >= 16;
  const int f = h * 14 + s, y = h * 16 + s - 16, ro = h * 4 + (s >> 2);
  const bool isr = s < 16 && ((s + h) & 3) == 0 && ro < 28;
  const bool tile = isq;
  const bool leader = (!(NOHS & 1) && b == NWG - 1 && w == 1), member = (w == 0);

  const unsigned pa = isy ? (w < 4 ? area + A_O1 + w * 1024 + l16 : (w == 7 && y < 112) ? area + A_F + l16 : OOB) : isr ? area + A_Y2 + w * 1024 + l16 : OOB;
  const unsigned mo_ = area_of(16 + (lane & 8) + (lane >> 4)) + A_O1 + s * 128 + (lane & 7) * 16;
  auto mf = [&](int p) -> unsigned {
    const int fd = (f & ~3) + ((p >> 2) & 3);
    return (unsigned)((fd >> 4) * 32 + 16 + (fd & 15)) * AREA + A_F + (f & 3) * 256 + (p >> 4) * 64 + (p & 3) * 16;
  };
  auto my = [&](int p) -> unsigned { return (unsigned)((p >> 4) * 32 + (p & 12) + ((-(p >> 4)) & 3)) * AREA + A_Y2 + y * 64 + (p & 3) * 16; };
  v4u eo, ed;
  auto da_of = [&]() {
    const int p = (w - 2) * 64 + lane;
    unsigned da = (isy && p < 112) ? my(p) : OOB;
    return (w & 6) == 2 ? da : (w & 6) == 4 ? mo_ + (w - 4) * 4 * AREA : (w == 6 && isf) ? mf(lane) : OOB;
  };
  if (EO == 1) { eo = ld4<OA>(rws, pa, 0); SB(); }
  if (EO == 3 || (EO == 4 && !tile)) { eo = ld4<OA>(rws, pa, 0); ed = ld4<OB>(rws, da_of(), 0); SB(); }
  if (EO == 5) { ed = ld4<OB>(rws, da_of(), 0); SB(); }
  const v4u sent = {SENT, SENT, SENT, SENT};
  unsigned poll_off = lane == 1 ? WS_GO : lane < 4 ? WS_T - 32 + l16 : OOB;
  poll_off = lane == 0 ? WS_REQ + (b >> 2) * 16 : poll_off;
  poll_off = leader ? (ES ? lane * 4 * AREA + (lane & 3) * 256 : WS_ECHO + l16) : poll_off;
  auto echoes = [&](unsigned seen) {
    v4u r;
#pragma unroll
    for (int j = 0; j < 4; ++j) r[j] = __builtin_amdgcn_raw_buffer_load_b32(rws, (int)(poll_off | (seen >> j << 31)), j * AREA, 16);
    return r;
  };
  v4u pv = {0u, 0u, 0u, 0u}, e1 = {0u, 0u, 0u, 0u};
  unsigned go_ref = 0;
  int* const ctl = &s_ctl[lane];
  auto ctl_get = [&](int i) { return __builtin_amdgcn_readfirstlane(__hip_atomic_load(&s_ctl[i], __ATOMIC_RELAXED, __HIP_MEMORY_SCOPE_WORKGROUP)); };
  auto ctl_set = [&](int i) { __hip_atomic_store(&s_ctl[i], 1, __ATOMIC_RELAXED, __HIP_MEMORY_SCOPE_WORKGROUP); };
  const unsigned qoff = WS_Q + h * 2048 + l16;
  v4u qv0, qv1;
  if (member) {
    qv0 = ld4<16>(rws, qoff, 0);
    qv1 = ld4<16>(rws, qoff, 1024);
    SB();
    int c = lane == C_NEXT ? (isq ? TOFFQ : TOFF) * 128 : 0;
    c = lane == C_TICK ? 40 : c;
    c = lane == C_T0 ? (int)(__builtin_amdgcn_s_memrealtime() & 0x7fffffff) : c;
    __hip_atomic_store(ctl, c, __ATOMIC_RELAXED, __HIP_MEMORY_SCOPE_WORKGROUP);
    st1c(c, rws, (lane == C_T0 && (bi >> 3) == 12) ? WS_T + (bi & 7) * 4 : OOB);
  }
  *reinterpret_cast<v4u*>(&s_sc[w * 8][lane * 4]) = sent;
  int t0 = 0;
  auto now = [&]() __attribute__((always_inline)) { return ((int)__builtin_amdgcn_s_memrealtime() - t0) & 0x7fffff; };
  const int neg = lane == C_NEXT ? 0 : (int)0x80000000;
  const int xcc = __builtin_amdgcn_s_getreg(20 | (3 << 11));
  constexpr auto tkf = [](int t, int sh) { return TAU + sh * 128 / ((t - NP) / 2 - 8); };
  constexpr auto tvf = [](int t, int sh) { return TAU - sh * 128 / ((t - NP) / 2 + 8 - PL); };
  int tk = s < 8 ? tkf(TQ + 16, SQ) : s < 16 ? tkf(TF, SF_) : s < 22 ? tkf(TFY, SFY) : tkf(TY, SY);
  int tv = s < 8 ? tvf(TQ, SQ) : s < 16 ? tvf(TF, SF_) : s < 22 ? tvf(TFY, SFY) : tvf(TY, SY);
  int inc = lane == C_NEXT ? tk : 0;
  auto token = [&](int burst) __attribute__((always_inline)) {
    const int t = max(now() * 128 - burst, 0);
    __hip_atomic_fetch_max(ctl, t | neg, __ATOMIC_RELAXED, __HIP_MEMORY_SCOPE_WORKGROUP);
    return __hip_atomic_fetch_add(ctl, inc, __ATOMIC_RELAXED, __HIP_MEMORY_SCOPE_WORKGROUP);
  };
  auto due_of = [&](int x) { return __builtin_amdgcn_readlane(x, C_NEXT) >> 7; };
  auto until = [&](int due) __attribute__((always_inline)) {
#pragma unroll 1
    for (int it = 0; it < 4096 && now() < due; ++it) __builtin_amdgcn_s_sleep(1);
    SB();
  };
  unsigned qs0, qs1;
  auto q_there = [&]() { return __builtin_amdgcn_ballot_w64(((lowb(qv0) ^ qs0) & (lowb(qv1) ^ qs1)) != 0xFFFFFFFFu) == 0; };
  auto load_q = [&]() { qv0 = ld4<16>(rws, qoff, 0); qv1 = ld4<16>(rws, qoff, 1024); };
  auto publish_q = [&]() {
    *reinterpret_cast<v2u*>(&s_q[lane * 2]) = (v2u){__builtin_amdgcn_perm(qv0[1], qv0[0], 0x07060302u), __builtin_amdgcn_perm(qv0[3], qv0[2], 0x07060302u)};
    *reinterpret_cast<v2u*>(&s_q[128 + lane * 2]) = (v2u){__builtin_amdgcn_perm(qv1[1], qv1[0], 0x07060302u), __builtin_amdgcn_perm(qv1[3], qv1[2], 0x07060302u)};
    LDS_FENCE();
    __builtin_amdgcn_s_waitcnt(0xC07F);
    ctl_set(C_Q);
  };
  v4u pre[4][8], ring[8];
  auto unit0 = [&]() {
#pragma unroll
    for (int i = 0; i < 8; ++i) { pre[0][i] = ldk(ubase(w), i); SB(); }
    if (EO == 2) { eo = ld4<OA>(rws, pa, 0); SB(); }
  };
  SB();
  if (tile) {
    v4u tw[8];
    const int oct = 7 * w;
    constexpr int stride = NQ * 2;
    const rsrc_t rt = mkrs(wq, D * stride);
    const unsigned to = (unsigned)((oct + (lane >> 3)) * 8 * stride + (h * 8 + s) * 128 + (lane & 7) * 16) | lmask;
    const v4u xa = ld4<0>(rx, l16, 0);
    const v4u xb = ld4<0>(rx, (unsigned)(oct + (lane >> 3)) * 16 | lmask, 0);
    SB();
#pragma unroll
    for (int i = 0; i < 8; ++i) { tw[i] = ld4<2>(rt, to, i * stride); SB(); }
    if (EO == 4) { eo = ld4<OA>(rws, pa, 0); ed = ld4<OB>(rws, da_of(), 0); SB(); }
    LDS_FENCE(); bar(); LDS_FENCE();
    t0 = ctl_get(C_T0);
    SB();
    const f32x2 ls = ln_stats(xa, lane);
    f32x2 acc[4];
#pragma unroll
    for (int i = 0; i < 8; ++i) {
      const float xn = ln_apply(asf((i & 1) ? (xb[i >> 1] & 0xffff0000u) : (xb[i >> 1] << 16)), ls);
#pragma unroll
      for (int j = 0; j < 4; ++j) acc[j] = i ? acc[j] + unp(tw[i][j]) * xn : unp(tw[i][j]) * xn;
    }
    float y0 = tr32(acc[0].x, acc[2].x), y1 = tr32(acc[0].y, acc[2].y), y2 = tr32(acc[1].x, acc[3].x), y3 = tr32(acc[1].y, acc[3].y);
    y0 = tr16(y0, y2); y1 = tr16(y1, y3);
    const bool s_ = lane & 8;
    y0 = (s_ ? y1 : y0) + dpp<ROR8>(s_ ? y0 : y1);
    s_mv[w][(lane & 7) * 8 + (lane >> 3)] = y0;
    STAMP(6);
    token(QBURST);
    unit0();
    LDS_FENCE(); bar(); LDS_FENCE();
    if (member) {
      float a = 0.f;
#pragma unroll
      for (int k = 0; k < 8; ++k) a += s_mv[k][lane];
      s_qs[lane] = f2bf(a) << 16;
      LDS_FENCE();
      const v4u qs = *reinterpret_cast<const v4u*>(&s_qs[(lane & 15) * 4]) | (~((s & 4) ? qv1 : qv0) & 0xffffu);
      st4c(qs, rws, (lane >> 4) == (s & 3) ? qoff + (s >> 2) * 1024 : OOB);
    }
  } else if (leader) {
    e1 = ES ? echoes(0) : ld4<16>(rws, poll_off, 0);
    go_ref = __builtin_amdgcn_raw_buffer_load_b32(rws, (int)WS_GO, 0, 16);
  }
  SB();
  STAMP(5);
  SB();
  const u16* wa = isy ? wo + (size_t)y * 32 * D : wg;
  const u16* wb = isy ? wd + (size_t)(y < 112 ? (y >> 1) * 32 + (y & 1) * 4 : 0) * D : wu;
  asm volatile("" : "+s"(wa), "+s"(wb));
  bool ok = !member;
  static_for<0, 40>([&](auto N) {
    constexpr int n = N;
    if constexpr (n == 0) {
      if (!tile) unit0();
    }
    if constexpr (n == 8) {
      if (!tile) {
        LDS_FENCE(); bar(); LDS_FENCE();
        t0 = ctl_get(C_T0);
        if (leader) {
          e1 += 1u;
          go_ref += 1u;
          st4c(e1, rws, WS_REQ + l16);
        }
      }
    }
    if constexpr (n > 0 && n % 8 == 0) {
      const int x = token(isq ? QBURST : PBURST);
      if constexpr (n > QFIRST) {
        if (!ok) {
          ok = q_there();
          if (ok) publish_q();
        }
      }
      if constexpr (n == OPOS + 16) {
        *reinterpret_cast<v4u*>(&s_old[w][lane * 4]) = (int)pa < 0 ? sent : eo;
        *reinterpret_cast<v4u*>(&s_dst[w][lane * 4]) = ~ed;
        LDS_FENCE();
        __hip_atomic_fetch_add(ctl, lane == C_OLD ? 1 : 0, __ATOMIC_RELAXED, __HIP_MEMORY_SCOPE_WORKGROUP);
      }
      SB();
      until(due_of(x));
      if constexpr (n == 8) {
        if (member) { qs0 = lowb(qv0); qs1 = lowb(qv1); }
      }
      if constexpr (n >= QFIRST) {
        if (!ok) load_q();
      }
      if constexpr (n == OPOS) {
        if (EO == 0 || EO == 5) eo = ld4<OA>(rws, pa, 0);
        if (EO < 3) ed = ld4<OB>(rws, da_of(), 0);
      }
      SB();
    }
    if constexpr (n == QPOS) {
      STAMP(9);
      if (ES && leader) pv = echoes(0);
      else pv = ld4<16>(rws, (member || leader) ? poll_off : OOB, 0);
      SB();
    }
    if constexpr (n >= 8) {
      if constexpr (n >= PACE) WAITV(PACE + ((n >= QPOS && n < QPOS + PACE) ? 1 : 0) + ((n >= OPOS && n < OPOS + PACE) ? (EO == 0 ? 2 : (EO == 3 || EO == 4) ? 0 : 1) : 0));
      if constexpr (n < 32) pre[n >> 3][n & 7] = ldk(ubase((n & 56) + w), n & 7); else ring[n & 7] = ldk(ubase(32 + w), n & 7);
    }
    SB();
    if constexpr (n - PACE == 7 || n - PACE == 15 || n - PACE == 23 || n - PACE == 31) STAMP(16 + (n - PACE) / 8);
  });
  inc = lane == C_TICK ? 1 : inc;
  int tok = token(KBURST);
  STAMP(10);
  bool svc = member || leader;
  unsigned seenm = 0, last_echo = 0;
  bool echoed = false;
  auto service = [&]() {
    if (leader) {
      seenm |= (pv[0] == e1[0] ? 1u : 0u) | (pv[1] == e1[1] ? 2u : 0u) | (pv[2] == e1[2] ? 4u : 0u) | (pv[3] == e1[3] ? 8u : 0u);
      svc = __builtin_amdgcn_ballot_w64(seenm != 15u) != 0;
      st1c(go_ref, rws, (lane == 0 && !svc) ? WS_GO : OOB);
    } else {
      const unsigned req = (unsigned)__builtin_amdgcn_readfirstlane((int)((b & 2) ? ((b & 1) ? pv[3] : pv[2]) : ((b & 1) ? pv[1] : pv[0])));
      const unsigned go = (unsigned)__builtin_amdgcn_readlane((int)pv[0], 1);
      go_ref = echoed ? go_ref : go;
      svc = go == (unsigned)__builtin_amdgcn_readfirstlane((int)go_ref);
      if (NOHS & 1) svc = false;
      const bool rdy = ctl_get(C_OLD) == 8;
      if (ES && EL > 0) st4c((v4u){req, req, req, req}, rws, (lane < EL && svc && rdy && !(echoed && req == last_echo)) ? area + ((b >> 2) & 3) * 256 + l16 : OOB);
      else st1c(req, rws, (lane == 0 && svc && rdy && !(echoed && req == last_echo)) ? (ES ? area + ((b >> 2) & 3) * 256 : WS_ECHO + b * 4) : OOB);
      echoed |= rdy; last_echo = req;
    }
    if (ES && leader) pv = echoes(seenm);
    else pv = ld4<16>(rws, svc ? poll_off : OOB, 0);
    SB();
  };

  {
    if (!ok) {
      ok = q_there();
      if (ok) publish_q();
    }
#pragma unroll 1
    for (int it = 0; it < SPIN_CAP && !ctl_get(C_Q); ++it) {
      if (svc && it >= QTRIES) { service(); asm volatile("" :: "v"(pv)); }
      if (!ok) {
        load_q();
        ok = q_there() | !svc;
        if (ok) publish_q();
      } else {
        __builtin_amdgcn_s_sleep(1);
      }
    }
  }
  LDS_FENCE();
  v4u qb[8];
#pragma unroll
  for (int i = 0; i < 4; ++i) {
    const v4u z = {0u, 0u, 0u, 0u}, q = *reinterpret_cast<const v4u*>(&s_q[(lane & 8) * 16 + i * 32 + (lane >> 4) * 8 + (lane & 1) * 4]);
    qb[i] = (lane & 2) ? z : q; qb[4 + i] = (lane & 2) ? q : z;
  }
  v4u pm;
#pragma unroll
  for (int j = 0; j < 4; ++j) pm[j] = ((lane >> 1) & 3) == j ? ((lane & 1) ? 0xffff0000u : 0xffffu) : 0u;
  STAMP(1);
  SB();

  Att st;
  int sx = 20;
  int rank;
  lds4_t pw;
  lds1_t pml;
  int idle;
  auto last = [&]() __attribute__((always_inline)) {
    pml = (lds1_t)&s_ml[(lane >> 3) & 1][rank];
    pw = (lds4_t)&s_part[rank][lane * 4];
    idle = __builtin_amdgcn_readfirstlane(!isy && !svc && rank >= PM && (unsigned)(rank - SR) >= 2 && (!isf || (unsigned)(rank - FO) >= 4));
    asm volatile("" : "+v"(pw), "+v"(pml), "+s"(idle));
    SB();
  };
  auto put_ml = [&]() __attribute__((always_inline)) {
    float l = st.l;
    l += dpp<XOR1>(l); l += dpp<XOR2>(l); l = x32_add(x16_add(l));
    pml[0] = st.m; pml[8] = l;
    SB();
  };
  {
    float* const sc = &s_sc[0][(lane >> 3) * 4 + (lane & 3)];
    rsrc_t nra, nrb;
    unsigned nva, nvb, nba, nbb, nsa, nsb;
    int kind = 0, nu = 32 + w, cu;
    auto next = [&]() __attribute__((always_inline)) {
      cu = nu;
      const int t = __builtin_amdgcn_readlane(tok, C_TICK);
      const bool fx = t < 2 * nk, tw = t >= nt;
      kind = fx ? (t >= nk ? 1 : 0) : tw ? 3 : 2;
      nu = t >= nk ? t - nk : t;
      nba = (unsigned)(fx ? first + 2 * nu : first + t) * 4096u;
      nbb = fx ? nba + 4096 : nba;
      nra = selr(kind == 1, rv, rk);
      nrb = selr(kind == 0, rk, rv);
      nva = selu(kind == 1, vvo, kvo); nvb = selu(kind == 0, kvo, vvo);
      nsa = selu(kind == 1, 256, 128); nsb = selu(kind == 0, 128, 256);
      const int u = t + 8 - nk;
      inc = lane == C_NEXT ? (t + PL >= 2 * nk ? PP : (int)selu(u >= 0, tv, tk) + ((unsigned)(u + XA) < XA + XB ? TX : 0)) : inc;
      rank = t - nt;
      if (!tw) until(due_of(tok));
      SB();
    };
    auto ldn = [&](int i) { ring[i] = i < 4 ? ld4<2>(nra, nva, nba + i * nsa) : ld4<2>(nrb, nvb, nbb + (i - 4) * nsb); };
    float mx;
    if (member) {
      const v4u d = pv - (unsigned)t0;
      const bool bad = max(max(d[0] + 512u, d[1] + 512u), max(d[2] + 512u, d[3] + 512u)) > 1024u;
      const int sm = (int)((d[0] + d[1]) + (d[2] + d[3]));
      const int lead = (__builtin_amdgcn_ballot_w64(bad) & 12) ? 0 : __builtin_amdgcn_readlane(sm, 2) + __builtin_amdgcn_readlane(sm, 3) + DB;
      tk += (lead * DK) >> 5; tv += (lead * DK) >> 5;
      __hip_atomic_fetch_add(ctl, lane == C_LEAD ? (lead * DG) >> 5 : (lane == C_NEXT ? (lead * DS) >> 5 : 0), __ATOMIC_RELAXED, __HIP_MEMORY_SCOPE_WORKGROUP);
    }
    if (svc) service();
    {
      const float s0 = scores<8>(pre[0], qb, lane, [&](int) {});
      const float s1 = scores<8>(pre[1], qb, lane, [&](int) {});
      const float s2 = scores<8>(pre[2], qb, lane, [&](int) {});
      const float s3 = scores<8>(pre[3], qb, lane, [&](int) {});
      sc[w * 32] = s0; sc[(8 + w) * 32] = s1; sc[(16 + w) * 32] = s2; sc[(24 + w) * 32] = s3;
      mx = fmaxf(fmaxf(s0, s1), fmaxf(s2, s3));
      SB();
      STAMP(11);
    }
    const v4u xa2 = ld4<0>(rx, (isf && w == 7) ? l16 : OOB, 0);
    SB();
#pragma unroll 1
    while (kind == 0) {
      next();
      const float sv = scores<8>(ring, qb, lane, ldn);
      sc[cu * 32] = sv;
      tok = token(KBURST);
      mx = fmaxf(mx, sv);
      STAMP(sx); ++sx;
      if (svc) service();
      SB();
    }
    STAMP(7);
    if (isf && w == 7) {
      const f32x2 ls = ln_stats(xa2, lane);
      v4u xn;
#pragma unroll
      for (int j = 0; j < 4; ++j) {
        const f32x2 v = unp(xa2[j]);
        xn[j] = __builtin_amdgcn_perm(asu(ln_apply(v.y, ls)), asu(ln_apply(v.x, ls)), 0x07060302u);
      }
      *reinterpret_cast<v4u*>(&s_xn[lane * 4]) = xn;
      LDS_FENCE();
      ctl_set(C_FC);
      SB();
    }
    st.m = red_max(mx); st.l = 0.f;
    st.a[0] = st.a[1] = (f32x4){0.f, 0.f, 0.f, 0.f};
#pragma unroll 1
    while (kind == 1) {
      float sv;
      for (;;) {
        sv = __hip_atomic_load(&sc[nu * 32], __ATOMIC_RELAXED, __HIP_MEMORY_SCOPE_WORKGROUP);
        if (__builtin_amdgcn_ballot_w64(asu(sv) == SENT) == 0) break;
        __builtin_amdgcn_s_sleep(1);
      }
      if (__builtin_amdgcn_ballot_w64(sv > st.m) != 0) {
        const float m2 = fmaxf(st.m, red_max(sv));
        const float al = __builtin_amdgcn_exp2f((st.m - m2) * EXP_C);
        st.m = m2; st.l *= al;
        st.a[0] *= al; st.a[1] *= al;
      }
      const float p = __builtin_amdgcn_exp2f((sv - st.m) * EXP_C);
      st.l += p;
      SB();
      next();
      if (kind == 3) {
        last();
        put_ml();
        phase_v<8>(ring, p, pm, st, [&](int) {});
        break;
      }
      phase_v<8>(ring, p, pm, st, ldn);
      tok = token(VBURST);
      STAMP(sx); ++sx;
      if (svc) service();
      SB();
    }
    STAMP(8);
    if (kind == 2) {
      next();
#pragma unroll 1
    while (kind != 3) {
      if (UF) { nba = __builtin_amdgcn_readfirstlane(nba); nbb = __builtin_amdgcn_readfirstlane(nbb); nsa = __builtin_amdgcn_readfirstlane(nsa); nsb = __builtin_amdgcn_readfirstlane(nsb); }
      const float p = phase_k4(ring, qb, st, lane, ldn);
      phase_v<4>(ring, p, pm, st, ldn);
      tok = token(VBURST);
      STAMP(sx); ++sx;
      SB();
      next();
    }
      SB();
      last();
      const float p = phase_k4(ring, qb, st, lane, [&](int) {});
      phase_v<4>(ring, p, pm, st, [&](int) {});
      put_ml();
    }
    pw[0] = st.a[0]; pw[64] = st.a[1];
    LDS_FENCE();
    SB();
    STAMP(12);
    if (idle) {
      LDS_FENCE(); bar(); LDS_FENCE();
      STAMP(2);
    }
    SB();
#pragma unroll 1
    for (int it = 0; it < SPIN_CAP && svc; ++it) {
      service();
      asm volatile("" :: "v"(pv));
    }
    LDS_FENCE();
    WAITV(0);
  }
  const lds4_t pr = (lds4_t)&s_part[0][lane * 4], pmr = (lds4_t)&s_ml[(lane >> 3) & 1][0];
  if (rank < PM) __hip_atomic_fetch_add(ctl, lane == C_DONE ? 1 : 0, __ATOMIC_RELAXED, __HIP_MEMORY_SCOPE_WORKGROUP);
  SB();
  const v4u rw4 = {(unsigned)(size_t)ws, (unsigned)((size_t)ws >> 32), WS_END, 0x00020000u};
  const unsigned fj = isf ? rank - FO : 4;
  auto clear = [&](int k, bool more) __attribute__((always_inline)) {
#pragma unroll 1
    for (int it = 0; it < SPIN_CAP; ++it) {
      const int x = __hip_atomic_load(ctl, __ATOMIC_RELAXED, __HIP_MEMORY_SCOPE_WORKGROUP);
      if (__builtin_amdgcn_readlane(x, C_TICK) >= nt + 8 - k && (!more || (__builtin_amdgcn_readlane(x, C_FC) && __builtin_amdgcn_readlane(x, C_OLD) == 8))) break;
      __builtin_amdgcn_s_sleep(1);
    }
    LDS_FENCE();
    SB();
  };
  const unsigned half = isy ? w : rank - SR;
  if (isy && w >= 4) {
    v4u wt[6][2];
    const rsrc_t rwo = mkrs(wa, XW ? 0 : 32 * D * 2);
    const rsrc_t rwd = mkrs(wb, (y < 112 && !XW) ? 28 * D * 2 : 0);
    unsigned o0 = (lane >> 3) * (D * 2) + (w - 4) * 256 + (lane & 7) * 16, o1 = w == 7 ? OOB : o0 + 128;
    unsigned o2 = o0 + (lane >> 5) * (4 * D * 2), o3 = o1 + (lane >> 5) * (4 * D * 2);
    asm volatile("" : "+v"(o0), "+v"(o1), "+v"(o2), "+v"(o3));
    SB();
    LDS_FENCE(); bar(); LDS_FENCE();
    STAMP(2);
    if (YB) __builtin_amdgcn_s_barrier();
    if (YD) __builtin_amdgcn_s_sleep(YD);
    SB();
#pragma unroll
    for (int k = 0; k < 6; ++k) {
      const int i = (k + 4) % 6;
      wt[i][0] = i < 4 ? ld4<2>(rwo, o0, i * 8 * D * 2) : ld4<2>(rwd, o2, (i & 3) * 16 * D * 2);
      wt[i][1] = i < 4 ? ld4<2>(rwo, o1, i * 8 * D * 2) : ld4<2>(rwd, o3, (i & 3) * 16 * D * 2);
      SB();
    }
    const unsigned spare = __builtin_amdgcn_raw_buffer_load_b32(rwo, (int)OOB, 0, 0);
    SB();
    const int p = (w - 4) * 32 + (lane >> 4) * 8 + (lane & 7);
    v4u dt = *reinterpret_cast<const v4u*>(&s_dst[2][p * 4]) & 1u;
    unsigned to = ((lane & 8) || p >= 112) ? OOB : my(p);
    f32x4 acc[2] = {{0.f, 0.f, 0.f, 0.f}, {0.f, 0.f, 0.f, 0.f}};
    auto bop = [&](float a) -> v4u { return pm & __builtin_bit_cast(unsigned, (bf16x2){(__bf16)a, (__bf16)a}); };
    auto step = [&](int i, const v4u& pb) { acc[0] = mfma(wt[i][0], pb, acc[0]); acc[1] = mfma(wt[i][1], pb, acc[1]); };
    lds4_t po = (lds4_t)&s_o[0][(lane >> 3) * 4];
    lds1_t ph = (lds1_t)&s_h[(lane & 4) * 4 + ((lane >> 4) & 1) * 8 + (lane & 3) * 2 + (lane >> 5)];
    asm volatile("" : "+v"(dt), "+v"(to), "+v"(po), "+v"(ph));
    SB();
    const v4u old = *reinterpret_cast<const v4u*>(&s_old[7][lane * 4]);
    v4u dr = old;
    if (w == 7) {
      if (G3) until(G3 + __builtin_amdgcn_readlane(tok, C_LEAD));
      dr = poll<PD3, PG3, 0>(rw4, pa, old);
      const v4u d = dr & ~1u;
      float g = tr16(tr32(asf(d[0]), asf(d[2])), tr32(asf(d[1]), asf(d[3])));
      g = rbf(g + dpp<ROR8>(g));
      const float u = dpp<ROR4>(g);
      *ph = rbf(g / (1.0f + __expf(-g))) * u;
      SB();
    }
    STAMP(14);
    LDS_FENCE(); bar(); LDS_FENCE();
    STAMP(3);
    const f32x2 hv = *reinterpret_cast<const f32x2*>(&s_h[(lane >> 3) * 2]);
    const f32x4 ma = *reinterpret_cast<const f32x4*>(&s_ml2[0]), mb = *reinterpret_cast<const f32x4*>(&s_ml2[4]);
    f32x4 pq[4];
#pragma unroll
    for (int k = 0; k < 4; ++k) pq[k] = po[k * 8];
    SB();
    const v4u h0 = bop(hv.x), h1 = bop(hv.y);
    {
      step(4, h0); step(5, h1);
      asm volatile("" : "+v"(acc[0]), "+v"(acc[1]));
      SB();
    }
    const float M = fmx(fmx(fmx(mb[0], mb[1]), mb[2]), mb[3]);
    f32x4 e = (mb - M) * EXP_C;
#pragma unroll
    for (int k = 0; k < 4; ++k) e[k] = __builtin_amdgcn_exp2f(e[k]);
    const float inv = __builtin_amdgcn_rcpf(__builtin_fmaf(ma[0], e[0], ma[1] * e[1]) + __builtin_fmaf(ma[2], e[2], ma[3] * e[3]));
    f32x2 av[2];
#pragma unroll
    for (int j = 0; j < 2; ++j) av[j] = (f32x2){pq[0][2 * j], pq[0][2 * j + 1]} * e[0];
#pragma unroll
    for (int k = 1; k < 4; ++k) {
#pragma unroll
      for (int j = 0; j < 2; ++j) av[j] = __builtin_elementwise_fma((f32x2){pq[k][2 * j], pq[k][2 * j + 1]}, (f32x2){e[k], e[k]}, av[j]);
    }
    av[0] *= inv; av[1] *= inv;
    {
      v4u pb[4];
#pragma unroll
      for (int i = 0; i < 4; ++i) pb[i] = bop(av[i >> 1][i & 1]);
      if (FB) { asm volatile("" : "+v"(pb[0]), "+v"(pb[1]), "+v"(pb[2]), "+v"(pb[3])); SB(); }
#pragma unroll
      for (int i = 0; i < 4; ++i) step(i, pb[i]);
    }
    f32x4 z;
#pragma unroll
    for (int v = 0; v < 4; ++v) {
      const float a0 = asf((unsigned)__builtin_amdgcn_update_dpp((int)asu(acc[0][v]), (int)asu(acc[0][v]), ROR8, 0xc, 0xf, false));
      const float a1 = asf((unsigned)__builtin_amdgcn_update_dpp((int)asu(acc[1][v]), (int)asu(acc[1][v]), ROR8, 0xc, 0xf, false));
      z[v] = tr32(a0, a1);
    }
    __builtin_amdgcn_raw_buffer_store_b128(tag4(z, dt), rws, (int)to, 0, S2);
    SB();
    STAMP(4);
    asm volatile("" :: "{v198}"(spare));
    poll_end(dr);
    STAMP_OUT();
    __builtin_amdgcn_endpgm();
  }
  if (half < 2) {
    ldsu_t tp = (ldsu_t)&s_dst[4 + half][lane * 4];
    unsigned to = mo_ + half * 4 * AREA;
    lds4_t pp = pr + half * 64;
    asm volatile("" : "+v"(tp), "+v"(to), "+v"(pp));
    SB();
    f32x4 ml[4], pt[8], o = {0.f, 0.f, 0.f, 0.f};
    float M = -3.0e38f, L = 0.f;
    if (PM) {
#pragma unroll 1
      for (int it = 0; it < SPIN_CAP && ctl_get(C_DONE) < PM; ++it) {}
      LDS_FENCE();
#pragma unroll
      for (int k = 0; k < 4; ++k) if ((k & 1) * 4 < PM) ml[k] = pmr[k];
#pragma unroll
      for (int k = 0; k < PM; ++k) pt[k] = pp[k * 128];
      M = ml[0][0];
#pragma unroll
      for (int k = 1; k < PM; ++k) M = fmx(M, ml[k >> 2][k & 3]);
#pragma unroll
      for (int k = 0; k < PM; ++k) {
        const float e = __builtin_amdgcn_exp2f((ml[k >> 2][k & 3] - M) * EXP_C);
        o += pt[k] * e;
        L += ml[2 + (k >> 2)][k & 3] * e;
      }
      asm volatile("" : "+v"(o), "+v"(M), "+v"(L));
      SB();
    }
    LDS_FENCE(); bar(); LDS_FENCE();
    STAMP(2);
#pragma unroll
    for (int k = 0; k < 4; ++k) if ((k & 1) * 4 + 4 > PM) ml[k] = pmr[k];
#pragma unroll
    for (int k = PM; k < 8; ++k) pt[k] = pp[k * 128];
    const v4u t = *tp;
    SB();
    {
      float m2 = M;
#pragma unroll
      for (int k = PM; k < 8; ++k) m2 = fmx(m2, ml[k >> 2][k & 3]);
      if (PM) {
        const float e = __builtin_amdgcn_exp2f((M - m2) * EXP_C);
        o *= e; L *= e;
      }
      M = m2;
#pragma unroll
      for (int k = PM; k < 8; ++k) {
        const float e = __builtin_amdgcn_exp2f((ml[k >> 2][k & 3] - M) * EXP_C);
        o += pt[k] * e;
        L += ml[2 + (k >> 2)][k & 3] * e;
      }
    }
    v4u msg;
#pragma unroll
    for (int j = 0; j < 3; ++j) msg[j] = tag<0x100>(__builtin_amdgcn_perm(asu(o[j]), asu(o[3]), 0x07060501u + j), t[j]);
    msg[3] = tag<1>(asu((lane & 1) ? L : M), t[3]);
    __builtin_amdgcn_raw_buffer_store_b128(msg, rws, (int)to, 0, S1);
    SB();
    if (!isy) STAMP(15);
  } else if (fj < 4) {
    unsigned fvo = ((lane >> 3) * (8 * F * 2) + (lane & 7) * 16) | lmask;
    const unsigned fb = ((f & 3) * 2 + (fj >> 1)) * (56 * F * 2) + (f >> 2) * 128;
    const rsrc_t rf = mkrs((fj & 1) ? wb : wa, FX ? 0 : D * F * 2);
    asm volatile("" : "+v"(fvo));
    clear(FK, true);
    if (FD) __builtin_amdgcn_s_sleep(FD);
    SB();
#pragma unroll
    for (int i = 0; i < 8; ++i) { ring[i] = ld4<2>(rf, fvo, fb + i * (F * 2)); SB(); }
    const v4u xn = *reinterpret_cast<const v4u*>(&s_xn[(((f & 3) * 2 + (fj >> 1)) * 7 + (lane >> 3)) * 4]);
    const int p = fj * 16 + ((lane >> 4) & 1) * 8 + (lane & 7);
    v4u t = *reinterpret_cast<const v4u*>(&s_dst[6][p * 4]);
    unsigned to = (lane & 40) ? OOB : mf(p);
    v4u pb[8];
#pragma unroll
    for (int i = 0; i < 8; ++i) {
      pb[i] = pm & __builtin_amdgcn_perm(xn[i >> 1], xn[i >> 1], (i & 1) ? 0x03020302u : 0x01000100u);
      asm volatile("" : "+v"(pb[i]));
    }
    asm volatile("" : "+v"(t), "+v"(to));
    SB();
    LDS_FENCE(); bar(); LDS_FENCE();
    SB();
    STAMP(2);
    f32x4 acc[2] = {{0.f, 0.f, 0.f, 0.f}, {0.f, 0.f, 0.f, 0.f}};
#pragma unroll
    for (int i = 0; i < 8; ++i) acc[i & 1] = mfma(ring[i], pb[i], acc[i & 1]);
    asm volatile("" :: "v"(pv));
    acc[0] += acc[1];
    f32x4 z;
#pragma unroll
    for (int v = 0; v < 4; ++v)
      z[v] = x32_add(asf((unsigned)__builtin_amdgcn_update_dpp((int)asu(acc[0][v]), (int)asu(acc[0][v]), ROR8, 0xc, 0xf, false)));
    __builtin_amdgcn_raw_buffer_store_b128(tag4(z, t), rws, (int)to, 0, S1);
    SB();
    STAMP(14);
  } else if (!idle) {
    LDS_FENCE(); bar(); LDS_FENCE();
    SB();
    STAMP(2);
  }
  const v4u old = *reinterpret_cast<const v4u*>(&s_old[w][lane * 4]);
  if (isy) {
    if (YB) { SB(); __builtin_amdgcn_s_barrier(); SB(); }
    lds1_t po = (lane & 8) ? (lds1_t)&s_ml2[w] : (lds1_t)&s_o[w][(lane & 7) * 4 + (lane >> 5) * 2 + ((lane >> 4) & 1)];
    lds1_t pq = (lds1_t)&s_ml2[4 + w];
    asm volatile("" : "+v"(po), "+v"(pq));
    if (G1) until(G1 + __builtin_amdgcn_readlane(tok, C_LEAD));
    STAMP(14);
    const v4u dr = poll<PD1, PG1, PS1>(rw4, pa, old);
    STAMP(15);
    const v4u d = dr & (v4u){~0x100u, ~0x100u, ~0x100u, ~1u};
    const float ot = dpp<XOR1>(asf(d[3]));
    const float m = (lane & 1) ? ot : asf(d[3]), l = (lane & 1) ? asf(d[3]) : ot;
    float M = fmx(m, dpp<ROR8>(m)); M = x16_mx(M); M = x32_mx(M);
    *pq = M;
    const float e = __builtin_amdgcn_exp2f((m - M) * EXP_C);
    const float v3 = asf(__builtin_amdgcn_perm(__builtin_amdgcn_perm(d[2], d[1], 0x04000c0cu), d[0], 0x0706000cu));
    float y0 = tr32(asf(d[0]) * e, asf(d[2]) * e), y1 = tr32(asf(d[1]) * e, v3 * e), L = x32_add(l * e);
    y0 = tr16(y0, y1); L = x16_add(L);
    const bool s_ = lane & 8;
    *po = (s_ ? L : y0) + dpp<ROR8>(s_ ? y0 : L);
    LDS_FENCE(); bar(); LDS_FENCE();
    STAMP(3);
    poll_end(dr);
    STAMP(4);
    STAMP_OUT();
    __builtin_amdgcn_endpgm();
  }
  if (isr) {
    const int col = (ro >> 1) * 32 + (ro & 1) * 4 + (lane & 3) * 8 + ((lane >> 2) & 3);
    float xo = asf((unsigned)xg[col] << 16);
    lds1_t po = (lds1_t)&s_r[(lane & 12) ? 8 : w][(lane & 3) * 4 + (lane >> 5) * 2 + ((lane >> 4) & 1)], pq = (lds1_t)&s_r[lane >> 4][lane & 15];
    const rsrc_t ro_ = mkrs(out, D * 2);
    unsigned oo = lane < 16 ? col * 2 : OOB;
    asm volatile("" : "+v"(xo), "+v"(po), "+v"(pq), "+v"(oo));
    if (G2) until(G2 + __builtin_amdgcn_readlane(tok, C_LEAD));
    const v4u dr = poll<PD2, PG2, PS2>(rw4, pa, old);
    const v4u d = dr & ~1u;
    float y0 = tr32(asf(d[0]), asf(d[2])), y1 = tr32(asf(d[1]), asf(d[3]));
    y0 = tr16(y0, y1);
    y0 += dpp<ROR8>(y0); y0 += dpp<ROR4>(y0);
    *po = y0;
    LDS_FENCE(); bar(); LDS_FENCE();
    if (w == 0) {
      float a = pq[0] + pq[64];
      a = x16_add(a); a = x32_add(a);
      __builtin_amdgcn_raw_buffer_store_b16((short)f2bf(xo + a), ro_, (int)oo, 0, SO);
    }
    STAMP(4);
    poll_end(dr);
  }
  if (!isr) STAMP(4);
  STAMP_OUT();
}

extern "C" void kernel_launch(void* const* d_in, const int* in_sizes, int n_in,
                              void* d_out, int out_size, void* d_ws, size_t ws_size,
                              hipStream_t stream) {
  fused_kernel<<<NWG, NT, 0, stream>>>((const u16*)d_in[0], (const u16*)d_in[1], (const u16*)d_in[2], (const u16*)d_in[3],
                                       (const u16*)d_in[4], (const u16*)d_in[6], (const u16*)d_in[7], (const u16*)d_in[8],
                                       (u16*)d_out, (char*)d_ws);
}
```

```cpp
#define PB 15
#define PO 600
#include <hip/hip_runtime.h>
#include <hip/hip_bf16.h>
#include <stdint.h>

#define QPOS 24
#define PACE 8
#define QTRIES 8
#define TQ 123
#define TF 131
#define TFY 129
#define TY 129
#define NP 4
#define TAU 3640
#define PP 3000
#define PL 8
#define TOFF 150
#define TOFFQ 180
#define QBURST 100000
#define PBURST 4000
#define KBURST 100000
#define VBURST 100000
#define QFIRST 24
#define OPOS 8
#define TWD 0
#define OA 16
#define OB 16
#define S1 16
#define S2 16
#define G1 3835
#define G2 3955
#define EO 3
#define PD1 2
#define PG1 6
#define PD2 2
#define PG2 6
#define PD3 2
#define PG3 6
#define G3 3845
#define SR 4
#define PS1 0
#define PS2 0
#define SO 0
#define DK 57
#define DS 0
#define DG 4
#define DB 0
#define SQ 0
#define SF_ 0
#define SFY 0
#define SY 0
#define XA 4
#define XB 4
#define TX 0
#define NOHS 0
#define ES 1
#define EL 8
#define SVM 0
#define SVL 0
#define FO 0
#define FP 1
#define NT3 1
#define UF 1
#define WV 1
#define FX 0
#define PM 5
#define FK 0
#define YB 1
#define FD 0
#define YD 0
#define FB 1
#define XW 0
#define DIAG 0
#define LITE 0

typedef unsigned v4u __attribute__((ext_vector_type(4)));
typedef unsigned v2u __attribute__((ext_vector_type(2)));
typedef float f32x4 __attribute__((ext_vector_type(4)));
typedef float f32x2 __attribute__((ext_vector_type(2)));
typedef __bf16 bf16x2 __attribute__((ext_vector_type(2)));
typedef __bf16 bf16x8 __attribute__((ext_vector_type(8)));
typedef unsigned short u16;
typedef __amdgpu_buffer_rsrc_t rsrc_t;
typedef __attribute__((address_space(3))) f32x4* lds4_t;
typedef __attribute__((address_space(3))) v4u* ldsu_t;
typedef __attribute__((address_space(3))) float* lds1_t;

constexpr int D = 448, SK = 32768, DH = 256, F = 1792, NQ = 4096;
constexpr int NWG = 256, NT = 512;
constexpr unsigned OOB = 0x80000000u;
constexpr unsigned SENT = 0xFFFFFFFFu;
constexpr unsigned AREA = 9216;
constexpr unsigned A_O1 = 1024;
constexpr unsigned A_F = 5120;
constexpr unsigned A_Y2 = 1024;
constexpr unsigned WS_REQ = NWG * AREA;
constexpr unsigned WS_ECHO = WS_REQ + 1024;
constexpr unsigned WS_GO = WS_ECHO + 1024;
constexpr unsigned WS_T = WS_GO + 256;
constexpr unsigned WS_Q = WS_T + 256;
constexpr unsigned WS_END = WS_Q + 8 * 2048;
constexpr int SPIN_CAP = 1 << 18;
constexpr int C_NEXT = 0, C_TICK = 1, C_GO = 2, C_Q = 3, C_T0 = 4, C_FC = 5, C_OLD = 6, C_DONE = 7, C_LEAD = 8;

__device__ __forceinline__ float asf(unsigned u) { return __builtin_bit_cast(float, u); }
__device__ __forceinline__ unsigned asu(float f) { return __builtin_bit_cast(unsigned, f); }
__device__ __forceinline__ float rbf(float f) { __bf16 b = (__bf16)f; return (float)b; }
__device__ __forceinline__ unsigned f2bf(float f) { __bf16 b = (__bf16)f; return (unsigned)__builtin_bit_cast(u16, b); }
__device__ __forceinline__ float dot2(unsigned k, unsigned q, float acc) {
  return __builtin_amdgcn_fdot2_f32_bf16(__builtin_bit_cast(bf16x2, k), __builtin_bit_cast(bf16x2, q), acc, false);
}
__device__ __forceinline__ f32x2 unp(unsigned u) { f32x2 r; r.x = asf(u << 16); r.y = asf(u & 0xffff0000u); return r; }
template <int CTRL> __device__ __forceinline__ float dpp(float x) {
  return asf((unsigned)__builtin_amdgcn_update_dpp(0, (int)asu(x), CTRL, 0xf, 0xf, true));
}
constexpr int XOR1 = 0xB1, XOR2 = 0x4E, XOR7 = 0x141, ROR8 = 0x128, ROR4 = 0x124, ROR2 = 0x122, BCAST = 0x150;
constexpr int QP_EVEN = 0xA0, QP_ODD = 0xF5;
__device__ __forceinline__ float x16_add(float x) { auto s = __builtin_amdgcn_permlane16_swap(asu(x), asu(x), false, false); return asf(s[0]) + asf(s[1]); }
__device__ __forceinline__ float x32_add(float x) { auto s = __builtin_amdgcn_permlane32_swap(asu(x), asu(x), false, false); return asf(s[0]) + asf(s[1]); }
__device__ __forceinline__ float x16_max(float x) { auto s = __builtin_amdgcn_permlane16_swap(asu(x), asu(x), false, false); return fmaxf(asf(s[0]), asf(s[1])); }
__device__ __forceinline__ float x32_max(float x) { auto s = __builtin_amdgcn_permlane32_swap(asu(x), asu(x), false, false); return fmaxf(asf(s[0]), asf(s[1])); }
__device__ __forceinline__ float tr32(float a, float b) { auto s = __builtin_amdgcn_permlane32_swap(asu(a), asu(b), false, false); return asf(s[0]) + asf(s[1]); }
__device__ __forceinline__ float tr16(float a, float b) { auto s = __builtin_amdgcn_permlane16_swap(asu(a), asu(b), false, false); return asf(s[0]) + asf(s[1]); }
__device__ __forceinline__ float fmx(float a, float b) { return __builtin_elementwise_maximum(a, b); }
__device__ __forceinline__ float x16_mx(float x) { auto s = __builtin_amdgcn_permlane16_swap(asu(x), asu(x), false, false); return fmx(asf(s[0]), asf(s[1])); }
__device__ __forceinline__ float x32_mx(float x) { auto s = __builtin_amdgcn_permlane32_swap(asu(x), asu(x), false, false); return fmx(asf(s[0]), asf(s[1])); }
__device__ __forceinline__ float wave_sum(float v) {
  v += dpp<XOR1>(v); v += dpp<XOR2>(v); v += dpp<XOR7>(v); v += dpp<ROR8>(v);
  v = x16_add(v); return x32_add(v);
}
__device__ __forceinline__ float wave_max(float v) {
  v = fmaxf(v, dpp<XOR1>(v)); v = fmaxf(v, dpp<XOR2>(v)); v = fmaxf(v, dpp<XOR7>(v)); v = fmaxf(v, dpp<ROR8>(v));
  v = x16_max(v); return x32_max(v);
}
__device__ __forceinline__ rsrc_t mkrs(const void* p, unsigned bytes) { return __builtin_amdgcn_make_buffer_rsrc((void*)p, 0, (int)bytes, 0x00020000); }
__device__ __forceinline__ rsrc_t selr(bool c, rsrc_t a, rsrc_t b) { return c ? a : b; }
__device__ __forceinline__ unsigned selu(bool c, unsigned a, unsigned b) { return c ? a : b; }
template <int AUX> __device__ __forceinline__ v4u ld4(rsrc_t r, unsigned vo, unsigned so) { return __builtin_amdgcn_raw_buffer_load_b128(r, (int)vo, (int)so, AUX); }
template <int AUX> __device__ __forceinline__ v2u ld2(rsrc_t r, unsigned vo, unsigned so) { return __builtin_amdgcn_raw_buffer_load_b64(r, (int)vo, (int)so, AUX); }
__device__ __forceinline__ void st4c(v4u v, rsrc_t r, unsigned vo) { __builtin_amdgcn_raw_buffer_store_b128(v, r, (int)vo, 0, 16); }
__device__ __forceinline__ void st1c(unsigned v, rsrc_t r, unsigned vo) { __builtin_amdgcn_raw_buffer_store_b32(v, r, (int)vo, 0, 16); }
template <unsigned M> __device__ __forceinline__ unsigned tag(unsigned u, unsigned t) { return (u & ~M) | (t & M); }
__device__ __forceinline__ v4u tag4(const f32x4& v, const v4u& t) { return (v4u){tag<1>(asu(v[0]), t[0]), tag<1>(asu(v[1]), t[1]), tag<1>(asu(v[2]), t[2]), tag<1>(asu(v[3]), t[3])}; }
__device__ __forceinline__ bool arrived(const v4u& v, const v4u& o) { return __builtin_amdgcn_ballot_w64(v[0] == o[0] || v[1] == o[1] || v[2] == o[2] || v[3] == o[3]) == 0; }
__device__ __forceinline__ unsigned lowb(const v4u& v) { return __builtin_amdgcn_perm(v[1], v[0], 0x0c0c0400u) | __builtin_amdgcn_perm(v[3], v[2], 0x04000c0cu); }
#define SB() __builtin_amdgcn_sched_barrier(0)
__device__ __forceinline__ void bar() { __builtin_amdgcn_s_waitcnt(0xC07F); __builtin_amdgcn_s_barrier(); }
#define WAITV(n) __builtin_amdgcn_s_waitcnt(((n) & 15) | (((n) >> 4) << 14) | 0x0F70)
template <int V> struct IC { static constexpr int value = V; constexpr operator int() const { return V; } };
template <int A, int B, class Fn> __device__ __forceinline__ void static_for(Fn&& f) { if constexpr (A < B) { f(IC<A>{}); static_for<A + 1, B>(f); } }
#define LDS_FENCE() asm volatile("" ::: "memory")

constexpr float EXP_C = 0.0625f * 1.4426950408889634f;

struct Att {
  float m, l;
  f32x4 a[2];
};
__device__ __forceinline__ f32x4 mfma(const v4u& a, const v4u& b, const f32x4& c) {
  return __builtin_amdgcn_mfma_f32_16x16x32_bf16(__builtin_bit_cast(bf16x8, a), __builtin_bit_cast(bf16x8, b), c, 0, 0, 0);
}
__device__ __forceinline__ float red_max(float v) { v = fmaxf(v, dpp<XOR1>(v)); v = fmaxf(v, dpp<XOR2>(v)); return x32_max(x16_max(v)); }

template <int NL, class ISSUE>
__device__ __forceinline__ float scores(v4u (&kb)[8], const v4u (&qb)[8], int lane, ISSUE issue) {
  f32x4 d = {0.f, 0.f, 0.f, 0.f};
#pragma unroll
  for (int i = 0; i < NL; ++i) {
    d = mfma(kb[i], qb[i], d);
    asm volatile("" : "+v"(d));
    issue(i);
    SB();
  }
  const bool odd = lane & 1;
  return rbf((odd ? d[3] : d[0]) + dpp<XOR1>(odd ? d[1] : d[2]));
}
template <class ISSUE>
__device__ __forceinline__ float phase_k4(v4u (&kb)[8], const v4u (&qb)[8], Att& st, int lane, ISSUE issue) {
  float s = scores<4>(kb, qb, lane, issue);
  s = (lane & 2) ? -__builtin_inff() : s;
  const float mn = fmaxf(st.m, red_max(s));
  const float al = __builtin_amdgcn_exp2f((st.m - mn) * EXP_C);
  const float p = __builtin_amdgcn_exp2f((s - mn) * EXP_C);
  st.m = mn; st.l = st.l * al + p;
  st.a[0] *= al; st.a[1] *= al;
  SB();
  return p;
}
template <int I, int O, class ISSUE>
__device__ __forceinline__ void pv_step(v4u (&vb)[8], unsigned pp, const v4u& pm, Att& st, ISSUE& issue) {
  const v4u pb = pm & (unsigned)__builtin_amdgcn_update_dpp(0, (int)pp, I * 0x55, 0xf, 0xf, true);
  st.a[0] = mfma(vb[O + 2 * I], pb, st.a[0]);
  asm volatile("" : "+v"(st.a[0]));
  issue(O + 2 * I);
  SB();
  st.a[1] = mfma(vb[O + 2 * I + 1], pb, st.a[1]);
  asm volatile("" : "+v"(st.a[1]));
  issue(O + 2 * I + 1);
  SB();
}
template <int NL, class ISSUE>
__device__ __forceinline__ void phase_v(v4u (&vb)[8], float p, const v4u& pm, Att& st, ISSUE issue) {
  constexpr int O = 8 - NL;
  const unsigned pp = __builtin_bit_cast(unsigned, (bf16x2){(__bf16)p, (__bf16)p});
  pv_step<0, O>(vb, pp, pm, st, issue); pv_step<1, O>(vb, pp, pm, st, issue);
  if constexpr (NL == 8) { pv_step<2, O>(vb, pp, pm, st, issue); pv_step<3, O>(vb, pp, pm, st, issue); }
}

template <int N, int GAP, int SL>
__device__ __forceinline__ v4u poll(const v4u& r, unsigned pa, const v4u& old) {
  v4u d;
  unsigned long long t0, t1, t2;
  int n = SPIN_CAP;
  asm volatile(
      "s_nop 4\n\t"
      "buffer_load_dwordx4 v[200:203], %5, %6, 0 offen sc1\n\t"
      ".rept %11\n\t"
      "s_sleep %12\n\t"
      "buffer_load_dwordx4 v[200:203], %5, %6, 0 offen sc1\n\t"
      ".endr\n"
      "1:\n\t"
      "s_waitcnt vmcnt(%11)\n\t"
      "v_cmp_eq_u32 vcc, v200, %7\n\t"
      "v_cmp_eq_u32 %1, v201, %8\n\t"
      "v_cmp_eq_u32 %2, v202, %9\n\t"
      "v_cmp_eq_u32 %3, v203, %10\n\t"
      "s_or_b64 vcc, vcc, %1\n\t"
      "s_or_b64 %2, %2, %3\n\t"
      "s_or_b64 exec, vcc, %2\n\t"
      "s_cbranch_scc0 2f\n\t"
      ".if %13\n\ts_sleep %13\n\t.endif\n\t"
      "buffer_load_dwordx4 v[200:203], %5, %6, 0 offen sc1\n\t"
      "s_sub_u32 %4, %4, 1\n\t"
      "s_cbranch_scc0 1b\n"
      "2:\n\t"
      "s_mov_b64 exec, -1"
      : "=&{v[200:203]}"(d), "=&s"(t0), "=&s"(t1), "=&s"(t2), "+s"(n)
      : "v"(pa), "s"(r), "v"(old[0]), "v"(old[1]), "v"(old[2]), "v"(old[3]), "n"(N - 1), "n"(GAP), "n"(SL)
      : "vcc", "scc", "memory");
  return d;
}
__device__ __forceinline__ void poll_end(const v4u& d) { asm volatile("s_waitcnt vmcnt(0)" :: "{v[200:203]}"(d)); }

#define STAMP(k) do { if ((k) == PB) { SB(); tpb = (int)__builtin_amdgcn_s_memrealtime() + PO; SB(); } } while (0)
#define STAMP_OUT() do { for (int it_ = 0; it_ < 100000 && (int)__builtin_amdgcn_s_memrealtime() - tpb < 0; ++it_) __builtin_amdgcn_s_sleep(1); } while (0)

__device__ __forceinline__ f32x2 ln_stats(const v4u& xa, int lane) {
  f32x2 xv[4];
  float sm = 0.f;
#pragma unroll
  for (int j = 0; j < 4; ++j) { xv[j] = unp(xa[j]); sm += xv[j].x + xv[j].y; }
  const float mean = wave_sum(sm) * (1.0f / D);
  float sq = 0.f;
#pragma unroll
  for (int j = 0; j < 4; ++j) { const f32x2 d = xv[j] - mean; sq += d.x * d.x + d.y * d.y; }
  sq = lane < 56 ? sq : 0.f;
  const float var = rbf(wave_sum(sq) * (1.0f / D));
  const float sd = rbf(sqrtf(rbf(var + 1e-5f)));
  return (f32x2){rbf(mean), 1.0f / sd};
}
__device__ __forceinline__ float ln_apply(float x, f32x2 st) { return rbf(rbf(x - st.x) * st.y); }

__global__ void __launch_bounds__(NT, 2)
fused_kernel(const u16* __restrict__ xg, const u16* __restrict__ kc, const u16* __restrict__ vc, const u16* __restrict__ wq,
             const u16* __restrict__ wo, const u16* __restrict__ wg, const u16* __restrict__ wu, const u16* __restrict__ wd,
             u16* __restrict__ out, char* ws) {
  __shared__ __attribute__((aligned(16))) float s_part[8][512];
  __shared__ __attribute__((aligned(16))) float s_mv[8][64];
  __shared__ __attribute__((aligned(16))) unsigned s_q[256];
  __shared__ __attribute__((aligned(16))) unsigned s_qs[64];
  __shared__ __attribute__((aligned(16))) unsigned s_old[8][256];
  __shared__ __attribute__((aligned(16))) unsigned s_dst[8][256];
  __shared__ __attribute__((aligned(16))) float s_ml[2][16];
  __shared__ __attribute__((aligned(16))) float s_o[4][32];
  __shared__ __attribute__((aligned(16))) float s_ml2[16];
  __shared__ __attribute__((aligned(16))) unsigned s_xn[256];
  __shared__ __attribute__((aligned(16))) float s_h[32];
  __shared__ __attribute__((aligned(16))) float s_r[9][16];
  __shared__ __attribute__((aligned(16))) float s_sc[64][32];
  __shared__ int s_ctl[64];
  const int tid = threadIdx.x, lane = tid & 63, w = __builtin_amdgcn_readfirstlane(tid >> 6);
  const int bi = blockIdx.x, h = (((bi & 1) ? bi + 5 : bi) & 6) | (bi >> 7), s = ((bi >> 3) & 15) * 2 + (bi & 1);
  const int b = h * 32 + s;
  static_assert(8 * TQ + 8 * TF + 6 * TFY + 10 * TY == 4096 && TQ - NP > 80 && TF - NP > 80 && TFY - NP > 80 && TY - NP > 80, "tickets");
  static_assert(TQ - NP <= 128 && TF - NP <= 128 && TFY - NP <= 128 && TY - NP <= 128, "tickets");
  const int nt = s < 8 ? TQ : s < 16 ? TF : s < 22 ? TFY : TY, nk = (nt - NP) >> 1;
  const int first = s * TY - min(s, 22) * (TY - TFY) - min(s, 16) * (TFY - TF) - min(s, 8) * (TF - TQ);
  asm volatile("" :: "s"(xg), "s"(kc), "s"(vc), "s"(wq), "s"(wo), "s"(wg), "s"(wu), "s"(wd), "s"(out), "s"(ws));
  int tpb = (int)__builtin_amdgcn_s_memrealtime();
  STAMP(0);
  const rsrc_t rx = mkrs(xg, D * 2);
  const rsrc_t rk = mkrs(kc + (size_t)h * SK * DH, SK * DH * 2);
  const rsrc_t rv = mkrs(vc + (size_t)h * SK * DH, SK * DH * 2);
  const rsrc_t rws = mkrs(ws, WS_END);
  const unsigned l16 = lane * 16;
  auto ubase = [&](int u) -> unsigned { return (unsigned)(first + 2 * u) * 4096u; };
  const unsigned kvo = ((lane >> 1) & 7) * 512 + (lane >> 4) * 32 + (lane & 1) * 16, vvo = (lane >> 4) * 1024 + (lane & 15) * 16;
  auto ldk = [&](unsigned base, int i) { return ld4<2>(rk, kvo + (i & 3) * 128, base + (i >> 2) * 4096); };
  const unsigned area = (unsigned)b * AREA;
  auto area_of = [&](int ss) -> unsigned { return (unsigned)(h * 32 + ss) * AREA; };
  const unsigned lmask = lane < 56 ? 0u : OOB;

  const bool isq = s < 8, isf = s < 14, isy = s >= 16;
  const int f = h * 14 + s, y = h * 16 + s - 16, ro = h * 4 + (s >> 2);
  const bool isr = s < 16 && ((s + h) & 3) == 0 && ro < 28;
  const bool tile = isq;
  const bool leader = (!(NOHS & 1) && b == NWG - 1 && w == 1), member = (w == 0);

  const unsigned pa = isy ? (w < 4 ? area + A_O1 + w * 1024 + l16 : (w == 7 && y < 112) ? area + A_F + l16 : OOB) : isr ? area + A_Y2 + w * 1024 + l16 : OOB;
  const unsigned mo_ = area_of(16 + (lane & 8) + (lane >> 4)) + A_O1 + s * 128 + (lane & 7) * 16;
  auto mf = [&](int p) -> unsigned {
    const int fd = (f & ~3) + ((p >> 2) & 3);
    return (unsigned)((fd >> 4) * 32 + 16 + (fd & 15)) * AREA + A_F + (f & 3) * 256 + (p >> 4) * 64 + (p & 3) * 16;
  };
  auto my = [&](int p) -> unsigned { return (unsigned)((p >> 4) * 32 + (p & 12) + ((-(p >> 4)) & 3)) * AREA + A_Y2 + y * 64 + (p & 3) * 16; };
  v4u eo, ed;
  auto da_of = [&]() {
    const int p = (w - 2) * 64 + lane;
    unsigned da = (isy && p < 112) ? my(p) : OOB;
    return (w & 6) == 2 ? da : (w & 6) == 4 ? mo_ + (w - 4) * 4 * AREA : (w == 6 && isf) ? mf(lane) : OOB;
  };
  if (EO == 1) { eo = ld4<OA>(rws, pa, 0); SB(); }
  if (EO == 3 || (EO == 4 && !tile)) { eo = ld4<OA>(rws, pa, 0); ed = ld4<OB>(rws, da_of(), 0); SB(); }
  if (EO == 5) { ed = ld4<OB>(rws, da_of(), 0); SB(); }
  const v4u sent = {SENT, SENT, SENT, SENT};
  unsigned poll_off = lane == 1 ? WS_GO : lane < 4 ? WS_T - 32 + l16 : OOB;
  poll_off = lane == 0 ? WS_REQ + (b >> 2) * 16 : poll_off;
  poll_off = leader ? (ES ? lane * 4 * AREA + (lane & 3) * 256 : WS_ECHO + l16) : poll_off;
  auto echoes = [&](unsigned seen) {
    v4u r;
#pragma unroll
    for (int j = 0; j < 4; ++j) r[j] = __builtin_amdgcn_raw_buffer_load_b32(rws, (int)(poll_off | (seen >> j << 31)), j * AREA, 16);
    return r;
  };
  v4u pv = {0u, 0u, 0u, 0u}, e1 = {0u, 0u, 0u, 0u};
  unsigned go_ref = 0;
  int* const ctl = &s_ctl[lane];
  auto ctl_get = [&](int i) { return __builtin_amdgcn_readfirstlane(__hip_atomic_load(&s_ctl[i], __ATOMIC_RELAXED, __HIP_MEMORY_SCOPE_WORKGROUP)); };
  auto ctl_set = [&](int i) { __hip_atomic_store(&s_ctl[i], 1, __ATOMIC_RELAXED, __HIP_MEMORY_SCOPE_WORKGROUP); };
  const unsigned qoff = WS_Q + h * 2048 + l16;
  v4u qv0, qv1;
  if (member) {
    qv0 = ld4<16>(rws, qoff, 0);
    qv1 = ld4<16>(rws, qoff, 1024);
    SB();
    int c = lane == C_NEXT ? (isq ? TOFFQ : TOFF) * 128 : 0;
    c = lane == C_TICK ? 40 : c;
    c = lane == C_T0 ? (int)(__builtin_amdgcn_s_memrealtime() & 0x7fffffff) : c;
    __hip_atomic_store(ctl, c, __ATOMIC_RELAXED, __HIP_MEMORY_SCOPE_WORKGROUP);
    st1c(c, rws, (lane == C_T0 && (bi >> 3) == 12) ? WS_T + (bi & 7) * 4 : OOB);
  }
  *reinterpret_cast<v4u*>(&s_sc[w * 8][lane * 4]) = sent;
  int t0 = 0;
  auto now = [&]() __attribute__((always_inline)) { return ((int)__builtin_amdgcn_s_memrealtime() - t0) & 0x7fffff; };
  const int neg = lane == C_NEXT ? 0 : (int)0x80000000;
  const int xcc = __builtin_amdgcn_s_getreg(20 | (3 << 11));
  constexpr auto tkf = [](int t, int sh) { return TAU + sh * 128 / ((t - NP) / 2 - 8); };
  constexpr auto tvf = [](int t, int sh) { return TAU - sh * 128 / ((t - NP) / 2 + 8 - PL); };
  int tk = s < 8 ? tkf(TQ + 16, SQ) : s < 16 ? tkf(TF, SF_) : s < 22 ? tkf(TFY, SFY) : tkf(TY, SY);
  int tv = s < 8 ? tvf(TQ, SQ) : s < 16 ? tvf(TF, SF_) : s < 22 ? tvf(TFY, SFY) : tvf(TY, SY);
  int inc = lane == C_NEXT ? tk : 0;
  auto token = [&](int burst) __attribute__((always_inline)) {
    const int t = max(now() * 128 - burst, 0);
    __hip_atomic_fetch_max(ctl, t | neg, __ATOMIC_RELAXED, __HIP_MEMORY_SCOPE_WORKGROUP);
    return __hip_atomic_fetch_add(ctl, inc, __ATOMIC_RELAXED, __HIP_MEMORY_SCOPE_WORKGROUP);
  };
  auto due_of = [&](int x) { return __builtin_amdgcn_readlane(x, C_NEXT) >> 7; };
  auto until = [&](int due) __attribute__((always_inline)) {
#pragma unroll 1
    for (int it = 0; it < 4096 && now() < due; ++it) __builtin_amdgcn_s_sleep(1);
    SB();
  };
  unsigned qs0, qs1;
  auto q_there = [&]() { return __builtin_amdgcn_ballot_w64(((lowb(qv0) ^ qs0) & (lowb(qv1) ^ qs1)) != 0xFFFFFFFFu) == 0; };
  auto load_q = [&]() { qv0 = ld4<16>(rws, qoff, 0); qv1 = ld4<16>(rws, qoff, 1024); };
  auto publish_q = [&]() {
    *reinterpret_cast<v2u*>(&s_q[lane * 2]) = (v2u){__builtin_amdgcn_perm(qv0[1], qv0[0], 0x07060302u), __builtin_amdgcn_perm(qv0[3], qv0[2], 0x07060302u)};
    *reinterpret_cast<v2u*>(&s_q[128 + lane * 2]) = (v2u){__builtin_amdgcn_perm(qv1[1], qv1[0], 0x07060302u), __builtin_amdgcn_perm(qv1[3], qv1[2], 0x07060302u)};
    LDS_FENCE();
    __builtin_amdgcn_s_waitcnt(0xC07F);
    ctl_set(C_Q);
  };
  v4u pre[4][8], ring[8];
  auto unit0 = [&]() {
#pragma unroll
    for (int i = 0; i < 8; ++i) { pre[0][i] = ldk(ubase(w), i); SB(); }
    if (EO == 2) { eo = ld4<OA>(rws, pa, 0); SB(); }
  };
  SB();
  if (tile) {
    v4u tw[8];
    const int oct = 7 * w;
    constexpr int stride = NQ * 2;
    const rsrc_t rt = mkrs(wq, D * stride);
    const unsigned to = (unsigned)((oct + (lane >> 3)) * 8 * stride + (h * 8 + s) * 128 + (lane & 7) * 16) | lmask;
    const v4u xa = ld4<0>(rx, l16, 0);
    const v4u xb = ld4<0>(rx, (unsigned)(oct + (lane >> 3)) * 16 | lmask, 0);
    SB();
#pragma unroll
    for (int i = 0; i < 8; ++i) { tw[i] = ld4<2>(rt, to, i * stride); SB(); }
    if (EO == 4) { eo = ld4<OA>(rws, pa, 0); ed = ld4<OB>(rws, da_of(), 0); SB(); }
    LDS_FENCE(); bar(); LDS_FENCE();
    t0 = ctl_get(C_T0);
    SB();
    const f32x2 ls = ln_stats(xa, lane);
    f32x2 acc[4];
#pragma unroll
    for (int i = 0; i < 8; ++i) {
      const float xn = ln_apply(asf((i & 1) ? (xb[i >> 1] & 0xffff0000u) : (xb[i >> 1] << 16)), ls);
#pragma unroll
      for (int j = 0; j < 4; ++j) acc[j] = i ? acc[j] + unp(tw[i][j]) * xn : unp(tw[i][j]) * xn;
    }
    float y0 = tr32(acc[0].x, acc[2].x), y1 = tr32(acc[0].y, acc[2].y), y2 = tr32(acc[1].x, acc[3].x), y3 = tr32(acc[1].y, acc[3].y);
    y0 = tr16(y0, y2); y1 = tr16(y1, y3);
    const bool s_ = lane & 8;
    y0 = (s_ ? y1 : y0) + dpp<ROR8>(s_ ? y0 : y1);
    s_mv[w][(lane & 7) * 8 + (lane >> 3)] = y0;
    STAMP(6);
    token(QBURST);
    unit0();
    LDS_FENCE(); bar(); LDS_FENCE();
    if (member) {
      float a = 0.f;
#pragma unroll
      for (int k = 0; k < 8; ++k) a += s_mv[k][lane];
      s_qs[lane] = f2bf(a) << 16;
      LDS_FENCE();
      const v4u qs = *reinterpret_cast<const v4u*>(&s_qs[(lane & 15) * 4]) | (~((s & 4) ? qv1 : qv0) & 0xffffu);
      st4c(qs, rws, (lane >> 4) == (s & 3) ? qoff + (s >> 2) * 1024 : OOB);
    }
  } else if (leader) {
    e1 = ES ? echoes(0) : ld4<16>(rws, poll_off, 0);
    go_ref = __builtin_amdgcn_raw_buffer_load_b32(rws, (int)WS_GO, 0, 16);
  }
  SB();
  STAMP(5);
  SB();
  const u16* wa = isy ? wo + (size_t)y * 32 * D : wg;
  const u16* wb = isy ? wd + (size_t)(y < 112 ? (y >> 1) * 32 + (y & 1) * 4 : 0) * D : wu;
  asm volatile("" : "+s"(wa), "+s"(wb));
  bool ok = !member;
  static_for<0, 40>([&](auto N) {
    constexpr int n = N;
    if constexpr (n == 0) {
      if (!tile) unit0();
    }
    if constexpr (n == 8) {
      if (!tile) {
        LDS_FENCE(); bar(); LDS_FENCE();
        t0 = ctl_get(C_T0);
        if (leader) {
          e1 += 1u;
          go_ref += 1u;
          st4c(e1, rws, WS_REQ + l16);
        }
      }
    }
    if constexpr (n > 0 && n % 8 == 0) {
      const int x = token(isq ? QBURST : PBURST);
      if constexpr (n > QFIRST) {
        if (!ok) {
          ok = q_there();
          if (ok) publish_q();
        }
      }
      if constexpr (n == OPOS + 16) {
        *reinterpret_cast<v4u*>(&s_old[w][lane * 4]) = (int)pa < 0 ? sent : eo;
        *reinterpret_cast<v4u*>(&s_dst[w][lane * 4]) = ~ed;
        LDS_FENCE();
        __hip_atomic_fetch_add(ctl, lane == C_OLD ? 1 : 0, __ATOMIC_RELAXED, __HIP_MEMORY_SCOPE_WORKGROUP);
      }
      SB();
      until(due_of(x));
      if constexpr (n == 8) {
        if (member) { qs0 = lowb(qv0); qs1 = lowb(qv1); }
      }
      if constexpr (n >= QFIRST) {
        if (!ok) load_q();
      }
      if constexpr (n == OPOS) {
        if (EO == 0 || EO == 5) eo = ld4<OA>(rws, pa, 0);
        if (EO < 3) ed = ld4<OB>(rws, da_of(), 0);
      }
      SB();
    }
    if constexpr (n == QPOS) {
      STAMP(9);
      if (ES && leader) pv = echoes(0);
      else pv = ld4<16>(rws, (member || leader) ? poll_off : OOB, 0);
      SB();
    }
    if constexpr (n >= 8) {
      if constexpr (n >= PACE) WAITV(PACE + ((n >= QPOS && n < QPOS + PACE) ? 1 : 0) + ((n >= OPOS && n < OPOS + PACE) ? (EO == 0 ? 2 : (EO == 3 || EO == 4) ? 0 : 1) : 0));
      if constexpr (n < 32) pre[n >> 3][n & 7] = ldk(ubase((n & 56) + w), n & 7); else ring[n & 7] = ldk(ubase(32 + w), n & 7);
    }
    SB();
    if constexpr (n - PACE == 7 || n - PACE == 15 || n - PACE == 23 || n - PACE == 31) STAMP(16 + (n - PACE) / 8);
  });
  inc = lane == C_TICK ? 1 : inc;
  int tok = token(KBURST);
  STAMP(10);
  bool svc = member || leader;
  unsigned seenm = 0, last_echo = 0;
  bool echoed = false;
  auto service = [&]() {
    if (leader) {
      seenm |= (pv[0] == e1[0] ? 1u : 0u) | (pv[1] == e1[1] ? 2u : 0u) | (pv[2] == e1[2] ? 4u : 0u) | (pv[3] == e1[3] ? 8u : 0u);
      svc = __builtin_amdgcn_ballot_w64(seenm != 15u) != 0;
      st1c(go_ref, rws, (lane == 0 && !svc) ? WS_GO : OOB);
    } else {
      const unsigned req = (unsigned)__builtin_amdgcn_readfirstlane((int)((b & 2) ? ((b & 1) ? pv[3] : pv[2]) : ((b & 1) ? pv[1] : pv[0])));
      const unsigned go = (unsigned)__builtin_amdgcn_readlane((int)pv[0], 1);
      go_ref = echoed ? go_ref : go;
      svc = go == (unsigned)__builtin_amdgcn_readfirstlane((int)go_ref);
      if (NOHS & 1) svc = false;
      const bool rdy = ctl_get(C_OLD) == 8;
      if (ES && EL > 0) st4c((v4u){req, req, req, req}, rws, (lane < EL && svc && rdy && !(echoed && req == last_echo)) ? area + ((b >> 2) & 3) * 256 + l16 : OOB);
      else st1c(req, rws, (lane == 0 && svc && rdy && !(echoed && req == last_echo)) ? (ES ? area + ((b >> 2) & 3) * 256 : WS_ECHO + b * 4) : OOB);
      echoed |= rdy; last_echo = req;
    }
    if (ES && leader) pv = echoes(seenm);
    else pv = ld4<16>(rws, svc ? poll_off : OOB, 0);
    SB();
  };

  {
    if (!ok) {
      ok = q_there();
      if (ok) publish_q();
    }
#pragma unroll 1
    for (int it = 0; it < SPIN_CAP && !ctl_get(C_Q); ++it) {
      if (svc && it >= QTRIES) { service(); asm volatile("" :: "v"(pv)); }
      if (!ok) {
        load_q();
        ok = q_there() | !svc;
        if (ok) publish_q();
      } else {
        __builtin_amdgcn_s_sleep(1);
      }
    }
  }
  LDS_FENCE();
  v4u qb[8];
#pragma unroll
  for (int i = 0; i < 4; ++i) {
    const v4u z = {0u, 0u, 0u, 0u}, q = *reinterpret_cast<const v4u*>(&s_q[(lane & 8) * 16 + i * 32 + (lane >> 4) * 8 + (lane & 1) * 4]);
    qb[i] = (lane & 2) ? z : q; qb[4 + i] = (lane & 2) ? q : z;
  }
  v4u pm;
#pragma unroll
  for (int j = 0; j < 4; ++j) pm[j] = ((lane >> 1) & 3) == j ? ((lane & 1) ? 0xffff0000u : 0xffffu) : 0u;
  STAMP(1);
  SB();

  Att st;
  int sx = 20;
  int rank;
  lds4_t pw;
  lds1_t pml;
  int idle;
  auto last = [&]() __attribute__((always_inline)) {
    pml = (lds1_t)&s_ml[(lane >> 3) & 1][rank];
    pw = (lds4_t)&s_part[rank][lane * 4];
    idle = __builtin_amdgcn_readfirstlane(!isy && !svc && rank >= PM && (unsigned)(rank - SR) >= 2 && (!isf || (unsigned)(rank - FO) >= 4));
    asm volatile("" : "+v"(pw), "+v"(pml), "+s"(idle));
    SB();
  };
  auto put_ml = [&]() __attribute__((always_inline)) {
    float l = st.l;
    l += dpp<XOR1>(l); l += dpp<XOR2>(l); l = x32_add(x16_add(l));
    pml[0] = st.m; pml[8] = l;
    SB();
  };
  {
    float* const sc = &s_sc[0][(lane >> 3) * 4 + (lane & 3)];
    rsrc_t nra, nrb;
    unsigned nva, nvb, nba, nbb, nsa, nsb;
    int kind = 0, nu = 32 + w, cu;
    auto next = [&]() __attribute__((always_inline)) {
      cu = nu;
      const int t = __builtin_amdgcn_readlane(tok, C_TICK);
      const bool fx = t < 2 * nk, tw = t >= nt;
      kind = fx ? (t >= nk ? 1 : 0) : tw ? 3 : 2;
      nu = t >= nk ? t - nk : t;
      nba = (unsigned)(fx ? first + 2 * nu : first + t) * 4096u;
      nbb = fx ? nba + 4096 : nba;
      nra = selr(kind == 1, rv, rk);
      nrb = selr(kind == 0, rk, rv);
      nva = selu(kind == 1, vvo, kvo); nvb = selu(kind == 0, kvo, vvo);
      nsa = selu(kind == 1, 256, 128); nsb = selu(kind == 0, 128, 256);
      const int u = t + 8 - nk;
      inc = lane == C_NEXT ? (t + PL >= 2 * nk ? PP : (int)selu(u >= 0, tv, tk) + ((unsigned)(u + XA) < XA + XB ? TX : 0)) : inc;
      rank = t - nt;
      if (!tw) until(due_of(tok));
      SB();
    };
    auto ldn = [&](int i) { ring[i] = i < 4 ? ld4<2>(nra, nva, nba + i * nsa) : ld4<2>(nrb, nvb, nbb + (i - 4) * nsb); };
    float mx;
    if (member) {
      const v4u d = pv - (unsigned)t0;
      const bool bad = max(max(d[0] + 512u, d[1] + 512u), max(d[2] + 512u, d[3] + 512u)) > 1024u;
      const int sm = (int)((d[0] + d[1]) + (d[2] + d[3]));
      const int lead = (__builtin_amdgcn_ballot_w64(bad) & 12) ? 0 : __builtin_amdgcn_readlane(sm, 2) + __builtin_amdgcn_readlane(sm, 3) + DB;
      tk += (lead * DK) >> 5; tv += (lead * DK) >> 5;
      __hip_atomic_fetch_add(ctl, lane == C_LEAD ? (lead * DG) >> 5 : (lane == C_NEXT ? (lead * DS) >> 5 : 0), __ATOMIC_RELAXED, __HIP_MEMORY_SCOPE_WORKGROUP);
    }
    if (svc) service();
    {
      const float s0 = scores<8>(pre[0], qb, lane, [&](int) {});
      const float s1 = scores<8>(pre[1], qb, lane, [&](int) {});
      const float s2 = scores<8>(pre[2], qb, lane, [&](int) {});
      const float s3 = scores<8>(pre[3], qb, lane, [&](int) {});
      sc[w * 32] = s0; sc[(8 + w) * 32] = s1; sc[(16 + w) * 32] = s2; sc[(24 + w) * 32] = s3;
      mx = fmaxf(fmaxf(s0, s1), fmaxf(s2, s3));
      SB();
      STAMP(11);
    }
    const v4u xa2 = ld4<0>(rx, (isf && w == 7) ? l16 : OOB, 0);
    SB();
#pragma unroll 1
    while (kind == 0) {
      next();
      const float sv = scores<8>(ring, qb, lane, ldn);
      sc[cu * 32] = sv;
      tok = token(KBURST);
      mx = fmaxf(mx, sv);
      STAMP(sx); ++sx;
      if (svc) service();
      SB();
    }
    STAMP(7);
    if (isf && w == 7) {
      const f32x2 ls = ln_stats(xa2, lane);
      v4u xn;
#pragma unroll
      for (int j = 0; j < 4; ++j) {
        const f32x2 v = unp(xa2[j]);
        xn[j] = __builtin_amdgcn_perm(asu(ln_apply(v.y, ls)), asu(ln_apply(v.x, ls)), 0x07060302u);
      }
      *reinterpret_cast<v4u*>(&s_xn[lane * 4]) = xn;
      LDS_FENCE();
      ctl_set(C_FC);
      SB();
    }
    st.m = red_max(mx); st.l = 0.f;
    st.a[0] = st.a[1] = (f32x4){0.f, 0.f, 0.f, 0.f};
#pragma unroll 1
    while (kind == 1) {
      float sv;
      for (;;) {
        sv = __hip_atomic_load(&sc[nu * 32], __ATOMIC_RELAXED, __HIP_MEMORY_SCOPE_WORKGROUP);
        if (__builtin_amdgcn_ballot_w64(asu(sv) == SENT) == 0) break;
        __builtin_amdgcn_s_sleep(1);
      }
      if (__builtin_amdgcn_ballot_w64(sv > st.m) != 0) {
        const float m2 = fmaxf(st.m, red_max(sv));
        const float al = __builtin_amdgcn_exp2f((st.m - m2) * EXP_C);
        st.m = m2; st.l *= al;
        st.a[0] *= al; st.a[1] *= al;
      }
      const float p = __builtin_amdgcn_exp2f((sv - st.m) * EXP_C);
      st.l += p;
      SB();
      next();
      if (kind == 3) {
        last();
        put_ml();
        phase_v<8>(ring, p, pm, st, [&](int) {});
        break;
      }
      phase_v<8>(ring, p, pm, st, ldn);
      tok = token(VBURST);
      STAMP(sx); ++sx;
      if (svc) service();
      SB();
    }
    STAMP(8);
    if (kind == 2) {
      next();
#pragma unroll 1
    while (kind != 3) {
      if (UF) { nba = __builtin_amdgcn_readfirstlane(nba); nbb = __builtin_amdgcn_readfirstlane(nbb); nsa = __builtin_amdgcn_readfirstlane(nsa); nsb = __builtin_amdgcn_readfirstlane(nsb); }
      const float p = phase_k4(ring, qb, st, lane, ldn);
      phase_v<4>(ring, p, pm, st, ldn);
      tok = token(VBURST);
      STAMP(sx); ++sx;
      SB();
      next();
    }
      SB();
      last();
      const float p = phase_k4(ring, qb, st, lane, [&](int) {});
      phase_v<4>(ring, p, pm, st, [&](int) {});
      put_ml();
    }
    pw[0] = st.a[0]; pw[64] = st.a[1];
    LDS_FENCE();
    SB();
    STAMP(12);
    if (idle) {
      LDS_FENCE(); bar(); LDS_FENCE();
      STAMP(2);
    }
    SB();
#pragma unroll 1
    for (int it = 0; it < SPIN_CAP && svc; ++it) {
      service();
      asm volatile("" :: "v"(pv));
    }
    LDS_FENCE();
    WAITV(0);
  }
  const lds4_t pr = (lds4_t)&s_part[0][lane * 4], pmr = (lds4_t)&s_ml[(lane >> 3) & 1][0];
  if (rank < PM) __hip_atomic_fetch_add(ctl, lane == C_DONE ? 1 : 0, __ATOMIC_RELAXED, __HIP_MEMORY_SCOPE_WORKGROUP);
  SB();
  const v4u rw4 = {(unsigned)(size_t)ws, (unsigned)((size_t)ws >> 32), WS_END, 0x00020000u};
  const unsigned fj = isf ? rank - FO : 4;
  auto clear = [&](int k, bool more) __attribute__((always_inline)) {
#pragma unroll 1
    for (int it = 0; it < SPIN_CAP; ++it) {
      const int x = __hip_atomic_load(ctl, __ATOMIC_RELAXED, __HIP_MEMORY_SCOPE_WORKGROUP);
      if (__builtin_amdgcn_readlane(x, C_TICK) >= nt + 8 - k && (!more || (__builtin_amdgcn_readlane(x, C_FC) && __builtin_amdgcn_readlane(x, C_OLD) == 8))) break;
      __builtin_amdgcn_s_sleep(1);
    }
    LDS_FENCE();
    SB();
  };
  const unsigned half = isy ? w : rank - SR;
  if (isy && w >= 4) {
    v4u wt[6][2];
    const rsrc_t rwo = mkrs(wa, XW ? 0 : 32 * D * 2);
    const rsrc_t rwd = mkrs(wb, (y < 112 && !XW) ? 28 * D * 2 : 0);
    unsigned o0 = (lane >> 3) * (D * 2) + (w - 4) * 256 + (lane & 7) * 16, o1 = w == 7 ? OOB : o0 + 128;
    unsigned o2 = o0 + (lane >> 5) * (4 * D * 2), o3 = o1 + (lane >> 5) * (4 * D * 2);
    asm volatile("" : "+v"(o0), "+v"(o1), "+v"(o2), "+v"(o3));
    SB();
    LDS_FENCE(); bar(); LDS_FENCE();
    STAMP(2);
    if (YB) __builtin_amdgcn_s_barrier();
    if (YD) __builtin_amdgcn_s_sleep(YD);
    SB();
#pragma unroll
    for (int k = 0; k < 6; ++k) {
      const int i = (k + 4) % 6;
      wt[i][0] = i < 4 ? ld4<2>(rwo, o0, i * 8 * D * 2) : ld4<2>(rwd, o2, (i & 3) * 16 * D * 2);
      wt[i][1] = i < 4 ? ld4<2>(rwo, o1, i * 8 * D * 2) : ld4<2>(rwd, o3, (i & 3) * 16 * D * 2);
      SB();
    }
    const unsigned spare = __builtin_amdgcn_raw_buffer_load_b32(rwo, (int)OOB, 0, 0);
    SB();
    const int p = (w - 4) * 32 + (lane >> 4) * 8 + (lane & 7);
    v4u dt = *reinterpret_cast<const v4u*>(&s_dst[2][p * 4]) & 1u;
    unsigned to = ((lane & 8) || p >= 112) ? OOB : my(p);
    f32x4 acc[2] = {{0.f, 0.f, 0.f, 0.f}, {0.f, 0.f, 0.f, 0.f}};
    auto bop = [&](float a) -> v4u { return pm & __builtin_bit_cast(unsigned, (bf16x2){(__bf16)a, (__bf16)a}); };
    auto step = [&](int i, const v4u& pb) { acc[0] = mfma(wt[i][0], pb, acc[0]); acc[1] = mfma(wt[i][1], pb, acc[1]); };
    lds4_t po = (lds4_t)&s_o[0][(lane >> 3) * 4];
    lds1_t ph = (lds1_t)&s_h[(lane & 4) * 4 + ((lane >> 4) & 1) * 8 + (lane & 3) * 2 + (lane >> 5)];
    asm volatile("" : "+v"(dt), "+v"(to), "+v"(po), "+v"(ph));
    SB();
    const v4u old = *reinterpret_cast<const v4u*>(&s_old[7][lane * 4]);
    v4u dr = old;
    if (w == 7) {
      if (G3) until(G3 + __builtin_amdgcn_readlane(tok, C_LEAD));
      dr = poll<PD3, PG3, 0>(rw4, pa, old);
      const v4u d = dr & ~1u;
      float g = tr16(tr32(asf(d[0]), asf(d[2])), tr32(asf(d[1]), asf(d[3])));
      g = rbf(g + dpp<ROR8>(g));
      const float u = dpp<ROR4>(g);
      *ph = rbf(g / (1.0f + __expf(-g))) * u;
      SB();
    }
    STAMP(14);
    LDS_FENCE(); bar(); LDS_FENCE();
    STAMP(3);
    const f32x2 hv = *reinterpret_cast<const f32x2*>(&s_h[(lane >> 3) * 2]);
    const f32x4 ma = *reinterpret_cast<const f32x4*>(&s_ml2[0]), mb = *reinterpret_cast<const f32x4*>(&s_ml2[4]);
    f32x4 pq[4];
#pragma unroll
    for (int k = 0; k < 4; ++k) pq[k] = po[k * 8];
    SB();
    const v4u h0 = bop(hv.x), h1 = bop(hv.y);
    {
      step(4, h0); step(5, h1);
      asm volatile("" : "+v"(acc[0]), "+v"(acc[1]));
      SB();
    }
    const float M = fmx(fmx(fmx(mb[0], mb[1]), mb[2]), mb[3]);
    f32x4 e = (mb - M) * EXP_C;
#pragma unroll
    for (int k = 0; k < 4; ++k) e[k] = __builtin_amdgcn_exp2f(e[k]);
    const float inv = __builtin_amdgcn_rcpf(__builtin_fmaf(ma[0], e[0], ma[1] * e[1]) + __builtin_fmaf(ma[2], e[2], ma[3] * e[3]));
    f32x2 av[2];
#pragma unroll
    for (int j = 0; j < 2; ++j) av[j] = (f32x2){pq[0][2 * j], pq[0][2 * j + 1]} * e[0];
#pragma unroll
    for (int k = 1; k < 4; ++k) {
#pragma unroll
      for (int j = 0; j < 2; ++j) av[j] = __builtin_elementwise_fma((f32x2){pq[k][2 * j], pq[k][2 * j + 1]}, (f32x2){e[k], e[k]}, av[j]);
    }
    av[0] *= inv; av[1] *= inv;
    {
      v4u pb[4];
#pragma unroll
      for (int i = 0; i < 4; ++i) pb[i] = bop(av[i >> 1][i & 1]);
      if (FB) { asm volatile("" : "+v"(pb[0]), "+v"(pb[1]), "+v"(pb[2]), "+v"(pb[3])); SB(); }
#pragma unroll
      for (int i = 0; i < 4; ++i) step(i, pb[i]);
    }
    f32x4 z;
#pragma unroll
    for (int v = 0; v < 4; ++v) {
      const float a0 = asf((unsigned)__builtin_amdgcn_update_dpp((int)asu(acc[0][v]), (int)asu(acc[0][v]), ROR8, 0xc, 0xf, false));
      const float a1 = asf((unsigned)__builtin_amdgcn_update_dpp((int)asu(acc[1][v]), (int)asu(acc[1][v]), ROR8, 0xc, 0xf, false));
      z[v] = tr32(a0, a1);
    }
    __builtin_amdgcn_raw_buffer_store_b128(tag4(z, dt), rws, (int)to, 0, S2);
    SB();
    STAMP(4);
    asm volatile("" :: "{v198}"(spare));
    poll_end(dr);
    STAMP_OUT();
    __builtin_amdgcn_endpgm();
  }
  if (half < 2) {
    ldsu_t tp = (ldsu_t)&s_dst[4 + half][lane * 4];
    unsigned to = mo_ + half * 4 * AREA;
    lds4_t pp = pr + half * 64;
    asm volatile("" : "+v"(tp), "+v"(to), "+v"(pp));
    SB();
    f32x4 ml[4], pt[8], o = {0.f, 0.f, 0.f, 0.f};
    float M = -3.0e38f, L = 0.f;
    if (PM) {
#pragma unroll 1
      for (int it = 0; it < SPIN_CAP && ctl_get(C_DONE) < PM; ++it) {}
      LDS_FENCE();
#pragma unroll
      for (int k = 0; k < 4; ++k) if ((k & 1) * 4 < PM) ml[k] = pmr[k];
#pragma unroll
      for (int k = 0; k < PM; ++k) pt[k] = pp[k * 128];
      M = ml[0][0];
#pragma unroll
      for (int k = 1; k < PM; ++k) M = fmx(M, ml[k >> 2][k & 3]);
#pragma unroll
      for (int k = 0; k < PM; ++k) {
        const float e = __builtin_amdgcn_exp2f((ml[k >> 2][k & 3] - M) * EXP_C);
        o += pt[k] * e;
        L += ml[2 + (k >> 2)][k & 3] * e;
      }
      asm volatile("" : "+v"(o), "+v"(M), "+v"(L));
      SB();
    }
    LDS_FENCE(); bar(); LDS_FENCE();
    STAMP(2);
#pragma unroll
    for (int k = 0; k < 4; ++k) if ((k & 1) * 4 + 4 > PM) ml[k] = pmr[k];
#pragma unroll
    for (int k = PM; k < 8; ++k) pt[k] = pp[k * 128];
    const v4u t = *tp;
    SB();
    {
      float m2 = M;
#pragma unroll
      for (int k = PM; k < 8; ++k) m2 = fmx(m2, ml[k >> 2][k & 3]);
      if (PM) {
        const float e = __builtin_amdgcn_exp2f((M - m2) * EXP_C);
        o *= e; L *= e;
      }
      M = m2;
#pragma unroll
      for (int k = PM; k < 8; ++k) {
        const float e = __builtin_amdgcn_exp2f((ml[k >> 2][k & 3] - M) * EXP_C);
        o += pt[k] * e;
        L += ml[2 + (k >> 2)][k & 3] * e;
      }
    }
    v4u msg;
#pragma unroll
    for (int j = 0; j < 3; ++j) msg[j] = tag<0x100>(__builtin_amdgcn_perm(asu(o[j]), asu(o[3]), 0x07060501u + j), t[j]);
    msg[3] = tag<1>(asu((lane & 1) ? L : M), t[3]);
    __builtin_amdgcn_raw_buffer_store_b128(msg, rws, (int)to, 0, S1);
    SB();
    if (!isy) STAMP(15);
  } else if (fj < 4) {
    unsigned fvo = ((lane >> 3) * (8 * F * 2) + (lane & 7) * 16) | lmask;
    const unsigned fb = ((f & 3) * 2 + (fj >> 1)) * (56 * F * 2) + (f >> 2) * 128;
    const rsrc_t rf = mkrs((fj & 1) ? wb : wa, FX ? 0 : D * F * 2);
    asm volatile("" : "+v"(fvo));
    clear(FK, true);
    if (FD) __builtin_amdgcn_s_sleep(FD);
    SB();
#pragma unroll
    for (int i = 0; i < 8; ++i) { ring[i] = ld4<2>(rf, fvo, fb + i * (F * 2)); SB(); }
    const v4u xn = *reinterpret_cast<const v4u*>(&s_xn[(((f & 3) * 2 + (fj >> 1)) * 7 + (lane >> 3)) * 4]);
    const int p = fj * 16 + ((lane >> 4) & 1) * 8 + (lane & 7);
    v4u t = *reinterpret_cast<const v4u*>(&s_dst[6][p * 4]);
    unsigned to = (lane & 40) ? OOB : mf(p);
    v4u pb[8];
#pragma unroll
    for (int i = 0; i < 8; ++i) {
      pb[i] = pm & __builtin_amdgcn_perm(xn[i >> 1], xn[i >> 1], (i & 1) ? 0x03020302u : 0x01000100u);
      asm volatile("" : "+v"(pb[i]));
    }
    asm volatile("" : "+v"(t), "+v"(to));
    SB();
    LDS_FENCE(); bar(); LDS_FENCE();
    SB();
    STAMP(2);
    f32x4 acc[2] = {{0.f, 0.f, 0.f, 0.f}, {0.f, 0.f, 0.f, 0.f}};
#pragma unroll
    for (int i = 0; i < 8; ++i) acc[i & 1] = mfma(ring[i], pb[i], acc[i & 1]);
    asm volatile("" :: "v"(pv));
    acc[0] += acc[1];
    f32x4 z;
#pragma unroll
    for (int v = 0; v < 4; ++v)
      z[v] = x32_add(asf((unsigned)__builtin_amdgcn_update_dpp((int)asu(acc[0][v]), (int)asu(acc[0][v]), ROR8, 0xc, 0xf, false)));
    __builtin_amdgcn_raw_buffer_store_b128(tag4(z, t), rws, (int)to, 0, S1);
    SB();
    STAMP(14);
  } else if (!idle) {
    LDS_FENCE(); bar(); LDS_FENCE();
    SB();
    STAMP(2);
  }
  const v4u old = *reinterpret_cast<const v4u*>(&s_old[w][lane * 4]);
  if (isy) {
    if (YB) { SB(); __builtin_amdgcn_s_barrier(); SB(); }
    lds1_t po = (lane & 8) ? (lds1_t)&s_ml2[w] : (lds1_t)&s_o[w][(lane & 7) * 4 + (lane >> 5) * 2 + ((lane >> 4) & 1)];
    lds1_t pq = (lds1_t)&s_ml2[4 + w];
    asm volatile("" : "+v"(po), "+v"(pq));
    if (G1) until(G1 + __builtin_amdgcn_readlane(tok, C_LEAD));
    STAMP(14);
    const v4u dr = poll<PD1, PG1, PS1>(rw4, pa, old);
    STAMP(15);
    const v4u d = dr & (v4u){~0x100u, ~0x100u, ~0x100u, ~1u};
    const float ot = dpp<XOR1>(asf(d[3]));
    const float m = (lane & 1) ? ot : asf(d[3]), l = (lane & 1) ? asf(d[3]) : ot;
    float M = fmx(m, dpp<ROR8>(m)); M = x16_mx(M); M = x32_mx(M);
    *pq = M;
    const float e = __builtin_amdgcn_exp2f((m - M) * EXP_C);
    const float v3 = asf(__builtin_amdgcn_perm(__builtin_amdgcn_perm(d[2], d[1], 0x04000c0cu), d[0], 0x0706000cu));
    float y0 = tr32(asf(d[0]) * e, asf(d[2]) * e), y1 = tr32(asf(d[1]) * e, v3 * e), L = x32_add(l * e);
    y0 = tr16(y0, y1); L = x16_add(L);
    const bool s_ = lane & 8;
    *po = (s_ ? L : y0) + dpp<ROR8>(s_ ? y0 : L);
    LDS_FENCE(); bar(); LDS_FENCE();
    STAMP(3);
    poll_end(dr);
    STAMP(4);
    STAMP_OUT();
    __builtin_amdgcn_endpgm();
  }
  if (isr) {
    const int col = (ro >> 1) * 32 + (ro & 1) * 4 + (lane & 3) * 8 + ((lane >> 2) & 3);
    float xo = asf((unsigned)xg[col] << 16);
    lds1_t po = (lds1_t)&s_r[(lane & 12) ? 8 : w][(lane & 3) * 4 + (lane >> 5) * 2 + ((lane >> 4) & 1)], pq = (lds1_t)&s_r[lane >> 4][lane & 15];
    const rsrc_t ro_ = mkrs(out, D * 2);
    unsigned oo = lane < 16 ? col * 2 : OOB;
    asm volatile("" : "+v"(xo), "+v"(po), "+v"(pq), "+v"(oo));
    if (G2) until(G2 + __builtin_amdgcn_readlane(tok, C_LEAD));
    const v4u dr = poll<PD2, PG2, PS2>(rw4, pa, old);
    const v4u d = dr & ~1u;
    float y0 = tr32(asf(d[0]), asf(d[2])), y1 = tr32(asf(d[1]), asf(d[3]));
    y0 = tr16(y0, y1);
    y0 += dpp<ROR8>(y0); y0 += dpp<ROR4>(y0);
    *po = y0;
    LDS_FENCE(); bar(); LDS_FENCE();
    if (w == 0) {
      float a = pq[0] + pq[64];
      a = x16_add(a); a = x32_add(a);
      __builtin_amdgcn_raw_buffer_store_b16((short)f2bf(xo + a), ro_, (int)oo, 0, SO);
    }
    STAMP(4);
    poll_end(dr);
  }
  if (!isr) STAMP(4);
  STAMP_OUT();
}

extern "C" void kernel_launch(void* const* d_in, const int* in_sizes, int n_in,
                              void* d_out, int out_size, void* d_ws, size_t ws_size,
                              hipStream_t stream) {
  fused_kernel<<<NWG, NT, 0, stream>>>((const u16*)d_in[0], (const u16*)d_in[1], (const u16*)d_in[2], (const u16*)d_in[3],
                                       (const u16*)d_in[4], (const u16*)d_in[6], (const u16*)d_in[7], (const u16*)d_in[8],
                                       (u16*)d_out, (char*)d_ws);
}
```

```cpp
#define PB 14
#define PO 600
#define PC s < 16
#include <hip/hip_runtime.h>
#include <hip/hip_bf16.h>
#include <stdint.h>

#define SWP 0
#define LD 1
#define LU 0
#define LB 1
#define LP 8
#define SKF 0
#define SL0 0
#define GB 1
#define YS 22
#define YE 0
#define YK 0
#define QPOS 24
#define PACE 8
#define QTRIES 8
#define TQ 123
#define TF 131
#define TFY 129
#define TY 129
#define NP 4
#define TAU 3640
#define PP 3000
#define PL 12
#define TOFF 150
#define TOFFQ 180
#define QBURST 100000
#define PBURST 4000
#define KBURST 100000
#define VBURST 100000
#define QFIRST 24
#define OPOS 8
#define TWD 0
#define OA 16
#define OB 16
#define S1 16
#define S2 16
#define G1 3835
#define G2 3955
#define EO 3
#define PD1 2
#define PG1 6
#define PD2 2
#define PG2 6
#define PD3 2
#define PG3 6
#define G3 3845
#define SR 4
#define PS1 0
#define PS2 0
#define SO 0
#define DK 57
#define DS 0
#define DG 4
#define DB 0
#define SQ 0
#define SF_ 0
#define SFY 0
#define SY 0
#define XA 4
#define XB 4
#define TX 0
#define NOHS 0
#define ES 1
#define EL 8
#define SVM 0
#define SVL 0
#define FO 0
#define FP 1
#define NT3 1
#define UF 1
#define WV 1
#define FX 0
#define PM 5
#define FK 0
#define YB 1
#define FD 0
#define YD 0
#define FB 1
#define XW 0
#define DIAG 0
#define LITE 0

typedef unsigned v4u __attribute__((ext_vector_type(4)));
typedef unsigned v2u __attribute__((ext_vector_type(2)));
typedef float f32x4 __attribute__((ext_vector_type(4)));
typedef float f32x2 __attribute__((ext_vector_type(2)));
typedef __bf16 bf16x2 __attribute__((ext_vector_type(2)));
typedef __bf16 bf16x8 __attribute__((ext_vector_type(8)));
typedef unsigned short u16;
typedef __amdgpu_buffer_rsrc_t rsrc_t;
typedef __attribute__((address_space(3))) f32x4* lds4_t;
typedef __attribute__((address_space(3))) v4u* ldsu_t;
typedef __attribute__((address_space(3))) float* lds1_t;

constexpr int D = 448, SK = 32768, DH = 256, F = 1792, NQ = 4096;
constexpr int NWG = 256, NT = 512;
constexpr unsigned OOB = 0x80000000u;
constexpr unsigned SENT = 0xFFFFFFFFu;
constexpr unsigned AREA = 9216;
constexpr unsigned A_O1 = 1024;
constexpr unsigned A_F = 5120;
constexpr unsigned A_Y2 = 1024;
constexpr unsigned WS_REQ = NWG * AREA;
constexpr unsigned WS_ECHO = WS_REQ + 1024;
constexpr unsigned WS_GO = WS_ECHO + 1024;
constexpr unsigned WS_T = WS_GO + 256;
constexpr unsigned WS_Q = WS_T + 256;
constexpr unsigned WS_END = WS_Q + 8 * 2048;
constexpr int SPIN_CAP = 1 << 18;
constexpr int C_NEXT = 0, C_TICK = 1, C_GO = 2, C_Q = 3, C_T0 = 4, C_FC = 5, C_OLD = 6, C_DONE = 7, C_LEAD = 8, C_CNT = 9;

__device__ __forceinline__ float asf(unsigned u) { return __builtin_bit_cast(float, u); }
__device__ __forceinline__ unsigned asu(float f) { return __builtin_bit_cast(unsigned, f); }
__device__ __forceinline__ float rbf(float f) { __bf16 b = (__bf16)f; return (float)b; }
__device__ __forceinline__ unsigned f2bf(float f) { __bf16 b = (__bf16)f; return (unsigned)__builtin_bit_cast(u16, b); }
__device__ __forceinline__ float dot2(unsigned k, unsigned q, float acc) {
  return __builtin_amdgcn_fdot2_f32_bf16(__builtin_bit_cast(bf16x2, k), __builtin_bit_cast(bf16x2, q), acc, false);
}
__device__ __forceinline__ f32x2 unp(unsigned u) { f32x2 r; r.x = asf(u << 16); r.y = asf(u & 0xffff0000u); return r; }
template <int CTRL> __device__ __forceinline__ float dpp(float x) {
  return asf((unsigned)__builtin_amdgcn_update_dpp(0, (int)asu(x), CTRL, 0xf, 0xf, true));
}
constexpr int XOR1 = 0xB1, XOR2 = 0x4E, XOR7 = 0x141, ROR8 = 0x128, ROR4 = 0x124, ROR2 = 0x122, BCAST = 0x150;
constexpr int QP_EVEN = 0xA0, QP_ODD = 0xF5;
__device__ __forceinline__ float x16_add(float x) { auto s = __builtin_amdgcn_permlane16_swap(asu(x), asu(x), false, false); return asf(s[0]) + asf(s[1]); }
__device__ __forceinline__ float x32_add(float x) { auto s = __builtin_amdgcn_permlane32_swap(asu(x), asu(x), false, false); return asf(s[0]) + asf(s[1]); }
__device__ __forceinline__ float x16_max(float x) { auto s = __builtin_amdgcn_permlane16_swap(asu(x), asu(x), false, false); return fmaxf(asf(s[0]), asf(s[1])); }
__device__ __forceinline__ float x32_max(float x) { auto s = __builtin_amdgcn_permlane32_swap(asu(x), asu(x), false, false); return fmaxf(asf(s[0]), asf(s[1])); }
__device__ __forceinline__ float tr32(float a, float b) { auto s = __builtin_amdgcn_permlane32_swap(asu(a), asu(b), false, false); return asf(s[0]) + asf(s[1]); }
__device__ __forceinline__ float tr16(float a, float b) { auto s = __builtin_amdgcn_permlane16_swap(asu(a), asu(b), false, false); return asf(s[0]) + asf(s[1]); }
__device__ __forceinline__ float fmx(float a, float b) { return __builtin_elementwise_maximum(a, b); }
__device__ __forceinline__ float x16_mx(float x) { auto s = __builtin_amdgcn_permlane16_swap(asu(x), asu(x), false, false); return fmx(asf(s[0]), asf(s[1])); }
__device__ __forceinline__ float x32_mx(float x) { auto s = __builtin_amdgcn_permlane32_swap(asu(x), asu(x), false, false); return fmx(asf(s[0]), asf(s[1])); }
__device__ __forceinline__ float wave_sum(float v) {
  v += dpp<XOR1>(v); v += dpp<XOR2>(v); v += dpp<XOR7>(v); v += dpp<ROR8>(v);
  v = x16_add(v); return x32_add(v);
}
__device__ __forceinline__ float wave_max(float v) {
  v = fmaxf(v, dpp<XOR1>(v)); v = fmaxf(v, dpp<XOR2>(v)); v = fmaxf(v, dpp<XOR7>(v)); v = fmaxf(v, dpp<ROR8>(v));
  v = x16_max(v); return x32_max(v);
}
__device__ __forceinline__ rsrc_t mkrs(const void* p, unsigned bytes) { return __builtin_amdgcn_make_buffer_rsrc((void*)p, 0, (int)bytes, 0x00020000); }
__device__ __forceinline__ rsrc_t selr(bool c, rsrc_t a, rsrc_t b) { return c ? a : b; }
__device__ __forceinline__ unsigned selu(bool c, unsigned a, unsigned b) { return c ? a : b; }
template <int AUX> __device__ __forceinline__ v4u ld4(rsrc_t r, unsigned vo, unsigned so) { return __builtin_amdgcn_raw_buffer_load_b128(r, (int)vo, (int)so, AUX); }
template <int AUX> __device__ __forceinline__ v2u ld2(rsrc_t r, unsigned vo, unsigned so) { return __builtin_amdgcn_raw_buffer_load_b64(r, (int)vo, (int)so, AUX); }
__device__ __forceinline__ void st4c(v4u v, rsrc_t r, unsigned vo) { __builtin_amdgcn_raw_buffer_store_b128(v, r, (int)vo, 0, 16); }
__device__ __forceinline__ void st1c(unsigned v, rsrc_t r, unsigned vo) { __builtin_amdgcn_raw_buffer_store_b32(v, r, (int)vo, 0, 16); }
template <unsigned M> __device__ __forceinline__ unsigned tag(unsigned u, unsigned t) { return (u & ~M) | (t & M); }
__device__ __forceinline__ v4u tag4(const f32x4& v, const v4u& t) { return (v4u){tag<1>(asu(v[0]), t[0]), tag<1>(asu(v[1]), t[1]), tag<1>(asu(v[2]), t[2]), tag<1>(asu(v[3]), t[3])}; }
__device__ __forceinline__ bool arrived(const v4u& v, const v4u& o) { return __builtin_amdgcn_ballot_w64(v[0] == o[0] || v[1] == o[1] || v[2] == o[2] || v[3] == o[3]) == 0; }
__device__ __forceinline__ unsigned lowb(const v4u& v) { return __builtin_amdgcn_perm(v[1], v[0], 0x0c0c0400u) | __builtin_amdgcn_perm(v[3], v[2], 0x04000c0cu); }
#define SB() __builtin_amdgcn_sched_barrier(0)
__device__ __forceinline__ void bar() { __builtin_amdgcn_s_waitcnt(0xC07F); __builtin_amdgcn_s_barrier(); }
#define WAITV(n) __builtin_amdgcn_s_waitcnt(((n) & 15) | (((n) >> 4) << 14) | 0x0F70)
template <int V> struct IC { static constexpr int value = V; constexpr operator int() const { return V; } };
template <int A, int B, class Fn> __device__ __forceinline__ void static_for(Fn&& f) { if constexpr (A < B) { f(IC<A>{}); static_for<A + 1, B>(f); } }
#define LDS_FENCE() asm volatile("" ::: "memory")

constexpr float EXP_C = 0.0625f * 1.4426950408889634f;

struct Att {
  float m, l;
  f32x4 a[2];
};
__device__ __forceinline__ f32x4 mfma(const v4u& a, const v4u& b, const f32x4& c) {
  return __builtin_amdgcn_mfma_f32_16x16x32_bf16(__builtin_bit_cast(bf16x8, a), __builtin_bit_cast(bf16x8, b), c, 0, 0, 0);
}
__device__ __forceinline__ float red_max(float v) { v = fmaxf(v, dpp<XOR1>(v)); v = fmaxf(v, dpp<XOR2>(v)); return x32_max(x16_max(v)); }

template <int NL, class ISSUE>
__device__ __forceinline__ float scores(v4u (&kb)[8], const v4u (&qb)[8], int lane, ISSUE issue) {
  f32x4 d = {0.f, 0.f, 0.f, 0.f};
#pragma unroll
  for (int i = 0; i < NL; ++i) {
    d = mfma(kb[i], qb[i], d);
    asm volatile("" : "+v"(d));
    issue(i);
    SB();
  }
  const bool odd = lane & 1;
  return rbf((odd ? d[3] : d[0]) + dpp<XOR1>(odd ? d[1] : d[2]));
}
template <class ISSUE>
__device__ __forceinline__ float phase_k4(v4u (&kb)[8], const v4u (&qb)[8], Att& st, int lane, ISSUE issue) {
  float s = scores<4>(kb, qb, lane, issue);
  s = (lane & 2) ? -__builtin_inff() : s;
  const float mn = fmaxf(st.m, red_max(s));
  const float al = __builtin_amdgcn_exp2f((st.m - mn) * EXP_C);
  const float p = __builtin_amdgcn_exp2f((s - mn) * EXP_C);
  st.m = mn; st.l = st.l * al + p;
  st.a[0] *= al; st.a[1] *= al;
  SB();
  return p;
}
template <int I, int O, class ISSUE>
__device__ __forceinline__ void pv_step(v4u (&vb)[8], unsigned pp, const v4u& pm, Att& st, ISSUE& issue) {
  const v4u pb = pm & (unsigned)__builtin_amdgcn_update_dpp(0, (int)pp, I * 0x55, 0xf, 0xf, true);
  st.a[0] = mfma(vb[O + 2 * I], pb, st.a[0]);
  asm volatile("" : "+v"(st.a[0]));
  issue(O + 2 * I);
  SB();
  st.a[1] = mfma(vb[O + 2 * I + 1], pb, st.a[1]);
  asm volatile("" : "+v"(st.a[1]));
  issue(O + 2 * I + 1);
  SB();
}
template <int NL, class ISSUE>
__device__ __forceinline__ void phase_v(v4u (&vb)[8], float p, const v4u& pm, Att& st, ISSUE issue) {
  constexpr int O = 8 - NL;
  const unsigned pp = __builtin_bit_cast(unsigned, (bf16x2){(__bf16)p, (__bf16)p});
  pv_step<0, O>(vb, pp, pm, st, issue); pv_step<1, O>(vb, pp, pm, st, issue);
  if constexpr (NL == 8) { pv_step<2, O>(vb, pp, pm, st, issue); pv_step<3, O>(vb, pp, pm, st, issue); }
}

template <int N, int GAP, int SL>
__device__ __forceinline__ v4u poll(const v4u& r, unsigned pa, const v4u& old) {
  v4u d;
  unsigned long long t0, t1, t2;
  int n = SPIN_CAP;
  asm volatile(
      "s_nop 4\n\t"
      "buffer_load_dwordx4 v[200:203], %5, %6, 0 offen sc1\n\t"
      ".rept %11\n\t"
      "s_sleep %12\n\t"
      "buffer_load_dwordx4 v[200:203], %5, %6, 0 offen sc1\n\t"
      ".endr\n"
      "1:\n\t"
      "s_waitcnt vmcnt(%11)\n\t"
      "v_cmp_eq_u32 vcc, v200, %7\n\t"
      "v_cmp_eq_u32 %1, v201, %8\n\t"
      "v_cmp_eq_u32 %2, v202, %9\n\t"
      "v_cmp_eq_u32 %3, v203, %10\n\t"
      "s_or_b64 vcc, vcc, %1\n\t"
      "s_or_b64 %2, %2, %3\n\t"
      "s_or_b64 exec, vcc, %2\n\t"
      "s_cbranch_scc0 2f\n\t"
      ".if %13\n\ts_sleep %13\n\t.endif\n\t"
      "buffer_load_dwordx4 v[200:203], %5, %6, 0 offen sc1\n\t"
      "s_sub_u32 %4, %4, 1\n\t"
      "s_cbranch_scc0 1b\n"
      "2:\n\t"
      "s_mov_b64 exec, -1"
      : "=&{v[200:203]}"(d), "=&s"(t0), "=&s"(t1), "=&s"(t2), "+s"(n)
      : "v"(pa), "s"(r), "v"(old[0]), "v"(old[1]), "v"(old[2]), "v"(old[3]), "n"(N - 1), "n"(GAP), "n"(SL)
      : "vcc", "scc", "memory");
  return d;
}
__device__ __forceinline__ void poll_end(const v4u& d) { asm volatile("s_waitcnt vmcnt(0)" :: "{v[200:203]}"(d)); }

#define STAMP(k) do { if ((k) == PB && (PC)) { SB(); tpb = (int)__builtin_amdgcn_s_memrealtime() + PO; SB(); } } while (0)
#define STAMP_OUT() do { for (int it_ = 0; it_ < 100000 && (int)__builtin_amdgcn_s_memrealtime() - tpb < 0; ++it_) __builtin_amdgcn_s_sleep(1); } while (0)

__device__ __forceinline__ f32x2 ln_stats(const v4u& xa, int lane) {
  f32x2 xv[4];
  float sm = 0.f;
#pragma unroll
  for (int j = 0; j < 4; ++j) { xv[j] = unp(xa[j]); sm += xv[j].x + xv[j].y; }
  const float mean = wave_sum(sm) * (1.0f / D);
  float sq = 0.f;
#pragma unroll
  for (int j = 0; j < 4; ++j) { const f32x2 d = xv[j] - mean; sq += d.x * d.x + d.y * d.y; }
  sq = lane < 56 ? sq : 0.f;
  const float var = rbf(wave_sum(sq) * (1.0f / D));
  const float sd = rbf(sqrtf(rbf(var + 1e-5f)));
  return (f32x2){rbf(mean), 1.0f / sd};
}
__device__ __forceinline__ float ln_apply(float x, f32x2 st) { return rbf(rbf(x - st.x) * st.y); }

__global__ void __launch_bounds__(NT, 2)
fused_kernel(const u16* __restrict__ xg, const u16* __restrict__ kc, const u16* __restrict__ vc, const u16* __restrict__ wq,
             const u16* __restrict__ wo, const u16* __restrict__ wg, const u16* __restrict__ wu, const u16* __restrict__ wd,
             u16* __restrict__ out, char* ws) {
  __shared__ __attribute__((aligned(16))) float s_part[8][512];
  __shared__ __attribute__((aligned(16))) float s_mv[8][64];
  __shared__ __attribute__((aligned(16))) unsigned s_q[256];
  __shared__ __attribute__((aligned(16))) unsigned s_qs[64];
  __shared__ __attribute__((aligned(16))) unsigned s_old[8][256];
  __shared__ __attribute__((aligned(16))) unsigned s_dst[8][256];
  __shared__ __attribute__((aligned(16))) float s_ml[2][16];
  __shared__ __attribute__((aligned(16))) float s_o[4][32];
  __shared__ __attribute__((aligned(16))) float s_ml2[16];
  __shared__ __attribute__((aligned(16))) unsigned s_xn[256];
  __shared__ __attribute__((aligned(16))) float s_h[32];
  __shared__ __attribute__((aligned(16))) float s_r[9][16];
  __shared__ __attribute__((aligned(16))) float s_sc[64][32];
  __shared__ int s_ctl[64];
  const int tid = threadIdx.x, lane = tid & 63, w = __builtin_amdgcn_readfirstlane(tid >> 6);
  const int bi = SWP == 2 ? (blockIdx.x & ~7) | ((blockIdx.x + 7) & 7) : blockIdx.x ^ SWP, h = (((bi & 1) ? bi + 5 : bi) & 6) | (bi >> 7), s = ((bi >> 3) & 15) * 2 + (bi & 1);
  const int b = h * 32 + s;
  static_assert(8 * TQ + 8 * TF + (YS - 16) * TFY + (32 - YS) * TY == 4096 && TQ - NP > 80 && TF - NP > 80 && TFY - NP > 80 && TY - NP > 80, "tickets");
  static_assert(TQ - NP <= 128 && TF - NP <= 128 && TFY - NP <= 128 && TY - NP <= 128, "tickets");
  const int nt = s < 8 ? TQ : s < 16 ? TF : s < YS ? TFY : TY, nk = (nt - NP) >> 1;
  const int first = s * TY - min(s, YS) * (TY - TFY) - min(s, 16) * (TFY - TF) - min(s, 8) * (TF - TQ);
  asm volatile("" :: "s"(xg), "s"(kc), "s"(vc), "s"(wq), "s"(wo), "s"(wg), "s"(wu), "s"(wd), "s"(out), "s"(ws));
  int tpb = (int)__builtin_amdgcn_s_memrealtime();
  STAMP(0);
  const rsrc_t rx = mkrs(xg, D * 2);
  const rsrc_t rk = mkrs(kc + (size_t)h * SK * DH, SK * DH * 2);
  const rsrc_t rv = mkrs(vc + (size_t)h * SK * DH, SK * DH * 2);
  const rsrc_t rws = mkrs(ws, WS_END);
  const unsigned l16 = lane * 16;
  auto ubase = [&](int u) -> unsigned { return (unsigned)(first + 2 * u) * 4096u; };
  const unsigned kvo = ((lane >> 1) & 7) * 512 + (lane >> 4) * 32 + (lane & 1) * 16, vvo = (lane >> 4) * 1024 + (lane & 15) * 16;
  auto ldk = [&](unsigned base, int i) { return ld4<2>(rk, kvo + (i & 3) * 128, base + (i >> 2) * 4096); };
  const unsigned area = (unsigned)b * AREA;
  auto area_of = [&](int ss) -> unsigned { return (unsigned)(h * 32 + ss) * AREA; };
  const unsigned lmask = lane < 56 ? 0u : OOB;

  const bool isq = s < 8, isf = s < 14, isy = s >= 16;
  const int f = h * 14 + s, y = h * 16 + s - 16, ro = h * 4 + (s >> 2);
  const bool isr = s < 16 && ((s + h) & 3) == 0 && ro < 28;
  const bool tile = isq;
  const bool leader = (!(NOHS & 1) && b == NWG - 1 && w == 1), member = (w == 0);

  const unsigned pa = isy ? (w < 4 ? area + A_O1 + w * 1024 + l16 : (w == 7 && y < 112) ? area + A_F + l16 : OOB) : isr ? area + A_Y2 + w * 1024 + l16 : OOB;
  const unsigned mo_ = area_of(16 + (lane & 8) + (lane >> 4)) + A_O1 + s * 128 + (lane & 7) * 16;
  auto mf = [&](int p) -> unsigned {
    const int fd = (f & ~3) + ((p >> 2) & 3);
    return (unsigned)((fd >> 4) * 32 + 16 + (fd & 15)) * AREA + A_F + (f & 3) * 256 + (p >> 4) * 64 + (p & 3) * 16;
  };
  auto my = [&](int p) -> unsigned { return (unsigned)((p >> 4) * 32 + (p & 12) + ((-(p >> 4)) & 3)) * AREA + A_Y2 + y * 64 + (p & 3) * 16; };
  v4u eo, ed;
  auto da_of = [&]() {
    const int p = (w - 2) * 64 + lane;
    unsigned da = (isy && p < 112) ? my(p) : OOB;
    return (w & 6) == 2 ? da : (w & 6) == 4 ? mo_ + (w - 4) * 4 * AREA : (w == 6 && isf) ? mf(lane) : OOB;
  };
  if (EO == 1) { eo = ld4<OA>(rws, pa, 0); SB(); }
  if (EO == 3 || (EO == 4 && !tile)) { eo = ld4<OA>(rws, pa, 0); ed = ld4<OB>(rws, da_of(), 0); SB(); }
  if (EO == 5) { ed = ld4<OB>(rws, da_of(), 0); SB(); }
  const v4u sent = {SENT, SENT, SENT, SENT};
  unsigned poll_off = lane == 1 ? WS_GO : lane < 4 ? WS_T - 32 + l16 : OOB;
  poll_off = lane == 0 ? WS_REQ + (b >> 2) * 16 : poll_off;
  poll_off = leader ? (ES ? lane * 4 * AREA + (lane & 3) * 256 : WS_ECHO + l16) : poll_off;
  auto echoes = [&](unsigned seen) {
    v4u r;
#pragma unroll
    for (int j = 0; j < 4; ++j) r[j] = __builtin_amdgcn_raw_buffer_load_b32(rws, (int)(poll_off | (seen >> j << 31)), j * AREA, 16);
    return r;
  };
  v4u pv = {0u, 0u, 0u, 0u}, e1 = {0u, 0u, 0u, 0u};
  unsigned go_ref = 0;
  int* const ctl = &s_ctl[lane];
  auto ctl_get = [&](int i) { return __builtin_amdgcn_readfirstlane(__hip_atomic_load(&s_ctl[i], __ATOMIC_RELAXED, __HIP_MEMORY_SCOPE_WORKGROUP)); };
  auto ctl_set = [&](int i) { __hip_atomic_store(&s_ctl[i], 1, __ATOMIC_RELAXED, __HIP_MEMORY_SCOPE_WORKGROUP); };
  const unsigned qoff = WS_Q + h * 2048 + l16;
  v4u qv0, qv1;
  if (member) {
    qv0 = ld4<16>(rws, qoff, 0);
    qv1 = ld4<16>(rws, qoff, 1024);
    SB();
    int c = lane == C_NEXT ? (isq ? TOFFQ : TOFF) * 128 : 0;
    c = lane == C_TICK ? 40 : c;
    c = lane == C_T0 ? (int)(__builtin_amdgcn_s_memrealtime() & 0x7fffffff) : c;
    __hip_atomic_store(ctl, c, __ATOMIC_RELAXED, __HIP_MEMORY_SCOPE_WORKGROUP);
    st1c(c, rws, (lane == C_T0 && (bi >> 3) == 12) ? WS_T + (bi & 7) * 4 : OOB);
  }
  *reinterpret_cast<v4u*>(&s_sc[w * 8][lane * 4]) = sent;
  int t0 = 0;
  auto now = [&]() __attribute__((always_inline)) { return ((int)__builtin_amdgcn_s_memrealtime() - t0) & 0x7fffff; };
  const int neg = lane == C_NEXT ? 0 : (int)0x80000000;
  const int xcc = __builtin_amdgcn_s_getreg(20 | (3 << 11));
  constexpr auto tkf = [](int t, int sh) { return TAU + sh * 128 / ((t - NP) / 2 - 8); };
  constexpr auto tvf = [](int t, int sh) { return TAU - sh * 128 / ((t - NP) / 2 + 8 - PL); };
  int tk = s < 8 ? tkf(TQ + 16, SQ) : s < 16 ? tkf(TF, SF_) : s < YS ? tkf(TFY, SFY) : tkf(TY, SY);
  int tv = s < 8 ? tvf(TQ, SQ) : s < 16 ? tvf(TF, SF_) : s < YS ? tvf(TFY, SFY) : tvf(TY, SY);
  int inc = lane == C_NEXT ? tk : lane == C_CNT ? 1 : 0;
  int lost = 0;
  auto token = [&](int burst, bool early = false) __attribute__((always_inline)) {
    const int t = max(now() * 128 - burst, 0);
    if (GB && early) {
      const int o = __hip_atomic_fetch_max(ctl, t | neg, __ATOMIC_RELAXED, __HIP_MEMORY_SCOPE_WORKGROUP);
      lost += max(o, t | neg) - o;
    } else {
      __hip_atomic_fetch_max(ctl, t | neg, __ATOMIC_RELAXED, __HIP_MEMORY_SCOPE_WORKGROUP);
    }
    return __hip_atomic_fetch_add(ctl, inc, __ATOMIC_RELAXED, __HIP_MEMORY_SCOPE_WORKGROUP);
  };
  auto due_of = [&](int x) { return __builtin_amdgcn_readlane(x, C_NEXT) >> 7; };
  auto until = [&](int due) __attribute__((always_inline)) {
#pragma unroll 1
    for (int it = 0; it < 4096 && now() < due; ++it) __builtin_amdgcn_s_sleep(1);
    SB();
  };
  unsigned qs0, qs1;
  auto q_there = [&]() { return __builtin_amdgcn_ballot_w64(((lowb(qv0) ^ qs0) & (lowb(qv1) ^ qs1)) != 0xFFFFFFFFu) == 0; };
  auto load_q = [&]() { qv0 = ld4<16>(rws, qoff, 0); qv1 = ld4<16>(rws, qoff, 1024); };
  auto publish_q = [&]() {
    *reinterpret_cast<v2u*>(&s_q[lane * 2]) = (v2u){__builtin_amdgcn_perm(qv0[1], qv0[0], 0x07060302u), __builtin_amdgcn_perm(qv0[3], qv0[2], 0x07060302u)};
    *reinterpret_cast<v2u*>(&s_q[128 + lane * 2]) = (v2u){__builtin_amdgcn_perm(qv1[1], qv1[0], 0x07060302u), __builtin_amdgcn_perm(qv1[3], qv1[2], 0x07060302u)};
    LDS_FENCE();
    __builtin_amdgcn_s_waitcnt(0xC07F);
    ctl_set(C_Q);
  };
  v4u pre[4][8], ring[8];
  auto unit0 = [&]() {
#pragma unroll
    for (int i = 0; i < 8; ++i) { pre[0][i] = ldk(ubase(w), i); SB(); }
    if (EO == 2) { eo = ld4<OA>(rws, pa, 0); SB(); }
  };
  SB();
  if (tile) {
    v4u tw[8];
    const int oct = 7 * w;
    constexpr int stride = NQ * 2;
    const rsrc_t rt = mkrs(wq, D * stride);
    const unsigned to = (unsigned)((oct + (lane >> 3)) * 8 * stride + (h * 8 + s) * 128 + (lane & 7) * 16) | lmask;
    const v4u xa = ld4<0>(rx, l16, 0);
    const v4u xb = ld4<0>(rx, (unsigned)(oct + (lane >> 3)) * 16 | lmask, 0);
    SB();
#pragma unroll
    for (int i = 0; i < 8; ++i) { tw[i] = ld4<2>(rt, to, i * stride); SB(); }
    if (EO == 4) { eo = ld4<OA>(rws, pa, 0); ed = ld4<OB>(rws, da_of(), 0); SB(); }
    LDS_FENCE(); bar(); LDS_FENCE();
    t0 = ctl_get(C_T0);
    SB();
    const f32x2 ls = ln_stats(xa, lane);
    f32x2 acc[4];
#pragma unroll
    for (int i = 0; i < 8; ++i) {
      const float xn = ln_apply(asf((i & 1) ? (xb[i >> 1] & 0xffff0000u) : (xb[i >> 1] << 16)), ls);
#pragma unroll
      for (int j = 0; j < 4; ++j) acc[j] = i ? acc[j] + unp(tw[i][j]) * xn : unp(tw[i][j]) * xn;
    }
    float y0 = tr32(acc[0].x, acc[2].x), y1 = tr32(acc[0].y, acc[2].y), y2 = tr32(acc[1].x, acc[3].x), y3 = tr32(acc[1].y, acc[3].y);
    y0 = tr16(y0, y2); y1 = tr16(y1, y3);
    const bool s_ = lane & 8;
    y0 = (s_ ? y1 : y0) + dpp<ROR8>(s_ ? y0 : y1);
    s_mv[w][(lane & 7) * 8 + (lane >> 3)] = y0;
    STAMP(6);
    token(QBURST);
    unit0();
    LDS_FENCE(); bar(); LDS_FENCE();
    if (member) {
      float a = 0.f;
#pragma unroll
      for (int k = 0; k < 8; ++k) a += s_mv[k][lane];
      s_qs[lane] = f2bf(a) << 16;
      LDS_FENCE();
      const v4u qs = *reinterpret_cast<const v4u*>(&s_qs[(lane & 15) * 4]) | (~((s & 4) ? qv1 : qv0) & 0xffffu);
      st4c(qs, rws, (lane >> 4) == (s & 3) ? qoff + (s >> 2) * 1024 : OOB);
    }
  } else if (leader && !LU) {
    e1 = ES ? echoes(0) : ld4<16>(rws, poll_off, 0);
    go_ref = __builtin_amdgcn_raw_buffer_load_b32(rws, (int)WS_GO, 0, 16);
  }
  SB();
  STAMP(5);
  SB();
  const u16* wa = isy ? wo + (size_t)y * 32 * D : wg;
  const u16* wb = isy ? wd + (size_t)(y < 112 ? (y >> 1) * 32 + (y & 1) * 4 : 0) * D : wu;
  asm volatile("" : "+s"(wa), "+s"(wb));
  bool ok = !member;
  static_for<0, 40>([&](auto N) {
    constexpr int n = N;
    if constexpr (n == 0) {
      if (!tile) unit0();
      if (LU && leader) {
        e1 = echoes(0);
        go_ref = __builtin_amdgcn_raw_buffer_load_b32(rws, (int)WS_GO, 0, 16);
      }
    }
    if constexpr (n == 8) {
      if (!tile) {
        LDS_FENCE(); bar(); LDS_FENCE();
        t0 = ctl_get(C_T0);
      }
    }
    if constexpr (n == LP) {
      if (leader) {
        e1 += 1u;
        go_ref += 1u;
        st4c(e1, rws, WS_REQ + l16);
      }
    }
    if constexpr (n > 0 && n % 8 == 0) {
      const int x = token((isq || (LB && b == NWG - 1)) ? QBURST : PBURST, true);
      if constexpr (n > QFIRST) {
        if (!ok) {
          ok = q_there();
          if (ok) publish_q();
        }
      }
      if constexpr (n == OPOS + 16) {
        *reinterpret_cast<v4u*>(&s_old[w][lane * 4]) = (int)pa < 0 ? sent : eo;
        *reinterpret_cast<v4u*>(&s_dst[w][lane * 4]) = ~ed;
        LDS_FENCE();
        __hip_atomic_fetch_add(ctl, lane == C_OLD ? 1 : 0, __ATOMIC_RELAXED, __HIP_MEMORY_SCOPE_WORKGROUP);
      }
      SB();
      until(due_of(x));
      if constexpr (n == 8) {
        if (member) { qs0 = lowb(qv0); qs1 = lowb(qv1); }
      }
      if constexpr (n >= QFIRST) {
        if (!ok) load_q();
      }
      if constexpr (n == OPOS) {
        if (EO == 0 || EO == 5) eo = ld4<OA>(rws, pa, 0);
        if (EO < 3) ed = ld4<OB>(rws, da_of(), 0);
      }
      SB();
    }
    if constexpr (n == QPOS) {
      STAMP(9);
      if (ES && leader) pv = LD ? ~e1 : echoes(0);
      else pv = ld4<16>(rws, (member || leader) ? poll_off : OOB, 0);
      SB();
    }
    if constexpr (n >= 8) {
      if constexpr (n >= PACE) WAITV(PACE + ((n >= QPOS && n < QPOS + PACE) ? 1 : 0) + ((n >= OPOS && n < OPOS + PACE) ? (EO == 0 ? 2 : (EO == 3 || EO == 4) ? 0 : 1) : 0));
      if constexpr (n < 32) pre[n >> 3][n & 7] = ldk(ubase((n & 56) + w), n & 7); else ring[n & 7] = ldk(ubase(32 + w), n & 7);
    }
    SB();
    if constexpr (n - PACE == 7 || n - PACE == 15 || n - PACE == 23 || n - PACE == 31) STAMP(16 + (n - PACE) / 8);
  });
  inc = lane == C_TICK ? 1 : inc;
  if (GB == 1) __hip_atomic_fetch_add(ctl, -lost, __ATOMIC_RELAXED, __HIP_MEMORY_SCOPE_WORKGROUP);
  int tok = token(KBURST);
  STAMP(10);
  bool svc = member || leader;
  unsigned seenm = 0, last_echo = 0;
  bool echoed = false;
  auto service = [&]() {
    if (leader) {
      seenm |= (pv[0] == e1[0] ? 1u : 0u) | (pv[1] == e1[1] ? 2u : 0u) | (pv[2] == e1[2] ? 4u : 0u) | (pv[3] == e1[3] ? 8u : 0u);
      svc = __builtin_amdgcn_ballot_w64(seenm != 15u) != 0;
      st1c(go_ref, rws, (lane == 0 && !svc) ? WS_GO : OOB);
    } else {
      const unsigned req = (unsigned)__builtin_amdgcn_readfirstlane((int)((b & 2) ? ((b & 1) ? pv[3] : pv[2]) : ((b & 1) ? pv[1] : pv[0])));
      const unsigned go = (unsigned)__builtin_amdgcn_readlane((int)pv[0], 1);
      go_ref = echoed ? go_ref : go;
      svc = go == (unsigned)__builtin_amdgcn_readfirstlane((int)go_ref);
      if (NOHS & 1) svc = false;
      const bool rdy = ctl_get(C_OLD) == 8;
      if (ES && EL > 0) st4c((v4u){req, req, req, req}, rws, (lane < EL && svc && rdy && !(echoed && req == last_echo)) ? area + ((b >> 2) & 3) * 256 + l16 : OOB);
      else st1c(req, rws, (lane == 0 && svc && rdy && !(echoed && req == last_echo)) ? (ES ? area + ((b >> 2) & 3) * 256 : WS_ECHO + b * 4) : OOB);
      echoed |= rdy; last_echo = req;
    }
    if (ES && leader) pv = echoes(seenm);
    else pv = ld4<16>(rws, svc ? poll_off : OOB, 0);
    SB();
  };

  {
    if (!ok) {
      ok = q_there();
      if (ok) publish_q();
    }
#pragma unroll 1
    for (int it = 0; it < SPIN_CAP && !ctl_get(C_Q); ++it) {
      if (svc && it >= QTRIES) { service(); asm volatile("" :: "v"(pv)); }
      if (!ok) {
        load_q();
        ok = q_there() | !svc;
        if (ok) publish_q();
      } else {
        __builtin_amdgcn_s_sleep(1);
      }
    }
  }
  LDS_FENCE();
  v4u qb[8];
#pragma unroll
  for (int i = 0; i < 4; ++i) {
    const v4u z = {0u, 0u, 0u, 0u}, q = *reinterpret_cast<const v4u*>(&s_q[(lane & 8) * 16 + i * 32 + (lane >> 4) * 8 + (lane & 1) * 4]);
    qb[i] = (lane & 2) ? z : q; qb[4 + i] = (lane & 2) ? q : z;
  }
  v4u pm;
#pragma unroll
  for (int j = 0; j < 4; ++j) pm[j] = ((lane >> 1) & 3) == j ? ((lane & 1) ? 0xffff0000u : 0xffffu) : 0u;
  STAMP(1);
  SB();

  Att st;
  int sx = 20;
  int rank;
  lds4_t pw;
  lds1_t pml;
  int idle;
  auto last = [&]() __attribute__((always_inline)) {
    pml = (lds1_t)&s_ml[(lane >> 3) & 1][rank];
    pw = (lds4_t)&s_part[rank][lane * 4];
    idle = __builtin_amdgcn_readfirstlane(!isy && !svc && rank >= PM && (unsigned)(rank - SR) >= 2 && (!isf || (unsigned)(rank - FO) >= 4));
    asm volatile("" : "+v"(pw), "+v"(pml), "+s"(idle));
    SB();
  };
  auto put_ml = [&]() __attribute__((always_inline)) {
    float l = st.l;
    l += dpp<XOR1>(l); l += dpp<XOR2>(l); l = x32_add(x16_add(l));
    pml[0] = st.m; pml[8] = l;
    SB();
  };
  {
    float* const sc = &s_sc[0][(lane >> 3) * 4 + (lane & 3)];
    rsrc_t nra, nrb;
    unsigned nva, nvb, nba, nbb, nsa, nsb;
    int kind = 0, nu = 32 + w, cu;
    auto next = [&]() __attribute__((always_inline)) {
      cu = nu;
      const int t = __builtin_amdgcn_readlane(tok, C_TICK);
      const bool fx = t < 2 * nk, tw = t >= nt;
      kind = fx ? (t >= nk ? 1 : 0) : tw ? 3 : 2;
      nu = t >= nk ? t - nk : t;
      nba = (unsigned)(fx ? first + 2 * nu : first + t) * 4096u;
      nbb = fx ? nba + 4096 : nba;
      nra = selr(kind == 1, rv, rk);
      nrb = selr(kind == 0, rk, rv);
      nva = selu(kind == 1, vvo, kvo); nvb = selu(kind == 0, kvo, vvo);
      nsa = selu(kind == 1, 256, 128); nsb = selu(kind == 0, 128, 256);
      const int u = t + 8 - nk;
      inc = lane == C_NEXT ? (t + PL >= 2 * nk ? PP : (int)selu(u >= 0, tv, tk) + ((unsigned)(u + XA) < XA + XB ? TX : 0)) : inc;
      rank = t - nt;
      if (!tw) until(due_of(tok));
      SB();
    };
    auto ldn = [&](int i) { ring[i] = i < 4 ? ld4<2>(nra, nva, nba + i * nsa) : ld4<2>(nrb, nvb, nbb + (i - 4) * nsb); };
    float mx;
    if (member) {
      const v4u d = pv - (unsigned)t0;
      const bool bad = max(max(d[0] + 512u, d[1] + 512u), max(d[2] + 512u, d[3] + 512u)) > 1024u;
      const int sm = (int)((d[0] + d[1]) + (d[2] + d[3]));
      const int lead = (__builtin_amdgcn_ballot_w64(bad) & 12) ? 0 : __builtin_amdgcn_readlane(sm, 2) + __builtin_amdgcn_readlane(sm, 3) + DB;
      const int slip = __builtin_amdgcn_readlane(tok, C_NEXT) - __builtin_amdgcn_readlane(tok, C_CNT) * tk - (isq ? TOFFQ : TOFF + SL0) * 128;
      const int adj = ((lead * DK) >> 5) - ((slip * SKF) >> 10);
      tk += adj; tv += adj;
      __hip_atomic_fetch_add(ctl, lane == C_LEAD ? (lead * DG) >> 5 : (lane == C_NEXT ? (lead * DS) >> 5 : 0), __ATOMIC_RELAXED, __HIP_MEMORY_SCOPE_WORKGROUP);
    }
    if (GB == 2) __hip_atomic_fetch_add(ctl, -lost, __ATOMIC_RELAXED, __HIP_MEMORY_SCOPE_WORKGROUP);
    if (svc && !(LD && leader)) service();
    {
      const float s0 = scores<8>(pre[0], qb, lane, [&](int) {});
      const float s1 = scores<8>(pre[1], qb, lane, [&](int) {});
      const float s2 = scores<8>(pre[2], qb, lane, [&](int) {});
      const float s3 = scores<8>(pre[3], qb, lane, [&](int) {});
      sc[w * 32] = s0; sc[(8 + w) * 32] = s1; sc[(16 + w) * 32] = s2; sc[(24 + w) * 32] = s3;
      mx = fmaxf(fmaxf(s0, s1), fmaxf(s2, s3));
      SB();
      STAMP(11);
    }
    const v4u xa2 = ld4<0>(rx, (isf && w == 7) ? l16 : OOB, 0);
    SB();
#pragma unroll 1
    while (kind == 0) {
      next();
      const float sv = scores<8>(ring, qb, lane, ldn);
      sc[cu * 32] = sv;
      tok = token(KBURST);
      mx = fmaxf(mx, sv);
      STAMP(sx); ++sx;
      if (svc && !(LD && leader)) service();
      SB();
    }
    STAMP(7);
    if (GB == 3) __hip_atomic_fetch_add(ctl, -lost, __ATOMIC_RELAXED, __HIP_MEMORY_SCOPE_WORKGROUP);
    if (LD == 1 && leader) { pv = echoes(seenm); SB(); }
    if (isf && w == 7) {
      const f32x2 ls = ln_stats(xa2, lane);
      v4u xn;
#pragma unroll
      for (int j = 0; j < 4; ++j) {
        const f32x2 v = unp(xa2[j]);
        xn[j] = __builtin_amdgcn_perm(asu(ln_apply(v.y, ls)), asu(ln_apply(v.x, ls)), 0x07060302u);
      }
      *reinterpret_cast<v4u*>(&s_xn[lane * 4]) = xn;
      LDS_FENCE();
      ctl_set(C_FC);
      SB();
    }
    st.m = red_max(mx); st.l = 0.f;
    st.a[0] = st.a[1] = (f32x4){0.f, 0.f, 0.f, 0.f};
#pragma unroll 1
    while (kind == 1) {
      float sv;
      for (;;) {
        sv = __hip_atomic_load(&sc[nu * 32], __ATOMIC_RELAXED, __HIP_MEMORY_SCOPE_WORKGROUP);
        if (__builtin_amdgcn_ballot_w64(asu(sv) == SENT) == 0) break;
        __builtin_amdgcn_s_sleep(1);
      }
      if (__builtin_amdgcn_ballot_w64(sv > st.m) != 0) {
        const float m2 = fmaxf(st.m, red_max(sv));
        const float al = __builtin_amdgcn_exp2f((st.m - m2) * EXP_C);
        st.m = m2; st.l *= al;
        st.a[0] *= al; st.a[1] *= al;
      }
      const float p = __builtin_amdgcn_exp2f((sv - st.m) * EXP_C);
      st.l += p;
      SB();
      next();
      if (kind == 3) {
        last();
        put_ml();
        phase_v<8>(ring, p, pm, st, [&](int) {});
        break;
      }
      phase_v<8>(ring, p, pm, st, ldn);
      tok = token(VBURST);
      STAMP(sx); ++sx;
      if (svc) service();
      SB();
    }
    STAMP(8);
    if (kind == 2) {
      next();
#pragma unroll 1
    while (kind != 3) {
      if (UF) { nba = __builtin_amdgcn_readfirstlane(nba); nbb = __builtin_amdgcn_readfirstlane(nbb); nsa = __builtin_amdgcn_readfirstlane(nsa); nsb = __builtin_amdgcn_readfirstlane(nsb); }
      const float p = phase_k4(ring, qb, st, lane, ldn);
      phase_v<4>(ring, p, pm, st, ldn);
      tok = token(VBURST);
      STAMP(sx); ++sx;
      SB();
      next();
    }
      SB();
      last();
      const float p = phase_k4(ring, qb, st, lane, [&](int) {});
      phase_v<4>(ring, p, pm, st, [&](int) {});
      put_ml();
    }
    pw[0] = st.a[0]; pw[64] = st.a[1];
    LDS_FENCE();
    SB();
    STAMP(12);
    if (idle) {
      LDS_FENCE(); bar(); LDS_FENCE();
      STAMP(2);
    }
    SB();
#pragma unroll 1
    for (int it = 0; it < SPIN_CAP && svc; ++it) {
      service();
      asm volatile("" :: "v"(pv));
    }
    LDS_FENCE();
    WAITV(0);
  }
  const lds4_t pr = (lds4_t)&s_part[0][lane * 4], pmr = (lds4_t)&s_ml[(lane >> 3) & 1][0];
  if (rank < PM) __hip_atomic_fetch_add(ctl, lane == C_DONE ? 1 : 0, __ATOMIC_RELAXED, __HIP_MEMORY_SCOPE_WORKGROUP);
  SB();
  const v4u rw4 = {(unsigned)(size_t)ws, (unsigned)((size_t)ws >> 32), WS_END, 0x00020000u};
  const unsigned fj = isf ? rank - FO : 4;
  auto clear = [&](int k, bool more) __attribute__((always_inline)) {
#pragma unroll 1
    for (int it = 0; it < SPIN_CAP; ++it) {
      const int x = __hip_atomic_load(ctl, __ATOMIC_RELAXED, __HIP_MEMORY_SCOPE_WORKGROUP);
      if (__builtin_amdgcn_readlane(x, C_TICK) >= nt + 8 - k && (!more || (__builtin_amdgcn_readlane(x, C_FC) && __builtin_amdgcn_readlane(x, C_OLD) == 8))) break;
      __builtin_amdgcn_s_sleep(1);
    }
    LDS_FENCE();
    SB();
  };
  const unsigned half = isy ? w : rank - SR;
  if (isy && w >= 4) {
    v4u wt[6][2];
    const rsrc_t rwo = mkrs(wa, XW ? 0 : 32 * D * 2);
    const rsrc_t rwd = mkrs(wb, (y < 112 && !XW) ? 28 * D * 2 : 0);
    unsigned o0 = (lane >> 3) * (D * 2) + (w - 4) * 256 + (lane & 7) * 16, o1 = w == 7 ? OOB : o0 + 128;
    unsigned o2 = o0 + (lane >> 5) * (4 * D * 2), o3 = o1 + (lane >> 5) * (4 * D * 2);
    asm volatile("" : "+v"(o0), "+v"(o1), "+v"(o2), "+v"(o3));
    SB();
    auto ldw = [&](int k) __attribute__((always_inline)) {
      const int i = (k + 4) % 6;
      wt[i][0] = i < 4 ? ld4<2>(rwo, o0, i * 8 * D * 2) : ld4<2>(rwd, o2, (i & 3) * 16 * D * 2);
      wt[i][1] = i < 4 ? ld4<2>(rwo, o1, i * 8 * D * 2) : ld4<2>(rwd, o3, (i & 3) * 16 * D * 2);
      SB();
    };
    if (YE) {
      clear(YK, false);
#pragma unroll
      for (int k = 0; k < YE; ++k) ldw(k);
    }
    LDS_FENCE(); bar(); LDS_FENCE();
    STAMP(2);
    if (YB) __builtin_amdgcn_s_barrier();
    if (YD) __builtin_amdgcn_s_sleep(YD);
    SB();
#pragma unroll
    for (int k = YE; k < 6; ++k) ldw(k);
    const unsigned spare = __builtin_amdgcn_raw_buffer_load_b32(rwo, (int)OOB, 0, 0);
    SB();
    const int p = (w - 4) * 32 + (lane >> 4) * 8 + (lane & 7);
    v4u dt = *reinterpret_cast<const v4u*>(&s_dst[2][p * 4]) & 1u;
    unsigned to = ((lane & 8) || p >= 112) ? OOB : my(p);
    f32x4 acc[2] = {{0.f, 0.f, 0.f, 0.f}, {0.f, 0.f, 0.f, 0.f}};
    auto bop = [&](float a) -> v4u { return pm & __builtin_bit_cast(unsigned, (bf16x2){(__bf16)a, (__bf16)a}); };
    auto step = [&](int i, const v4u& pb) { acc[0] = mfma(wt[i][0], pb, acc[0]); acc[1] = mfma(wt[i][1], pb, acc[1]); };
    lds4_t po = (lds4_t)&s_o[0][(lane >> 3) * 4];
    lds1_t ph = (lds1_t)&s_h[(lane & 4) * 4 + ((lane >> 4) & 1) * 8 + (lane & 3) * 2 + (lane >> 5)];
    asm volatile("" : "+v"(dt), "+v"(to), "+v"(po), "+v"(ph));
    SB();
    const v4u old = *reinterpret_cast<const v4u*>(&s_old[7][lane * 4]);
    v4u dr = old;
    if (w == 7) {
      if (G3) until(G3 + __builtin_amdgcn_readlane(tok, C_LEAD));
      dr = poll<PD3, PG3, 0>(rw4, pa, old);
      const v4u d = dr & ~1u;
      float g = tr16(tr32(asf(d[0]), asf(d[2])), tr32(asf(d[1]), asf(d[3])));
      g = rbf(g + dpp<ROR8>(g));
      const float u = dpp<ROR4>(g);
      *ph = rbf(g / (1.0f + __expf(-g))) * u;
      SB();
    }
    STAMP(14);
    LDS_FENCE(); bar(); LDS_FENCE();
    STAMP(3);
    const f32x2 hv = *reinterpret_cast<const f32x2*>(&s_h[(lane >> 3) * 2]);
    const f32x4 ma = *reinterpret_cast<const f32x4*>(&s_ml2[0]), mb = *reinterpret_cast<const f32x4*>(&s_ml2[4]);
    f32x4 pq[4];
#pragma unroll
    for (int k = 0; k < 4; ++k) pq[k] = po[k * 8];
    SB();
    const v4u h0 = bop(hv.x), h1 = bop(hv.y);
    {
      step(4, h0); step(5, h1);
      asm volatile("" : "+v"(acc[0]), "+v"(acc[1]));
      SB();
    }
    const float M = fmx(fmx(fmx(mb[0], mb[1]), mb[2]), mb[3]);
    f32x4 e = (mb - M) * EXP_C;
#pragma unroll
    for (int k = 0; k < 4; ++k) e[k] = __builtin_amdgcn_exp2f(e[k]);
    const float inv = __builtin_amdgcn_rcpf(__builtin_fmaf(ma[0], e[0], ma[1] * e[1]) + __builtin_fmaf(ma[2], e[2], ma[3] * e[3]));
    f32x2 av[2];
#pragma unroll
    for (int j = 0; j < 2; ++j) av[j] = (f32x2){pq[0][2 * j], pq[0][2 * j + 1]} * e[0];
#pragma unroll
    for (int k = 1; k < 4; ++k) {
#pragma unroll
      for (int j = 0; j < 2; ++j) av[j] = __builtin_elementwise_fma((f32x2){pq[k][2 * j], pq[k][2 * j + 1]}, (f32x2){e[k], e[k]}, av[j]);
    }
    av[0] *= inv; av[1] *= inv;
    {
      v4u pb[4];
#pragma unroll
      for (int i = 0; i < 4; ++i) pb[i] = bop(av[i >> 1][i & 1]);
      if (FB) { asm volatile("" : "+v"(pb[0]), "+v"(pb[1]), "+v"(pb[2]), "+v"(pb[3])); SB(); }
#pragma unroll
      for (int i = 0; i < 4; ++i) step(i, pb[i]);
    }
    f32x4 z;
#pragma unroll
    for (int v = 0; v < 4; ++v) {
      const float a0 = asf((unsigned)__builtin_amdgcn_update_dpp((int)asu(acc[0][v]), (int)asu(acc[0][v]), ROR8, 0xc, 0xf, false));
      const float a1 = asf((unsigned)__builtin_amdgcn_update_dpp((int)asu(acc[1][v]), (int)asu(acc[1][v]), ROR8, 0xc, 0xf, false));
      z[v] = tr32(a0, a1);
    }
    __builtin_amdgcn_raw_buffer_store_b128(tag4(z, dt), rws, (int)to, 0, S2);
    SB();
    STAMP(4);
    asm volatile("" :: "{v198}"(spare));
    poll_end(dr);
    STAMP_OUT();
    __builtin_amdgcn_endpgm();
  }
  if (half < 2) {
    ldsu_t tp = (ldsu_t)&s_dst[4 + half][lane * 4];
    unsigned to = mo_ + half * 4 * AREA;
    lds4_t pp = pr + half * 64;
    asm volatile("" : "+v"(tp), "+v"(to), "+v"(pp));
    SB();
    f32x4 ml[4], pt[8], o = {0.f, 0.f, 0.f, 0.f};
    float M = -3.0e38f, L = 0.f;
    if (PM) {
#pragma unroll 1
      for (int it = 0; it < SPIN_CAP && ctl_get(C_DONE) < PM; ++it) {}
      LDS_FENCE();
#pragma unroll
      for (int k = 0; k < 4; ++k) if ((k & 1) * 4 < PM) ml[k] = pmr[k];
#pragma unroll
      for (int k = 0; k < PM; ++k) pt[k] = pp[k * 128];
      M = ml[0][0];
#pragma unroll
      for (int k = 1; k < PM; ++k) M = fmx(M, ml[k >> 2][k & 3]);
#pragma unroll
      for (int k = 0; k < PM; ++k) {
        const float e = __builtin_amdgcn_exp2f((ml[k >> 2][k & 3] - M) * EXP_C);
        o += pt[k] * e;
        L += ml[2 + (k >> 2)][k & 3] * e;
      }
      asm volatile("" : "+v"(o), "+v"(M), "+v"(L));
      SB();
    }
    LDS_FENCE(); bar(); LDS_FENCE();
    STAMP(2);
#pragma unroll
    for (int k = 0; k < 4; ++k) if ((k & 1) * 4 + 4 > PM) ml[k] = pmr[k];
#pragma unroll
    for (int k = PM; k < 8; ++k) pt[k] = pp[k * 128];
    const v4u t = *tp;
    SB();
    {
      float m2 = M;
#pragma unroll
      for (int k = PM; k < 8; ++k) m2 = fmx(m2, ml[k >> 2][k & 3]);
      if (PM) {
        const float e = __builtin_amdgcn_exp2f((M - m2) * EXP_C);
        o *= e; L *= e;
      }
      M = m2;
#pragma unroll
      for (int k = PM; k < 8; ++k) {
        const float e = __builtin_amdgcn_exp2f((ml[k >> 2][k & 3] - M) * EXP_C);
        o += pt[k] * e;
        L += ml[2 + (k >> 2)][k & 3] * e;
      }
    }
    v4u msg;
#pragma unroll
    for (int j = 0; j < 3; ++j) msg[j] = tag<0x100>(__builtin_amdgcn_perm(asu(o[j]), asu(o[3]), 0x07060501u + j), t[j]);
    msg[3] = tag<1>(asu((lane & 1) ? L : M), t[3]);
    __builtin_amdgcn_raw_buffer_store_b128(msg, rws, (int)to, 0, S1);
    SB();
    if (!isy) STAMP(15);
  } else if (fj < 4) {
    unsigned fvo = ((lane >> 3) * (8 * F * 2) + (lane & 7) * 16) | lmask;
    const unsigned fb = ((f & 3) * 2 + (fj >> 1)) * (56 * F * 2) + (f >> 2) * 128;
    const rsrc_t rf = mkrs((fj & 1) ? wb : wa, FX ? 0 : D * F * 2);
    asm volatile("" : "+v"(fvo));
    clear(FK, true);
    if (FD) __builtin_amdgcn_s_sleep(FD);
    SB();
#pragma unroll
    for (int i = 0; i < 8; ++i) { ring[i] = ld4<2>(rf, fvo, fb + i * (F * 2)); SB(); }
    const v4u xn = *reinterpret_cast<const v4u*>(&s_xn[(((f & 3) * 2 + (fj >> 1)) * 7 + (lane >> 3)) * 4]);
    const int p = fj * 16 + ((lane >> 4) & 1) * 8 + (lane & 7);
    v4u t = *reinterpret_cast<const v4u*>(&s_dst[6][p * 4]);
    unsigned to = (lane & 40) ? OOB : mf(p);
    v4u pb[8];
#pragma unroll
    for (int i = 0; i < 8; ++i) {
      pb[i] = pm & __builtin_amdgcn_perm(xn[i >> 1], xn[i >> 1], (i & 1) ? 0x03020302u : 0x01000100u);
      asm volatile("" : "+v"(pb[i]));
    }
    asm volatile("" : "+v"(t), "+v"(to));
    SB();
    LDS_FENCE(); bar(); LDS_FENCE();
    SB();
    STAMP(2);
    f32x4 acc[2] = {{0.f, 0.f, 0.f, 0.f}, {0.f, 0.f, 0.f, 0.f}};
#pragma unroll
    for (int i = 0; i < 8; ++i) acc[i & 1] = mfma(ring[i], pb[i], acc[i & 1]);
    asm volatile("" :: "v"(pv));
    acc[0] += acc[1];
    f32x4 z;
#pragma unroll
    for (int v = 0; v < 4; ++v)
      z[v] = x32_add(asf((unsigned)__builtin_amdgcn_update_dpp((int)asu(acc[0][v]), (int)asu(acc[0][v]), ROR8, 0xc, 0xf, false)));
    __builtin_amdgcn_raw_buffer_store_b128(tag4(z, t), rws, (int)to, 0, S1);
    SB();
    STAMP(14);
  } else if (!idle) {
    LDS_FENCE(); bar(); LDS_FENCE();
    SB();
    STAMP(2);
  }
  const v4u old = *reinterpret_cast<const v4u*>(&s_old[w][lane * 4]);
  if (isy) {
    if (YB) { SB(); __builtin_amdgcn_s_barrier(); SB(); }
    lds1_t po = (lane & 8) ? (lds1_t)&s_ml2[w] : (lds1_t)&s_o[w][(lane & 7) * 4 + (lane >> 5) * 2 + ((lane >> 4) & 1)];
    lds1_t pq = (lds1_t)&s_ml2[4 + w];
    asm volatile("" : "+v"(po), "+v"(pq));
    if (G1) until(G1 + __builtin_amdgcn_readlane(tok, C_LEAD));
    STAMP(14);
    const v4u dr = poll<PD1, PG1, PS1>(rw4, pa, old);
    STAMP(15);
    const v4u d = dr & (v4u){~0x100u, ~0x100u, ~0x100u, ~1u};
    const float ot = dpp<XOR1>(asf(d[3]));
    const float m = (lane & 1) ? ot : asf(d[3]), l = (lane & 1) ? asf(d[3]) : ot;
    float M = fmx(m, dpp<ROR8>(m)); M = x16_mx(M); M = x32_mx(M);
    *pq = M;
    const float e = __builtin_amdgcn_exp2f((m - M) * EXP_C);
    const float v3 = asf(__builtin_amdgcn_perm(__builtin_amdgcn_perm(d[2], d[1], 0x04000c0cu), d[0], 0x0706000cu));
    float y0 = tr32(asf(d[0]) * e, asf(d[2]) * e), y1 = tr32(asf(d[1]) * e, v3 * e), L = x32_add(l * e);
    y0 = tr16(y0, y1); L = x16_add(L);
    const bool s_ = lane & 8;
    *po = (s_ ? L : y0) + dpp<ROR8>(s_ ? y0 : L);
    LDS_FENCE(); bar(); LDS_FENCE();
    STAMP(3);
    poll_end(dr);
    STAMP(4);
    STAMP_OUT();
    __builtin_amdgcn_endpgm();
  }
  if (isr) {
    const int col = (ro >> 1) * 32 + (ro & 1) * 4 + (lane & 3) * 8 + ((lane >> 2) & 3);
    float xo = asf((unsigned)xg[col] << 16);
    lds1_t po = (lds1_t)&s_r[(lane & 12) ? 8 : w][(lane & 3) * 4 + (lane >> 5) * 2 + ((lane >> 4) & 1)], pq = (lds1_t)&s_r[lane >> 4][lane & 15];
    const rsrc_t ro_ = mkrs(out, D * 2);
    unsigned oo = lane < 16 ? col * 2 : OOB;
    asm volatile("" : "+v"(xo), "+v"(po), "+v"(pq), "+v"(oo));
    if (G2) until(G2 + __builtin_amdgcn_readlane(tok, C_LEAD));
    const v4u dr = poll<PD2, PG2, PS2>(rw4, pa, old);
    const v4u d = dr & ~1u;
    float y0 = tr32(asf(d[0]), asf(d[2])), y1 = tr32(asf(d[1]), asf(d[3]));
    y0 = tr16(y0, y1);
    y0 += dpp<ROR8>(y0); y0 += dpp<ROR4>(y0);
    *po = y0;
    LDS_FENCE(); bar(); LDS_FENCE();
    if (w == 0) {
      float a = pq[0] + pq[64];
      a = x16_add(a); a = x32_add(a);
      __builtin_amdgcn_raw_buffer_store_b16((short)f2bf(xo + a), ro_, (int)oo, 0, SO);
    }
    STAMP(4);
    poll_end(dr);
  }
  if (!isr) STAMP(4);
  STAMP_OUT();
}

extern "C" void kernel_launch(void* const* d_in, const int* in_sizes, int n_in,
                              void* d_out, int out_size, void* d_ws, size_t ws_size,
                              hipStream_t stream) {
  fused_kernel<<<NWG, NT, 0, stream>>>((const u16*)d_in[0], (const u16*)d_in[1], (const u16*)d_in[2], (const u16*)d_in[3],
                                       (const u16*)d_in[4], (const u16*)d_in[6], (const u16*)d_in[7], (const u16*)d_in[8],
                                       (u16*)d_out, (char*)d_ws);
}
```

```cpp
#include <hip/hip_runtime.h>
#include <hip/hip_bf16.h>
#include <stdint.h>

#define SWP 0
#define LD 1
#define LU 0
#define LB 1
#define LP 8
#define SKF 0
#define SL0 0
#define GB 1
#define YS 22
#define YE 0
#define YK 0
#define QPOS 24
#define PACE 8
#define QTRIES 8
#define TQ 123
#define TF 131
#define TFY 129
#define TY 129
#define NP 4
#define TAU 3640
#define PP 3000
#define PL 12
#define TOFF 150
#define TOFFQ 180
#define QBURST 100000
#define PBURST 4000
#define KBURST 100000
#define VBURST 100000
#define QFIRST 24
#define OPOS 8
#define TWD 0
#define OA 16
#define OB 16
#define S1 16
#define S2 16
#define G1 3835
#define G2 3955
#define EO 3
#define PD1 2
#define PG1 6
#define PD2 2
#define PG2 6
#define PD3 2
#define PG3 6
#define G3 3845
#define SR 4
#define PS1 0
#define PS2 0
#define SO 0
#define DK 57
#define DS 0
#define DG 4
#define DB 0
#define SQ 0
#define SF_ 0
#define SFY 0
#define SY 0
#define XA 4
#define XB 4
#define TX 0
#define NOHS 0
#define ES 1
#define EL 8
#define SVM 0
#define SVL 0
#define FO 0
#define FP 1
#define NT3 1
#define UF 1
#define WV 1
#define FX 0
#define PM 5
#define FK 0
#define YB 1
#define FD 0
#define YD 0
#define FB 1
#define XW 0
#define DIAG 0
#define LITE 0

typedef unsigned v4u __attribute__((ext_vector_type(4)));
typedef unsigned v2u __attribute__((ext_vector_type(2)));
typedef float f32x4 __attribute__((ext_vector_type(4)));
typedef float f32x2 __attribute__((ext_vector_type(2)));
typedef __bf16 bf16x2 __attribute__((ext_vector_type(2)));
typedef __bf16 bf16x8 __attribute__((ext_vector_type(8)));
typedef unsigned short u16;
typedef __amdgpu_buffer_rsrc_t rsrc_t;
typedef __attribute__((address_space(3))) f32x4* lds4_t;
typedef __attribute__((address_space(3))) v4u* ldsu_t;
typedef __attribute__((address_space(3))) float* lds1_t;

constexpr int D = 448, SK = 32768, DH = 256, F = 1792, NQ = 4096;
constexpr int NWG = 256, NT = 512;
constexpr unsigned OOB = 0x80000000u;
constexpr unsigned SENT = 0xFFFFFFFFu;
constexpr unsigned AREA = 9216;
constexpr unsigned A_O1 = 1024;
constexpr unsigned A_F = 5120;
constexpr unsigned A_Y2 = 1024;
constexpr unsigned WS_REQ = NWG * AREA;
constexpr unsigned WS_ECHO = WS_REQ + 1024;
constexpr unsigned WS_GO = WS_ECHO + 1024;
constexpr unsigned WS_T = WS_GO + 256;
constexpr unsigned WS_Q = WS_T + 256;
constexpr unsigned WS_END = WS_Q + 8 * 2048;
constexpr int SPIN_CAP = 1 << 18;
constexpr int C_NEXT = 0, C_TICK = 1, C_GO = 2, C_Q = 3, C_T0 = 4, C_FC = 5, C_OLD = 6, C_DONE = 7, C_LEAD = 8, C_CNT = 9;

__device__ __forceinline__ float asf(unsigned u) { return __builtin_bit_cast(float, u); }
__device__ __forceinline__ unsigned asu(float f) { return __builtin_bit_cast(unsigned, f); }
__device__ __forceinline__ float rbf(float f) { __bf16 b = (__bf16)f; return (float)b; }
__device__ __forceinline__ unsigned f2bf(float f) { __bf16 b = (__bf16)f; return (unsigned)__builtin_bit_cast(u16, b); }
__device__ __forceinline__ float dot2(unsigned k, unsigned q, float acc) {
  return __builtin_amdgcn_fdot2_f32_bf16(__builtin_bit_cast(bf16x2, k), __builtin_bit_cast(bf16x2, q), acc, false);
}
__device__ __forceinline__ f32x2 unp(unsigned u) { f32x2 r; r.x = asf(u << 16); r.y = asf(u & 0xffff0000u); return r; }
template <int CTRL> __device__ __forceinline__ float dpp(float x) {
  return asf((unsigned)__builtin_amdgcn_update_dpp(0, (int)asu(x), CTRL, 0xf, 0xf, true));
}
constexpr int XOR1 = 0xB1, XOR2 = 0x4E, XOR7 = 0x141, ROR8 = 0x128, ROR4 = 0x124, ROR2 = 0x122, BCAST = 0x150;
constexpr int QP_EVEN = 0xA0, QP_ODD = 0xF5;
__device__ __forceinline__ float x16_add(float x) { auto s = __builtin_amdgcn_permlane16_swap(asu(x), asu(x), false, false); return asf(s[0]) + asf(s[1]); }
__device__ __forceinline__ float x32_add(float x) { auto s = __builtin_amdgcn_permlane32_swap(asu(x), asu(x), false, false); return asf(s[0]) + asf(s[1]); }
__device__ __forceinline__ float x16_max(float x) { auto s = __builtin_amdgcn_permlane16_swap(asu(x), asu(x), false, false); return fmaxf(asf(s[0]), asf(s[1])); }
__device__ __forceinline__ float x32_max(float x) { auto s = __builtin_amdgcn_permlane32_swap(asu(x), asu(x), false, false); return fmaxf(asf(s[0]), asf(s[1])); }
__device__ __forceinline__ float tr32(float a, float b) { auto s = __builtin_amdgcn_permlane32_swap(asu(a), asu(b), false, false); return asf(s[0]) + asf(s[1]); }
__device__ __forceinline__ float tr16(float a, float b) { auto s = __builtin_amdgcn_permlane16_swap(asu(a), asu(b), false, false); return asf(s[0]) + asf(s[1]); }
__device__ __forceinline__ float fmx(float a, float b) { return __builtin_elementwise_maximum(a, b); }
__device__ __forceinline__ float x16_mx(float x) { auto s = __builtin_amdgcn_permlane16_swap(asu(x), asu(x), false, false); return fmx(asf(s[0]), asf(s[1])); }
__device__ __forceinline__ float x32_mx(float x) { auto s = __builtin_amdgcn_permlane32_swap(asu(x), asu(x), false, false); return fmx(asf(s[0]), asf(s[1])); }
__device__ __forceinline__ float wave_sum(float v) {
  v += dpp<XOR1>(v); v += dpp<XOR2>(v); v += dpp<XOR7>(v); v += dpp<ROR8>(v);
  v = x16_add(v); return x32_add(v);
}
__device__ __forceinline__ float wave_max(float v) {
  v = fmaxf(v, dpp<XOR1>(v)); v = fmaxf(v, dpp<XOR2>(v)); v = fmaxf(v, dpp<XOR7>(v)); v = fmaxf(v, dpp<ROR8>(v));
  v = x16_max(v); return x32_max(v);
}
__device__ __forceinline__ rsrc_t mkrs(const void* p, unsigned bytes) { return __builtin_amdgcn_make_buffer_rsrc((void*)p, 0, (int)bytes, 0x00020000); }
__device__ __forceinline__ rsrc_t selr(bool c, rsrc_t a, rsrc_t b) { return c ? a : b; }
__device__ __forceinline__ unsigned selu(bool c, unsigned a, unsigned b) { return c ? a : b; }
template <int AUX> __device__ __forceinline__ v4u ld4(rsrc_t r, unsigned vo, unsigned so) { return __builtin_amdgcn_raw_buffer_load_b128(r, (int)vo, (int)so, AUX); }
template <int AUX> __device__ __forceinline__ v2u ld2(rsrc_t r, unsigned vo, unsigned so) { return __builtin_amdgcn_raw_buffer_load_b64(r, (int)vo, (int)so, AUX); }
__device__ __forceinline__ void st4c(v4u v, rsrc_t r, unsigned vo) { __builtin_amdgcn_raw_buffer_store_b128(v, r, (int)vo, 0, 16); }
__device__ __forceinline__ void st1c(unsigned v, rsrc_t r, unsigned vo) { __builtin_amdgcn_raw_buffer_store_b32(v, r, (int)vo, 0, 16); }
template <unsigned M> __device__ __forceinline__ unsigned tag(unsigned u, unsigned t) { return (u & ~M) | (t & M); }
__device__ __forceinline__ v4u tag4(const f32x4& v, const v4u& t) { return (v4u){tag<1>(asu(v[0]), t[0]), tag<1>(asu(v[1]), t[1]), tag<1>(asu(v[2]), t[2]), tag<1>(asu(v[3]), t[3])}; }
__device__ __forceinline__ bool arrived(const v4u& v, const v4u& o) { return __builtin_amdgcn_ballot_w64(v[0] == o[0] || v[1] == o[1] || v[2] == o[2] || v[3] == o[3]) == 0; }
__device__ __forceinline__ unsigned lowb(const v4u& v) { return __builtin_amdgcn_perm(v[1], v[0], 0x0c0c0400u) | __builtin_amdgcn_perm(v[3], v[2], 0x04000c0cu); }
#define SB() __builtin_amdgcn_sched_barrier(0)
__device__ __forceinline__ void bar() { __builtin_amdgcn_s_waitcnt(0xC07F); __builtin_amdgcn_s_barrier(); }
#define WAITV(n) __builtin_amdgcn_s_waitcnt(((n) & 15) | (((n) >> 4) << 14) | 0x0F70)
template <int V> struct IC { static constexpr int value = V; constexpr operator int() const { return V; } };
template <int A, int B, class Fn> __device__ __forceinline__ void static_for(Fn&& f) { if constexpr (A < B) { f(IC<A>{}); static_for<A + 1, B>(f); } }
#define LDS_FENCE() asm volatile("" ::: "memory")

constexpr float EXP_C = 0.0625f * 1.4426950408889634f;

struct Att {
  float m, l;
  f32x4 a[2];
};
__device__ __forceinline__ f32x4 mfma(const v4u& a, const v4u& b, const f32x4& c) {
  return __builtin_amdgcn_mfma_f32_16x16x32_bf16(__builtin_bit_cast(bf16x8, a), __builtin_bit_cast(bf16x8, b), c, 0, 0, 0);
}
__device__ __forceinline__ float red_max(float v) { v = fmaxf(v, dpp<XOR1>(v)); v = fmaxf(v, dpp<XOR2>(v)); return x32_max(x16_max(v)); }

template <int NL, class ISSUE>
__device__ __forceinline__ float scores(v4u (&kb)[8], const v4u (&qb)[8], int lane, ISSUE issue) {
  f32x4 d = {0.f, 0.f, 0.f, 0.f};
#pragma unroll
  for (int i = 0; i < NL; ++i) {
    d = mfma(kb[i], qb[i], d);
    asm volatile("" : "+v"(d));
    issue(i);
    SB();
  }
  const bool odd = lane & 1;
  return rbf((odd ? d[3] : d[0]) + dpp<XOR1>(odd ? d[1] : d[2]));
}
template <class ISSUE>
__device__ __forceinline__ float phase_k4(v4u (&kb)[8], const v4u (&qb)[8], Att& st, int lane, ISSUE issue) {
  float s = scores<4>(kb, qb, lane, issue);
  s = (lane & 2) ? -__builtin_inff() : s;
  const float mn = fmaxf(st.m, red_max(s));
  const float al = __builtin_amdgcn_exp2f((st.m - mn) * EXP_C);
  const float p = __builtin_amdgcn_exp2f((s - mn) * EXP_C);
  st.m = mn; st.l = st.l * al + p;
  st.a[0] *= al; st.a[1] *= al;
  SB();
  return p;
}
template <int I, int O, class ISSUE>
__device__ __forceinline__ void pv_step(v4u (&vb)[8], unsigned pp, const v4u& pm, Att& st, ISSUE& issue) {
  const v4u pb = pm & (unsigned)__builtin_amdgcn_update_dpp(0, (int)pp, I * 0x55, 0xf, 0xf, true);
  st.a[0] = mfma(vb[O + 2 * I], pb, st.a[0]);
  asm volatile("" : "+v"(st.a[0]));
  issue(O + 2 * I);
  SB();
  st.a[1] = mfma(vb[O + 2 * I + 1], pb, st.a[1]);
  asm volatile("" : "+v"(st.a[1]));
  issue(O + 2 * I + 1);
  SB();
}
template <int NL, class ISSUE>
__device__ __forceinline__ void phase_v(v4u (&vb)[8], float p, const v4u& pm, Att& st, ISSUE issue) {
  constexpr int O = 8 - NL;
  const unsigned pp = __builtin_bit_cast(unsigned, (bf16x2){(__bf16)p, (__bf16)p});
  pv_step<0, O>(vb, pp, pm, st, issue); pv_step<1, O>(vb, pp, pm, st, issue);
  if constexpr (NL == 8) { pv_step<2, O>(vb, pp, pm, st, issue); pv_step<3, O>(vb, pp, pm, st, issue); }
}

template <int N, int GAP, int SL>
__device__ __forceinline__ v4u poll(const v4u& r, unsigned pa, const v4u& old) {
  v4u d;
  unsigned long long t0, t1, t2;
  int n = SPIN_CAP;
  asm volatile(
      "s_nop 4\n\t"
      "buffer_load_dwordx4 v[200:203], %5, %6, 0 offen sc1\n\t"
      ".rept %11\n\t"
      "s_sleep %12\n\t"
      "buffer_load_dwordx4 v[200:203], %5, %6, 0 offen sc1\n\t"
      ".endr\n"
      "1:\n\t"
      "s_waitcnt vmcnt(%11)\n\t"
      "v_cmp_eq_u32 vcc, v200, %7\n\t"
      "v_cmp_eq_u32 %1, v201, %8\n\t"
      "v_cmp_eq_u32 %2, v202, %9\n\t"
      "v_cmp_eq_u32 %3, v203, %10\n\t"
      "s_or_b64 vcc, vcc, %1\n\t"
      "s_or_b64 %2, %2, %3\n\t"
      "s_or_b64 exec, vcc, %2\n\t"
      "s_cbranch_scc0 2f\n\t"
      ".if %13\n\ts_sleep %13\n\t.endif\n\t"
      "buffer_load_dwordx4 v[200:203], %5, %6, 0 offen sc1\n\t"
      "s_sub_u32 %4, %4, 1\n\t"
      "s_cbranch_scc0 1b\n"
      "2:\n\t"
      "s_mov_b64 exec, -1"
      : "=&{v[200:203]}"(d), "=&s"(t0), "=&s"(t1), "=&s"(t2), "+s"(n)
      : "v"(pa), "s"(r), "v"(old[0]), "v"(old[1]), "v"(old[2]), "v"(old[3]), "n"(N - 1), "n"(GAP), "n"(SL)
      : "vcc", "scc", "memory");
  return d;
}
__device__ __forceinline__ void poll_end(const v4u& d) { asm volatile("s_waitcnt vmcnt(0)" :: "{v[200:203]}"(d)); }

#define STAMP(k) do {} while (0)
#define STAMP_OUT() do {} while (0)

__device__ __forceinline__ f32x2 ln_stats(const v4u& xa, int lane) {
  f32x2 xv[4];
  float sm = 0.f;
#pragma unroll
  for (int j = 0; j < 4; ++j) { xv[j] = unp(xa[j]); sm += xv[j].x + xv[j].y; }
  const float mean = wave_sum(sm) * (1.0f / D);
  float sq = 0.f;
#pragma unroll
  for (int j = 0; j < 4; ++j) { const f32x2 d = xv[j] - mean; sq += d.x * d.x + d.y * d.y; }
  sq = lane < 56 ? sq : 0.f;
  const float var = rbf(wave_sum(sq) * (1.0f / D));
  const float sd = rbf(sqrtf(rbf(var + 1e-5f)));
  return (f32x2){rbf(mean), 1.0f / sd};
}
__device__ __forceinline__ float ln_apply(float x, f32x2 st) { return rbf(rbf(x - st.x) * st.y); }

__global__ void __launch_bounds__(NT, 2)
fused_kernel(const u16* __restrict__ xg, const u16* __restrict__ kc, const u16* __restrict__ vc, const u16* __restrict__ wq,
             const u16* __restrict__ wo, const u16* __restrict__ wg, const u16* __restrict__ wu, const u16* __restrict__ wd,
             u16* __restrict__ out, char* ws) {
  __shared__ __attribute__((aligned(16))) float s_part[8][512];
  __shared__ __attribute__((aligned(16))) float s_mv[8][64];
  __shared__ __attribute__((aligned(16))) unsigned s_q[256];
  __shared__ __attribute__((aligned(16))) unsigned s_qs[64];
  __shared__ __attribute__((aligned(16))) unsigned s_old[8][256];
  __shared__ __attribute__((aligned(16))) unsigned s_dst[8][256];
  __shared__ __attribute__((aligned(16))) float s_ml[2][16];
  __shared__ __attribute__((aligned(16))) float s_o[4][32];
  __shared__ __attribute__((aligned(16))) float s_ml2[16];
  __shared__ __attribute__((aligned(16))) unsigned s_xn[256];
  __shared__ __attribute__((aligned(16))) float s_h[32];
  __shared__ __attribute__((aligned(16))) float s_r[9][16];
  __shared__ __attribute__((aligned(16))) float s_sc[64][32];
  __shared__ int s_ctl[64];
  const int tid = threadIdx.x, lane = tid & 63, w = __builtin_amdgcn_readfirstlane(tid >> 6);
  const int bi = SWP == 2 ? (blockIdx.x & ~7) | ((blockIdx.x + 7) & 7) : blockIdx.x ^ SWP, h = (((bi & 1) ? bi + 5 : bi) & 6) | (bi >> 7), s = ((bi >> 3) & 15) * 2 + (bi & 1);
  const int b = h * 32 + s;
  static_assert(8 * TQ + 8 * TF + (YS - 16) * TFY + (32 - YS) * TY == 4096 && TQ - NP > 80 && TF - NP > 80 && TFY - NP > 80 && TY - NP > 80, "tickets");
  static_assert(TQ - NP <= 128 && TF - NP <= 128 && TFY - NP <= 128 && TY - NP <= 128, "tickets");
  const int nt = s < 8 ? TQ : s < 16 ? TF : s < YS ? TFY : TY, nk = (nt - NP) >> 1;
  const int first = s * TY - min(s, YS) * (TY - TFY) - min(s, 16) * (TFY - TF) - min(s, 8) * (TF - TQ);
  asm volatile("" :: "s"(xg), "s"(kc), "s"(vc), "s"(wq), "s"(wo), "s"(wg), "s"(wu), "s"(wd), "s"(out), "s"(ws));
  STAMP(0);
  const rsrc_t rx = mkrs(xg, D * 2);
  const rsrc_t rk = mkrs(kc + (size_t)h * SK * DH, SK * DH * 2);
  const rsrc_t rv = mkrs(vc + (size_t)h * SK * DH, SK * DH * 2);
  const rsrc_t rws = mkrs(ws, WS_END);
  const unsigned l16 = lane * 16;
  auto ubase = [&](int u) -> unsigned { return (unsigned)(first + 2 * u) * 4096u; };
  const unsigned kvo = ((lane >> 1) & 7) * 512 + (lane >> 4) * 32 + (lane & 1) * 16, vvo = (lane >> 4) * 1024 + (lane & 15) * 16;
  auto ldk = [&](unsigned base, int i) { return ld4<2>(rk, kvo + (i & 3) * 128, base + (i >> 2) * 4096); };
  const unsigned area = (unsigned)b * AREA;
  auto area_of = [&](int ss) -> unsigned { return (unsigned)(h * 32 + ss) * AREA; };
  const unsigned lmask = lane < 56 ? 0u : OOB;

  const bool isq = s < 8, isf = s < 14, isy = s >= 16;
  const int f = h * 14 + s, y = h * 16 + s - 16, ro = h * 4 + (s >> 2);
  const bool isr = s < 16 && ((s + h) & 3) == 0 && ro < 28;
  const bool tile = isq;
  const bool leader = (!(NOHS & 1) && b == NWG - 1 && w == 1), member = (w == 0);

  const unsigned pa = isy ? (w < 4 ? area + A_O1 + w * 1024 + l16 : (w == 7 && y < 112) ? area + A_F + l16 : OOB) : isr ? area + A_Y2 + w * 1024 + l16 : OOB;
  const unsigned mo_ = area_of(16 + (lane & 8) + (lane >> 4)) + A_O1 + s * 128 + (lane & 7) * 16;
  auto mf = [&](int p) -> unsigned {
    const int fd = (f & ~3) + ((p >> 2) & 3);
    return (unsigned)((fd >> 4) * 32 + 16 + (fd & 15)) * AREA + A_F + (f & 3) * 256 + (p >> 4) * 64 + (p & 3) * 16;
  };
  auto my = [&](int p) -> unsigned { return (unsigned)((p >> 4) * 32 + (p & 12) + ((-(p >> 4)) & 3)) * AREA + A_Y2 + y * 64 + (p & 3) * 16; };
  v4u eo, ed;
  auto da_of = [&]() {
    const int p = (w - 2) * 64 + lane;
    unsigned da = (isy && p < 112) ? my(p) : OOB;
    return (w & 6) == 2 ? da : (w & 6) == 4 ? mo_ + (w - 4) * 4 * AREA : (w == 6 && isf) ? mf(lane) : OOB;
  };
  if (EO == 1) { eo = ld4<OA>(rws, pa, 0); SB(); }
  if (EO == 3 || (EO == 4 && !tile)) { eo = ld4<OA>(rws, pa, 0); ed = ld4<OB>(rws, da_of(), 0); SB(); }
  if (EO == 5) { ed = ld4<OB>(rws, da_of(), 0); SB(); }
  const v4u sent = {SENT, SENT, SENT, SENT};
  unsigned poll_off = lane == 1 ? WS_GO : lane < 4 ? WS_T - 32 + l16 : OOB;
  poll_off = lane == 0 ? WS_REQ + (b >> 2) * 16 : poll_off;
  poll_off = leader ? (ES ? lane * 4 * AREA + (lane & 3) * 256 : WS_ECHO + l16) : poll_off;
  auto echoes = [&](unsigned seen) {
    v4u r;
#pragma unroll
    for (int j = 0; j < 4; ++j) r[j] = __builtin_amdgcn_raw_buffer_load_b32(rws, (int)(poll_off | (seen >> j << 31)), j * AREA, 16);
    return r;
  };
  v4u pv = {0u, 0u, 0u, 0u}, e1 = {0u, 0u, 0u, 0u};
  unsigned go_ref = 0;
  int* const ctl = &s_ctl[lane];
  auto ctl_get = [&](int i) { return __builtin_amdgcn_readfirstlane(__hip_atomic_load(&s_ctl[i], __ATOMIC_RELAXED, __HIP_MEMORY_SCOPE_WORKGROUP)); };
  auto ctl_set = [&](int i) { __hip_atomic_store(&s_ctl[i], 1, __ATOMIC_RELAXED, __HIP_MEMORY_SCOPE_WORKGROUP); };
  const unsigned qoff = WS_Q + h * 2048 + l16;
  v4u qv0, qv1;
  if (member) {
    qv0 = ld4<16>(rws, qoff, 0);
    qv1 = ld4<16>(rws, qoff, 1024);
    SB();
    int c = lane == C_NEXT ? (isq ? TOFFQ : TOFF) * 128 : 0;
    c = lane == C_TICK ? 40 : c;
    c = lane == C_T0 ? (int)(__builtin_amdgcn_s_memrealtime() & 0x7fffffff) : c;
    __hip_atomic_store(ctl, c, __ATOMIC_RELAXED, __HIP_MEMORY_SCOPE_WORKGROUP);
    st1c(c, rws, (lane == C_T0 && (bi >> 3) == 12) ? WS_T + (bi & 7) * 4 : OOB);
  }
  *reinterpret_cast<v4u*>(&s_sc[w * 8][lane * 4]) = sent;
  int t0 = 0;
  auto now = [&]() __attribute__((always_inline)) { return ((int)__builtin_amdgcn_s_memrealtime() - t0) & 0x7fffff; };
  const int neg = lane == C_NEXT ? 0 : (int)0x80000000;
  const int xcc = __builtin_amdgcn_s_getreg(20 | (3 << 11));
  constexpr auto tkf = [](int t, int sh) { return TAU + sh * 128 / ((t - NP) / 2 - 8); };
  constexpr auto tvf = [](int t, int sh) { return TAU - sh * 128 / ((t - NP) / 2 + 8 - PL); };
  int tk = s < 8 ? tkf(TQ + 16, SQ) : s < 16 ? tkf(TF, SF_) : s < YS ? tkf(TFY, SFY) : tkf(TY, SY);
  int tv = s < 8 ? tvf(TQ, SQ) : s < 16 ? tvf(TF, SF_) : s < YS ? tvf(TFY, SFY) : tvf(TY, SY);
  int inc = lane == C_NEXT ? tk : lane == C_CNT ? 1 : 0;
  int lost = 0;
  auto token = [&](int burst, bool early = false) __attribute__((always_inline)) {
    const int t = max(now() * 128 - burst, 0);
    if (GB && early) {
      const int o = __hip_atomic_fetch_max(ctl, t | neg, __ATOMIC_RELAXED, __HIP_MEMORY_SCOPE_WORKGROUP);
      lost += max(o, t | neg) - o;
    } else {
      __hip_atomic_fetch_max(ctl, t | neg, __ATOMIC_RELAXED, __HIP_MEMORY_SCOPE_WORKGROUP);
    }
    return __hip_atomic_fetch_add(ctl, inc, __ATOMIC_RELAXED, __HIP_MEMORY_SCOPE_WORKGROUP);
  };
  auto due_of = [&](int x) { return __builtin_amdgcn_readlane(x, C_NEXT) >> 7; };
  auto until = [&](int due) __attribute__((always_inline)) {
#pragma unroll 1
    for (int it = 0; it < 4096 && now() < due; ++it) __builtin_amdgcn_s_sleep(1);
    SB();
  };
  unsigned qs0, qs1;
  auto q_there = [&]() { return __builtin_amdgcn_ballot_w64(((lowb(qv0) ^ qs0) & (lowb(qv1) ^ qs1)) != 0xFFFFFFFFu) == 0; };
  auto load_q = [&]() { qv0 = ld4<16>(rws, qoff, 0); qv1 = ld4<16>(rws, qoff, 1024); };
  auto publish_q = [&]() {
    *reinterpret_cast<v2u*>(&s_q[lane * 2]) = (v2u){__builtin_amdgcn_perm(qv0[1], qv0[0], 0x07060302u), __builtin_amdgcn_perm(qv0[3], qv0[2], 0x07060302u)};
    *reinterpret_cast<v2u*>(&s_q[128 + lane * 2]) = (v2u){__builtin_amdgcn_perm(qv1[1], qv1[0], 0x07060302u), __builtin_amdgcn_perm(qv1[3], qv1[2], 0x07060302u)};
    LDS_FENCE();
    __builtin_amdgcn_s_waitcnt(0xC07F);
    ctl_set(C_Q);
  };
  v4u pre[4][8], ring[8];
  auto unit0 = [&]() {
#pragma unroll
    for (int i = 0; i < 8; ++i) { pre[0][i] = ldk(ubase(w), i); SB(); }
    if (EO == 2) { eo = ld4<OA>(rws, pa, 0); SB(); }
  };
  SB();
  if (tile) {
    v4u tw[8];
    const int oct = 7 * w;
    constexpr int stride = NQ * 2;
    const rsrc_t rt = mkrs(wq, D * stride);
    const unsigned to = (unsigned)((oct + (lane >> 3)) * 8 * stride + (h * 8 + s) * 128 + (lane & 7) * 16) | lmask;
    const v4u xa = ld4<0>(rx, l16, 0);
    const v4u xb = ld4<0>(rx, (unsigned)(oct + (lane >> 3)) * 16 | lmask, 0);
    SB();
#pragma unroll
    for (int i = 0; i < 8; ++i) { tw[i] = ld4<2>(rt, to, i * stride); SB(); }
    if (EO == 4) { eo = ld4<OA>(rws, pa, 0); ed = ld4<OB>(rws, da_of(), 0); SB(); }
    LDS_FENCE(); bar(); LDS_FENCE();
    t0 = ctl_get(C_T0);
    SB();
    const f32x2 ls = ln_stats(xa, lane);
    f32x2 acc[4];
#pragma unroll
    for (int i = 0; i < 8; ++i) {
      const float xn = ln_apply(asf((i & 1) ? (xb[i >> 1] & 0xffff0000u) : (xb[i >> 1] << 16)), ls);
#pragma unroll
      for (int j = 0; j < 4; ++j) acc[j] = i ? acc[j] + unp(tw[i][j]) * xn : unp(tw[i][j]) * xn;
    }
    float y0 = tr32(acc[0].x, acc[2].x), y1 = tr32(acc[0].y, acc[2].y), y2 = tr32(acc[1].x, acc[3].x), y3 = tr32(acc[1].y, acc[3].y);
    y0 = tr16(y0, y2); y1 = tr16(y1, y3);
    const bool s_ = lane & 8;
    y0 = (s_ ? y1 : y0) + dpp<ROR8>(s_ ? y0 : y1);
    s_mv[w][(lane & 7) * 8 + (lane >> 3)] = y0;
    STAMP(6);
    token(QBURST);
    unit0();
    LDS_FENCE(); bar(); LDS_FENCE();
    if (member) {
      float a = 0.f;
#pragma unroll
      for (int k = 0; k < 8; ++k) a += s_mv[k][lane];
      s_qs[lane] = f2bf(a) << 16;
      LDS_FENCE();
      const v4u qs = *reinterpret_cast<const v4u*>(&s_qs[(lane & 15) * 4]) | (~((s & 4) ? qv1 : qv0) & 0xffffu);
      st4c(qs, rws, (lane >> 4) == (s & 3) ? qoff + (s >> 2) * 1024 : OOB);
    }
  } else if (leader && !LU) {
    e1 = ES ? echoes(0) : ld4<16>(rws, poll_off, 0);
    go_ref = __builtin_amdgcn_raw_buffer_load_b32(rws, (int)WS_GO, 0, 16);
  }
  SB();
  STAMP(5);
  SB();
  const u16* wa = isy ? wo + (size_t)y * 32 * D : wg;
  const u16* wb = isy ? wd + (size_t)(y < 112 ? (y >> 1) * 32 + (y & 1) * 4 : 0) * D : wu;
  asm volatile("" : "+s"(wa), "+s"(wb));
  bool ok = !member;
  static_for<0, 40>([&](auto N) {
    constexpr int n = N;
    if constexpr (n == 0) {
      if (!tile) unit0();
      if (LU && leader) {
        e1 = echoes(0);
        go_ref = __builtin_amdgcn_raw_buffer_load_b32(rws, (int)WS_GO, 0, 16);
      }
    }
    if constexpr (n == 8) {
      if (!tile) {
        LDS_FENCE(); bar(); LDS_FENCE();
        t0 = ctl_get(C_T0);
      }
    }
    if constexpr (n == LP) {
      if (leader) {
        e1 += 1u;
        go_ref += 1u;
        st4c(e1, rws, WS_REQ + l16);
      }
    }
    if constexpr (n > 0 && n % 8 == 0) {
      const int x = token((isq || (LB && b == NWG - 1)) ? QBURST : PBURST, true);
      if constexpr (n > QFIRST) {
        if (!ok) {
          ok = q_there();
          if (ok) publish_q();
        }
      }
      if constexpr (n == OPOS + 16) {
        *reinterpret_cast<v4u*>(&s_old[w][lane * 4]) = (int)pa < 0 ? sent : eo;
        *reinterpret_cast<v4u*>(&s_dst[w][lane * 4]) = ~ed;
        LDS_FENCE();
        __hip_atomic_fetch_add(ctl, lane == C_OLD ? 1 : 0, __ATOMIC_RELAXED, __HIP_MEMORY_SCOPE_WORKGROUP);
      }
      SB();
      until(due_of(x));
      if constexpr (n == 8) {
        if (member) { qs0 = lowb(qv0); qs1 = lowb(qv1); }
      }
      if constexpr (n >= QFIRST) {
        if (!ok) load_q();
      }
      if constexpr (n == OPOS) {
        if (EO == 0 || EO == 5) eo = ld4<OA>(rws, pa, 0);
        if (EO < 3) ed = ld4<OB>(rws, da_of(), 0);
      }
      SB();
    }
    if constexpr (n == QPOS) {
      STAMP(9);
      if (ES && leader) pv = LD ? ~e1 : echoes(0);
      else pv = ld4<16>(rws, (member || leader) ? poll_off : OOB, 0);
      SB();
    }
    if constexpr (n >= 8) {
      if constexpr (n >= PACE) WAITV(PACE + ((n >= QPOS && n < QPOS + PACE) ? 1 : 0) + ((n >= OPOS && n < OPOS + PACE) ? (EO == 0 ? 2 : (EO == 3 || EO == 4) ? 0 : 1) : 0));
      if constexpr (n < 32) pre[n >> 3][n & 7] = ldk(ubase((n & 56) + w), n & 7); else ring[n & 7] = ldk(ubase(32 + w), n & 7);
    }
    SB();
    if constexpr (n - PACE == 7 || n - PACE == 15 || n - PACE == 23 || n - PACE == 31) STAMP(16 + (n - PACE) / 8);
  });
  inc = lane == C_TICK ? 1 : inc;
  if (GB == 1) __hip_atomic_fetch_add(ctl, -lost, __ATOMIC_RELAXED, __HIP_MEMORY_SCOPE_WORKGROUP);
  int tok = token(KBURST);
  STAMP(10);
  bool svc = member || leader;
  unsigned seenm = 0, last_echo = 0;
  bool echoed = false;
  auto service = [&]() {
    if (leader) {
      seenm |= (pv[0] == e1[0] ? 1u : 0u) | (pv[1] == e1[1] ? 2u : 0u) | (pv[2] == e1[2] ? 4u : 0u) | (pv[3] == e1[3] ? 8u : 0u);
      svc = __builtin_amdgcn_ballot_w64(seenm != 15u) != 0;
      st1c(go_ref, rws, (lane == 0 && !svc) ? WS_GO : OOB);
    } else {
      const unsigned req = (unsigned)__builtin_amdgcn_readfirstlane((int)((b & 2) ? ((b & 1) ? pv[3] : pv[2]) : ((b & 1) ? pv[1] : pv[0])));
      const unsigned go = (unsigned)__builtin_amdgcn_readlane((int)pv[0], 1);
      go_ref = echoed ? go_ref : go;
      svc = go == (unsigned)__builtin_amdgcn_readfirstlane((int)go_ref);
      if (NOHS & 1) svc = false;
      const bool rdy = ctl_get(C_OLD) == 8;
      if (ES && EL > 0) st4c((v4u){req, req, req, req}, rws, (lane < EL && svc && rdy && !(echoed && req == last_echo)) ? area + ((b >> 2) & 3) * 256 + l16 : OOB);
      else st1c(req, rws, (lane == 0 && svc && rdy && !(echoed && req == last_echo)) ? (ES ? area + ((b >> 2) & 3) * 256 : WS_ECHO + b * 4) : OOB);
      echoed |= rdy; last_echo = req;
    }
    if (ES && leader) pv = echoes(seenm);
    else pv = ld4<16>(rws, svc ? poll_off : OOB, 0);
    SB();
  };

  {
    if (!ok) {
      ok = q_there();
      if (ok) publish_q();
    }
#pragma unroll 1
    for (int it = 0; it < SPIN_CAP && !ctl_get(C_Q); ++it) {
      if (svc && it >= QTRIES) { service(); asm volatile("" :: "v"(pv)); }
      if (!ok) {
        load_q();
        ok = q_there() | !svc;
        if (ok) publish_q();
      } else {
        __builtin_amdgcn_s_sleep(1);
      }
    }
  }
  LDS_FENCE();
  v4u qb[8];
#pragma unroll
  for (int i = 0; i < 4; ++i) {
    const v4u z = {0u, 0u, 0u, 0u}, q = *reinterpret_cast<const v4u*>(&s_q[(lane & 8) * 16 + i * 32 + (lane >> 4) * 8 + (lane & 1) * 4]);
    qb[i] = (lane & 2) ? z : q; qb[4 + i] = (lane & 2) ? q : z;
  }
  v4u pm;
#pragma unroll
  for (int j = 0; j < 4; ++j) pm[j] = ((lane >> 1) & 3) == j ? ((lane & 1) ? 0xffff0000u : 0xffffu) : 0u;
  STAMP(1);
  SB();

  Att st;
  int sx = 20;
  int rank;
  lds4_t pw;
  lds1_t pml;
  int idle;
  auto last = [&]() __attribute__((always_inline)) {
    pml = (lds1_t)&s_ml[(lane >> 3) & 1][rank];
    pw = (lds4_t)&s_part[rank][lane * 4];
    idle = __builtin_amdgcn_readfirstlane(!isy && !svc && rank >= PM && (unsigned)(rank - SR) >= 2 && (!isf || (unsigned)(rank - FO) >= 4));
    asm volatile("" : "+v"(pw), "+v"(pml), "+s"(idle));
    SB();
  };
  auto put_ml = [&]() __attribute__((always_inline)) {
    float l = st.l;
    l += dpp<XOR1>(l); l += dpp<XOR2>(l); l = x32_add(x16_add(l));
    pml[0] = st.m; pml[8] = l;
    SB();
  };
  {
    float* const sc = &s_sc[0][(lane >> 3) * 4 + (lane & 3)];
    rsrc_t nra, nrb;
    unsigned nva, nvb, nba, nbb, nsa, nsb;
    int kind = 0, nu = 32 + w, cu;
    auto next = [&]() __attribute__((always_inline)) {
      cu = nu;
      const int t = __builtin_amdgcn_readlane(tok, C_TICK);
      const bool fx = t < 2 * nk, tw = t >= nt;
      kind = fx ? (t >= nk ? 1 : 0) : tw ? 3 : 2;
      nu = t >= nk ? t - nk : t;
      nba = (unsigned)(fx ? first + 2 * nu : first + t) * 4096u;
      nbb = fx ? nba + 4096 : nba;
      nra = selr(kind == 1, rv, rk);
      nrb = selr(kind == 0, rk, rv);
      nva = selu(kind == 1, vvo, kvo); nvb = selu(kind == 0, kvo, vvo);
      nsa = selu(kind == 1, 256, 128); nsb = selu(kind == 0, 128, 256);
      const int u = t + 8 - nk;
      inc = lane == C_NEXT ? (t + PL >= 2 * nk ? PP : (int)selu(u >= 0, tv, tk) + ((unsigned)(u + XA) < XA + XB ? TX : 0)) : inc;
      rank = t - nt;
      if (!tw) until(due_of(tok));
      SB();
    };
    auto ldn = [&](int i) { ring[i] = i < 4 ? ld4<2>(nra, nva, nba + i * nsa) : ld4<2>(nrb, nvb, nbb + (i - 4) * nsb); };
    float mx;
    if (member) {
      const v4u d = pv - (unsigned)t0;
      const bool bad = max(max(d[0] + 512u, d[1] + 512u), max(d[2] + 512u, d[3] + 512u)) > 1024u;
      const int sm = (int)((d[0] + d[1]) + (d[2] + d[3]));
      const int lead = (__builtin_amdgcn_ballot_w64(bad) & 12) ? 0 : __builtin_amdgcn_readlane(sm, 2) + __builtin_amdgcn_readlane(sm, 3) + DB;
      const int slip = __builtin_amdgcn_readlane(tok, C_NEXT) - __builtin_amdgcn_readlane(tok, C_CNT) * tk - (isq ? TOFFQ : TOFF + SL0) * 128;
      const int adj = ((lead * DK) >> 5) - ((slip * SKF) >> 10);
      tk += adj; tv += adj;
      __hip_atomic_fetch_add(ctl, lane == C_LEAD ? (lead * DG) >> 5 : (lane == C_NEXT ? (lead * DS) >> 5 : 0), __ATOMIC_RELAXED, __HIP_MEMORY_SCOPE_WORKGROUP);
    }
    if (GB == 2) __hip_atomic_fetch_add(ctl, -lost, __ATOMIC_RELAXED, __HIP_MEMORY_SCOPE_WORKGROUP);
    if (svc && !(LD && leader)) service();
    {
      const float s0 = scores<8>(pre[0], qb, lane, [&](int) {});
      const float s1 = scores<8>(pre[1], qb, lane, [&](int) {});
      const float s2 = scores<8>(pre[2], qb, lane, [&](int) {});
      const float s3 = scores<8>(pre[3], qb, lane, [&](int) {});
      sc[w * 32] = s0; sc[(8 + w) * 32] = s1; sc[(16 + w) * 32] = s2; sc[(24 + w) * 32] = s3;
      mx = fmaxf(fmaxf(s0, s1), fmaxf(s2, s3));
      SB();
      STAMP(11);
    }
    const v4u xa2 = ld4<0>(rx, (isf && w == 7) ? l16 : OOB, 0);
    SB();
#pragma unroll 1
    while (kind == 0) {
      next();
      const float sv = scores<8>(ring, qb, lane, ldn);
      sc[cu * 32] = sv;
      tok = token(KBURST);
      mx = fmaxf(mx, sv);
      STAMP(sx); ++sx;
      if (svc && !(LD && leader)) service();
      SB();
    }
    STAMP(7);
    if (GB == 3) __hip_atomic_fetch_add(ctl, -lost, __ATOMIC_RELAXED, __HIP_MEMORY_SCOPE_WORKGROUP);
    if (LD == 1 && leader) { pv = echoes(seenm); SB(); }
    if (isf && w == 7) {
      const f32x2 ls = ln_stats(xa2, lane);
      v4u xn;
#pragma unroll
      for (int j = 0; j < 4; ++j) {
        const f32x2 v = unp(xa2[j]);
        xn[j] = __builtin_amdgcn_perm(asu(ln_apply(v.y, ls)), asu(ln_apply(v.x, ls)), 0x07060302u);
      }
      *reinterpret_cast<v4u*>(&s_xn[lane * 4]) = xn;
      LDS_FENCE();
      ctl_set(C_FC);
      SB();
    }
    st.m = red_max(mx); st.l = 0.f;
    st.a[0] = st.a[1] = (f32x4){0.f, 0.f, 0.f, 0.f};
#pragma unroll 1
    while (kind == 1) {
      float sv;
      for (;;) {
        sv = __hip_atomic_load(&sc[nu * 32], __ATOMIC_RELAXED, __HIP_MEMORY_SCOPE_WORKGROUP);
        if (__builtin_amdgcn_ballot_w64(asu(sv) == SENT) == 0) break;
        __builtin_amdgcn_s_sleep(1);
      }
      if (__builtin_amdgcn_ballot_w64(sv > st.m) != 0) {
        const float m2 = fmaxf(st.m, red_max(sv));
        const float al = __builtin_amdgcn_exp2f((st.m - m2) * EXP_C);
        st.m = m2; st.l *= al;
        st.a[0] *= al; st.a[1] *= al;
      }
      const float p = __builtin_amdgcn_exp2f((sv - st.m) * EXP_C);
      st.l += p;
      SB();
      next();
      if (kind == 3) {
        last();
        put_ml();
        phase_v<8>(ring, p, pm, st, [&](int) {});
        break;
      }
      phase_v<8>(ring, p, pm, st, ldn);
      tok = token(VBURST);
      STAMP(sx); ++sx;
      if (svc) service();
      SB();
    }
    STAMP(8);
    if (kind == 2) {
      next();
#pragma unroll 1
    while (kind != 3) {
      if (UF) { nba = __builtin_amdgcn_readfirstlane(nba); nbb = __builtin_amdgcn_readfirstlane(nbb); nsa = __builtin_amdgcn_readfirstlane(nsa); nsb = __builtin_amdgcn_readfirstlane(nsb); }
      const float p = phase_k4(ring, qb, st, lane, ldn);
      phase_v<4>(ring, p, pm, st, ldn);
      tok = token(VBURST);
      STAMP(sx); ++sx;
      SB();
      next();
    }
      SB();
      last();
      const float p = phase_k4(ring, qb, st, lane, [&](int) {});
      phase_v<4>(ring, p, pm, st, [&](int) {});
      put_ml();
    }
    pw[0] = st.a[0]; pw[64] = st.a[1];
    LDS_FENCE();
    SB();
    STAMP(12);
    if (idle) {
      LDS_FENCE(); bar(); LDS_FENCE();
      STAMP(2);
    }
    SB();
#pragma unroll 1
    for (int it = 0; it < SPIN_CAP && svc; ++it) {
      service();
      asm volatile("" :: "v"(pv));
    }
    LDS_FENCE();
    WAITV(0);
  }
  const lds4_t pr = (lds4_t)&s_part[0][lane * 4], pmr = (lds4_t)&s_ml[(lane >> 3) & 1][0];
  if (rank < PM) __hip_atomic_fetch_add(ctl, lane == C_DONE ? 1 : 0, __ATOMIC_RELAXED, __HIP_MEMORY_SCOPE_WORKGROUP);
  SB();
  const v4u rw4 = {(unsigned)(size_t)ws, (unsigned)((size_t)ws >> 32), WS_END, 0x00020000u};
  const unsigned fj = isf ? rank - FO : 4;
  auto clear = [&](int k, bool more) __attribute__((always_inline)) {
#pragma unroll 1
    for (int it = 0; it < SPIN_CAP; ++it) {
      const int x = __hip_atomic_load(ctl, __ATOMIC_RELAXED, __HIP_MEMORY_SCOPE_WORKGROUP);
      if (__builtin_amdgcn_readlane(x, C_TICK) >= nt + 8 - k && (!more || (__builtin_amdgcn_readlane(x, C_FC) && __builtin_amdgcn_readlane(x, C_OLD) == 8))) break;
      __builtin_amdgcn_s_sleep(1);
    }
    LDS_FENCE();
    SB();
  };
  const unsigned half = isy ? w : rank - SR;
  if (isy && w >= 4) {
    v4u wt[6][2];
    const rsrc_t rwo = mkrs(wa, XW ? 0 : 32 * D * 2);
    const rsrc_t rwd = mkrs(wb, (y < 112 && !XW) ? 28 * D * 2 : 0);
    unsigned o0 = (lane >> 3) * (D * 2) + (w - 4) * 256 + (lane & 7) * 16, o1 = w == 7 ? OOB : o0 + 128;
    unsigned o2 = o0 + (lane >> 5) * (4 * D * 2), o3 = o1 + (lane >> 5) * (4 * D * 2);
    asm volatile("" : "+v"(o0), "+v"(o1), "+v"(o2), "+v"(o3));
    SB();
    auto ldw = [&](int k) __attribute__((always_inline)) {
      const int i = (k + 4) % 6;
      wt[i][0] = i < 4 ? ld4<2>(rwo, o0, i * 8 * D * 2) : ld4<2>(rwd, o2, (i & 3) * 16 * D * 2);
      wt[i][1] = i < 4 ? ld4<2>(rwo, o1, i * 8 * D * 2) : ld4<2>(rwd, o3, (i & 3) * 16 * D * 2);
      SB();
    };
    if (YE) {
      clear(YK, false);
#pragma unroll
      for (int k = 0; k < YE; ++k) ldw(k);
    }
    LDS_FENCE(); bar(); LDS_FENCE();
    STAMP(2);
    if (YB) __builtin_amdgcn_s_barrier();
    if (YD) __builtin_amdgcn_s_sleep(YD);
    SB();
#pragma unroll
    for (int k = YE; k < 6; ++k) ldw(k);
    const unsigned spare = __builtin_amdgcn_raw_buffer_load_b32(rwo, (int)OOB, 0, 0);
    SB();
    const int p = (w - 4) * 32 + (lane >> 4) * 8 + (lane & 7);
    v4u dt = *reinterpret_cast<const v4u*>(&s_dst[2][p * 4]) & 1u;
    unsigned to = ((lane & 8) || p >= 112) ? OOB : my(p);
    f32x4 acc[2] = {{0.f, 0.f, 0.f, 0.f}, {0.f, 0.f, 0.f, 0.f}};
    auto bop = [&](float a) -> v4u { return pm & __builtin_bit_cast(unsigned, (bf16x2){(__bf16)a, (__bf16)a}); };
    auto step = [&](int i, const v4u& pb) { acc[0] = mfma(wt[i][0], pb, acc[0]); acc[1] = mfma(wt[i][1], pb, acc[1]); };
    lds4_t po = (lds4_t)&s_o[0][(lane >> 3) * 4];
    lds1_t ph = (lds1_t)&s_h[(lane & 4) * 4 + ((lane >> 4) & 1) * 8 + (lane & 3) * 2 + (lane >> 5)];
    asm volatile("" : "+v"(dt), "+v"(to), "+v"(po), "+v"(ph));
    SB();
    const v4u old = *reinterpret_cast<const v4u*>(&s_old[7][lane * 4]);
    v4u dr = old;
    if (w == 7) {
      if (G3) until(G3 + __builtin_amdgcn_readlane(tok, C_LEAD));
      dr = poll<PD3, PG3, 0>(rw4, pa, old);
      const v4u d = dr & ~1u;
      float g = tr16(tr32(asf(d[0]), asf(d[2])), tr32(asf(d[1]), asf(d[3])));
      g = rbf(g + dpp<ROR8>(g));
      const float u = dpp<ROR4>(g);
      *ph = rbf(g / (1.0f + __expf(-g))) * u;
      SB();
    }
    STAMP(14);
    LDS_FENCE(); bar(); LDS_FENCE();
    STAMP(3);
    const f32x2 hv = *reinterpret_cast<const f32x2*>(&s_h[(lane >> 3) * 2]);
    const f32x4 ma = *reinterpret_cast<const f32x4*>(&s_ml2[0]), mb = *reinterpret_cast<const f32x4*>(&s_ml2[4]);
    f32x4 pq[4];
#pragma unroll
    for (int k = 0; k < 4; ++k) pq[k] = po[k * 8];
    SB();
    const v4u h0 = bop(hv.x), h1 = bop(hv.y);
    {
      step(4, h0); step(5, h1);
      asm volatile("" : "+v"(acc[0]), "+v"(acc[1]));
      SB();
    }
    const float M = fmx(fmx(fmx(mb[0], mb[1]), mb[2]), mb[3]);
    f32x4 e = (mb - M) * EXP_C;
#pragma unroll
    for (int k = 0; k < 4; ++k) e[k] = __builtin_amdgcn_exp2f(e[k]);
    const float inv = __builtin_amdgcn_rcpf(__builtin_fmaf(ma[0], e[0], ma[1] * e[1]) + __builtin_fmaf(ma[2], e[2], ma[3] * e[3]));
    f32x2 av[2];
#pragma unroll
    for (int j = 0; j < 2; ++j) av[j] = (f32x2){pq[0][2 * j], pq[0][2 * j + 1]} * e[0];
#pragma unroll
    for (int k = 1; k < 4; ++k) {
#pragma unroll
      for (int j = 0; j < 2; ++j) av[j] = __builtin_elementwise_fma((f32x2){pq[k][2 * j], pq[k][2 * j + 1]}, (f32x2){e[k], e[k]}, av[j]);
    }
    av[0] *= inv; av[1] *= inv;
    {
      v4u pb[4];
#pragma unroll
      for (int i = 0; i < 4; ++i) pb[i] = bop(av[i >> 1][i & 1]);
      if (FB) { asm volatile("" : "+v"(pb[0]), "+v"(pb[1]), "+v"(pb[2]), "+v"(pb[3])); SB(); }
#pragma unroll
      for (int i = 0; i < 4; ++i) step(i, pb[i]);
    }
    f32x4 z;
#pragma unroll
    for (int v = 0; v < 4; ++v) {
      const float a0 = asf((unsigned)__builtin_amdgcn_update_dpp((int)asu(acc[0][v]), (int)asu(acc[0][v]), ROR8, 0xc, 0xf, false));
      const float a1 = asf((unsigned)__builtin_amdgcn_update_dpp((int)asu(acc[1][v]), (int)asu(acc[1][v]), ROR8, 0xc, 0xf, false));
      z[v] = tr32(a0, a1);
    }
    __builtin_amdgcn_raw_buffer_store_b128(tag4(z, dt), rws, (int)to, 0, S2);
    SB();
    STAMP(4);
    asm volatile("" :: "{v198}"(spare));
    poll_end(dr);
    STAMP_OUT();
    __builtin_amdgcn_endpgm();
  }
  if (half < 2) {
    ldsu_t tp = (ldsu_t)&s_dst[4 + half][lane * 4];
    unsigned to = mo_ + half * 4 * AREA;
    lds4_t pp = pr + half * 64;
    asm volatile("" : "+v"(tp), "+v"(to), "+v"(pp));
    SB();
    f32x4 ml[4], pt[8], o = {0.f, 0.f, 0.f, 0.f};
    float M = -3.0e38f, L = 0.f;
    if (PM) {
#pragma unroll 1
      for (int it = 0; it < SPIN_CAP && ctl_get(C_DONE) < PM; ++it) {}
      LDS_FENCE();
#pragma unroll
      for (int k = 0; k < 4; ++k) if ((k & 1) * 4 < PM) ml[k] = pmr[k];
#pragma unroll
      for (int k = 0; k < PM; ++k) pt[k] = pp[k * 128];
      M = ml[0][0];
#pragma unroll
      for (int k = 1; k < PM; ++k) M = fmx(M, ml[k >> 2][k & 3]);
#pragma unroll
      for (int k = 0; k < PM; ++k) {
        const float e = __builtin_amdgcn_exp2f((ml[k >> 2][k & 3] - M) * EXP_C);
        o += pt[k] * e;
        L += ml[2 + (k >> 2)][k & 3] * e;
      }
      asm volatile("" : "+v"(o), "+v"(M), "+v"(L));
      SB();
    }
    LDS_FENCE(); bar(); LDS_FENCE();
    STAMP(2);
#pragma unroll
    for (int k = 0; k < 4; ++k) if ((k & 1) * 4 + 4 > PM) ml[k] = pmr[k];
#pragma unroll
    for (int k = PM; k < 8; ++k) pt[k] = pp[k * 128];
    const v4u t = *tp;
    SB();
    {
      float m2 = M;
#pragma unroll
      for (int k = PM; k < 8; ++k) m2 = fmx(m2, ml[k >> 2][k & 3]);
      if (PM) {
        const float e = __builtin_amdgcn_exp2f((M - m2) * EXP_C);
        o *= e; L *= e;
      }
      M = m2;
#pragma unroll
      for (int k = PM; k < 8; ++k) {
        const float e = __builtin_amdgcn_exp2f((ml[k >> 2][k & 3] - M) * EXP_C);
        o += pt[k] * e;
        L += ml[2 + (k >> 2)][k & 3] * e;
      }
    }
    v4u msg;
#pragma unroll
    for (int j = 0; j < 3; ++j) msg[j] = tag<0x100>(__builtin_amdgcn_perm(asu(o[j]), asu(o[3]), 0x07060501u + j), t[j]);
    msg[3] = tag<1>(asu((lane & 1) ? L : M), t[3]);
    __builtin_amdgcn_raw_buffer_store_b128(msg, rws, (int)to, 0, S1);
    SB();
    if (!isy) STAMP(15);
  } else if (fj < 4) {
    unsigned fvo = ((lane >> 3) * (8 * F * 2) + (lane & 7) * 16) | lmask;
    const unsigned fb = ((f & 3) * 2 + (fj >> 1)) * (56 * F * 2) + (f >> 2) * 128;
    const rsrc_t rf = mkrs((fj & 1) ? wb : wa, FX ? 0 : D * F * 2);
    asm volatile("" : "+v"(fvo));
    clear(FK, true);
    if (FD) __builtin_amdgcn_s_sleep(FD);
    SB();
#pragma unroll
    for (int i = 0; i < 8; ++i) { ring[i] = ld4<2>(rf, fvo, fb + i * (F * 2)); SB(); }
    const v4u xn = *reinterpret_cast<const v4u*>(&s_xn[(((f & 3) * 2 + (fj >> 1)) * 7 + (lane >> 3)) * 4]);
    const int p = fj * 16 + ((lane >> 4) & 1) * 8 + (lane & 7);
    v4u t = *reinterpret_cast<const v4u*>(&s_dst[6][p * 4]);
    unsigned to = (lane & 40) ? OOB : mf(p);
    v4u pb[8];
#pragma unroll
    for (int i = 0; i < 8; ++i) {
      pb[i] = pm & __builtin_amdgcn_perm(xn[i >> 1], xn[i >> 1], (i & 1) ? 0x03020302u : 0x01000100u);
      asm volatile("" : "+v"(pb[i]));
    }
    asm volatile("" : "+v"(t), "+v"(to));
    SB();
    LDS_FENCE(); bar(); LDS_FENCE();
    SB();
    STAMP(2);
    f32x4 acc[2] = {{0.f, 0.f, 0.f, 0.f}, {0.f, 0.f, 0.f, 0.f}};
#pragma unroll
    for (int i = 0; i < 8; ++i) acc[i & 1] = mfma(ring[i], pb[i], acc[i & 1]);
    asm volatile("" :: "v"(pv));
    acc[0] += acc[1];
    f32x4 z;
#pragma unroll
    for (int v = 0; v < 4; ++v)
      z[v] = x32_add(asf((unsigned)__builtin_amdgcn_update_dpp((int)asu(acc[0][v]), (int)asu(acc[0][v]), ROR8, 0xc, 0xf, false)));
    __builtin_amdgcn_raw_buffer_store_b128(tag4(z, t), rws, (int)to, 0, S1);
    SB();
    STAMP(14);
  } else if (!idle) {
    LDS_FENCE(); bar(); LDS_FENCE();
    SB();
    STAMP(2);
  }
  const v4u old = *reinterpret_cast<const v4u*>(&s_old[w][lane * 4]);
  if (isy) {
    if (YB) { SB(); __builtin_amdgcn_s_barrier(); SB(); }
    lds1_t po = (lane & 8) ? (lds1_t)&s_ml2[w] : (lds1_t)&s_o[w][(lane & 7) * 4 + (lane >> 5) * 2 + ((lane >> 4) & 1)];
    lds1_t pq = (lds1_t)&s_ml2[4 + w];
    asm volatile("" : "+v"(po), "+v"(pq));
    if (G1) until(G1 + __builtin_amdgcn_readlane(tok, C_LEAD));
    STAMP(14);
    const v4u dr = poll<PD1, PG1, PS1>(rw4, pa, old);
    STAMP(15);
    const v4u d = dr & (v4u){~0x100u, ~0x100u, ~0x100u, ~1u};
    const float ot = dpp<XOR1>(asf(d[3]));
    const float m = (lane & 1) ? ot : asf(d[3]), l = (lane & 1) ? asf(d[3]) : ot;
    float M = fmx(m, dpp<ROR8>(m)); M = x16_mx(M); M = x32_mx(M);
    *pq = M;
    const float e = __builtin_amdgcn_exp2f((m - M) * EXP_C);
    const float v3 = asf(__builtin_amdgcn_perm(__builtin_amdgcn_perm(d[2], d[1], 0x04000c0cu), d[0], 0x0706000cu));
    float y0 = tr32(asf(d[0]) * e, asf(d[2]) * e), y1 = tr32(asf(d[1]) * e, v3 * e), L = x32_add(l * e);
    y0 = tr16(y0, y1); L = x16_add(L);
    const bool s_ = lane & 8;
    *po = (s_ ? L : y0) + dpp<ROR8>(s_ ? y0 : L);
    LDS_FENCE(); bar(); LDS_FENCE();
    STAMP(3);
    poll_end(dr);
    STAMP(4);
    STAMP_OUT();
    __builtin_amdgcn_endpgm();
  }
  if (isr) {
    const int col = (ro >> 1) * 32 + (ro & 1) * 4 + (lane & 3) * 8 + ((lane >> 2) & 3);
    float xo = asf((unsigned)xg[col] << 16);
    lds1_t po = (lds1_t)&s_r[(lane & 12) ? 8 : w][(lane & 3) * 4 + (lane >> 5) * 2 + ((lane >> 4) & 1)], pq = (lds1_t)&s_r[lane >> 4][lane & 15];
    const rsrc_t ro_ = mkrs(out, D * 2);
    unsigned oo = lane < 16 ? col * 2 : OOB;
    asm volatile("" : "+v"(xo), "+v"(po), "+v"(pq), "+v"(oo));
    if (G2) until(G2 + __builtin_amdgcn_readlane(tok, C_LEAD));
    const v4u dr = poll<PD2, PG2, PS2>(rw4, pa, old);
    const v4u d = dr & ~1u;
    float y0 = tr32(asf(d[0]), asf(d[2])), y1 = tr32(asf(d[1]), asf(d[3]));
    y0 = tr16(y0, y1);
    y0 += dpp<ROR8>(y0); y0 += dpp<ROR4>(y0);
    *po = y0;
    LDS_FENCE(); bar(); LDS_FENCE();
    if (w == 0) {
      float a = pq[0] + pq[64];
      a = x16_add(a); a = x32_add(a);
      __builtin_amdgcn_raw_buffer_store_b16((short)f2bf(xo + a), ro_, (int)oo, 0, SO);
    }
    STAMP(4);
    poll_end(dr);
  }
  if (!isr) STAMP(4);
  STAMP_OUT();
}

extern "C" void kernel_launch(void* const* d_in, const int* in_sizes, int n_in,
                              void* d_out, int out_size, void* d_ws, size_t ws_size,
                              hipStream_t stream) {
  fused_kernel<<<NWG, NT, 0, stream>>>((const u16*)d_in[0], (const u16*)d_in[1], (const u16*)d_in[2], (const u16*)d_in[3],
                                       (const u16*)d_in[4], (const u16*)d_in[6], (const u16*)d_in[7], (const u16*)d_in[8],
                                       (u16*)d_out, (char*)d_ws);
}
```

```cpp
#define PA 46
#define PB 14
#define PC s == 9 && h == 3
#include <hip/hip_runtime.h>
#include <hip/hip_bf16.h>
#include <stdint.h>

#define SWP 0
#define LD 1
#define LU 0
#define LB 1
#define LP 8
#define SKF 0
#define SL0 0
#define GB 1
#define YS 22
#define YE 0
#define YK 0
#define FE 0
#define QPOS 24
#define PACE 8
#define QTRIES 8
#define TQ 123
#define TF 131
#define TFY 129
#define TY 129
#define NP 4
#define TAU 3640
#define PP 3000
#define PL 12
#define TOFF 150
#define TOFFQ 180
#define QBURST 100000
#define PBURST 4000
#define KBURST 100000
#define VBURST 100000
#define QFIRST 24
#define OPOS 8
#define TWD 0
#define OA 16
#define OB 16
#define S1 16
#define S2 16
#define G1 3835
#define G2 3955
#define EO 3
#define PD1 2
#define PG1 6
#define PD2 2
#define PG2 6
#define PD3 2
#define PG3 6
#define G3 3845
#define SR 4
#define PS1 0
#define PS2 0
#define SO 0
#define DK 57
#define DS 0
#define DG 4
#define DB 0
#define SQ 0
#define SF_ 0
#define SFY 0
#define SY 0
#define XA 4
#define XB 4
#define TX 0
#define NOHS 0
#define ES 1
#define EL 8
#define SVM 0
#define SVL 0
#define FO 0
#define FP 1
#define NT3 1
#define UF 1
#define WV 1
#define FX 0
#define PM 5
#define FK 0
#define YB 1
#define FD 0
#define YD 0
#define FB 1
#define XW 0
#define DIAG 0
#define LITE 0

typedef unsigned v4u __attribute__((ext_vector_type(4)));
typedef unsigned v2u __attribute__((ext_vector_type(2)));
typedef float f32x4 __attribute__((ext_vector_type(4)));
typedef float f32x2 __attribute__((ext_vector_type(2)));
typedef __bf16 bf16x2 __attribute__((ext_vector_type(2)));
typedef __bf16 bf16x8 __attribute__((ext_vector_type(8)));
typedef unsigned short u16;
typedef __amdgpu_buffer_rsrc_t rsrc_t;
typedef __attribute__((address_space(3))) f32x4* lds4_t;
typedef __attribute__((address_space(3))) v4u* ldsu_t;
typedef __attribute__((address_space(3))) float* lds1_t;

constexpr int D = 448, SK = 32768, DH = 256, F = 1792, NQ = 4096;
constexpr int NWG = 256, NT = 512;
constexpr unsigned OOB = 0x80000000u;
constexpr unsigned SENT = 0xFFFFFFFFu;
constexpr unsigned AREA = 9216;
constexpr unsigned A_O1 = 1024;
constexpr unsigned A_F = 5120;
constexpr unsigned A_Y2 = 1024;
constexpr unsigned WS_REQ = NWG * AREA;
constexpr unsigned WS_ECHO = WS_REQ + 1024;
constexpr unsigned WS_GO = WS_ECHO + 1024;
constexpr unsigned WS_T = WS_GO + 256;
constexpr unsigned WS_Q = WS_T + 256;
constexpr unsigned WS_END = WS_Q + 8 * 2048;
constexpr int SPIN_CAP = 1 << 18;
constexpr int C_NEXT = 0, C_TICK = 1, C_GO = 2, C_Q = 3, C_T0 = 4, C_FC = 5, C_OLD = 6, C_DONE = 7, C_LEAD = 8, C_CNT = 9;

__device__ __forceinline__ float asf(unsigned u) { return __builtin_bit_cast(float, u); }
__device__ __forceinline__ unsigned asu(float f) { return __builtin_bit_cast(unsigned, f); }
__device__ __forceinline__ float rbf(float f) { __bf16 b = (__bf16)f; return (float)b; }
__device__ __forceinline__ unsigned f2bf(float f) { __bf16 b = (__bf16)f; return (unsigned)__builtin_bit_cast(u16, b); }
__device__ __forceinline__ float dot2(unsigned k, unsigned q, float acc) {
  return __builtin_amdgcn_fdot2_f32_bf16(__builtin_bit_cast(bf16x2, k), __builtin_bit_cast(bf16x2, q), acc, false);
}
__device__ __forceinline__ f32x2 unp(unsigned u) { f32x2 r; r.x = asf(u << 16); r.y = asf(u & 0xffff0000u); return r; }
template <int CTRL> __device__ __forceinline__ float dpp(float x) {
  return asf((unsigned)__builtin_amdgcn_update_dpp(0, (int)asu(x), CTRL, 0xf, 0xf, true));
}
constexpr int XOR1 = 0xB1, XOR2 = 0x4E, XOR7 = 0x141, ROR8 = 0x128, ROR4 = 0x124, ROR2 = 0x122, BCAST = 0x150;
constexpr int QP_EVEN = 0xA0, QP_ODD = 0xF5;
__device__ __forceinline__ float x16_add(float x) { auto s = __builtin_amdgcn_permlane16_swap(asu(x), asu(x), false, false); return asf(s[0]) + asf(s[1]); }
__device__ __forceinline__ float x32_add(float x) { auto s = __builtin_amdgcn_permlane32_swap(asu(x), asu(x), false, false); return asf(s[0]) + asf(s[1]); }
__device__ __forceinline__ float x16_max(float x) { auto s = __builtin_amdgcn_permlane16_swap(asu(x), asu(x), false, false); return fmaxf(asf(s[0]), asf(s[1])); }
__device__ __forceinline__ float x32_max(float x) { auto s = __builtin_amdgcn_permlane32_swap(asu(x), asu(x), false, false); return fmaxf(asf(s[0]), asf(s[1])); }
__device__ __forceinline__ float tr32(float a, float b) { auto s = __builtin_amdgcn_permlane32_swap(asu(a), asu(b), false, false); return asf(s[0]) + asf(s[1]); }
__device__ __forceinline__ float tr16(float a, float b) { auto s = __builtin_amdgcn_permlane16_swap(asu(a), asu(b), false, false); return asf(s[0]) + asf(s[1]); }
__device__ __forceinline__ float fmx(float a, float b) { return __builtin_elementwise_maximum(a, b); }
__device__ __forceinline__ float x16_mx(float x) { auto s = __builtin_amdgcn_permlane16_swap(asu(x), asu(x), false, false); return fmx(asf(s[0]), asf(s[1])); }
__device__ __forceinline__ float x32_mx(float x) { auto s = __builtin_amdgcn_permlane32_swap(asu(x), asu(x), false, false); return fmx(asf(s[0]), asf(s[1])); }
__device__ __forceinline__ float wave_sum(float v) {
  v += dpp<XOR1>(v); v += dpp<XOR2>(v); v += dpp<XOR7>(v); v += dpp<ROR8>(v);
  v = x16_add(v); return x32_add(v);
}
__device__ __forceinline__ float wave_max(float v) {
  v = fmaxf(v, dpp<XOR1>(v)); v = fmaxf(v, dpp<XOR2>(v)); v = fmaxf(v, dpp<XOR7>(v)); v = fmaxf(v, dpp<ROR8>(v));
  v = x16_max(v); return x32_max(v);
}
__device__ __forceinline__ rsrc_t mkrs(const void* p, unsigned bytes) { return __builtin_amdgcn_make_buffer_rsrc((void*)p, 0, (int)bytes, 0x00020000); }
__device__ __forceinline__ rsrc_t selr(bool c, rsrc_t a, rsrc_t b) { return c ? a : b; }
__device__ __forceinline__ unsigned selu(bool c, unsigned a, unsigned b) { return c ? a : b; }
template <int AUX> __device__ __forceinline__ v4u ld4(rsrc_t r, unsigned vo, unsigned so) { return __builtin_amdgcn_raw_buffer_load_b128(r, (int)vo, (int)so, AUX); }
template <int AUX> __device__ __forceinline__ v2u ld2(rsrc_t r, unsigned vo, unsigned so) { return __builtin_amdgcn_raw_buffer_load_b64(r, (int)vo, (int)so, AUX); }
__device__ __forceinline__ void st4c(v4u v, rsrc_t r, unsigned vo) { __builtin_amdgcn_raw_buffer_store_b128(v, r, (int)vo, 0, 16); }
__device__ __forceinline__ void st1c(unsigned v, rsrc_t r, unsigned vo) { __builtin_amdgcn_raw_buffer_store_b32(v, r, (int)vo, 0, 16); }
template <unsigned M> __device__ __forceinline__ unsigned tag(unsigned u, unsigned t) { return (u & ~M) | (t & M); }
__device__ __forceinline__ v4u tag4(const f32x4& v, const v4u& t) { return (v4u){tag<1>(asu(v[0]), t[0]), tag<1>(asu(v[1]), t[1]), tag<1>(asu(v[2]), t[2]), tag<1>(asu(v[3]), t[3])}; }
__device__ __forceinline__ bool arrived(const v4u& v, const v4u& o) { return __builtin_amdgcn_ballot_w64(v[0] == o[0] || v[1] == o[1] || v[2] == o[2] || v[3] == o[3]) == 0; }
__device__ __forceinline__ unsigned lowb(const v4u& v) { return __builtin_amdgcn_perm(v[1], v[0], 0x0c0c0400u) | __builtin_amdgcn_perm(v[3], v[2], 0x04000c0cu); }
#define SB() __builtin_amdgcn_sched_barrier(0)
__device__ __forceinline__ void bar() { __builtin_amdgcn_s_waitcnt(0xC07F); __builtin_amdgcn_s_barrier(); }
#define WAITV(n) __builtin_amdgcn_s_waitcnt(((n) & 15) | (((n) >> 4) << 14) | 0x0F70)
template <int V> struct IC { static constexpr int value = V; constexpr operator int() const { return V; } };
template <int A, int B, class Fn> __device__ __forceinline__ void static_for(Fn&& f) { if constexpr (A < B) { f(IC<A>{}); static_for<A + 1, B>(f); } }
#define LDS_FENCE() asm volatile("" ::: "memory")

constexpr float EXP_C = 0.0625f * 1.4426950408889634f;

struct Att {
  float m, l;
  f32x4 a[2];
};
__device__ __forceinline__ f32x4 mfma(const v4u& a, const v4u& b, const f32x4& c) {
  return __builtin_amdgcn_mfma_f32_16x16x32_bf16(__builtin_bit_cast(bf16x8, a), __builtin_bit_cast(bf16x8, b), c, 0, 0, 0);
}
__device__ __forceinline__ float red_max(float v) { v = fmaxf(v, dpp<XOR1>(v)); v = fmaxf(v, dpp<XOR2>(v)); return x32_max(x16_max(v)); }

template <int NL, class ISSUE>
__device__ __forceinline__ float scores(v4u (&kb)[8], const v4u (&qb)[8], int lane, ISSUE issue) {
  f32x4 d = {0.f, 0.f, 0.f, 0.f};
#pragma unroll
  for (int i = 0; i < NL; ++i) {
    d = mfma(kb[i], qb[i], d);
    asm volatile("" : "+v"(d));
    issue(i);
    SB();
  }
  const bool odd = lane & 1;
  return rbf((odd ? d[3] : d[0]) + dpp<XOR1>(odd ? d[1] : d[2]));
}
template <class ISSUE>
__device__ __forceinline__ float phase_k4(v4u (&kb)[8], const v4u (&qb)[8], Att& st, int lane, ISSUE issue) {
  float s = scores<4>(kb, qb, lane, issue);
  s = (lane & 2) ? -__builtin_inff() : s;
  const float mn = fmaxf(st.m, red_max(s));
  const float al = __builtin_amdgcn_exp2f((st.m - mn) * EXP_C);
  const float p = __builtin_amdgcn_exp2f((s - mn) * EXP_C);
  st.m = mn; st.l = st.l * al + p;
  st.a[0] *= al; st.a[1] *= al;
  SB();
  return p;
}
template <int I, int O, class ISSUE>
__device__ __forceinline__ void pv_step(v4u (&vb)[8], unsigned pp, const v4u& pm, Att& st, ISSUE& issue) {
  const v4u pb = pm & (unsigned)__builtin_amdgcn_update_dpp(0, (int)pp, I * 0x55, 0xf, 0xf, true);
  st.a[0] = mfma(vb[O + 2 * I], pb, st.a[0]);
  asm volatile("" : "+v"(st.a[0]));
  issue(O + 2 * I);
  SB();
  st.a[1] = mfma(vb[O + 2 * I + 1], pb, st.a[1]);
  asm volatile("" : "+v"(st.a[1]));
  issue(O + 2 * I + 1);
  SB();
}
template <int NL, class ISSUE>
__device__ __forceinline__ void phase_v(v4u (&vb)[8], float p, const v4u& pm, Att& st, ISSUE issue) {
  constexpr int O = 8 - NL;
  const unsigned pp = __builtin_bit_cast(unsigned, (bf16x2){(__bf16)p, (__bf16)p});
  pv_step<0, O>(vb, pp, pm, st, issue); pv_step<1, O>(vb, pp, pm, st, issue);
  if constexpr (NL == 8) { pv_step<2, O>(vb, pp, pm, st, issue); pv_step<3, O>(vb, pp, pm, st, issue); }
}

template <int N, int GAP, int SL>
__device__ __forceinline__ v4u poll(const v4u& r, unsigned pa, const v4u& old) {
  v4u d;
  unsigned long long t0, t1, t2;
  int n = SPIN_CAP;
  asm volatile(
      "s_nop 4\n\t"
      "buffer_load_dwordx4 v[200:203], %5, %6, 0 offen sc1\n\t"
      ".rept %11\n\t"
      "s_sleep %12\n\t"
      "buffer_load_dwordx4 v[200:203], %5, %6, 0 offen sc1\n\t"
      ".endr\n"
      "1:\n\t"
      "s_waitcnt vmcnt(%11)\n\t"
      "v_cmp_eq_u32 vcc, v200, %7\n\t"
      "v_cmp_eq_u32 %1, v201, %8\n\t"
      "v_cmp_eq_u32 %2, v202, %9\n\t"
      "v_cmp_eq_u32 %3, v203, %10\n\t"
      "s_or_b64 vcc, vcc, %1\n\t"
      "s_or_b64 %2, %2, %3\n\t"
      "s_or_b64 exec, vcc, %2\n\t"
      "s_cbranch_scc0 2f\n\t"
      ".if %13\n\ts_sleep %13\n\t.endif\n\t"
      "buffer_load_dwordx4 v[200:203], %5, %6, 0 offen sc1\n\t"
      "s_sub_u32 %4, %4, 1\n\t"
      "s_cbranch_scc0 1b\n"
      "2:\n\t"
      "s_mov_b64 exec, -1"
      : "=&{v[200:203]}"(d), "=&s"(t0), "=&s"(t1), "=&s"(t2), "+s"(n)
      : "v"(pa), "s"(r), "v"(old[0]), "v"(old[1]), "v"(old[2]), "v"(old[3]), "n"(N - 1), "n"(GAP), "n"(SL)
      : "vcc", "scc", "memory");
  return d;
}
__device__ __forceinline__ void poll_end(const v4u& d) { asm volatile("s_waitcnt vmcnt(0)" :: "{v[200:203]}"(d)); }

#define STAMP(k) do { if ((k) == PA && (PC)) { SB(); tpa = (int)__builtin_amdgcn_s_memrealtime(); SB(); } if ((k) == PB && (PC)) { SB(); tpb += 5 * ((int)__builtin_amdgcn_s_memrealtime() - tpa); SB(); } } while (0)
#define STAMP_OUT() do { for (int it_ = 0; it_ < 100000 && (int)__builtin_amdgcn_s_memrealtime() - tpb < 0; ++it_) __builtin_amdgcn_s_sleep(1); } while (0)

__device__ __forceinline__ f32x2 ln_stats(const v4u& xa, int lane) {
  f32x2 xv[4];
  float sm = 0.f;
#pragma unroll
  for (int j = 0; j < 4; ++j) { xv[j] = unp(xa[j]); sm += xv[j].x + xv[j].y; }
  const float mean = wave_sum(sm) * (1.0f / D);
  float sq = 0.f;
#pragma unroll
  for (int j = 0; j < 4; ++j) { const f32x2 d = xv[j] - mean; sq += d.x * d.x + d.y * d.y; }
  sq = lane < 56 ? sq : 0.f;
  const float var = rbf(wave_sum(sq) * (1.0f / D));
  const float sd = rbf(sqrtf(rbf(var + 1e-5f)));
  return (f32x2){rbf(mean), 1.0f / sd};
}
__device__ __forceinline__ float ln_apply(float x, f32x2 st) { return rbf(rbf(x - st.x) * st.y); }

__global__ void __launch_bounds__(NT, 2)
fused_kernel(const u16* __restrict__ xg, const u16* __restrict__ kc, const u16* __restrict__ vc, const u16* __restrict__ wq,
             const u16* __restrict__ wo, const u16* __restrict__ wg, const u16* __restrict__ wu, const u16* __restrict__ wd,
             u16* __restrict__ out, char* ws) {
  __shared__ __attribute__((aligned(16))) float s_part[8][512];
  __shared__ __attribute__((aligned(16))) float s_mv[8][64];
  __shared__ __attribute__((aligned(16))) unsigned s_q[256];
  __shared__ __attribute__((aligned(16))) unsigned s_qs[64];
  __shared__ __attribute__((aligned(16))) unsigned s_old[8][256];
  __shared__ __attribute__((aligned(16))) unsigned s_dst[8][256];
  __shared__ __attribute__((aligned(16))) float s_ml[2][16];
  __shared__ __attribute__((aligned(16))) float s_o[4][32];
  __shared__ __attribute__((aligned(16))) float s_ml2[16];
  __shared__ __attribute__((aligned(16))) unsigned s_xn[256];
  __shared__ __attribute__((aligned(16))) float s_h[32];
  __shared__ __attribute__((aligned(16))) float s_r[9][16];
  __shared__ __attribute__((aligned(16))) float s_sc[64][32];
  __shared__ int s_ctl[64];
  const int tid = threadIdx.x, lane = tid & 63, w = __builtin_amdgcn_readfirstlane(tid >> 6);
  const int bi = SWP == 2 ? (blockIdx.x & ~7) | ((blockIdx.x + 7) & 7) : blockIdx.x ^ SWP, h = (((bi & 1) ? bi + 5 : bi) & 6) | (bi >> 7), s = ((bi >> 3) & 15) * 2 + (bi & 1);
  const int b = h * 32 + s;
  static_assert(8 * TQ + 8 * TF + (YS - 16) * TFY + (32 - YS) * TY == 4096 && TQ - NP > 80 && TF - NP > 80 && TFY - NP > 80 && TY - NP > 80, "tickets");
  static_assert(TQ - NP <= 128 && TF - NP <= 128 && TFY - NP <= 128 && TY - NP <= 128, "tickets");
  const int nt = s < 8 ? TQ : s < 16 ? TF : s < YS ? TFY : TY, nk = (nt - NP) >> 1;
  const int first = s * TY - min(s, YS) * (TY - TFY) - min(s, 16) * (TFY - TF) - min(s, 8) * (TF - TQ);
  asm volatile("" :: "s"(xg), "s"(kc), "s"(vc), "s"(wq), "s"(wo), "s"(wg), "s"(wu), "s"(wd), "s"(out), "s"(ws));
  int tpb = (int)__builtin_amdgcn_s_memrealtime() + 4600, tpa = 0;
  STAMP(0);
  const rsrc_t rx = mkrs(xg, D * 2);
  const rsrc_t rk = mkrs(kc + (size_t)h * SK * DH, SK * DH * 2);
  const rsrc_t rv = mkrs(vc + (size_t)h * SK * DH, SK * DH * 2);
  const rsrc_t rws = mkrs(ws, WS_END);
  const unsigned l16 = lane * 16;
  auto ubase = [&](int u) -> unsigned { return (unsigned)(first + 2 * u) * 4096u; };
  const unsigned kvo = ((lane >> 1) & 7) * 512 + (lane >> 4) * 32 + (lane & 1) * 16, vvo = (lane >> 4) * 1024 + (lane & 15) * 16;
  auto ldk = [&](unsigned base, int i) { return ld4<2>(rk, kvo + (i & 3) * 128, base + (i >> 2) * 4096); };
  const unsigned area = (unsigned)b * AREA;
  auto area_of = [&](int ss) -> unsigned { return (unsigned)(h * 32 + ss) * AREA; };
  const unsigned lmask = lane < 56 ? 0u : OOB;

  const bool isq = s < 8, isf = s < 14, isy = s >= 16;
  const int f = h * 14 + s, y = h * 16 + s - 16, ro = h * 4 + (s >> 2);
  const bool isr = s < 16 && ((s + h) & 3) == 0 && ro < 28;
  const bool tile = isq;
  const bool leader = (!(NOHS & 1) && b == NWG - 1 && w == 1), member = (w == 0);

  const unsigned pa = isy ? (w < 4 ? area + A_O1 + w * 1024 + l16 : (w == 7 && y < 112) ? area + A_F + l16 : OOB) : isr ? area + A_Y2 + w * 1024 + l16 : OOB;
  const unsigned mo_ = area_of(16 + (lane & 8) + (lane >> 4)) + A_O1 + s * 128 + (lane & 7) * 16;
  auto mf = [&](int p) -> unsigned {
    const int fd = (f & ~3) + ((p >> 2) & 3);
    return (unsigned)((fd >> 4) * 32 + 16 + (fd & 15)) * AREA + A_F + (f & 3) * 256 + (p >> 4) * 64 + (p & 3) * 16;
  };
  auto my = [&](int p) -> unsigned { return (unsigned)((p >> 4) * 32 + (p & 12) + ((-(p >> 4)) & 3)) * AREA + A_Y2 + y * 64 + (p & 3) * 16; };
  v4u eo, ed;
  auto da_of = [&]() {
    const int p = (w - 2) * 64 + lane;
    unsigned da = (isy && p < 112) ? my(p) : OOB;
    return (w & 6) == 2 ? da : (w & 6) == 4 ? mo_ + (w - 4) * 4 * AREA : (w == 6 && isf) ? mf(lane) : OOB;
  };
  if (EO == 1) { eo = ld4<OA>(rws, pa, 0); SB(); }
  if (EO == 3 || (EO == 4 && !tile)) { eo = ld4<OA>(rws, pa, 0); ed = ld4<OB>(rws, da_of(), 0); SB(); }
  if (EO == 5) { ed = ld4<OB>(rws, da_of(), 0); SB(); }
  const v4u sent = {SENT, SENT, SENT, SENT};
  unsigned poll_off = lane == 1 ? WS_GO : lane < 4 ? WS_T - 32 + l16 : OOB;
  poll_off = lane == 0 ? WS_REQ + (b >> 2) * 16 : poll_off;
  poll_off = leader ? (ES ? lane * 4 * AREA + (lane & 3) * 256 : WS_ECHO + l16) : poll_off;
  auto echoes = [&](unsigned seen) {
    v4u r;
#pragma unroll
    for (int j = 0; j < 4; ++j) r[j] = __builtin_amdgcn_raw_buffer_load_b32(rws, (int)(poll_off | (seen >> j << 31)), j * AREA, 16);
    return r;
  };
  v4u pv = {0u, 0u, 0u, 0u}, e1 = {0u, 0u, 0u, 0u};
  unsigned go_ref = 0;
  int* const ctl = &s_ctl[lane];
  auto ctl_get = [&](int i) { return __builtin_amdgcn_readfirstlane(__hip_atomic_load(&s_ctl[i], __ATOMIC_RELAXED, __HIP_MEMORY_SCOPE_WORKGROUP)); };
  auto ctl_set = [&](int i) { __hip_atomic_store(&s_ctl[i], 1, __ATOMIC_RELAXED, __HIP_MEMORY_SCOPE_WORKGROUP); };
  const unsigned qoff = WS_Q + h * 2048 + l16;
  v4u qv0, qv1;
  if (member) {
    qv0 = ld4<16>(rws, qoff, 0);
    qv1 = ld4<16>(rws, qoff, 1024);
    SB();
    int c = lane == C_NEXT ? (isq ? TOFFQ : TOFF) * 128 : 0;
    c = lane == C_TICK ? 40 : c;
    c = lane == C_T0 ? (int)(__builtin_amdgcn_s_memrealtime() & 0x7fffffff) : c;
    __hip_atomic_store(ctl, c, __ATOMIC_RELAXED, __HIP_MEMORY_SCOPE_WORKGROUP);
    st1c(c, rws, (lane == C_T0 && (bi >> 3) == 12) ? WS_T + (bi & 7) * 4 : OOB);
  }
  *reinterpret_cast<v4u*>(&s_sc[w * 8][lane * 4]) = sent;
  int t0 = 0;
  auto now = [&]() __attribute__((always_inline)) { return ((int)__builtin_amdgcn_s_memrealtime() - t0) & 0x7fffff; };
  const int neg = lane == C_NEXT ? 0 : (int)0x80000000;
  const int xcc = __builtin_amdgcn_s_getreg(20 | (3 << 11));
  constexpr auto tkf = [](int t, int sh) { return TAU + sh * 128 / ((t - NP) / 2 - 8); };
  constexpr auto tvf = [](int t, int sh) { return TAU - sh * 128 / ((t - NP) / 2 + 8 - PL); };
  int tk = s < 8 ? tkf(TQ + 16, SQ) : s < 16 ? tkf(TF, SF_) : s < YS ? tkf(TFY, SFY) : tkf(TY, SY);
  int tv = s < 8 ? tvf(TQ, SQ) : s < 16 ? tvf(TF, SF_) : s < YS ? tvf(TFY, SFY) : tvf(TY, SY);
  int inc = lane == C_NEXT ? tk : lane == C_CNT ? 1 : 0;
  int lost = 0;
  auto token = [&](int burst, bool early = false) __attribute__((always_inline)) {
    const int t = max(now() * 128 - burst, 0);
    if (GB && early) {
      const int o = __hip_atomic_fetch_max(ctl, t | neg, __ATOMIC_RELAXED, __HIP_MEMORY_SCOPE_WORKGROUP);
      lost += max(o, t | neg) - o;
    } else {
      __hip_atomic_fetch_max(ctl, t | neg, __ATOMIC_RELAXED, __HIP_MEMORY_SCOPE_WORKGROUP);
    }
    return __hip_atomic_fetch_add(ctl, inc, __ATOMIC_RELAXED, __HIP_MEMORY_SCOPE_WORKGROUP);
  };
  auto due_of = [&](int x) { return __builtin_amdgcn_readlane(x, C_NEXT) >> 7; };
  auto until = [&](int due) __attribute__((always_inline)) {
#pragma unroll 1
    for (int it = 0; it < 4096 && now() < due; ++it) __builtin_amdgcn_s_sleep(1);
    SB();
  };
  unsigned qs0, qs1;
  auto q_there = [&]() { return __builtin_amdgcn_ballot_w64(((lowb(qv0) ^ qs0) & (lowb(qv1) ^ qs1)) != 0xFFFFFFFFu) == 0; };
  auto load_q = [&]() { qv0 = ld4<16>(rws, qoff, 0); qv1 = ld4<16>(rws, qoff, 1024); };
  auto publish_q = [&]() {
    *reinterpret_cast<v2u*>(&s_q[lane * 2]) = (v2u){__builtin_amdgcn_perm(qv0[1], qv0[0], 0x07060302u), __builtin_amdgcn_perm(qv0[3], qv0[2], 0x07060302u)};
    *reinterpret_cast<v2u*>(&s_q[128 + lane * 2]) = (v2u){__builtin_amdgcn_perm(qv1[1], qv1[0], 0x07060302u), __builtin_amdgcn_perm(qv1[3], qv1[2], 0x07060302u)};
    LDS_FENCE();
    __builtin_amdgcn_s_waitcnt(0xC07F);
    ctl_set(C_Q);
  };
  v4u pre[4][8], ring[8];
  auto unit0 = [&]() {
#pragma unroll
    for (int i = 0; i < 8; ++i) { pre[0][i] = ldk(ubase(w), i); SB(); }
    if (EO == 2) { eo = ld4<OA>(rws, pa, 0); SB(); }
  };
  SB();
  if (tile) {
    v4u tw[8];
    const int oct = 7 * w;
    constexpr int stride = NQ * 2;
    const rsrc_t rt = mkrs(wq, D * stride);
    const unsigned to = (unsigned)((oct + (lane >> 3)) * 8 * stride + (h * 8 + s) * 128 + (lane & 7) * 16) | lmask;
    const v4u xa = ld4<0>(rx, l16, 0);
    const v4u xb = ld4<0>(rx, (unsigned)(oct + (lane >> 3)) * 16 | lmask, 0);
    SB();
#pragma unroll
    for (int i = 0; i < 8; ++i) { tw[i] = ld4<2>(rt, to, i * stride); SB(); }
    if (EO == 4) { eo = ld4<OA>(rws, pa, 0); ed = ld4<OB>(rws, da_of(), 0); SB(); }
    LDS_FENCE(); bar(); LDS_FENCE();
    t0 = ctl_get(C_T0);
    SB();
    const f32x2 ls = ln_stats(xa, lane);
    f32x2 acc[4];
#pragma unroll
    for (int i = 0; i < 8; ++i) {
      const float xn = ln_apply(asf((i & 1) ? (xb[i >> 1] & 0xffff0000u) : (xb[i >> 1] << 16)), ls);
#pragma unroll
      for (int j = 0; j < 4; ++j) acc[j] = i ? acc[j] + unp(tw[i][j]) * xn : unp(tw[i][j]) * xn;
    }
    float y0 = tr32(acc[0].x, acc[2].x), y1 = tr32(acc[0].y, acc[2].y), y2 = tr32(acc[1].x, acc[3].x), y3 = tr32(acc[1].y, acc[3].y);
    y0 = tr16(y0, y2); y1 = tr16(y1, y3);
    const bool s_ = lane & 8;
    y0 = (s_ ? y1 : y0) + dpp<ROR8>(s_ ? y0 : y1);
    s_mv[w][(lane & 7) * 8 + (lane >> 3)] = y0;
    STAMP(6);
    token(QBURST);
    unit0();
    LDS_FENCE(); bar(); LDS_FENCE();
    if (member) {
      float a = 0.f;
#pragma unroll
      for (int k = 0; k < 8; ++k) a += s_mv[k][lane];
      s_qs[lane] = f2bf(a) << 16;
      LDS_FENCE();
      const v4u qs = *reinterpret_cast<const v4u*>(&s_qs[(lane & 15) * 4]) | (~((s & 4) ? qv1 : qv0) & 0xffffu);
      st4c(qs, rws, (lane >> 4) == (s & 3) ? qoff + (s >> 2) * 1024 : OOB);
    }
  } else if (leader && !LU) {
    e1 = ES ? echoes(0) : ld4<16>(rws, poll_off, 0);
    go_ref = __builtin_amdgcn_raw_buffer_load_b32(rws, (int)WS_GO, 0, 16);
  }
  SB();
  STAMP(5);
  SB();
  const u16* wa = isy ? wo + (size_t)y * 32 * D : wg;
  const u16* wb = isy ? wd + (size_t)(y < 112 ? (y >> 1) * 32 + (y & 1) * 4 : 0) * D : wu;
  asm volatile("" : "+s"(wa), "+s"(wb));
  bool ok = !member;
  static_for<0, 40>([&](auto N) {
    constexpr int n = N;
    if constexpr (n == 0) {
      if (!tile) unit0();
      if (LU && leader) {
        e1 = echoes(0);
        go_ref = __builtin_amdgcn_raw_buffer_load_b32(rws, (int)WS_GO, 0, 16);
      }
    }
    if constexpr (n == 8) {
      if (!tile) {
        LDS_FENCE(); bar(); LDS_FENCE();
        t0 = ctl_get(C_T0);
      }
    }
    if constexpr (n == LP) {
      if (leader) {
        e1 += 1u;
        go_ref += 1u;
        st4c(e1, rws, WS_REQ + l16);
      }
    }
    if constexpr (n > 0 && n % 8 == 0) {
      const int x = token((isq || (LB && b == NWG - 1)) ? QBURST : PBURST, true);
      if constexpr (n > QFIRST) {
        if (!ok) {
          ok = q_there();
          if (ok) publish_q();
        }
      }
      if constexpr (n == OPOS + 16) {
        *reinterpret_cast<v4u*>(&s_old[w][lane * 4]) = (int)pa < 0 ? sent : eo;
        *reinterpret_cast<v4u*>(&s_dst[w][lane * 4]) = ~ed;
        LDS_FENCE();
        __hip_atomic_fetch_add(ctl, lane == C_OLD ? 1 : 0, __ATOMIC_RELAXED, __HIP_MEMORY_SCOPE_WORKGROUP);
      }
      SB();
      until(due_of(x));
      if constexpr (n == 8) {
        if (member) { qs0 = lowb(qv0); qs1 = lowb(qv1); }
      }
      if constexpr (n >= QFIRST) {
        if (!ok) load_q();
      }
      if constexpr (n == OPOS) {
        if (EO == 0 || EO == 5) eo = ld4<OA>(rws, pa, 0);
        if (EO < 3) ed = ld4<OB>(rws, da_of(), 0);
      }
      SB();
    }
    if constexpr (n == QPOS) {
      STAMP(9);
      if (ES && leader) pv = LD ? ~e1 : echoes(0);
      else pv = ld4<16>(rws, (member || leader) ? poll_off : OOB, 0);
      SB();
    }
    if constexpr (n >= 8) {
      if constexpr (n >= PACE) WAITV(PACE + ((n >= QPOS && n < QPOS + PACE) ? 1 : 0) + ((n >= OPOS && n < OPOS + PACE) ? (EO == 0 ? 2 : (EO == 3 || EO == 4) ? 0 : 1) : 0));
      if constexpr (n < 32) pre[n >> 3][n & 7] = ldk(ubase((n & 56) + w), n & 7); else ring[n & 7] = ldk(ubase(32 + w), n & 7);
    }
    SB();
    if constexpr (n - PACE == 7 || n - PACE == 15 || n - PACE == 23 || n - PACE == 31) STAMP(16 + (n - PACE) / 8);
  });
  inc = lane == C_TICK ? 1 : inc;
  if (GB == 1) __hip_atomic_fetch_add(ctl, -lost, __ATOMIC_RELAXED, __HIP_MEMORY_SCOPE_WORKGROUP);
  int tok = token(KBURST);
  STAMP(10);
  bool svc = member || leader;
  unsigned seenm = 0, last_echo = 0;
  bool echoed = false;
  auto service = [&]() {
    if (leader) {
      seenm |= (pv[0] == e1[0] ? 1u : 0u) | (pv[1] == e1[1] ? 2u : 0u) | (pv[2] == e1[2] ? 4u : 0u) | (pv[3] == e1[3] ? 8u : 0u);
      svc = __builtin_amdgcn_ballot_w64(seenm != 15u) != 0;
      st1c(go_ref, rws, (lane == 0 && !svc) ? WS_GO : OOB);
    } else {
      const unsigned req = (unsigned)__builtin_amdgcn_readfirstlane((int)((b & 2) ? ((b & 1) ? pv[3] : pv[2]) : ((b & 1) ? pv[1] : pv[0])));
      const unsigned go = (unsigned)__builtin_amdgcn_readlane((int)pv[0], 1);
      go_ref = echoed ? go_ref : go;
      svc = go == (unsigned)__builtin_amdgcn_readfirstlane((int)go_ref);
      if (NOHS & 1) svc = false;
      const bool rdy = ctl_get(C_OLD) == 8;
      if (ES && EL > 0) st4c((v4u){req, req, req, req}, rws, (lane < EL && svc && rdy && !(echoed && req == last_echo)) ? area + ((b >> 2) & 3) * 256 + l16 : OOB);
      else st1c(req, rws, (lane == 0 && svc && rdy && !(echoed && req == last_echo)) ? (ES ? area + ((b >> 2) & 3) * 256 : WS_ECHO + b * 4) : OOB);
      echoed |= rdy; last_echo = req;
    }
    if (ES && leader) pv = echoes(seenm);
    else pv = ld4<16>(rws, svc ? poll_off : OOB, 0);
    SB();
  };

  {
    if (!ok) {
      ok = q_there();
      if (ok) publish_q();
    }
#pragma unroll 1
    for (int it = 0; it < SPIN_CAP && !ctl_get(C_Q); ++it) {
      if (svc && it >= QTRIES) { service(); asm volatile("" :: "v"(pv)); }
      if (!ok) {
        load_q();
        ok = q_there() | !svc;
        if (ok) publish_q();
      } else {
        __builtin_amdgcn_s_sleep(1);
      }
    }
  }
  LDS_FENCE();
  v4u qb[8];
#pragma unroll
  for (int i = 0; i < 4; ++i) {
    const v4u z = {0u, 0u, 0u, 0u}, q = *reinterpret_cast<const v4u*>(&s_q[(lane & 8) * 16 + i * 32 + (lane >> 4) * 8 + (lane & 1) * 4]);
    qb[i] = (lane & 2) ? z : q; qb[4 + i] = (lane & 2) ? q : z;
  }
  v4u pm;
#pragma unroll
  for (int j = 0; j < 4; ++j) pm[j] = ((lane >> 1) & 3) == j ? ((lane & 1) ? 0xffff0000u : 0xffffu) : 0u;
  STAMP(1);
  SB();

  Att st;
  int sx = 20;
  int rank;
  lds4_t pw;
  lds1_t pml;
  int idle;
  auto last = [&]() __attribute__((always_inline)) {
    pml = (lds1_t)&s_ml[(lane >> 3) & 1][rank];
    pw = (lds4_t)&s_part[rank][lane * 4];
    idle = __builtin_amdgcn_readfirstlane(!isy && !svc && rank >= PM && (unsigned)(rank - SR) >= 2 && (!isf || (unsigned)(rank - FO) >= 4));
    asm volatile("" : "+v"(pw), "+v"(pml), "+s"(idle));
    SB();
  };
  auto put_ml = [&]() __attribute__((always_inline)) {
    float l = st.l;
    l += dpp<XOR1>(l); l += dpp<XOR2>(l); l = x32_add(x16_add(l));
    pml[0] = st.m; pml[8] = l;
    SB();
  };
  {
    float* const sc = &s_sc[0][(lane >> 3) * 4 + (lane & 3)];
    rsrc_t nra, nrb;
    unsigned nva, nvb, nba, nbb, nsa, nsb;
    int kind = 0, nu = 32 + w, cu;
    auto next = [&]() __attribute__((always_inline)) {
      cu = nu;
      const int t = __builtin_amdgcn_readlane(tok, C_TICK);
      const bool fx = t < 2 * nk, tw = t >= nt;
      kind = fx ? (t >= nk ? 1 : 0) : tw ? 3 : 2;
      nu = t >= nk ? t - nk : t;
      nba = (unsigned)(fx ? first + 2 * nu : first + t) * 4096u;
      nbb = fx ? nba + 4096 : nba;
      nra = selr(kind == 1, rv, rk);
      nrb = selr(kind == 0, rk, rv);
      nva = selu(kind == 1, vvo, kvo); nvb = selu(kind == 0, kvo, vvo);
      nsa = selu(kind == 1, 256, 128); nsb = selu(kind == 0, 128, 256);
      const int u = t + 8 - nk;
      inc = lane == C_NEXT ? (t + PL >= 2 * nk ? PP : (int)selu(u >= 0, tv, tk) + ((unsigned)(u + XA) < XA + XB ? TX : 0)) : inc;
      rank = t - nt;
      if (!tw) until(due_of(tok));
      SB();
    };
    auto ldn = [&](int i) { ring[i] = i < 4 ? ld4<2>(nra, nva, nba + i * nsa) : ld4<2>(nrb, nvb, nbb + (i - 4) * nsb); };
    float mx;
    if (member) {
      const v4u d = pv - (unsigned)t0;
      const bool bad = max(max(d[0] + 512u, d[1] + 512u), max(d[2] + 512u, d[3] + 512u)) > 1024u;
      const int sm = (int)((d[0] + d[1]) + (d[2] + d[3]));
      const int lead = (__builtin_amdgcn_ballot_w64(bad) & 12) ? 0 : __builtin_amdgcn_readlane(sm, 2) + __builtin_amdgcn_readlane(sm, 3) + DB;
      const int slip = __builtin_amdgcn_readlane(tok, C_NEXT) - __builtin_amdgcn_readlane(tok, C_CNT) * tk - (isq ? TOFFQ : TOFF + SL0) * 128;
      const int adj = ((lead * DK) >> 5) - ((slip * SKF) >> 10);
      tk += adj; tv += adj;
      __hip_atomic_fetch_add(ctl, lane == C_LEAD ? (lead * DG) >> 5 : (lane == C_NEXT ? (lead * DS) >> 5 : 0), __ATOMIC_RELAXED, __HIP_MEMORY_SCOPE_WORKGROUP);
    }
    if (GB == 2) __hip_atomic_fetch_add(ctl, -lost, __ATOMIC_RELAXED, __HIP_MEMORY_SCOPE_WORKGROUP);
    if (svc && !(LD && leader)) service();
    {
      const float s0 = scores<8>(pre[0], qb, lane, [&](int) {});
      const float s1 = scores<8>(pre[1], qb, lane, [&](int) {});
      const float s2 = scores<8>(pre[2], qb, lane, [&](int) {});
      const float s3 = scores<8>(pre[3], qb, lane, [&](int) {});
      sc[w * 32] = s0; sc[(8 + w) * 32] = s1; sc[(16 + w) * 32] = s2; sc[(24 + w) * 32] = s3;
      mx = fmaxf(fmaxf(s0, s1), fmaxf(s2, s3));
      SB();
      STAMP(11);
    }
    const v4u xa2 = ld4<0>(rx, (isf && w == 7) ? l16 : OOB, 0);
    SB();
#pragma unroll 1
    while (kind == 0) {
      next();
      const float sv = scores<8>(ring, qb, lane, ldn);
      sc[cu * 32] = sv;
      tok = token(KBURST);
      mx = fmaxf(mx, sv);
      STAMP(sx); ++sx;
      if (svc && !(LD && leader)) service();
      SB();
    }
    STAMP(7);
    if (GB == 3) __hip_atomic_fetch_add(ctl, -lost, __ATOMIC_RELAXED, __HIP_MEMORY_SCOPE_WORKGROUP);
    if (LD == 1 && leader) { pv = echoes(seenm); SB(); }
    if (isf && w == 7) {
      const f32x2 ls = ln_stats(xa2, lane);
      v4u xn;
#pragma unroll
      for (int j = 0; j < 4; ++j) {
        const f32x2 v = unp(xa2[j]);
        xn[j] = __builtin_amdgcn_perm(asu(ln_apply(v.y, ls)), asu(ln_apply(v.x, ls)), 0x07060302u);
      }
      *reinterpret_cast<v4u*>(&s_xn[lane * 4]) = xn;
      LDS_FENCE();
      ctl_set(C_FC);
      SB();
    }
    st.m = red_max(mx); st.l = 0.f;
    st.a[0] = st.a[1] = (f32x4){0.f, 0.f, 0.f, 0.f};
#pragma unroll 1
    while (kind == 1) {
      float sv;
      for (;;) {
        sv = __hip_atomic_load(&sc[nu * 32], __ATOMIC_RELAXED, __HIP_MEMORY_SCOPE_WORKGROUP);
        if (__builtin_amdgcn_ballot_w64(asu(sv) == SENT) == 0) break;
        __builtin_amdgcn_s_sleep(1);
      }
      if (__builtin_amdgcn_ballot_w64(sv > st.m) != 0) {
        const float m2 = fmaxf(st.m, red_max(sv));
        const float al = __builtin_amdgcn_exp2f((st.m - m2) * EXP_C);
        st.m = m2; st.l *= al;
        st.a[0] *= al; st.a[1] *= al;
      }
      const float p = __builtin_amdgcn_exp2f((sv - st.m) * EXP_C);
      st.l += p;
      SB();
      next();
      if (kind == 3) {
        last();
        put_ml();
        phase_v<8>(ring, p, pm, st, [&](int) {});
        break;
      }
      phase_v<8>(ring, p, pm, st, ldn);
      tok = token(VBURST);
      STAMP(sx); ++sx;
      if (svc) service();
      SB();
    }
    STAMP(8);
    if (kind == 2) {
      next();
#pragma unroll 1
    while (kind != 3) {
      if (UF) { nba = __builtin_amdgcn_readfirstlane(nba); nbb = __builtin_amdgcn_readfirstlane(nbb); nsa = __builtin_amdgcn_readfirstlane(nsa); nsb = __builtin_amdgcn_readfirstlane(nsb); }
      const float p = phase_k4(ring, qb, st, lane, ldn);
      phase_v<4>(ring, p, pm, st, ldn);
      tok = token(VBURST);
      STAMP(sx); ++sx;
      SB();
      next();
    }
      SB();
      last();
      const float p = phase_k4(ring, qb, st, lane, [&](int) {});
      phase_v<4>(ring, p, pm, st, [&](int) {});
      put_ml();
    }
    pw[0] = st.a[0]; pw[64] = st.a[1];
    LDS_FENCE();
    SB();
    STAMP(12);
    if (idle) {
      LDS_FENCE(); bar(); LDS_FENCE();
      STAMP(2);
    }
    SB();
#pragma unroll 1
    for (int it = 0; it < SPIN_CAP && svc; ++it) {
      service();
      asm volatile("" :: "v"(pv));
    }
    LDS_FENCE();
    WAITV(0);
  }
  const lds4_t pr = (lds4_t)&s_part[0][lane * 4], pmr = (lds4_t)&s_ml[(lane >> 3) & 1][0];
  if (rank < PM) __hip_atomic_fetch_add(ctl, lane == C_DONE ? 1 : 0, __ATOMIC_RELAXED, __HIP_MEMORY_SCOPE_WORKGROUP);
  SB();
  const v4u rw4 = {(unsigned)(size_t)ws, (unsigned)((size_t)ws >> 32), WS_END, 0x00020000u};
  const unsigned fj = isf ? rank - FO : 4;
  auto clear = [&](int k, bool more) __attribute__((always_inline)) {
#pragma unroll 1
    for (int it = 0; it < SPIN_CAP; ++it) {
      const int x = __hip_atomic_load(ctl, __ATOMIC_RELAXED, __HIP_MEMORY_SCOPE_WORKGROUP);
      if (__builtin_amdgcn_readlane(x, C_TICK) >= nt + 8 - k && (!more || (__builtin_amdgcn_readlane(x, C_FC) && __builtin_amdgcn_readlane(x, C_OLD) == 8))) break;
      __builtin_amdgcn_s_sleep(1);
    }
    LDS_FENCE();
    SB();
  };
  const unsigned half = isy ? w : rank - SR;
  if (isy && w >= 4) {
    v4u wt[6][2];
    const rsrc_t rwo = mkrs(wa, XW ? 0 : 32 * D * 2);
    const rsrc_t rwd = mkrs(wb, (y < 112 && !XW) ? 28 * D * 2 : 0);
    unsigned o0 = (lane >> 3) * (D * 2) + (w - 4) * 256 + (lane & 7) * 16, o1 = w == 7 ? OOB : o0 + 128;
    unsigned o2 = o0 + (lane >> 5) * (4 * D * 2), o3 = o1 + (lane >> 5) * (4 * D * 2);
    asm volatile("" : "+v"(o0), "+v"(o1), "+v"(o2), "+v"(o3));
    SB();
    auto ldw = [&](int k) __attribute__((always_inline)) {
      const int i = (k + 4) % 6;
      wt[i][0] = i < 4 ? ld4<2>(rwo, o0, i * 8 * D * 2) : ld4<2>(rwd, o2, (i & 3) * 16 * D * 2);
      wt[i][1] = i < 4 ? ld4<2>(rwo, o1, i * 8 * D * 2) : ld4<2>(rwd, o3, (i & 3) * 16 * D * 2);
      SB();
    };
    if (YE) {
      clear(YK, false);
#pragma unroll
      for (int k = 0; k < YE; ++k) ldw(k);
    }
    LDS_FENCE(); bar(); LDS_FENCE();
    STAMP(2);
    if (YB) __builtin_amdgcn_s_barrier();
    if (YD) __builtin_amdgcn_s_sleep(YD);
    SB();
#pragma unroll
    for (int k = YE; k < 6; ++k) ldw(k);
    const unsigned spare = __builtin_amdgcn_raw_buffer_load_b32(rwo, (int)OOB, 0, 0);
    SB();
    const int p = (w - 4) * 32 + (lane >> 4) * 8 + (lane & 7);
    v4u dt = *reinterpret_cast<const v4u*>(&s_dst[2][p * 4]) & 1u;
    unsigned to = ((lane & 8) || p >= 112) ? OOB : my(p);
    f32x4 acc[2] = {{0.f, 0.f, 0.f, 0.f}, {0.f, 0.f, 0.f, 0.f}};
    auto bop = [&](float a) -> v4u { return pm & __builtin_bit_cast(unsigned, (bf16x2){(__bf16)a, (__bf16)a}); };
    auto step = [&](int i, const v4u& pb) { acc[0] = mfma(wt[i][0], pb, acc[0]); acc[1] = mfma(wt[i][1], pb, acc[1]); };
    lds4_t po = (lds4_t)&s_o[0][(lane >> 3) * 4];
    lds1_t ph = (lds1_t)&s_h[(lane & 4) * 4 + ((lane >> 4) & 1) * 8 + (lane & 3) * 2 + (lane >> 5)];
    asm volatile("" : "+v"(dt), "+v"(to), "+v"(po), "+v"(ph));
    SB();
    const v4u old = *reinterpret_cast<const v4u*>(&s_old[7][lane * 4]);
    v4u dr = old;
    if (w == 7) {
      if (G3) until(G3 + __builtin_amdgcn_readlane(tok, C_LEAD));
      dr = poll<PD3, PG3, 0>(rw4, pa, old);
      const v4u d = dr & ~1u;
      float g = tr16(tr32(asf(d[0]), asf(d[2])), tr32(asf(d[1]), asf(d[3])));
      g = rbf(g + dpp<ROR8>(g));
      const float u = dpp<ROR4>(g);
      *ph = rbf(g / (1.0f + __expf(-g))) * u;
      SB();
    }
    STAMP(14);
    LDS_FENCE(); bar(); LDS_FENCE();
    STAMP(3);
    const f32x2 hv = *reinterpret_cast<const f32x2*>(&s_h[(lane >> 3) * 2]);
    const f32x4 ma = *reinterpret_cast<const f32x4*>(&s_ml2[0]), mb = *reinterpret_cast<const f32x4*>(&s_ml2[4]);
    f32x4 pq[4];
#pragma unroll
    for (int k = 0; k < 4; ++k) pq[k] = po[k * 8];
    SB();
    const v4u h0 = bop(hv.x), h1 = bop(hv.y);
    {
      step(4, h0); step(5, h1);
      asm volatile("" : "+v"(acc[0]), "+v"(acc[1]));
      SB();
    }
    const float M = fmx(fmx(fmx(mb[0], mb[1]), mb[2]), mb[3]);
    f32x4 e = (mb - M) * EXP_C;
#pragma unroll
    for (int k = 0; k < 4; ++k) e[k] = __builtin_amdgcn_exp2f(e[k]);
    const float inv = __builtin_amdgcn_rcpf(__builtin_fmaf(ma[0], e[0], ma[1] * e[1]) + __builtin_fmaf(ma[2], e[2], ma[3] * e[3]));
    f32x2 av[2];
#pragma unroll
    for (int j = 0; j < 2; ++j) av[j] = (f32x2){pq[0][2 * j], pq[0][2 * j + 1]} * e[0];
#pragma unroll
    for (int k = 1; k < 4; ++k) {
#pragma unroll
      for (int j = 0; j < 2; ++j) av[j] = __builtin_elementwise_fma((f32x2){pq[k][2 * j], pq[k][2 * j + 1]}, (f32x2){e[k], e[k]}, av[j]);
    }
    av[0] *= inv; av[1] *= inv;
    {
      v4u pb[4];
#pragma unroll
      for (int i = 0; i < 4; ++i) pb[i] = bop(av[i >> 1][i & 1]);
      if (FB) { asm volatile("" : "+v"(pb[0]), "+v"(pb[1]), "+v"(pb[2]), "+v"(pb[3])); SB(); }
#pragma unroll
      for (int i = 0; i < 4; ++i) step(i, pb[i]);
    }
    f32x4 z;
#pragma unroll
    for (int v = 0; v < 4; ++v) {
      const float a0 = asf((unsigned)__builtin_amdgcn_update_dpp((int)asu(acc[0][v]), (int)asu(acc[0][v]), ROR8, 0xc, 0xf, false));
      const float a1 = asf((unsigned)__builtin_amdgcn_update_dpp((int)asu(acc[1][v]), (int)asu(acc[1][v]), ROR8, 0xc, 0xf, false));
      z[v] = tr32(a0, a1);
    }
    __builtin_amdgcn_raw_buffer_store_b128(tag4(z, dt), rws, (int)to, 0, S2);
    SB();
    STAMP(4);
    asm volatile("" :: "{v198}"(spare));
    poll_end(dr);
    STAMP_OUT();
    __builtin_amdgcn_endpgm();
  }
  if (half < 2) {
    ldsu_t tp = (ldsu_t)&s_dst[4 + half][lane * 4];
    unsigned to = mo_ + half * 4 * AREA;
    lds4_t pp = pr + half * 64;
    asm volatile("" : "+v"(tp), "+v"(to), "+v"(pp));
    SB();
    f32x4 ml[4], pt[8], o = {0.f, 0.f, 0.f, 0.f};
    float M = -3.0e38f, L = 0.f;
    if (PM) {
#pragma unroll 1
      for (int it = 0; it < SPIN_CAP && ctl_get(C_DONE) < PM; ++it) {}
      LDS_FENCE();
#pragma unroll
      for (int k = 0; k < 4; ++k) if ((k & 1) * 4 < PM) ml[k] = pmr[k];
#pragma unroll
      for (int k = 0; k < PM; ++k) pt[k] = pp[k * 128];
      M = ml[0][0];
#pragma unroll
      for (int k = 1; k < PM; ++k) M = fmx(M, ml[k >> 2][k & 3]);
#pragma unroll
      for (int k = 0; k < PM; ++k) {
        const float e = __builtin_amdgcn_exp2f((ml[k >> 2][k & 3] - M) * EXP_C);
        o += pt[k] * e;
        L += ml[2 + (k >> 2)][k & 3] * e;
      }
      asm volatile("" : "+v"(o), "+v"(M), "+v"(L));
      SB();
    }
    LDS_FENCE(); bar(); LDS_FENCE();
    STAMP(2);
#pragma unroll
    for (int k = 0; k < 4; ++k) if ((k & 1) * 4 + 4 > PM) ml[k] = pmr[k];
#pragma unroll
    for (int k = PM; k < 8; ++k) pt[k] = pp[k * 128];
    const v4u t = *tp;
    SB();
    {
      float m2 = M;
#pragma unroll
      for (int k = PM; k < 8; ++k) m2 = fmx(m2, ml[k >> 2][k & 3]);
      if (PM) {
        const float e = __builtin_amdgcn_exp2f((M - m2) * EXP_C);
        o *= e; L *= e;
      }
      M = m2;
#pragma unroll
      for (int k = PM; k < 8; ++k) {
        const float e = __builtin_amdgcn_exp2f((ml[k >> 2][k & 3] - M) * EXP_C);
        o += pt[k] * e;
        L += ml[2 + (k >> 2)][k & 3] * e;
      }
    }
    v4u msg;
#pragma unroll
    for (int j = 0; j < 3; ++j) msg[j] = tag<0x100>(__builtin_amdgcn_perm(asu(o[j]), asu(o[3]), 0x07060501u + j), t[j]);
    msg[3] = tag<1>(asu((lane & 1) ? L : M), t[3]);
    __builtin_amdgcn_raw_buffer_store_b128(msg, rws, (int)to, 0, S1);
    SB();
    if (!isy) STAMP(15);
  } else if (fj < 4) {
    unsigned fvo = ((lane >> 3) * (8 * F * 2) + (lane & 7) * 16) | lmask;
    const unsigned fb = ((f & 3) * 2 + (fj >> 1)) * (56 * F * 2) + (f >> 2) * 128;
    const rsrc_t rf = mkrs((fj & 1) ? wb : wa, FX ? 0 : D * F * 2);
    asm volatile("" : "+v"(fvo));
#pragma unroll
    for (int i = 0; i < FE; ++i) { ring[i] = ld4<2>(rf, fvo, fb + i * (F * 2)); SB(); }
    clear(FK, true);
    if (FD) __builtin_amdgcn_s_sleep(FD);
    SB();
    STAMP(46);
#pragma unroll
    for (int i = FE; i < 8; ++i) { ring[i] = ld4<2>(rf, fvo, fb + i * (F * 2)); SB(); }
    const v4u xn = *reinterpret_cast<const v4u*>(&s_xn[(((f & 3) * 2 + (fj >> 1)) * 7 + (lane >> 3)) * 4]);
    const int p = fj * 16 + ((lane >> 4) & 1) * 8 + (lane & 7);
    v4u t = *reinterpret_cast<const v4u*>(&s_dst[6][p * 4]);
    unsigned to = (lane & 40) ? OOB : mf(p);
    v4u pb[8];
#pragma unroll
    for (int i = 0; i < 8; ++i) {
      pb[i] = pm & __builtin_amdgcn_perm(xn[i >> 1], xn[i >> 1], (i & 1) ? 0x03020302u : 0x01000100u);
      asm volatile("" : "+v"(pb[i]));
    }
    asm volatile("" : "+v"(t), "+v"(to));
    SB();
    LDS_FENCE(); bar(); LDS_FENCE();
    SB();
    STAMP(2);
    f32x4 acc[2] = {{0.f, 0.f, 0.f, 0.f}, {0.f, 0.f, 0.f, 0.f}};
#pragma unroll
    for (int i = 0; i < 8; ++i) acc[i & 1] = mfma(ring[i], pb[i], acc[i & 1]);
    asm volatile("" :: "v"(pv));
    acc[0] += acc[1];
    f32x4 z;
#pragma unroll
    for (int v = 0; v < 4; ++v)
      z[v] = x32_add(asf((unsigned)__builtin_amdgcn_update_dpp((int)asu(acc[0][v]), (int)asu(acc[0][v]), ROR8, 0xc, 0xf, false)));
    __builtin_amdgcn_raw_buffer_store_b128(tag4(z, t), rws, (int)to, 0, S1);
    SB();
    STAMP(14);
  } else if (!idle) {
    LDS_FENCE(); bar(); LDS_FENCE();
    SB();
    STAMP(2);
  }
  const v4u old = *reinterpret_cast<const v4u*>(&s_old[w][lane * 4]);
  if (isy) {
    if (YB) { SB(); __builtin_amdgcn_s_barrier(); SB(); }
    lds1_t po = (lane & 8) ? (lds1_t)&s_ml2[w] : (lds1_t)&s_o[w][(lane & 7) * 4 + (lane >> 5) * 2 + ((lane >> 4) & 1)];
    lds1_t pq = (lds1_t)&s_ml2[4 + w];
    asm volatile("" : "+v"(po), "+v"(pq));
    if (G1) until(G1 + __builtin_amdgcn_readlane(tok, C_LEAD));
    STAMP(14);
    const v4u dr = poll<PD1, PG1, PS1>(rw4, pa, old);
    STAMP(15);
    const v4u d = dr & (v4u){~0x100u, ~0x100u, ~0x100u, ~1u};
    const float ot = dpp<XOR1>(asf(d[3]));
    const float m = (lane & 1) ? ot : asf(d[3]), l = (lane & 1) ? asf(d[3]) : ot;
    float M = fmx(m, dpp<ROR8>(m)); M = x16_mx(M); M = x32_mx(M);
    *pq = M;
    const float e = __builtin_amdgcn_exp2f((m - M) * EXP_C);
    const float v3 = asf(__builtin_amdgcn_perm(__builtin_amdgcn_perm(d[2], d[1], 0x04000c0cu), d[0], 0x0706000cu));
    float y0 = tr32(asf(d[0]) * e, asf(d[2]) * e), y1 = tr32(asf(d[1]) * e, v3 * e), L = x32_add(l * e);
    y0 = tr16(y0, y1); L = x16_add(L);
    const bool s_ = lane & 8;
    *po = (s_ ? L : y0) + dpp<ROR8>(s_ ? y0 : L);
    LDS_FENCE(); bar(); LDS_FENCE();
    STAMP(3);
    poll_end(dr);
    STAMP(4);
    STAMP_OUT();
    __builtin_amdgcn_endpgm();
  }
  if (isr) {
    const int col = (ro >> 1) * 32 + (ro & 1) * 4 + (lane & 3) * 8 + ((lane >> 2) & 3);
    float xo = asf((unsigned)xg[col] << 16);
    lds1_t po = (lds1_t)&s_r[(lane & 12) ? 8 : w][(lane & 3) * 4 + (lane >> 5) * 2 + ((lane >> 4) & 1)], pq = (lds1_t)&s_r[lane >> 4][lane & 15];
    const rsrc_t ro_ = mkrs(out, D * 2);
    unsigned oo = lane < 16 ? col * 2 : OOB;
    asm volatile("" : "+v"(xo), "+v"(po), "+v"(pq), "+v"(oo));
    if (G2) until(G2 + __builtin_amdgcn_readlane(tok, C_LEAD));
    const v4u dr = poll<PD2, PG2, PS2>(rw4, pa, old);
    const v4u d = dr & ~1u;
    float y0 = tr32(asf(d[0]), asf(d[2])), y1 = tr32(asf(d[1]), asf(d[3]));
    y0 = tr16(y0, y1);
    y0 += dpp<ROR8>(y0); y0 += dpp<ROR4>(y0);
    *po = y0;
    LDS_FENCE(); bar(); LDS_FENCE();
    if (w == 0) {
      float a = pq[0] + pq[64];
      a = x16_add(a); a = x32_add(a);
      __builtin_amdgcn_raw_buffer_store_b16((short)f2bf(xo + a), ro_, (int)oo, 0, SO);
    }
    STAMP(4);
    poll_end(dr);
  }
  if (!isr) STAMP(4);
  STAMP_OUT();
}

extern "C" void kernel_launch(void* const* d_in, const int* in_sizes, int n_in,
                              void* d_out, int out_size, void* d_ws, size_t ws_size,
                              hipStream_t stream) {
  fused_kernel<<<NWG, NT, 0, stream>>>((const u16*)d_in[0], (const u16*)d_in[1], (const u16*)d_in[2], (const u16*)d_in[3],
                                       (const u16*)d_in[4], (const u16*)d_in[6], (const u16*)d_in[7], (const u16*)d_in[8],
                                       (u16*)d_out, (char*)d_ws);
}
```

```cpp
#include <hip/hip_runtime.h>
#include <hip/hip_bf16.h>
#include <stdint.h>

#define SWP 0
#define LD 1
#define LU 0
#define LB 1
#define LP 8
#define SKF 0
#define SL0 0
#define GB 1
#define YS 22
#define YE 0
#define YK 0
#define FE 0
#define QPOS 24
#define PACE 8
#define QTRIES 8
#define TQ 123
#define TF 131
#define TFY 129
#define TY 129
#define NP 4
#define TAU 3640
#define PP 3000
#define PL 12
#define TOFF 150
#define TOFFQ 180
#define QBURST 100000
#define PBURST 4000
#define KBURST 100000
#define VBURST 100000
#define QFIRST 24
#define OPOS 8
#define TWD 0
#define OA 16
#define OB 16
#define S1 16
#define S2 16
#define G1 3835
#define G2 3955
#define EO 3
#define PD1 2
#define PG1 6
#define PD2 2
#define PG2 6
#define PD3 2
#define PG3 6
#define G3 3845
#define SR 4
#define PS1 0
#define PS2 0
#define SO 0
#define DK 57
#define DS 0
#define DG 4
#define DB 0
#define SQ 0
#define SF_ 0
#define SFY 0
#define SY 0
#define XA 4
#define XB 4
#define TX 0
#define NOHS 0
#define ES 1
#define EL 8
#define SVM 0
#define SVL 0
#define FO 0
#define FP 1
#define NT3 1
#define UF 1
#define WV 1
#define FX 0
#define PM 5
#define FK 0
#define YB 1
#define FD 0
#define YD 0
#define FB 1
#define XW 0
#define DIAG 0
#define LITE 0

typedef unsigned v4u __attribute__((ext_vector_type(4)));
typedef unsigned v2u __attribute__((ext_vector_type(2)));
typedef float f32x4 __attribute__((ext_vector_type(4)));
typedef float f32x2 __attribute__((ext_vector_type(2)));
typedef __bf16 bf16x2 __attribute__((ext_vector_type(2)));
typedef __bf16 bf16x8 __attribute__((ext_vector_type(8)));
typedef unsigned short u16;
typedef __amdgpu_buffer_rsrc_t rsrc_t;
typedef __attribute__((address_space(3))) f32x4* lds4_t;
typedef __attribute__((address_space(3))) v4u* ldsu_t;
typedef __attribute__((address_space(3))) float* lds1_t;

constexpr int D = 448, SK = 32768, DH = 256, F = 1792, NQ = 4096;
constexpr int NWG = 256, NT = 512;
constexpr unsigned OOB = 0x80000000u;
constexpr unsigned SENT = 0xFFFFFFFFu;
constexpr unsigned AREA = 9216;
constexpr unsigned A_O1 = 1024;
constexpr unsigned A_F = 5120;
constexpr unsigned A_Y2 = 1024;
constexpr unsigned WS_REQ = NWG * AREA;
constexpr unsigned WS_ECHO = WS_REQ + 1024;
constexpr unsigned WS_GO = WS_ECHO + 1024;
constexpr unsigned WS_T = WS_GO + 256;
constexpr unsigned WS_Q = WS_T + 256;
constexpr unsigned WS_END = WS_Q + 8 * 2048;
constexpr int SPIN_CAP = 1 << 18;
constexpr int C_NEXT = 0, C_TICK = 1, C_GO = 2, C_Q = 3, C_T0 = 4, C_FC = 5, C_OLD = 6, C_DONE = 7, C_LEAD = 8, C_CNT = 9;

__device__ __forceinline__ float asf(unsigned u) { return __builtin_bit_cast(float, u); }
__device__ __forceinline__ unsigned asu(float f) { return __builtin_bit_cast(unsigned, f); }
__device__ __forceinline__ float rbf(float f) { __bf16 b = (__bf16)f; return (float)b; }
__device__ __forceinline__ unsigned f2bf(float f) { __bf16 b = (__bf16)f; return (unsigned)__builtin_bit_cast(u16, b); }
__device__ __forceinline__ float dot2(unsigned k, unsigned q, float acc) {
  return __builtin_amdgcn_fdot2_f32_bf16(__builtin_bit_cast(bf16x2, k), __builtin_bit_cast(bf16x2, q), acc, false);
}
__device__ __forceinline__ f32x2 unp(unsigned u) { f32x2 r; r.x = asf(u << 16); r.y = asf(u & 0xffff0000u); return r; }
template <int CTRL> __device__ __forceinline__ float dpp(float x) {
  return asf((unsigned)__builtin_amdgcn_update_dpp(0, (int)asu(x), CTRL, 0xf, 0xf, true));
}
constexpr int XOR1 = 0xB1, XOR2 = 0x4E, XOR7 = 0x141, ROR8 = 0x128, ROR4 = 0x124, ROR2 = 0x122, BCAST = 0x150;
constexpr int QP_EVEN = 0xA0, QP_ODD = 0xF5;
__device__ __forceinline__ float x16_add(float x) { auto s = __builtin_amdgcn_permlane16_swap(asu(x), asu(x), false, false); return asf(s[0]) + asf(s[1]); }
__device__ __forceinline__ float x32_add(float x) { auto s = __builtin_amdgcn_permlane32_swap(asu(x), asu(x), false, false); return asf(s[0]) + asf(s[1]); }
__device__ __forceinline__ float x16_max(float x) { auto s = __builtin_amdgcn_permlane16_swap(asu(x), asu(x), false, false); return fmaxf(asf(s[0]), asf(s[1])); }
__device__ __forceinline__ float x32_max(float x) { auto s = __builtin_amdgcn_permlane32_swap(asu(x), asu(x), false, false); return fmaxf(asf(s[0]), asf(s[1])); }
__device__ __forceinline__ float tr32(float a, float b) { auto s = __builtin_amdgcn_permlane32_swap(asu(a), asu(b), false, false); return asf(s[0]) + asf(s[1]); }
__device__ __forceinline__ float tr16(float a, float b) { auto s = __builtin_amdgcn_permlane16_swap(asu(a), asu(b), false, false); return asf(s[0]) + asf(s[1]); }
__device__ __forceinline__ float fmx(float a, float b) { return __builtin_elementwise_maximum(a, b); }
__device__ __forceinline__ float x16_mx(float x) { auto s = __builtin_amdgcn_permlane16_swap(asu(x), asu(x), false, false); return fmx(asf(s[0]), asf(s[1])); }
__device__ __forceinline__ float x32_mx(float x) { auto s = __builtin_amdgcn_permlane32_swap(asu(x), asu(x), false, false); return fmx(asf(s[0]), asf(s[1])); }
__device__ __forceinline__ float wave_sum(float v) {
  v += dpp<XOR1>(v); v += dpp<XOR2>(v); v += dpp<XOR7>(v); v += dpp<ROR8>(v);
  v = x16_add(v); return x32_add(v);
}
__device__ __forceinline__ float wave_max(float v) {
  v = fmaxf(v, dpp<XOR1>(v)); v = fmaxf(v, dpp<XOR2>(v)); v = fmaxf(v, dpp<XOR7>(v)); v = fmaxf(v, dpp<ROR8>(v));
  v = x16_max(v); return x32_max(v);
}
__device__ __forceinline__ rsrc_t mkrs(const void* p, unsigned bytes) { return __builtin_amdgcn_make_buffer_rsrc((void*)p, 0, (int)bytes, 0x00020000); }
__device__ __forceinline__ rsrc_t selr(bool c, rsrc_t a, rsrc_t b) { return c ? a : b; }
__device__ __forceinline__ unsigned selu(bool c, unsigned a, unsigned b) { return c ? a : b; }
template <int AUX> __device__ __forceinline__ v4u ld4(rsrc_t r, unsigned vo, unsigned so) { return __builtin_amdgcn_raw_buffer_load_b128(r, (int)vo, (int)so, AUX); }
template <int AUX> __device__ __forceinline__ v2u ld2(rsrc_t r, unsigned vo, unsigned so) { return __builtin_amdgcn_raw_buffer_load_b64(r, (int)vo, (int)so, AUX); }
__device__ __forceinline__ void st4c(v4u v, rsrc_t r, unsigned vo) { __builtin_amdgcn_raw_buffer_store_b128(v, r, (int)vo, 0, 16); }
__device__ __forceinline__ void st1c(unsigned v, rsrc_t r, unsigned vo) { __builtin_amdgcn_raw_buffer_store_b32(v, r, (int)vo, 0, 16); }
template <unsigned M> __device__ __forceinline__ unsigned tag(unsigned u, unsigned t) { return (u & ~M) | (t & M); }
__device__ __forceinline__ v4u tag4(const f32x4& v, const v4u& t) { return (v4u){tag<1>(asu(v[0]), t[0]), tag<1>(asu(v[1]), t[1]), tag<1>(asu(v[2]), t[2]), tag<1>(asu(v[3]), t[3])}; }
__device__ __forceinline__ bool arrived(const v4u& v, const v4u& o) { return __builtin_amdgcn_ballot_w64(v[0] == o[0] || v[1] == o[1] || v[2] == o[2] || v[3] == o[3]) == 0; }
__device__ __forceinline__ unsigned lowb(const v4u& v) { return __builtin_amdgcn_perm(v[1], v[0], 0x0c0c0400u) | __builtin_amdgcn_perm(v[3], v[2], 0x04000c0cu); }
#define SB() __builtin_amdgcn_sched_barrier(0)
__device__ __forceinline__ void bar() { __builtin_amdgcn_s_waitcnt(0xC07F); __builtin_amdgcn_s_barrier(); }
#define WAITV(n) __builtin_amdgcn_s_waitcnt(((n) & 15) | (((n) >> 4) << 14) | 0x0F70)
template <int V> struct IC { static constexpr int value = V; constexpr operator int() const { return V; } };
template <int A, int B, class Fn> __device__ __forceinline__ void static_for(Fn&& f) { if constexpr (A < B) { f(IC<A>{}); static_for<A + 1, B>(f); } }
#define LDS_FENCE() asm volatile("" ::: "memory")

constexpr float EXP_C = 0.0625f * 1.4426950408889634f;

struct Att {
  float m, l;
  f32x4 a[2];
};
__device__ __forceinline__ f32x4 mfma(const v4u& a, const v4u& b, const f32x4& c) {
  return __builtin_amdgcn_mfma_f32_16x16x32_bf16(__builtin_bit_cast(bf16x8, a), __builtin_bit_cast(bf16x8, b), c, 0, 0, 0);
}
__device__ __forceinline__ float red_max(float v) { v = fmaxf(v, dpp<XOR1>(v)); v = fmaxf(v, dpp<XOR2>(v)); return x32_max(x16_max(v)); }

template <int NL, class ISSUE>
__device__ __forceinline__ float scores(v4u (&kb)[8], const v4u (&qb)[8], int lane, ISSUE issue) {
  f32x4 d = {0.f, 0.f, 0.f, 0.f};
#pragma unroll
  for (int i = 0; i < NL; ++i) {
    d = mfma(kb[i], qb[i], d);
    asm volatile("" : "+v"(d));
    issue(i);
    SB();
  }
  const bool odd = lane & 1;
  return rbf((odd ? d[3] : d[0]) + dpp<XOR1>(odd ? d[1] : d[2]));
}
template <class ISSUE>
__device__ __forceinline__ float phase_k4(v4u (&kb)[8], const v4u (&qb)[8], Att& st, int lane, ISSUE issue) {
  float s = scores<4>(kb, qb, lane, issue);
  s = (lane & 2) ? -__builtin_inff() : s;
  const float mn = fmaxf(st.m, red_max(s));
  const float al = __builtin_amdgcn_exp2f((st.m - mn) * EXP_C);
  const float p = __builtin_amdgcn_exp2f((s - mn) * EXP_C);
  st.m = mn; st.l = st.l * al + p;
  st.a[0] *= al; st.a[1] *= al;
  SB();
  return p;
}
template <int I, int O, class ISSUE>
__device__ __forceinline__ void pv_step(v4u (&vb)[8], unsigned pp, const v4u& pm, Att& st, ISSUE& issue) {
  const v4u pb = pm & (unsigned)__builtin_amdgcn_update_dpp(0, (int)pp, I * 0x55, 0xf, 0xf, true);
  st.a[0] = mfma(vb[O + 2 * I], pb, st.a[0]);
  asm volatile("" : "+v"(st.a[0]));
  issue(O + 2 * I);
  SB();
  st.a[1] = mfma(vb[O + 2 * I + 1], pb, st.a[1]);
  asm volatile("" : "+v"(st.a[1]));
  issue(O + 2 * I + 1);
  SB();
}
template <int NL, class ISSUE>
__device__ __forceinline__ void phase_v(v4u (&vb)[8], float p, const v4u& pm, Att& st, ISSUE issue) {
  constexpr int O = 8 - NL;
  const unsigned pp = __builtin_bit_cast(unsigned, (bf16x2){(__bf16)p, (__bf16)p});
  pv_step<0, O>(vb, pp, pm, st, issue); pv_step<1, O>(vb, pp, pm, st, issue);
  if constexpr (NL == 8) { pv_step<2, O>(vb, pp, pm, st, issue); pv_step<3, O>(vb, pp, pm, st, issue); }
}

template <int N, int GAP, int SL>
__device__ __forceinline__ v4u poll(const v4u& r, unsigned pa, const v4u& old) {
  v4u d;
  unsigned long long t0, t1, t2;
  int n = SPIN_CAP;
  asm volatile(
      "s_nop 4\n\t"
      "buffer_load_dwordx4 v[200:203], %5, %6, 0 offen sc1\n\t"
      ".rept %11\n\t"
      "s_sleep %12\n\t"
      "buffer_load_dwordx4 v[200:203], %5, %6, 0 offen sc1\n\t"
      ".endr\n"
      "1:\n\t"
      "s_waitcnt vmcnt(%11)\n\t"
      "v_cmp_eq_u32 vcc, v200, %7\n\t"
      "v_cmp_eq_u32 %1, v201, %8\n\t"
      "v_cmp_eq_u32 %2, v202, %9\n\t"
      "v_cmp_eq_u32 %3, v203, %10\n\t"
      "s_or_b64 vcc, vcc, %1\n\t"
      "s_or_b64 %2, %2, %3\n\t"
      "s_or_b64 exec, vcc, %2\n\t"
      "s_cbranch_scc0 2f\n\t"
      ".if %13\n\ts_sleep %13\n\t.endif\n\t"
      "buffer_load_dwordx4 v[200:203], %5, %6, 0 offen sc1\n\t"
      "s_sub_u32 %4, %4, 1\n\t"
      "s_cbranch_scc0 1b\n"
      "2:\n\t"
      "s_mov_b64 exec, -1"
      : "=&{v[200:203]}"(d), "=&s"(t0), "=&s"(t1), "=&s"(t2), "+s"(n)
      : "v"(pa), "s"(r), "v"(old[0]), "v"(old[1]), "v"(old[2]), "v"(old[3]), "n"(N - 1), "n"(GAP), "n"(SL)
      : "vcc", "scc", "memory");
  return d;
}
__device__ __forceinline__ void poll_end(const v4u& d) { asm volatile("s_waitcnt vmcnt(0)" :: "{v[200:203]}"(d)); }

#define STAMP(k) do {} while (0)
#define STAMP_OUT() do {} while (0)

__device__ __forceinline__ f32x2 ln_stats(const v4u& xa, int lane) {
  f32x2 xv[4];
  float sm = 0.f;
#pragma unroll
  for (int j = 0; j < 4; ++j) { xv[j] = unp(xa[j]); sm += xv[j].x + xv[j].y; }
  const float mean = wave_sum(sm) * (1.0f / D);
  float sq = 0.f;
#pragma unroll
  for (int j = 0; j < 4; ++j) { const f32x2 d = xv[j] - mean; sq += d.x * d.x + d.y * d.y; }
  sq = lane < 56 ? sq : 0.f;
  const float var = rbf(wave_sum(sq) * (1.0f / D));
  const float sd = rbf(sqrtf(rbf(var + 1e-5f)));
  return (f32x2){rbf(mean), 1.0f / sd};
}
__device__ __forceinline__ float ln_apply(float x, f32x2 st) { return rbf(rbf(x - st.x) * st.y); }

__global__ void __launch_bounds__(NT, 2)
fused_kernel(const u16* __restrict__ xg, const u16* __restrict__ kc, const u16* __restrict__ vc, const u16* __restrict__ wq,
             const u16* __restrict__ wo, const u16* __restrict__ wg, const u16* __restrict__ wu, const u16* __restrict__ wd,
             u16* __restrict__ out, char* ws) {
  __shared__ __attribute__((aligned(16))) float s_part[8][512];
  __shared__ __attribute__((aligned(16))) float s_mv[8][64];
  __shared__ __attribute__((aligned(16))) unsigned s_q[256];
  __shared__ __attribute__((aligned(16))) unsigned s_qs[64];
  __shared__ __attribute__((aligned(16))) unsigned s_old[8][256];
  __shared__ __attribute__((aligned(16))) unsigned s_dst[8][256];
  __shared__ __attribute__((aligned(16))) float s_ml[2][16];
  __shared__ __attribute__((aligned(16))) float s_o[4][32];
  __shared__ __attribute__((aligned(16))) float s_ml2[16];
  __shared__ __attribute__((aligned(16))) unsigned s_xn[256];
  __shared__ __attribute__((aligned(16))) float s_h[32];
  __shared__ __attribute__((aligned(16))) float s_r[9][16];
  __shared__ __attribute__((aligned(16))) float s_sc[64][32];
  __shared__ int s_ctl[64];
  const int tid = threadIdx.x, lane = tid & 63, w = __builtin_amdgcn_readfirstlane(tid >> 6);
  const int bi = SWP == 2 ? (blockIdx.x & ~7) | ((blockIdx.x + 7) & 7) : blockIdx.x ^ SWP, h = (((bi & 1) ? bi + 5 : bi) & 6) | (bi >> 7), s = ((bi >> 3) & 15) * 2 + (bi & 1);
  const int b = h * 32 + s;
  static_assert(8 * TQ + 8 * TF + (YS - 16) * TFY + (32 - YS) * TY == 4096 && TQ - NP > 80 && TF - NP > 80 && TFY - NP > 80 && TY - NP > 80, "tickets");
  static_assert(TQ - NP <= 128 && TF - NP <= 128 && TFY - NP <= 128 && TY - NP <= 128, "tickets");
  const int nt = s < 8 ? TQ : s < 16 ? TF : s < YS ? TFY : TY, nk = (nt - NP) >> 1;
  const int first = s * TY - min(s, YS) * (TY - TFY) - min(s, 16) * (TFY - TF) - min(s, 8) * (TF - TQ);
  asm volatile("" :: "s"(xg), "s"(kc), "s"(vc), "s"(wq), "s"(wo), "s"(wg), "s"(wu), "s"(wd), "s"(out), "s"(ws));
  STAMP(0);
  const rsrc_t rx = mkrs(xg, D * 2);
  const rsrc_t rk = mkrs(kc + (size_t)h * SK * DH, SK * DH * 2);
  const rsrc_t rv = mkrs(vc + (size_t)h * SK * DH, SK * DH * 2);
  const rsrc_t rws = mkrs(ws, WS_END);
  const unsigned l16 = lane * 16;
  auto ubase = [&](int u) -> unsigned { return (unsigned)(first + 2 * u) * 4096u; };
  const unsigned kvo = ((lane >> 1) & 7) * 512 + (lane >> 4) * 32 + (lane & 1) * 16, vvo = (lane >> 4) * 1024 + (lane & 15) * 16;
  auto ldk = [&](unsigned base, int i) { return ld4<2>(rk, kvo + (i & 3) * 128, base + (i >> 2) * 4096); };
  const unsigned area = (unsigned)b * AREA;
  auto area_of = [&](int ss) -> unsigned { return (unsigned)(h * 32 + ss) * AREA; };
  const unsigned lmask = lane < 56 ? 0u : OOB;

  const bool isq = s < 8, isf = s < 14, isy = s >= 16;
  const int f = h * 14 + s, y = h * 16 + s - 16, ro = h * 4 + (s >> 2);
  const bool isr = s < 16 && ((s + h) & 3) == 0 && ro < 28;
  const bool tile = isq;
  const bool leader = (!(NOHS & 1) && b == NWG - 1 && w == 1), member = (w == 0);

  const unsigned pa = isy ? (w < 4 ? area + A_O1 + w * 1024 + l16 : (w == 7 && y < 112) ? area + A_F + l16 : OOB) : isr ? area + A_Y2 + w * 1024 + l16 : OOB;
  const unsigned mo_ = area_of(16 + (lane & 8) + (lane >> 4)) + A_O1 + s * 128 + (lane & 7) * 16;
  auto mf = [&](int p) -> unsigned {
    const int fd = (f & ~3) + ((p >> 2) & 3);
    return (unsigned)((fd >> 4) * 32 + 16 + (fd & 15)) * AREA + A_F + (f & 3) * 256 + (p >> 4) * 64 + (p & 3) * 16;
  };
  auto my = [&](int p) -> unsigned { return (unsigned)((p >> 4) * 32 + (p & 12) + ((-(p >> 4)) & 3)) * AREA + A_Y2 + y * 64 + (p & 3) * 16; };
  v4u eo, ed;
  auto da_of = [&]() {
    const int p = (w - 2) * 64 + lane;
    unsigned da = (isy && p < 112) ? my(p) : OOB;
    return (w & 6) == 2 ? da : (w & 6) == 4 ? mo_ + (w - 4) * 4 * AREA : (w == 6 && isf) ? mf(lane) : OOB;
  };
  if (EO == 1) { eo = ld4<OA>(rws, pa, 0); SB(); }
  if (EO == 3 || (EO == 4 && !tile)) { eo = ld4<OA>(rws, pa, 0); ed = ld4<OB>(rws, da_of(), 0); SB(); }
  if (EO == 5) { ed = ld4<OB>(rws, da_of(), 0); SB(); }
  const v4u sent = {SENT, SENT, SENT, SENT};
  unsigned poll_off = lane == 1 ? WS_GO : lane < 4 ? WS_T - 32 + l16 : OOB;
  poll_off = lane == 0 ? WS_REQ + (b >> 2) * 16 : poll_off;
  poll_off = leader ? (ES ? lane * 4 * AREA + (lane & 3) * 256 : WS_ECHO + l16) : poll_off;
  auto echoes = [&](unsigned seen) {
    v4u r;
#pragma unroll
    for (int j = 0; j < 4; ++j) r[j] = __builtin_amdgcn_raw_buffer_load_b32(rws, (int)(poll_off | (seen >> j << 31)), j * AREA, 16);
    return r;
  };
  v4u pv = {0u, 0u, 0u, 0u}, e1 = {0u, 0u, 0u, 0u};
  unsigned go_ref = 0;
  int* const ctl = &s_ctl[lane];
  auto ctl_get = [&](int i) { return __builtin_amdgcn_readfirstlane(__hip_atomic_load(&s_ctl[i], __ATOMIC_RELAXED, __HIP_MEMORY_SCOPE_WORKGROUP)); };
  auto ctl_set = [&](int i) { __hip_atomic_store(&s_ctl[i], 1, __ATOMIC_RELAXED, __HIP_MEMORY_SCOPE_WORKGROUP); };
  const unsigned qoff = WS_Q + h * 2048 + l16;
  v4u qv0, qv1;
  if (member) {
    qv0 = ld4<16>(rws, qoff, 0);
    qv1 = ld4<16>(rws, qoff, 1024);
    SB();
    int c = lane == C_NEXT ? (isq ? TOFFQ : TOFF) * 128 : 0;
    c = lane == C_TICK ? 40 : c;
    c = lane == C_T0 ? (int)(__builtin_amdgcn_s_memrealtime() & 0x7fffffff) : c;
    __hip_atomic_store(ctl, c, __ATOMIC_RELAXED, __HIP_MEMORY_SCOPE_WORKGROUP);
    st1c(c, rws, (lane == C_T0 && (bi >> 3) == 12) ? WS_T + (bi & 7) * 4 : OOB);
  }
  *reinterpret_cast<v4u*>(&s_sc[w * 8][lane * 4]) = sent;
  int t0 = 0;
  auto now = [&]() __attribute__((always_inline)) { return ((int)__builtin_amdgcn_s_memrealtime() - t0) & 0x7fffff; };
  const int neg = lane == C_NEXT ? 0 : (int)0x80000000;
  const int xcc = __builtin_amdgcn_s_getreg(20 | (3 << 11));
  constexpr auto tkf = [](int t, int sh) { return TAU + sh * 128 / ((t - NP) / 2 - 8); };
  constexpr auto tvf = [](int t, int sh) { return TAU - sh * 128 / ((t - NP) / 2 + 8 - PL); };
  int tk = s < 8 ? tkf(TQ + 16, SQ) : s < 16 ? tkf(TF, SF_) : s < YS ? tkf(TFY, SFY) : tkf(TY, SY);
  int tv = s < 8 ? tvf(TQ, SQ) : s < 16 ? tvf(TF, SF_) : s < YS ? tvf(TFY, SFY) : tvf(TY, SY);
  int inc = lane == C_NEXT ? tk : lane == C_CNT ? 1 : 0;
  int lost = 0;
  auto token = [&](int burst, bool early = false) __attribute__((always_inline)) {
    const int t = max(now() * 128 - burst, 0);
    if (GB && early) {
      const int o = __hip_atomic_fetch_max(ctl, t | neg, __ATOMIC_RELAXED, __HIP_MEMORY_SCOPE_WORKGROUP);
      lost += max(o, t | neg) - o;
    } else {
      __hip_atomic_fetch_max(ctl, t | neg, __ATOMIC_RELAXED, __HIP_MEMORY_SCOPE_WORKGROUP);
    }
    return __hip_atomic_fetch_add(ctl, inc, __ATOMIC_RELAXED, __HIP_MEMORY_SCOPE_WORKGROUP);
  };
  auto due_of = [&](int x) { return __builtin_amdgcn_readlane(x, C_NEXT) >> 7; };
  auto until = [&](int due) __attribute__((always_inline)) {
#pragma unroll 1
    for (int it = 0; it < 4096 && now() < due; ++it) __builtin_amdgcn_s_sleep(1);
    SB();
  };
  unsigned qs0, qs1;
  auto q_there = [&]() { return __builtin_amdgcn_ballot_w64(((lowb(qv0) ^ qs0) & (lowb(qv1) ^ qs1)) != 0xFFFFFFFFu) == 0; };
  auto load_q = [&]() { qv0 = ld4<16>(rws, qoff, 0); qv1 = ld4<16>(rws, qoff, 1024); };
  auto publish_q = [&]() {
    *reinterpret_cast<v2u*>(&s_q[lane * 2]) = (v2u){__builtin_amdgcn_perm(qv0[1], qv0[0], 0x07060302u), __builtin_amdgcn_perm(qv0[3], qv0[2], 0x07060302u)};
    *reinterpret_cast<v2u*>(&s_q[128 + lane * 2]) = (v2u){__builtin_amdgcn_perm(qv1[1], qv1[0], 0x07060302u), __builtin_amdgcn_perm(qv1[3], qv1[2], 0x07060302u)};
    LDS_FENCE();
    __builtin_amdgcn_s_waitcnt(0xC07F);
    ctl_set(C_Q);
  };
  v4u pre[4][8], ring[8];
  auto unit0 = [&]() {
#pragma unroll
    for (int i = 0; i < 8; ++i) { pre[0][i] = ldk(ubase(w), i); SB(); }
    if (EO == 2) { eo = ld4<OA>(rws, pa, 0); SB(); }
  };
  SB();
  if (tile) {
    v4u tw[8];
    const int oct = 7 * w;
    constexpr int stride = NQ * 2;
    const rsrc_t rt = mkrs(wq, D * stride);
    const unsigned to = (unsigned)((oct + (lane >> 3)) * 8 * stride + (h * 8 + s) * 128 + (lane & 7) * 16) | lmask;
    const v4u xa = ld4<0>(rx, l16, 0);
    const v4u xb = ld4<0>(rx, (unsigned)(oct + (lane >> 3)) * 16 | lmask, 0);
    SB();
#pragma unroll
    for (int i = 0; i < 8; ++i) { tw[i] = ld4<2>(rt, to, i * stride); SB(); }
    if (EO == 4) { eo = ld4<OA>(rws, pa, 0); ed = ld4<OB>(rws, da_of(), 0); SB(); }
    LDS_FENCE(); bar(); LDS_FENCE();
    t0 = ctl_get(C_T0);
    SB();
    const f32x2 ls = ln_stats(xa, lane);
    f32x2 acc[4];
#pragma unroll
    for (int i = 0; i < 8; ++i) {
      const float xn = ln_apply(asf((i & 1) ? (xb[i >> 1] & 0xffff0000u) : (xb[i >> 1] << 16)), ls);
#pragma unroll
      for (int j = 0; j < 4; ++j) acc[j] = i ? acc[j] + unp(tw[i][j]) * xn : unp(tw[i][j]) * xn;
    }
    float y0 = tr32(acc[0].x, acc[2].x), y1 = tr32(acc[0].y, acc[2].y), y2 = tr32(acc[1].x, acc[3].x), y3 = tr32(acc[1].y, acc[3].y);
    y0 = tr16(y0, y2); y1 = tr16(y1, y3);
    const bool s_ = lane & 8;
    y0 = (s_ ? y1 : y0) + dpp<ROR8>(s_ ? y0 : y1);
    s_mv[w][(lane & 7) * 8 + (lane >> 3)] = y0;
    STAMP(6);
    token(QBURST);
    unit0();
    LDS_FENCE(); bar(); LDS_FENCE();
    if (member) {
      float a = 0.f;
#pragma unroll
      for (int k = 0; k < 8; ++k) a += s_mv[k][lane];
      s_qs[lane] = f2bf(a) << 16;
      LDS_FENCE();
      const v4u qs = *reinterpret_cast<const v4u*>(&s_qs[(lane & 15) * 4]) | (~((s & 4) ? qv1 : qv0) & 0xffffu);
      st4c(qs, rws, (lane >> 4) == (s & 3) ? qoff + (s >> 2) * 1024 : OOB);
    }
  } else if (leader && !LU) {
    e1 = ES ? echoes(0) : ld4<16>(rws, poll_off, 0);
    go_ref = __builtin_amdgcn_raw_buffer_load_b32(rws, (int)WS_GO, 0, 16);
  }
  SB();
  STAMP(5);
  SB();
  const u16* wa = isy ? wo + (size_t)y * 32 * D : wg;
  const u16* wb = isy ? wd + (size_t)(y < 112 ? (y >> 1) * 32 + (y & 1) * 4 : 0) * D : wu;
  asm volatile("" : "+s"(wa), "+s"(wb));
  bool ok = !member;
  static_for<0, 40>([&](auto N) {
    constexpr int n = N;
    if constexpr (n == 0) {
      if (!tile) unit0();
      if (LU && leader) {
        e1 = echoes(0);
        go_ref = __builtin_amdgcn_raw_buffer_load_b32(rws, (int)WS_GO, 0, 16);
      }
    }
    if constexpr (n == 8) {
      if (!tile) {
        LDS_FENCE(); bar(); LDS_FENCE();
        t0 = ctl_get(C_T0);
      }
    }
    if constexpr (n == LP) {
      if (leader) {
        e1 += 1u;
        go_ref += 1u;
        st4c(e1, rws, WS_REQ + l16);
      }
    }
    if constexpr (n > 0 && n % 8 == 0) {
      const int x = token((isq || (LB && b == NWG - 1)) ? QBURST : PBURST, true);
      if constexpr (n > QFIRST) {
        if (!ok) {
          ok = q_there();
          if (ok) publish_q();
        }
      }
      if constexpr (n == OPOS + 16) {
        *reinterpret_cast<v4u*>(&s_old[w][lane * 4]) = (int)pa < 0 ? sent : eo;
        *reinterpret_cast<v4u*>(&s_dst[w][lane * 4]) = ~ed;
        LDS_FENCE();
        __hip_atomic_fetch_add(ctl, lane == C_OLD ? 1 : 0, __ATOMIC_RELAXED, __HIP_MEMORY_SCOPE_WORKGROUP);
      }
      SB();
      until(due_of(x));
      if constexpr (n == 8) {
        if (member) { qs0 = lowb(qv0); qs1 = lowb(qv1); }
      }
      if constexpr (n >= QFIRST) {
        if (!ok) load_q();
      }
      if constexpr (n == OPOS) {
        if (EO == 0 || EO == 5) eo = ld4<OA>(rws, pa, 0);
        if (EO < 3) ed = ld4<OB>(rws, da_of(), 0);
      }
      SB();
    }
    if constexpr (n == QPOS) {
      STAMP(9);
      if (ES && leader) pv = LD ? ~e1 : echoes(0);
      else pv = ld4<16>(rws, (member || leader) ? poll_off : OOB, 0);
      SB();
    }
    if constexpr (n >= 8) {
      if constexpr (n >= PACE) WAITV(PACE + ((n >= QPOS && n < QPOS + PACE) ? 1 : 0) + ((n >= OPOS && n < OPOS + PACE) ? (EO == 0 ? 2 : (EO == 3 || EO == 4) ? 0 : 1) : 0));
      if constexpr (n < 32) pre[n >> 3][n & 7] = ldk(ubase((n & 56) + w), n & 7); else ring[n & 7] = ldk(ubase(32 + w), n & 7);
    }
    SB();
    if constexpr (n - PACE == 7 || n - PACE == 15 || n - PACE == 23 || n - PACE == 31) STAMP(16 + (n - PACE) / 8);
  });
  inc = lane == C_TICK ? 1 : inc;
  if (GB == 1) __hip_atomic_fetch_add(ctl, -lost, __ATOMIC_RELAXED, __HIP_MEMORY_SCOPE_WORKGROUP);
  int tok = token(KBURST);
  STAMP(10);
  bool svc = member || leader;
  unsigned seenm = 0, last_echo = 0;
  bool echoed = false;
  auto service = [&]() {
    if (leader) {
      seenm |= (pv[0] == e1[0] ? 1u : 0u) | (pv[1] == e1[1] ? 2u : 0u) | (pv[2] == e1[2] ? 4u : 0u) | (pv[3] == e1[3] ? 8u : 0u);
      svc = __builtin_amdgcn_ballot_w64(seenm != 15u) != 0;
      st1c(go_ref, rws, (lane == 0 && !svc) ? WS_GO : OOB);
    } else {
      const unsigned req = (unsigned)__builtin_amdgcn_readfirstlane((int)((b & 2) ? ((b & 1) ? pv[3] : pv[2]) : ((b & 1) ? pv[1] : pv[0])));
      const unsigned go = (unsigned)__builtin_amdgcn_readlane((int)pv[0], 1);
      go_ref = echoed ? go_ref : go;
      svc = go == (unsigned)__builtin_amdgcn_readfirstlane((int)go_ref);
      if (NOHS & 1) svc = false;
      const bool rdy = ctl_get(C_OLD) == 8;
      if (ES && EL > 0) st4c((v4u){req, req, req, req}, rws, (lane < EL && svc && rdy && !(echoed && req == last_echo)) ? area + ((b >> 2) & 3) * 256 + l16 : OOB);
      else st1c(req, rws, (lane == 0 && svc && rdy && !(echoed && req == last_echo)) ? (ES ? area + ((b >> 2) & 3) * 256 : WS_ECHO + b * 4) : OOB);
      echoed |= rdy; last_echo = req;
    }
    if (ES && leader) pv = echoes(seenm);
    else pv = ld4<16>(rws, svc ? poll_off : OOB, 0);
    SB();
  };

  {
    if (!ok) {
      ok = q_there();
      if (ok) publish_q();
    }
#pragma unroll 1
    for (int it = 0; it < SPIN_CAP && !ctl_get(C_Q); ++it) {
      if (svc && it >= QTRIES) { service(); asm volatile("" :: "v"(pv)); }
      if (!ok) {
        load_q();
        ok = q_there() | !svc;
        if (ok) publish_q();
      } else {
        __builtin_amdgcn_s_sleep(1);
      }
    }
  }
  LDS_FENCE();
  v4u qb[8];
#pragma unroll
  for (int i = 0; i < 4; ++i) {
    const v4u z = {0u, 0u, 0u, 0u}, q = *reinterpret_cast<const v4u*>(&s_q[(lane & 8) * 16 + i * 32 + (lane >> 4) * 8 + (lane & 1) * 4]);
    qb[i] = (lane & 2) ? z : q; qb[4 + i] = (lane & 2) ? q : z;
  }
  v4u pm;
#pragma unroll
  for (int j = 0; j < 4; ++j) pm[j] = ((lane >> 1) & 3) == j ? ((lane & 1) ? 0xffff0000u : 0xffffu) : 0u;
  STAMP(1);
  SB();

  Att st;
  int sx = 20;
  int rank;
  lds4_t pw;
  lds1_t pml;
  int idle;
  auto last = [&]() __attribute__((always_inline)) {
    pml = (lds1_t)&s_ml[(lane >> 3) & 1][rank];
    pw = (lds4_t)&s_part[rank][lane * 4];
    idle = __builtin_amdgcn_readfirstlane(!isy && !svc && rank >= PM && (unsigned)(rank - SR) >= 2 && (!isf || (unsigned)(rank - FO) >= 4));
    asm volatile("" : "+v"(pw), "+v"(pml), "+s"(idle));
    SB();
  };
  auto put_ml = [&]() __attribute__((always_inline)) {
    float l = st.l;
    l += dpp<XOR1>(l); l += dpp<XOR2>(l); l = x32_add(x16_add(l));
    pml[0] = st.m; pml[8] = l;
    SB();
  };
  {
    float* const sc = &s_sc[0][(lane >> 3) * 4 + (lane & 3)];
    rsrc_t nra, nrb;
    unsigned nva, nvb, nba, nbb, nsa, nsb;
    int kind = 0, nu = 32 + w, cu;
    auto next = [&]() __attribute__((always_inline)) {
      cu = nu;
      const int t = __builtin_amdgcn_readlane(tok, C_TICK);
      const bool fx = t < 2 * nk, tw = t >= nt;
      kind = fx ? (t >= nk ? 1 : 0) : tw ? 3 : 2;
      nu = t >= nk ? t - nk : t;
      nba = (unsigned)(fx ? first + 2 * nu : first + t) * 4096u;
      nbb = fx ? nba + 4096 : nba;
      nra = selr(kind == 1, rv, rk);
      nrb = selr(kind == 0, rk, rv);
      nva = selu(kind == 1, vvo, kvo); nvb = selu(kind == 0, kvo, vvo);
      nsa = selu(kind == 1, 256, 128); nsb = selu(kind == 0, 128, 256);
      const int u = t + 8 - nk;
      inc = lane == C_NEXT ? (t + PL >= 2 * nk ? PP : (int)selu(u >= 0, tv, tk) + ((unsigned)(u + XA) < XA + XB ? TX : 0)) : inc;
      rank = t - nt;
      if (!tw) until(due_of(tok));
      SB();
    };
    auto ldn = [&](int i) { ring[i] = i < 4 ? ld4<2>(nra, nva, nba + i * nsa) : ld4<2>(nrb, nvb, nbb + (i - 4) * nsb); };
    float mx;
    if (member) {
      const v4u d = pv - (unsigned)t0;
      const bool bad = max(max(d[0] + 512u, d[1] + 512u), max(d[2] + 512u, d[3] + 512u)) > 1024u;
      const int sm = (int)((d[0] + d[1]) + (d[2] + d[3]));
      const int lead = (__builtin_amdgcn_ballot_w64(bad) & 12) ? 0 : __builtin_amdgcn_readlane(sm, 2) + __builtin_amdgcn_readlane(sm, 3) + DB;
      const int slip = __builtin_amdgcn_readlane(tok, C_NEXT) - __builtin_amdgcn_readlane(tok, C_CNT) * tk - (isq ? TOFFQ : TOFF + SL0) * 128;
      const int adj = ((lead * DK) >> 5) - ((slip * SKF) >> 10);
      tk += adj; tv += adj;
      __hip_atomic_fetch_add(ctl, lane == C_LEAD ? (lead * DG) >> 5 : (lane == C_NEXT ? (lead * DS) >> 5 : 0), __ATOMIC_RELAXED, __HIP_MEMORY_SCOPE_WORKGROUP);
    }
    if (GB == 2) __hip_atomic_fetch_add(ctl, -lost, __ATOMIC_RELAXED, __HIP_MEMORY_SCOPE_WORKGROUP);
    if (svc && !(LD && leader)) service();
    {
      const float s0 = scores<8>(pre[0], qb, lane, [&](int) {});
      const float s1 = scores<8>(pre[1], qb, lane, [&](int) {});
      const float s2 = scores<8>(pre[2], qb, lane, [&](int) {});
      const float s3 = scores<8>(pre[3], qb, lane, [&](int) {});
      sc[w * 32] = s0; sc[(8 + w) * 32] = s1; sc[(16 + w) * 32] = s2; sc[(24 + w) * 32] = s3;
      mx = fmaxf(fmaxf(s0, s1), fmaxf(s2, s3));
      SB();
      STAMP(11);
    }
    const v4u xa2 = ld4<0>(rx, (isf && w == 7) ? l16 : OOB, 0);
    SB();
#pragma unroll 1
    while (kind == 0) {
      next();
      const float sv = scores<8>(ring, qb, lane, ldn);
      sc[cu * 32] = sv;
      tok = token(KBURST);
      mx = fmaxf(mx, sv);
      STAMP(sx); ++sx;
      if (svc && !(LD && leader)) service();
      SB();
    }
    STAMP(7);
    if (GB == 3) __hip_atomic_fetch_add(ctl, -lost, __ATOMIC_RELAXED, __HIP_MEMORY_SCOPE_WORKGROUP);
    if (LD == 1 && leader) { pv = echoes(seenm); SB(); }
    if (isf && w == 7) {
      const f32x2 ls = ln_stats(xa2, lane);
      v4u xn;
#pragma unroll
      for (int j = 0; j < 4; ++j) {
        const f32x2 v = unp(xa2[j]);
        xn[j] = __builtin_amdgcn_perm(asu(ln_apply(v.y, ls)), asu(ln_apply(v.x, ls)), 0x07060302u);
      }
      *reinterpret_cast<v4u*>(&s_xn[lane * 4]) = xn;
      LDS_FENCE();
      ctl_set(C_FC);
      SB();
    }
    st.m = red_max(mx); st.l = 0.f;
    st.a[0] = st.a[1] = (f32x4){0.f, 0.f, 0.f, 0.f};
#pragma unroll 1
    while (kind == 1) {
      float sv;
      for (;;) {
        sv = __hip_atomic_load(&sc[nu * 32], __ATOMIC_RELAXED, __HIP_MEMORY_SCOPE_WORKGROUP);
        if (__builtin_amdgcn_ballot_w64(asu(sv) == SENT) == 0) break;
        __builtin_amdgcn_s_sleep(1);
      }
      if (__builtin_amdgcn_ballot_w64(sv > st.m) != 0) {
        const float m2 = fmaxf(st.m, red_max(sv));
        const float al = __builtin_amdgcn_exp2f((st.m - m2) * EXP_C);
        st.m = m2; st.l *= al;
        st.a[0] *= al; st.a[1] *= al;
      }
      const float p = __builtin_amdgcn_exp2f((sv - st.m) * EXP_C);
      st.l += p;
      SB();
      next();
      if (kind == 3) {
        last();
        put_ml();
        phase_v<8>(ring, p, pm, st, [&](int) {});
        break;
      }
      phase_v<8>(ring, p, pm, st, ldn);
      tok = token(VBURST);
      STAMP(sx); ++sx;
      if (svc) service();
      SB();
    }
    STAMP(8);
    if (kind == 2) {
      next();
#pragma unroll 1
    while (kind != 3) {
      if (UF) { nba = __builtin_amdgcn_readfirstlane(nba); nbb = __builtin_amdgcn_readfirstlane(nbb); nsa = __builtin_amdgcn_readfirstlane(nsa); nsb = __builtin_amdgcn_readfirstlane(nsb); }
      const float p = phase_k4(ring, qb, st, lane, ldn);
      phase_v<4>(ring, p, pm, st, ldn);
      tok = token(VBURST);
      STAMP(sx); ++sx;
      SB();
      next();
    }
      SB();
      last();
      const float p = phase_k4(ring, qb, st, lane, [&](int) {});
      phase_v<4>(ring, p, pm, st, [&](int) {});
      put_ml();
    }
    pw[0] = st.a[0]; pw[64] = st.a[1];
    LDS_FENCE();
    SB();
    STAMP(12);
    if (idle) {
      LDS_FENCE(); bar(); LDS_FENCE();
      STAMP(2);
    }
    SB();
#pragma unroll 1
    for (int it = 0; it < SPIN_CAP && svc; ++it) {
      service();
      asm volatile("" :: "v"(pv));
    }
    LDS_FENCE();
    WAITV(0);
  }
  const lds4_t pr = (lds4_t)&s_part[0][lane * 4], pmr = (lds4_t)&s_ml[(lane >> 3) & 1][0];
  if (rank < PM) __hip_atomic_fetch_add(ctl, lane == C_DONE ? 1 : 0, __ATOMIC_RELAXED, __HIP_MEMORY_SCOPE_WORKGROUP);
  SB();
  const v4u rw4 = {(unsigned)(size_t)ws, (unsigned)((size_t)ws >> 32), WS_END, 0x00020000u};
  const unsigned fj = isf ? rank - FO : 4;
  auto clear = [&](int k, bool more) __attribute__((always_inline)) {
#pragma unroll 1
    for (int it = 0; it < SPIN_CAP; ++it) {
      const int x = __hip_atomic_load(ctl, __ATOMIC_RELAXED, __HIP_MEMORY_SCOPE_WORKGROUP);
      if (__builtin_amdgcn_readlane(x, C_TICK) >= nt + 8 - k && (!more || (__builtin_amdgcn_readlane(x, C_FC) && __builtin_amdgcn_readlane(x, C_OLD) == 8))) break;
      __builtin_amdgcn_s_sleep(1);
    }
    LDS_FENCE();
    SB();
  };
  const unsigned half = isy ? w : rank - SR;
  if (isy && w >= 4) {
    v4u wt[6][2];
    const rsrc_t rwo = mkrs(wa, XW ? 0 : 32 * D * 2);
    const rsrc_t rwd = mkrs(wb, (y < 112 && !XW) ? 28 * D * 2 : 0);
    unsigned o0 = (lane >> 3) * (D * 2) + (w - 4) * 256 + (lane & 7) * 16, o1 = w == 7 ? OOB : o0 + 128;
    unsigned o2 = o0 + (lane >> 5) * (4 * D * 2), o3 = o1 + (lane >> 5) * (4 * D * 2);
    asm volatile("" : "+v"(o0), "+v"(o1), "+v"(o2), "+v"(o3));
    SB();
    auto ldw = [&](int k) __attribute__((always_inline)) {
      const int i = (k + 4) % 6;
      wt[i][0] = i < 4 ? ld4<2>(rwo, o0, i * 8 * D * 2) : ld4<2>(rwd, o2, (i & 3) * 16 * D * 2);
      wt[i][1] = i < 4 ? ld4<2>(rwo, o1, i * 8 * D * 2) : ld4<2>(rwd, o3, (i & 3) * 16 * D * 2);
      SB();
    };
    if (YE) {
      clear(YK, false);
#pragma unroll
      for (int k = 0; k < YE; ++k) ldw(k);
    }
    LDS_FENCE(); bar(); LDS_FENCE();
    STAMP(2);
    if (YB) __builtin_amdgcn_s_barrier();
    if (YD) __builtin_amdgcn_s_sleep(YD);
    SB();
#pragma unroll
    for (int k = YE; k < 6; ++k) ldw(k);
    const unsigned spare = __builtin_amdgcn_raw_buffer_load_b32(rwo, (int)OOB, 0, 0);
    SB();
    const int p = (w - 4) * 32 + (lane >> 4) * 8 + (lane & 7);
    v4u dt = *reinterpret_cast<const v4u*>(&s_dst[2][p * 4]) & 1u;
    unsigned to = ((lane & 8) || p >= 112) ? OOB : my(p);
    f32x4 acc[2] = {{0.f, 0.f, 0.f, 0.f}, {0.f, 0.f, 0.f, 0.f}};
    auto bop = [&](float a) -> v4u { return pm & __builtin_bit_cast(unsigned, (bf16x2){(__bf16)a, (__bf16)a}); };
    auto step = [&](int i, const v4u& pb) { acc[0] = mfma(wt[i][0], pb, acc[0]); acc[1] = mfma(wt[i][1], pb, acc[1]); };
    lds4_t po = (lds4_t)&s_o[0][(lane >> 3) * 4];
    lds1_t ph = (lds1_t)&s_h[(lane & 4) * 4 + ((lane >> 4) & 1) * 8 + (lane & 3) * 2 + (lane >> 5)];
    asm volatile("" : "+v"(dt), "+v"(to), "+v"(po), "+v"(ph));
    SB();
    const v4u old = *reinterpret_cast<const v4u*>(&s_old[7][lane * 4]);
    v4u dr = old;
    if (w == 7) {
      if (G3) until(G3 + __builtin_amdgcn_readlane(tok, C_LEAD));
      dr = poll<PD3, PG3, 0>(rw4, pa, old);
      const v4u d = dr & ~1u;
      float g = tr16(tr32(asf(d[0]), asf(d[2])), tr32(asf(d[1]), asf(d[3])));
      g = rbf(g + dpp<ROR8>(g));
      const float u = dpp<ROR4>(g);
      *ph = rbf(g / (1.0f + __expf(-g))) * u;
      SB();
    }
    STAMP(14);
    LDS_FENCE(); bar(); LDS_FENCE();
    STAMP(3);
    const f32x2 hv = *reinterpret_cast<const f32x2*>(&s_h[(lane >> 3) * 2]);
    const f32x4 ma = *reinterpret_cast<const f32x4*>(&s_ml2[0]), mb = *reinterpret_cast<const f32x4*>(&s_ml2[4]);
    f32x4 pq[4];
#pragma unroll
    for (int k = 0; k < 4; ++k) pq[k] = po[k * 8];
    SB();
    const v4u h0 = bop(hv.x), h1 = bop(hv.y);
    {
      step(4, h0); step(5, h1);
      asm volatile("" : "+v"(acc[0]), "+v"(acc[1]));
      SB();
    }
    const float M = fmx(fmx(fmx(mb[0], mb[1]), mb[2]), mb[3]);
    f32x4 e = (mb - M) * EXP_C;
#pragma unroll
    for (int k = 0; k < 4; ++k) e[k] = __builtin_amdgcn_exp2f(e[k]);
    const float inv = __builtin_amdgcn_rcpf(__builtin_fmaf(ma[0], e[0], ma[1] * e[1]) + __builtin_fmaf(ma[2], e[2], ma[3] * e[3]));
    f32x2 av[2];
#pragma unroll
    for (int j = 0; j < 2; ++j) av[j] = (f32x2){pq[0][2 * j], pq[0][2 * j + 1]} * e[0];
#pragma unroll
    for (int k = 1; k < 4; ++k) {
#pragma unroll
      for (int j = 0; j < 2; ++j) av[j] = __builtin_elementwise_fma((f32x2){pq[k][2 * j], pq[k][2 * j + 1]}, (f32x2){e[k], e[k]}, av[j]);
    }
    av[0] *= inv; av[1] *= inv;
    {
      v4u pb[4];
#pragma unroll
      for (int i = 0; i < 4; ++i) pb[i] = bop(av[i >> 1][i & 1]);
      if (FB) { asm volatile("" : "+v"(pb[0]), "+v"(pb[1]), "+v"(pb[2]), "+v"(pb[3])); SB(); }
#pragma unroll
      for (int i = 0; i < 4; ++i) step(i, pb[i]);
    }
    f32x4 z;
#pragma unroll
    for (int v = 0; v < 4; ++v) {
      const float a0 = asf((unsigned)__builtin_amdgcn_update_dpp((int)asu(acc[0][v]), (int)asu(acc[0][v]), ROR8, 0xc, 0xf, false));
      const float a1 = asf((unsigned)__builtin_amdgcn_update_dpp((int)asu(acc[1][v]), (int)asu(acc[1][v]), ROR8, 0xc, 0xf, false));
      z[v] = tr32(a0, a1);
    }
    __builtin_amdgcn_raw_buffer_store_b128(tag4(z, dt), rws, (int)to, 0, S2);
    SB();
    STAMP(4);
    asm volatile("" :: "{v198}"(spare));
    poll_end(dr);
    STAMP_OUT();
    __builtin_amdgcn_endpgm();
  }
  if (half < 2) {
    ldsu_t tp = (ldsu_t)&s_dst[4 + half][lane * 4];
    unsigned to = mo_ + half * 4 * AREA;
    lds4_t pp = pr + half * 64;
    asm volatile("" : "+v"(tp), "+v"(to), "+v"(pp));
    SB();
    f32x4 ml[4], pt[8], o = {0.f, 0.f, 0.f, 0.f};
    float M = -3.0e38f, L = 0.f;
    if (PM) {
#pragma unroll 1
      for (int it = 0; it < SPIN_CAP && ctl_get(C_DONE) < PM; ++it) {}
      LDS_FENCE();
#pragma unroll
      for (int k = 0; k < 4; ++k) if ((k & 1) * 4 < PM) ml[k] = pmr[k];
#pragma unroll
      for (int k = 0; k < PM; ++k) pt[k] = pp[k * 128];
      M = ml[0][0];
#pragma unroll
      for (int k = 1; k < PM; ++k) M = fmx(M, ml[k >> 2][k & 3]);
#pragma unroll
      for (int k = 0; k < PM; ++k) {
        const float e = __builtin_amdgcn_exp2f((ml[k >> 2][k & 3] - M) * EXP_C);
        o += pt[k] * e;
        L += ml[2 + (k >> 2)][k & 3] * e;
      }
      asm volatile("" : "+v"(o), "+v"(M), "+v"(L));
      SB();
    }
    LDS_FENCE(); bar(); LDS_FENCE();
    STAMP(2);
#pragma unroll
    for (int k = 0; k < 4; ++k) if ((k & 1) * 4 + 4 > PM) ml[k] = pmr[k];
#pragma unroll
    for (int k = PM; k < 8; ++k) pt[k] = pp[k * 128];
    const v4u t = *tp;
    SB();
    {
      float m2 = M;
#pragma unroll
      for (int k = PM; k < 8; ++k) m2 = fmx(m2, ml[k >> 2][k & 3]);
      if (PM) {
        const float e = __builtin_amdgcn_exp2f((M - m2) * EXP_C);
        o *= e; L *= e;
      }
      M = m2;
#pragma unroll
      for (int k = PM; k < 8; ++k) {
        const float e = __builtin_amdgcn_exp2f((ml[k >> 2][k & 3] - M) * EXP_C);
        o += pt[k] * e;
        L += ml[2 + (k >> 2)][k & 3] * e;
      }
    }
    v4u msg;
#pragma unroll
    for (int j = 0; j < 3; ++j) msg[j] = tag<0x100>(__builtin_amdgcn_perm(asu(o[j]), asu(o[3]), 0x07060501u + j), t[j]);
    msg[3] = tag<1>(asu((lane & 1) ? L : M), t[3]);
    __builtin_amdgcn_raw_buffer_store_b128(msg, rws, (int)to, 0, S1);
    SB();
    if (!isy) STAMP(15);
  } else if (fj < 4) {
    unsigned fvo = ((lane >> 3) * (8 * F * 2) + (lane & 7) * 16) | lmask;
    const unsigned fb = ((f & 3) * 2 + (fj >> 1)) * (56 * F * 2) + (f >> 2) * 128;
    const rsrc_t rf = mkrs((fj & 1) ? wb : wa, FX ? 0 : D * F * 2);
    asm volatile("" : "+v"(fvo));
#pragma unroll
    for (int i = 0; i < FE; ++i) { ring[i] = ld4<2>(rf, fvo, fb + i * (F * 2)); SB(); }
    clear(FK, true);
    if (FD) __builtin_amdgcn_s_sleep(FD);
    SB();
    STAMP(46);
#pragma unroll
    for (int i = FE; i < 8; ++i) { ring[i] = ld4<2>(rf, fvo, fb + i * (F * 2)); SB(); }
    const v4u xn = *reinterpret_cast<const v4u*>(&s_xn[(((f & 3) * 2 + (fj >> 1)) * 7 + (lane >> 3)) * 4]);
    const int p = fj * 16 + ((lane >> 4) & 1) * 8 + (lane & 7);
    v4u t = *reinterpret_cast<const v4u*>(&s_dst[6][p * 4]);
    unsigned to = (lane & 40) ? OOB : mf(p);
    v4u pb[8];
#pragma unroll
    for (int i = 0; i < 8; ++i) {
      pb[i] = pm & __builtin_amdgcn_perm(xn[i >> 1], xn[i >> 1], (i & 1) ? 0x03020302u : 0x01000100u);
      asm volatile("" : "+v"(pb[i]));
    }
    asm volatile("" : "+v"(t), "+v"(to));
    SB();
    LDS_FENCE(); bar(); LDS_FENCE();
    SB();
    STAMP(2);
    f32x4 acc[2] = {{0.f, 0.f, 0.f, 0.f}, {0.f, 0.f, 0.f, 0.f}};
#pragma unroll
    for (int i = 0; i < 8; ++i) acc[i & 1] = mfma(ring[i], pb[i], acc[i & 1]);
    asm volatile("" :: "v"(pv));
    acc[0] += acc[1];
    f32x4 z;
#pragma unroll
    for (int v = 0; v < 4; ++v)
      z[v] = x32_add(asf((unsigned)__builtin_amdgcn_update_dpp((int)asu(acc[0][v]), (int)asu(acc[0][v]), ROR8, 0xc, 0xf, false)));
    __builtin_amdgcn_raw_buffer_store_b128(tag4(z, t), rws, (int)to, 0, S1);
    SB();
    STAMP(14);
  } else if (!idle) {
    LDS_FENCE(); bar(); LDS_FENCE();
    SB();
    STAMP(2);
  }
  const v4u old = *reinterpret_cast<const v4u*>(&s_old[w][lane * 4]);
  if (isy) {
    if (YB) { SB(); __builtin_amdgcn_s_barrier(); SB(); }
    lds1_t po = (lane & 8) ? (lds1_t)&s_ml2[w] : (lds1_t)&s_o[w][(lane & 7) * 4 + (lane >> 5) * 2 + ((lane >> 4) & 1)];
    lds1_t pq = (lds1_t)&s_ml2[4 + w];
    asm volatile("" : "+v"(po), "+v"(pq));
    if (G1) until(G1 + __builtin_amdgcn_readlane(tok, C_LEAD));
    STAMP(14);
    const v4u dr = poll<PD1, PG1, PS1>(rw4, pa, old);
    STAMP(15);
    const v4u d = dr & (v4u){~0x100u, ~0x100u, ~0x100u, ~1u};
    const float ot = dpp<XOR1>(asf(d[3]));
    const float m = (lane & 1) ? ot : asf(d[3]), l = (lane & 1) ? asf(d[3]) : ot;
    float M = fmx(m, dpp<ROR8>(m)); M = x16_mx(M); M = x32_mx(M);
    *pq = M;
    const float e = __builtin_amdgcn_exp2f((m - M) * EXP_C);
    const float v3 = asf(__builtin_amdgcn_perm(__builtin_amdgcn_perm(d[2], d[1], 0x04000c0cu), d[0], 0x0706000cu));
    float y0 = tr32(asf(d[0]) * e, asf(d[2]) * e), y1 = tr32(asf(d[1]) * e, v3 * e), L = x32_add(l * e);
    y0 = tr16(y0, y1); L = x16_add(L);
    const bool s_ = lane & 8;
    *po = (s_ ? L : y0) + dpp<ROR8>(s_ ? y0 : L);
    LDS_FENCE(); bar(); LDS_FENCE();
    STAMP(3);
    poll_end(dr);
    STAMP(4);
    STAMP_OUT();
    __builtin_amdgcn_endpgm();
  }
  if (isr) {
    const int col = (ro >> 1) * 32 + (ro & 1) * 4 + (lane & 3) * 8 + ((lane >> 2) & 3);
    float xo = asf((unsigned)xg[col] << 16);
    lds1_t po = (lds1_t)&s_r[(lane & 12) ? 8 : w][(lane & 3) * 4 + (lane >> 5) * 2 + ((lane >> 4) & 1)], pq = (lds1_t)&s_r[lane >> 4][lane & 15];
    const rsrc_t ro_ = mkrs(out, D * 2);
    unsigned oo = lane < 16 ? col * 2 : OOB;
    asm volatile("" : "+v"(xo), "+v"(po), "+v"(pq), "+v"(oo));
    if (G2) until(G2 + __builtin_amdgcn_readlane(tok, C_LEAD));
    const v4u dr = poll<PD2, PG2, PS2>(rw4, pa, old);
    const v4u d = dr & ~1u;
    float y0 = tr32(asf(d[0]), asf(d[2])), y1 = tr32(asf(d[1]), asf(d[3]));
    y0 = tr16(y0, y1);
    y0 += dpp<ROR8>(y0); y0 += dpp<ROR4>(y0);
    *po = y0;
    LDS_FENCE(); bar(); LDS_FENCE();
    if (w == 0) {
      float a = pq[0] + pq[64];
      a = x16_add(a); a = x32_add(a);
      __builtin_amdgcn_raw_buffer_store_b16((short)f2bf(xo + a), ro_, (int)oo, 0, SO);
    }
    STAMP(4);
    poll_end(dr);
  }
  if (!isr) STAMP(4);
  STAMP_OUT();
}

extern "C" void kernel_launch(void* const* d_in, const int* in_sizes, int n_in,
                              void* d_out, int out_size, void* d_ws, size_t ws_size,
                              hipStream_t stream) {
  fused_kernel<<<NWG, NT, 0, stream>>>((const u16*)d_in[0], (const u16*)d_in[1], (const u16*)d_in[2], (const u16*)d_in[3],
                                       (const u16*)d_in[4], (const u16*)d_in[6], (const u16*)d_in[7], (const u16*)d_in[8],
                                       (u16*)d_out, (char*)d_ws);
}
```

```cpp
#include <hip/hip_runtime.h>
#include <hip/hip_bf16.h>
#include <stdint.h>

#define STK 2
#define SD 2
#define SV 0
#define DR 1
#define SWP 0
#define LD 1
#define LU 0
#define LB 1
#define LP 8
#define SKF 0
#define SL0 0
#define GB 1
#define YS 22
#define YE 0
#define YK 0
#define FE 0
#define QPOS 24
#define PACE 8
#define QTRIES 8
#define TQ 123
#define TF 131
#define TFY 129
#define TY 129
#define NP 4
#define TAU 3640
#define PP 3000
#define PL 12
#define TOFF 150
#define TOFFQ 180
#define QBURST 100000
#define PBURST 4000
#define KBURST 100000
#define VBURST 100000
#define QFIRST 24
#define OPOS 8
#define TWD 0
#define OA 16
#define OB 16
#define S1 16
#define S2 16
#define G1 3835
#define G2 3955
#define EO 3
#define PD1 2
#define PG1 6
#define PD2 2
#define PG2 6
#define PD3 2
#define PG3 6
#define G3 3845
#define SR 4
#define PS1 0
#define PS2 0
#define SO 0
#define DK 57
#define DS 0
#define DG 4
#define DB 0
#define SQ 0
#define SF_ 0
#define SFY 0
#define SY 0
#define XA 4
#define XB 4
#define TX 0
#define NOHS 0
#define ES 1
#define EL 8
#define SVM 0
#define SVL 0
#define FO 0
#define FP 1
#define NT3 1
#define UF 1
#define WV 1
#define FX 0
#define PM 5
#define FK 0
#define YB 1
#define FD 0
#define YD 0
#define FB 1
#define XW 0
#define DIAG 0
#define LITE 0

typedef unsigned v4u __attribute__((ext_vector_type(4)));
typedef unsigned v2u __attribute__((ext_vector_type(2)));
typedef float f32x4 __attribute__((ext_vector_type(4)));
typedef float f32x2 __attribute__((ext_vector_type(2)));
typedef __bf16 bf16x2 __attribute__((ext_vector_type(2)));
typedef __bf16 bf16x8 __attribute__((ext_vector_type(8)));
typedef unsigned short u16;
typedef __amdgpu_buffer_rsrc_t rsrc_t;
typedef __attribute__((address_space(3))) f32x4* lds4_t;
typedef __attribute__((address_space(3))) v4u* ldsu_t;
typedef __attribute__((address_space(3))) float* lds1_t;

constexpr int D = 448, SK = 32768, DH = 256, F = 1792, NQ = 4096;
constexpr int NWG = 256, NT = 512;
constexpr unsigned OOB = 0x80000000u;
constexpr unsigned SENT = 0xFFFFFFFFu;
constexpr unsigned AREA = 9216;
constexpr unsigned A_O1 = 1024;
constexpr unsigned A_F = 5120;
constexpr unsigned A_Y2 = 1024;
constexpr unsigned WS_REQ = NWG * AREA;
constexpr unsigned WS_ECHO = WS_REQ + 1024;
constexpr unsigned WS_GO = WS_ECHO + 1024;
constexpr unsigned WS_T = WS_GO + 256;
constexpr unsigned WS_Q = WS_T + 256;
constexpr unsigned WS_END = WS_Q + 8 * 2048;
constexpr int SPIN_CAP = 1 << 18;
constexpr int C_NEXT = 0, C_TICK = 1, C_GO = 2, C_Q = 3, C_T0 = 4, C_FC = 5, C_OLD = 6, C_DONE = 7, C_LEAD = 8, C_CNT = 9, C_KT = 10, C_FIN = 11;

__device__ __forceinline__ float asf(unsigned u) { return __builtin_bit_cast(float, u); }
__device__ __forceinline__ unsigned asu(float f) { return __builtin_bit_cast(unsigned, f); }
__device__ __forceinline__ float rbf(float f) { __bf16 b = (__bf16)f; return (float)b; }
__device__ __forceinline__ unsigned f2bf(float f) { __bf16 b = (__bf16)f; return (unsigned)__builtin_bit_cast(u16, b); }
__device__ __forceinline__ float dot2(unsigned k, unsigned q, float acc) {
  return __builtin_amdgcn_fdot2_f32_bf16(__builtin_bit_cast(bf16x2, k), __builtin_bit_cast(bf16x2, q), acc, false);
}
__device__ __forceinline__ f32x2 unp(unsigned u) { f32x2 r; r.x = asf(u << 16); r.y = asf(u & 0xffff0000u); return r; }
template <int CTRL> __device__ __forceinline__ float dpp(float x) {
  return asf((unsigned)__builtin_amdgcn_update_dpp(0, (int)asu(x), CTRL, 0xf, 0xf, true));
}
constexpr int XOR1 = 0xB1, XOR2 = 0x4E, XOR7 = 0x141, ROR8 = 0x128, ROR4 = 0x124, ROR2 = 0x122, BCAST = 0x150;
constexpr int QP_EVEN = 0xA0, QP_ODD = 0xF5;
__device__ __forceinline__ float x16_add(float x) { auto s = __builtin_amdgcn_permlane16_swap(asu(x), asu(x), false, false); return asf(s[0]) + asf(s[1]); }
__device__ __forceinline__ float x32_add(float x) { auto s = __builtin_amdgcn_permlane32_swap(asu(x), asu(x), false, false); return asf(s[0]) + asf(s[1]); }
__device__ __forceinline__ float x16_max(float x) { auto s = __builtin_amdgcn_permlane16_swap(asu(x), asu(x), false, false); return fmaxf(asf(s[0]), asf(s[1])); }
__device__ __forceinline__ float x32_max(float x) { auto s = __builtin_amdgcn_permlane32_swap(asu(x), asu(x), false, false); return fmaxf(asf(s[0]), asf(s[1])); }
__device__ __forceinline__ float tr32(float a, float b) { auto s = __builtin_amdgcn_permlane32_swap(asu(a), asu(b), false, false); return asf(s[0]) + asf(s[1]); }
__device__ __forceinline__ float tr16(float a, float b) { auto s = __builtin_amdgcn_permlane16_swap(asu(a), asu(b), false, false); return asf(s[0]) + asf(s[1]); }
__device__ __forceinline__ float fmx(float a, float b) { return __builtin_elementwise_maximum(a, b); }
__device__ __forceinline__ float x16_mx(float x) { auto s = __builtin_amdgcn_permlane16_swap(asu(x), asu(x), false, false); return fmx(asf(s[0]), asf(s[1])); }
__device__ __forceinline__ float x32_mx(float x) { auto s = __builtin_amdgcn_permlane32_swap(asu(x), asu(x), false, false); return fmx(asf(s[0]), asf(s[1])); }
__device__ __forceinline__ float wave_sum(float v) {
  v += dpp<XOR1>(v); v += dpp<XOR2>(v); v += dpp<XOR7>(v); v += dpp<ROR8>(v);
  v = x16_add(v); return x32_add(v);
}
__device__ __forceinline__ float wave_max(float v) {
  v = fmaxf(v, dpp<XOR1>(v)); v = fmaxf(v, dpp<XOR2>(v)); v = fmaxf(v, dpp<XOR7>(v)); v = fmaxf(v, dpp<ROR8>(v));
  v = x16_max(v); return x32_max(v);
}
__device__ __forceinline__ rsrc_t mkrs(const void* p, unsigned bytes) { return __builtin_amdgcn_make_buffer_rsrc((void*)p, 0, (int)bytes, 0x00020000); }
__device__ __forceinline__ rsrc_t selr(bool c, rsrc_t a, rsrc_t b) { return c ? a : b; }
__device__ __forceinline__ unsigned selu(bool c, unsigned a, unsigned b) { return c ? a : b; }
template <int AUX> __device__ __forceinline__ v4u ld4(rsrc_t r, unsigned vo, unsigned so) { return __builtin_amdgcn_raw_buffer_load_b128(r, (int)vo, (int)so, AUX); }
template <int AUX> __device__ __forceinline__ v2u ld2(rsrc_t r, unsigned vo, unsigned so) { return __builtin_amdgcn_raw_buffer_load_b64(r, (int)vo, (int)so, AUX); }
__device__ __forceinline__ void st4c(v4u v, rsrc_t r, unsigned vo) { __builtin_amdgcn_raw_buffer_store_b128(v, r, (int)vo, 0, 16); }
__device__ __forceinline__ void st1c(unsigned v, rsrc_t r, unsigned vo) { __builtin_amdgcn_raw_buffer_store_b32(v, r, (int)vo, 0, 16); }
template <unsigned M> __device__ __forceinline__ unsigned tag(unsigned u, unsigned t) { return (u & ~M) | (t & M); }
__device__ __forceinline__ v4u tag4(const f32x4& v, const v4u& t) { return (v4u){tag<1>(asu(v[0]), t[0]), tag<1>(asu(v[1]), t[1]), tag<1>(asu(v[2]), t[2]), tag<1>(asu(v[3]), t[3])}; }
__device__ __forceinline__ bool arrived(const v4u& v, const v4u& o) { return __builtin_amdgcn_ballot_w64(v[0] == o[0] || v[1] == o[1] || v[2] == o[2] || v[3] == o[3]) == 0; }
__device__ __forceinline__ unsigned lowb(const v4u& v) { return __builtin_amdgcn_perm(v[1], v[0], 0x0c0c0400u) | __builtin_amdgcn_perm(v[3], v[2], 0x04000c0cu); }
#define SB() __builtin_amdgcn_sched_barrier(0)
__device__ __forceinline__ void bar() { __builtin_amdgcn_s_waitcnt(0xC07F); __builtin_amdgcn_s_barrier(); }
#define WAITV(n) __builtin_amdgcn_s_waitcnt(((n) & 15) | (((n) >> 4) << 14) | 0x0F70)
template <int V> struct IC { static constexpr int value = V; constexpr operator int() const { return V; } };
template <int A, int B, class Fn> __device__ __forceinline__ void static_for(Fn&& f) { if constexpr (A < B) { f(IC<A>{}); static_for<A + 1, B>(f); } }
#define LDS_FENCE() asm volatile("" ::: "memory")

constexpr float EXP_C = 0.0625f * 1.4426950408889634f;

struct Att {
  float m, l;
  f32x4 a[2];
};
__device__ __forceinline__ f32x4 mfma(const v4u& a, const v4u& b, const f32x4& c) {
  return __builtin_amdgcn_mfma_f32_16x16x32_bf16(__builtin_bit_cast(bf16x8, a), __builtin_bit_cast(bf16x8, b), c, 0, 0, 0);
}
__device__ __forceinline__ float red_max(float v) { v = fmaxf(v, dpp<XOR1>(v)); v = fmaxf(v, dpp<XOR2>(v)); return x32_max(x16_max(v)); }

template <int NL, class ISSUE>
__device__ __forceinline__ float scores(v4u (&kb)[8], const v4u (&qb)[8], int lane, ISSUE issue) {
  f32x4 d = {0.f, 0.f, 0.f, 0.f};
#pragma unroll
  for (int i = 0; i < NL; ++i) {
    d = mfma(kb[i], qb[i], d);
    asm volatile("" : "+v"(d));
    issue(i);
    SB();
  }
  const bool odd = lane & 1;
  return rbf((odd ? d[3] : d[0]) + dpp<XOR1>(odd ? d[1] : d[2]));
}
template <class ISSUE>
__device__ __forceinline__ float phase_k4(v4u (&kb)[8], const v4u (&qb)[8], Att& st, int lane, ISSUE issue) {
  float s = scores<4>(kb, qb, lane, issue);
  s = (lane & 2) ? -__builtin_inff() : s;
  const float mn = fmaxf(st.m, red_max(s));
  const float al = __builtin_amdgcn_exp2f((st.m - mn) * EXP_C);
  const float p = __builtin_amdgcn_exp2f((s - mn) * EXP_C);
  st.m = mn; st.l = st.l * al + p;
  st.a[0] *= al; st.a[1] *= al;
  SB();
  return p;
}
template <int I, int O, class ISSUE>
__device__ __forceinline__ void pv_step(v4u (&vb)[8], unsigned pp, const v4u& pm, Att& st, ISSUE& issue) {
  const v4u pb = pm & (unsigned)__builtin_amdgcn_update_dpp(0, (int)pp, I * 0x55, 0xf, 0xf, true);
  st.a[0] = mfma(vb[O + 2 * I], pb, st.a[0]);
  asm volatile("" : "+v"(st.a[0]));
  issue(O + 2 * I);
  SB();
  st.a[1] = mfma(vb[O + 2 * I + 1], pb, st.a[1]);
  asm volatile("" : "+v"(st.a[1]));
  issue(O + 2 * I + 1);
  SB();
}
template <int NL, class ISSUE>
__device__ __forceinline__ void phase_v(v4u (&vb)[8], float p, const v4u& pm, Att& st, ISSUE issue) {
  constexpr int O = 8 - NL;
  const unsigned pp = __builtin_bit_cast(unsigned, (bf16x2){(__bf16)p, (__bf16)p});
  pv_step<0, O>(vb, pp, pm, st, issue); pv_step<1, O>(vb, pp, pm, st, issue);
  if constexpr (NL == 8) { pv_step<2, O>(vb, pp, pm, st, issue); pv_step<3, O>(vb, pp, pm, st, issue); }
}

template <int N, int GAP, int SL>
__device__ __forceinline__ v4u poll(const v4u& r, unsigned pa, const v4u& old) {
  v4u d;
  unsigned long long t0, t1, t2;
  int n = SPIN_CAP;
  asm volatile(
      "s_nop 4\n\t"
      "buffer_load_dwordx4 v[200:203], %5, %6, 0 offen sc1\n\t"
      ".rept %11\n\t"
      "s_sleep %12\n\t"
      "buffer_load_dwordx4 v[200:203], %5, %6, 0 offen sc1\n\t"
      ".endr\n"
      "1:\n\t"
      "s_waitcnt vmcnt(%11)\n\t"
      "v_cmp_eq_u32 vcc, v200, %7\n\t"
      "v_cmp_eq_u32 %1, v201, %8\n\t"
      "v_cmp_eq_u32 %2, v202, %9\n\t"
      "v_cmp_eq_u32 %3, v203, %10\n\t"
      "s_or_b64 vcc, vcc, %1\n\t"
      "s_or_b64 %2, %2, %3\n\t"
      "s_or_b64 exec, vcc, %2\n\t"
      "s_cbranch_scc0 2f\n\t"
      ".if %13\n\ts_sleep %13\n\t.endif\n\t"
      "buffer_load_dwordx4 v[200:203], %5, %6, 0 offen sc1\n\t"
      "s_sub_u32 %4, %4, 1\n\t"
      "s_cbranch_scc0 1b\n"
      "2:\n\t"
      "s_mov_b64 exec, -1"
      : "=&{v[200:203]}"(d), "=&s"(t0), "=&s"(t1), "=&s"(t2), "+s"(n)
      : "v"(pa), "s"(r), "v"(old[0]), "v"(old[1]), "v"(old[2]), "v"(old[3]), "n"(N - 1), "n"(GAP), "n"(SL)
      : "vcc", "scc", "memory");
  return d;
}
__device__ __forceinline__ void poll_end(const v4u& d) { asm volatile("s_waitcnt vmcnt(0)" :: "{v[200:203]}"(d)); }

#define STAMP(k) do {} while (0)
#define STAMP_OUT() do {} while (0)

__device__ __forceinline__ f32x2 ln_stats(const v4u& xa, int lane) {
  f32x2 xv[4];
  float sm = 0.f;
#pragma unroll
  for (int j = 0; j < 4; ++j) { xv[j] = unp(xa[j]); sm += xv[j].x + xv[j].y; }
  const float mean = wave_sum(sm) * (1.0f / D);
  float sq = 0.f;
#pragma unroll
  for (int j = 0; j < 4; ++j) { const f32x2 d = xv[j] - mean; sq += d.x * d.x + d.y * d.y; }
  sq = lane < 56 ? sq : 0.f;
  const float var = rbf(wave_sum(sq) * (1.0f / D));
  const float sd = rbf(sqrtf(rbf(var + 1e-5f)));
  return (f32x2){rbf(mean), 1.0f / sd};
}
__device__ __forceinline__ float ln_apply(float x, f32x2 st) { return rbf(rbf(x - st.x) * st.y); }

__global__ void __launch_bounds__(NT, 2)
fused_kernel(const u16* __restrict__ xg, const u16* __restrict__ kc, const u16* __restrict__ vc, const u16* __restrict__ wq,
             const u16* __restrict__ wo, const u16* __restrict__ wg, const u16* __restrict__ wu, const u16* __restrict__ wd,
             u16* __restrict__ out, char* ws) {
  __shared__ __attribute__((aligned(16))) float s_part[8][512];
  __shared__ __attribute__((aligned(16))) float s_mv[8][64];
  __shared__ __attribute__((aligned(16))) unsigned s_q[256];
  __shared__ __attribute__((aligned(16))) unsigned s_qs[64];
  __shared__ __attribute__((aligned(16))) unsigned s_old[8][256];
  __shared__ __attribute__((aligned(16))) unsigned s_dst[8][256];
  __shared__ __attribute__((aligned(16))) float s_ml[2][16];
  __shared__ __attribute__((aligned(16))) float s_o[4][32];
  __shared__ __attribute__((aligned(16))) float s_ml2[16];
  __shared__ __attribute__((aligned(16))) unsigned s_xn[256];
  __shared__ __attribute__((aligned(16))) float s_h[32];
  __shared__ __attribute__((aligned(16))) float s_r[9][16];
  __shared__ __attribute__((aligned(16))) float s_sc[64][32];
  __shared__ int s_ctl[64];
  const int tid = threadIdx.x, lane = tid & 63, w = __builtin_amdgcn_readfirstlane(tid >> 6);
  const int bi = SWP == 2 ? (blockIdx.x & ~7) | ((blockIdx.x + 7) & 7) : blockIdx.x ^ SWP, h = (((bi & 1) ? bi + 5 : bi) & 6) | (bi >> 7), s = ((bi >> 3) & 15) * 2 + (bi & 1);
  const int b = h * 32 + s;
  static_assert(8 * TQ + 8 * TF + (YS - 16) * TFY + (32 - YS) * TY == 4096 && TQ - NP > 80 && TF - NP > 80 && TFY - NP > 80 && TY - NP > 80, "tickets");
  static_assert(TQ - NP <= 128 && TF - NP <= 128 && TFY - NP <= 128 && TY - NP <= 128, "tickets");
  const int nt = s < 8 ? TQ : s < 16 ? TF : s < YS ? TFY : TY, nk = (nt - NP) >> 1;
  const int first = s * TY - min(s, YS) * (TY - TFY) - min(s, 16) * (TFY - TF) - min(s, 8) * (TF - TQ);
  asm volatile("" :: "s"(xg), "s"(kc), "s"(vc), "s"(wq), "s"(wo), "s"(wg), "s"(wu), "s"(wd), "s"(out), "s"(ws));
  STAMP(0);
  const rsrc_t rx = mkrs(xg, D * 2);
  const rsrc_t rk = mkrs(kc + (size_t)h * SK * DH, SK * DH * 2);
  const rsrc_t rv = mkrs(vc + (size_t)h * SK * DH, SK * DH * 2);
  const rsrc_t rws = mkrs(ws, WS_END);
  const unsigned l16 = lane * 16;
  auto ubase = [&](int u) -> unsigned { return (unsigned)(first + 2 * u) * 4096u; };
  const unsigned kvo = ((lane >> 1) & 7) * 512 + (lane >> 4) * 32 + (lane & 1) * 16, vvo = (lane >> 4) * 1024 + (lane & 15) * 16;
  auto ldk = [&](unsigned base, int i) { return ld4<2>(rk, kvo + (i & 3) * 128, base + (i >> 2) * 4096); };
  const unsigned area = (unsigned)b * AREA;
  auto area_of = [&](int ss) -> unsigned { return (unsigned)(h * 32 + ss) * AREA; };
  const unsigned lmask = lane < 56 ? 0u : OOB;

  const bool isq = s < 8, isf = s < 14, isy = s >= 16;
  const int f = h * 14 + s, y = h * 16 + s - 16, ro = h * 4 + (s >> 2);
  const bool isr = s < 16 && ((s + h) & 3) == 0 && ro < 28;
  const bool tile = isq;
  const bool leader = (!(NOHS & 1) && b == NWG - 1 && w == 1), member = (w == 0);

  const unsigned pa = isy ? (w < 4 ? area + A_O1 + w * 1024 + l16 : (w == 7 && y < 112) ? area + A_F + l16 : OOB) : isr ? area + A_Y2 + w * 1024 + l16 : OOB;
  const unsigned mo_ = area_of(16 + (lane & 8) + (lane >> 4)) + A_O1 + s * 128 + (lane & 7) * 16;
  auto mf = [&](int p) -> unsigned {
    const int fd = (f & ~3) + ((p >> 2) & 3);
    return (unsigned)((fd >> 4) * 32 + 16 + (fd & 15)) * AREA + A_F + (f & 3) * 256 + (p >> 4) * 64 + (p & 3) * 16;
  };
  auto my = [&](int p) -> unsigned { return (unsigned)((p >> 4) * 32 + (p & 12) + ((-(p >> 4)) & 3)) * AREA + A_Y2 + y * 64 + (p & 3) * 16; };
  v4u eo, ed;
  auto da_of = [&]() {
    const int p = (w - 2) * 64 + lane;
    unsigned da = (isy && p < 112) ? my(p) : OOB;
    return (w & 6) == 2 ? da : (w & 6) == 4 ? mo_ + (w - 4) * 4 * AREA : (w == 6 && isf) ? mf(lane) : OOB;
  };
  if (EO == 1) { eo = ld4<OA>(rws, pa, 0); SB(); }
  if (EO == 3 || (EO == 4 && !tile)) { eo = ld4<OA>(rws, pa, 0); ed = ld4<OB>(rws, da_of(), 0); SB(); }
  if (EO == 5) { ed = ld4<OB>(rws, da_of(), 0); SB(); }
  const v4u sent = {SENT, SENT, SENT, SENT};
  unsigned poll_off = lane == 1 ? WS_GO : lane < 4 ? WS_T - 32 + l16 : OOB;
  poll_off = lane == 0 ? WS_REQ + (b >> 2) * 16 : poll_off;
  poll_off = leader ? (ES ? lane * 4 * AREA + (lane & 3) * 256 : WS_ECHO + l16) : poll_off;
  auto echoes = [&](unsigned seen) {
    v4u r;
#pragma unroll
    for (int j = 0; j < 4; ++j) r[j] = __builtin_amdgcn_raw_buffer_load_b32(rws, (int)(poll_off | (seen >> j << 31)), j * AREA, 16);
    return r;
  };
  v4u pv = {0u, 0u, 0u, 0u}, e1 = {0u, 0u, 0u, 0u};
  unsigned go_ref = 0;
  int* const ctl = &s_ctl[lane];
  auto ctl_get = [&](int i) { return __builtin_amdgcn_readfirstlane(__hip_atomic_load(&s_ctl[i], __ATOMIC_RELAXED, __HIP_MEMORY_SCOPE_WORKGROUP)); };
  auto ctl_set = [&](int i) { __hip_atomic_store(&s_ctl[i], 1, __ATOMIC_RELAXED, __HIP_MEMORY_SCOPE_WORKGROUP); };
  const unsigned qoff = WS_Q + h * 2048 + l16;
  v4u qv0, qv1;
  if (member) {
    qv0 = ld4<16>(rws, qoff, 0);
    qv1 = ld4<16>(rws, qoff, 1024);
    SB();
    int c = lane == C_NEXT ? (isq ? TOFFQ : TOFF) * 128 : 0;
    c = (lane == C_TICK || lane == C_KT) ? 40 : c;
    c = lane == C_T0 ? (int)(__builtin_amdgcn_s_memrealtime() & 0x7fffffff) : c;
    __hip_atomic_store(ctl, c, __ATOMIC_RELAXED, __HIP_MEMORY_SCOPE_WORKGROUP);
    st1c(c, rws, (lane == C_T0 && (bi >> 3) == 12) ? WS_T + (bi & 7) * 4 : OOB);
  }
  *reinterpret_cast<v4u*>(&s_sc[w * 8][lane * 4]) = sent;
  int t0 = 0;
  auto now = [&]() __attribute__((always_inline)) { return ((int)__builtin_amdgcn_s_memrealtime() - t0) & 0x7fffff; };
  const int neg = lane == C_NEXT ? 0 : (int)0x80000000;
  const int xcc = __builtin_amdgcn_s_getreg(20 | (3 << 11));
  constexpr auto tkf = [](int t, int sh) { return TAU + sh * 128 / ((t - NP) / 2 - 8); };
  constexpr auto tvf = [](int t, int sh) { return TAU - sh * 128 / ((t - NP) / 2 + 8 - PL); };
  int tk = s < 8 ? tkf(TQ + 16, SQ) : s < 16 ? tkf(TF, SF_) : s < YS ? tkf(TFY, SFY) : tkf(TY, SY);
  int tv = s < 8 ? tvf(TQ, SQ) : s < 16 ? tvf(TF, SF_) : s < YS ? tvf(TFY, SFY) : tvf(TY, SY);
  int inc = lane == C_NEXT ? tk : lane == C_CNT ? 1 : 0;
  int lost = 0;
  auto token = [&](int burst, bool early = false) __attribute__((always_inline)) {
    const int t = max(now() * 128 - burst, 0);
    if (GB && early) {
      const int o = __hip_atomic_fetch_max(ctl, t | neg, __ATOMIC_RELAXED, __HIP_MEMORY_SCOPE_WORKGROUP);
      lost += max(o, t | neg) - o;
    } else {
      __hip_atomic_fetch_max(ctl, t | neg, __ATOMIC_RELAXED, __HIP_MEMORY_SCOPE_WORKGROUP);
    }
    return __hip_atomic_fetch_add(ctl, inc, __ATOMIC_RELAXED, __HIP_MEMORY_SCOPE_WORKGROUP);
  };
  auto due_of = [&](int x) { return __builtin_amdgcn_readlane(x, C_NEXT) >> 7; };
  auto until = [&](int due) __attribute__((always_inline)) {
#pragma unroll 1
    for (int it = 0; it < 4096 && now() < due; ++it) __builtin_amdgcn_s_sleep(1);
    SB();
  };
  unsigned qs0, qs1;
  auto q_there = [&]() { return __builtin_amdgcn_ballot_w64(((lowb(qv0) ^ qs0) & (lowb(qv1) ^ qs1)) != 0xFFFFFFFFu) == 0; };
  auto load_q = [&]() { qv0 = ld4<16>(rws, qoff, 0); qv1 = ld4<16>(rws, qoff, 1024); };
  auto publish_q = [&]() {
    *reinterpret_cast<v2u*>(&s_q[lane * 2]) = (v2u){__builtin_amdgcn_perm(qv0[1], qv0[0], 0x07060302u), __builtin_amdgcn_perm(qv0[3], qv0[2], 0x07060302u)};
    *reinterpret_cast<v2u*>(&s_q[128 + lane * 2]) = (v2u){__builtin_amdgcn_perm(qv1[1], qv1[0], 0x07060302u), __builtin_amdgcn_perm(qv1[3], qv1[2], 0x07060302u)};
    LDS_FENCE();
    __builtin_amdgcn_s_waitcnt(0xC07F);
    ctl_set(C_Q);
  };
  v4u pre[4][8], ring[8];
  auto unit0 = [&]() {
#pragma unroll
    for (int i = 0; i < 8; ++i) { pre[0][i] = ldk(ubase(w), i); SB(); }
    if (EO == 2) { eo = ld4<OA>(rws, pa, 0); SB(); }
  };
  SB();
  if (tile) {
    v4u tw[8];
    const int oct = 7 * w;
    constexpr int stride = NQ * 2;
    const rsrc_t rt = mkrs(wq, D * stride);
    const unsigned to = (unsigned)((oct + (lane >> 3)) * 8 * stride + (h * 8 + s) * 128 + (lane & 7) * 16) | lmask;
    const v4u xa = ld4<0>(rx, l16, 0);
    const v4u xb = ld4<0>(rx, (unsigned)(oct + (lane >> 3)) * 16 | lmask, 0);
    SB();
#pragma unroll
    for (int i = 0; i < 8; ++i) { tw[i] = ld4<2>(rt, to, i * stride); SB(); }
    if (EO == 4) { eo = ld4<OA>(rws, pa, 0); ed = ld4<OB>(rws, da_of(), 0); SB(); }
    LDS_FENCE(); bar(); LDS_FENCE();
    t0 = ctl_get(C_T0);
    SB();
    const f32x2 ls = ln_stats(xa, lane);
    f32x2 acc[4];
#pragma unroll
    for (int i = 0; i < 8; ++i) {
      const float xn = ln_apply(asf((i & 1) ? (xb[i >> 1] & 0xffff0000u) : (xb[i >> 1] << 16)), ls);
#pragma unroll
      for (int j = 0; j < 4; ++j) acc[j] = i ? acc[j] + unp(tw[i][j]) * xn : unp(tw[i][j]) * xn;
    }
    float y0 = tr32(acc[0].x, acc[2].x), y1 = tr32(acc[0].y, acc[2].y), y2 = tr32(acc[1].x, acc[3].x), y3 = tr32(acc[1].y, acc[3].y);
    y0 = tr16(y0, y2); y1 = tr16(y1, y3);
    const bool s_ = lane & 8;
    y0 = (s_ ? y1 : y0) + dpp<ROR8>(s_ ? y0 : y1);
    s_mv[w][(lane & 7) * 8 + (lane >> 3)] = y0;
    STAMP(6);
    token(QBURST);
    unit0();
    LDS_FENCE(); bar(); LDS_FENCE();
    if (member) {
      float a = 0.f;
#pragma unroll
      for (int k = 0; k < 8; ++k) a += s_mv[k][lane];
      s_qs[lane] = f2bf(a) << 16;
      LDS_FENCE();
      const v4u qs = *reinterpret_cast<const v4u*>(&s_qs[(lane & 15) * 4]) | (~((s & 4) ? qv1 : qv0) & 0xffffu);
      st4c(qs, rws, (lane >> 4) == (s & 3) ? qoff + (s >> 2) * 1024 : OOB);
    }
  } else if (leader && !LU) {
    e1 = ES ? echoes(0) : ld4<16>(rws, poll_off, 0);
    go_ref = __builtin_amdgcn_raw_buffer_load_b32(rws, (int)WS_GO, 0, 16);
  }
  SB();
  STAMP(5);
  SB();
  const u16* wa = isy ? wo + (size_t)y * 32 * D : wg;
  const u16* wb = isy ? wd + (size_t)(y < 112 ? (y >> 1) * 32 + (y & 1) * 4 : 0) * D : wu;
  asm volatile("" : "+s"(wa), "+s"(wb));
  bool ok = !member;
  static_for<0, 40>([&](auto N) {
    constexpr int n = N;
    if constexpr (n == 0) {
      if (!tile) unit0();
      if (LU && leader) {
        e1 = echoes(0);
        go_ref = __builtin_amdgcn_raw_buffer_load_b32(rws, (int)WS_GO, 0, 16);
      }
    }
    if constexpr (n == 8) {
      if (!tile) {
        LDS_FENCE(); bar(); LDS_FENCE();
        t0 = ctl_get(C_T0);
      }
    }
    if constexpr (n == LP) {
      if (leader) {
        e1 += 1u;
        go_ref += 1u;
        st4c(e1, rws, WS_REQ + l16);
      }
    }
    if constexpr (n > 0 && n % 8 == 0) {
      const int x = token((isq || (LB && b == NWG - 1)) ? QBURST : PBURST, true);
      if constexpr (n > QFIRST) {
        if (!ok) {
          ok = q_there();
          if (ok) publish_q();
        }
      }
      if constexpr (n == OPOS + 16) {
        *reinterpret_cast<v4u*>(&s_old[w][lane * 4]) = (int)pa < 0 ? sent : eo;
        *reinterpret_cast<v4u*>(&s_dst[w][lane * 4]) = ~ed;
        LDS_FENCE();
        __hip_atomic_fetch_add(ctl, lane == C_OLD ? 1 : 0, __ATOMIC_RELAXED, __HIP_MEMORY_SCOPE_WORKGROUP);
      }
      SB();
      until(due_of(x));
      if constexpr (n == 8) {
        if (member) { qs0 = lowb(qv0); qs1 = lowb(qv1); }
      }
      if constexpr (n >= QFIRST) {
        if (!ok) load_q();
      }
      if constexpr (n == OPOS) {
        if (EO == 0 || EO == 5) eo = ld4<OA>(rws, pa, 0);
        if (EO < 3) ed = ld4<OB>(rws, da_of(), 0);
      }
      SB();
    }
    if constexpr (n == QPOS) {
      STAMP(9);
      if (ES && leader) pv = LD ? ~e1 : echoes(0);
      else pv = ld4<16>(rws, (member || leader) ? poll_off : OOB, 0);
      SB();
    }
    if constexpr (n >= 8) {
      if constexpr (n >= PACE) WAITV(PACE + ((n >= QPOS && n < QPOS + PACE) ? 1 : 0) + ((n >= OPOS && n < OPOS + PACE) ? (EO == 0 ? 2 : (EO == 3 || EO == 4) ? 0 : 1) : 0));
      if constexpr (n < 32) pre[n >> 3][n & 7] = ldk(ubase((n & 56) + w), n & 7); else ring[n & 7] = ldk(ubase(32 + w), n & 7);
    }
    SB();
    if constexpr (n - PACE == 7 || n - PACE == 15 || n - PACE == 23 || n - PACE == 31) STAMP(16 + (n - PACE) / 8);
  });
  inc = (lane == C_TICK || lane == C_KT) ? 1 : inc;
  if (GB == 1) __hip_atomic_fetch_add(ctl, -lost, __ATOMIC_RELAXED, __HIP_MEMORY_SCOPE_WORKGROUP);
  int tok = token(KBURST);
  STAMP(10);
  bool svc = member || leader;
  unsigned seenm = 0, last_echo = 0;
  bool echoed = false;
  auto service = [&]() {
    if (leader) {
      seenm |= (pv[0] == e1[0] ? 1u : 0u) | (pv[1] == e1[1] ? 2u : 0u) | (pv[2] == e1[2] ? 4u : 0u) | (pv[3] == e1[3] ? 8u : 0u);
      svc = __builtin_amdgcn_ballot_w64(seenm != 15u) != 0;
      st1c(go_ref, rws, (lane == 0 && !svc) ? WS_GO : OOB);
    } else {
      const unsigned req = (unsigned)__builtin_amdgcn_readfirstlane((int)((b & 2) ? ((b & 1) ? pv[3] : pv[2]) : ((b & 1) ? pv[1] : pv[0])));
      const unsigned go = (unsigned)__builtin_amdgcn_readlane((int)pv[0], 1);
      go_ref = echoed ? go_ref : go;
      svc = go == (unsigned)__builtin_amdgcn_readfirstlane((int)go_ref);
      if (NOHS & 1) svc = false;
      const bool rdy = ctl_get(C_OLD) == 8;
      if (ES && EL > 0) st4c((v4u){req, req, req, req}, rws, (lane < EL && svc && rdy && !(echoed && req == last_echo)) ? area + ((b >> 2) & 3) * 256 + l16 : OOB);
      else st1c(req, rws, (lane == 0 && svc && rdy && !(echoed && req == last_echo)) ? (ES ? area + ((b >> 2) & 3) * 256 : WS_ECHO + b * 4) : OOB);
      echoed |= rdy; last_echo = req;
    }
    if (ES && leader) pv = echoes(seenm);
    else pv = ld4<16>(rws, svc ? poll_off : OOB, 0);
    SB();
  };

  {
    if (!ok) {
      ok = q_there();
      if (ok) publish_q();
    }
#pragma unroll 1
    for (int it = 0; it < SPIN_CAP && !ctl_get(C_Q); ++it) {
      if (svc && it >= QTRIES) { service(); asm volatile("" :: "v"(pv)); }
      if (!ok) {
        load_q();
        ok = q_there() | !svc;
        if (ok) publish_q();
      } else {
        __builtin_amdgcn_s_sleep(1);
      }
    }
  }
  LDS_FENCE();
  v4u qb[8];
#pragma unroll
  for (int i = 0; i < 4; ++i) {
    const v4u z = {0u, 0u, 0u, 0u}, q = *reinterpret_cast<const v4u*>(&s_q[(lane & 8) * 16 + i * 32 + (lane >> 4) * 8 + (lane & 1) * 4]);
    qb[i] = (lane & 2) ? z : q; qb[4 + i] = (lane & 2) ? q : z;
  }
  v4u pm;
#pragma unroll
  for (int j = 0; j < 4; ++j) pm[j] = ((lane >> 1) & 3) == j ? ((lane & 1) ? 0xffff0000u : 0xffffu) : 0u;
  STAMP(1);
  SB();

  Att st;
  int sx = 20;
  int rank, slot;
  lds4_t pw;
  lds1_t pml;
  int idle;
  auto last = [&]() __attribute__((always_inline)) {
    pml = (lds1_t)&s_ml[(lane >> 3) & 1][slot];
    pw = (lds4_t)&s_part[slot][lane * 4];
    idle = __builtin_amdgcn_readfirstlane(!isy && !svc && slot >= PM && (unsigned)(rank - SR) >= 2 && (!isf || (unsigned)(rank - FO) >= 4));
    asm volatile("" : "+v"(pw), "+v"(pml), "+s"(idle));
    SB();
  };
  auto put_ml = [&]() __attribute__((always_inline)) {
    float l = st.l;
    l += dpp<XOR1>(l); l += dpp<XOR2>(l); l = x32_add(x16_add(l));
    pml[0] = st.m; pml[8] = l;
    SB();
  };
  {
    float* const sc = &s_sc[0][(lane >> 3) * 4 + (lane & 3)];
    rsrc_t nra, nrb;
    unsigned nva, nvb, nba, nbb, nsa, nsb;
    int kind = 0, nu = 32 + w, cu, tc = 32 + w, sd = 0;
    auto next = [&]() __attribute__((always_inline)) {
      cu = nu;
      const bool dyn = !(STK == 2 && SD) || tc < nk;
      tc = (STK == 2 && tc < nk) ? __builtin_amdgcn_readlane(tok, C_KT) : tc + 8;
      const int t = STK ? tc : __builtin_amdgcn_readlane(tok, C_TICK);
      const bool fx = t < 2 * nk, tw = t >= nt;
      kind = fx ? (t >= nk ? 1 : 0) : tw ? 3 : 2;
      nu = t >= nk ? t - nk : t;
      nba = (unsigned)(fx ? first + 2 * nu : first + t) * 4096u;
      nbb = fx ? nba + 4096 : nba;
      nra = selr(kind == 1, rv, rk);
      nrb = selr(kind == 0, rk, rv);
      nva = selu(kind == 1, vvo, kvo); nvb = selu(kind == 0, kvo, vvo);
      nsa = selu(kind == 1, 256, 128); nsb = selu(kind == 0, 128, 256);
      const int u = t + 8 - nk;
      const int aj = __builtin_amdgcn_readlane(tok, C_LEAD) * DK / DG;
      inc = lane == C_NEXT ? (t + PL >= 2 * nk ? PP : (int)selu(u >= 0, tv + ((STK == 2 && SD == 2 && member) ? aj : 0), tk) + ((unsigned)(u + XA) < XA + XB ? TX : 0)) : inc;
      if (STK == 2) inc = lane == C_KT ? (t < nk) : inc;
      if (STK && DR) inc = lane == C_FIN ? (t + 8 >= nt) : inc;
      slot = t - nt;
      rank = (STK && DR) ? __builtin_amdgcn_readlane(tok, C_FIN) : slot;
      const int fast = min(max(t - (2 * nk - PL + 8), 0), 8);
      sd = dyn ? __builtin_amdgcn_readlane(tok, C_NEXT) : sd + 8 * tv - fast * (tv - PP) + aj - SV;
      if (!tw) until((SD == 2 ? min(sd, __builtin_amdgcn_readlane(tok, C_NEXT)) : sd) >> 7);
      SB();
    };
    auto ldn = [&](int i) { ring[i] = i < 4 ? ld4<2>(nra, nva, nba + i * nsa) : ld4<2>(nrb, nvb, nbb + (i - 4) * nsb); };
    float mx;
    if (member) {
      const v4u d = pv - (unsigned)t0;
      const bool bad = max(max(d[0] + 512u, d[1] + 512u), max(d[2] + 512u, d[3] + 512u)) > 1024u;
      const int sm = (int)((d[0] + d[1]) + (d[2] + d[3]));
      const int lead = (__builtin_amdgcn_ballot_w64(bad) & 12) ? 0 : __builtin_amdgcn_readlane(sm, 2) + __builtin_amdgcn_readlane(sm, 3) + DB;
      const int slip = __builtin_amdgcn_readlane(tok, C_NEXT) - __builtin_amdgcn_readlane(tok, C_CNT) * tk - (isq ? TOFFQ : TOFF + SL0) * 128;
      const int adj = ((lead * DK) >> 5) - ((slip * SKF) >> 10);
      tk += adj; if (!(STK == 2 && SD)) tv += adj;
      __hip_atomic_fetch_add(ctl, lane == C_LEAD ? (lead * DG) >> 5 : (lane == C_NEXT ? (lead * DS) >> 5 : 0), __ATOMIC_RELAXED, __HIP_MEMORY_SCOPE_WORKGROUP);
    }
    if (GB == 2) __hip_atomic_fetch_add(ctl, -lost, __ATOMIC_RELAXED, __HIP_MEMORY_SCOPE_WORKGROUP);
    if (svc && !(LD && leader)) service();
    {
      const float s0 = scores<8>(pre[0], qb, lane, [&](int) {});
      const float s1 = scores<8>(pre[1], qb, lane, [&](int) {});
      const float s2 = scores<8>(pre[2], qb, lane, [&](int) {});
      const float s3 = scores<8>(pre[3], qb, lane, [&](int) {});
      sc[w * 32] = s0; sc[(8 + w) * 32] = s1; sc[(16 + w) * 32] = s2; sc[(24 + w) * 32] = s3;
      mx = fmaxf(fmaxf(s0, s1), fmaxf(s2, s3));
      SB();
      STAMP(11);
    }
    const v4u xa2 = ld4<0>(rx, (isf && w == 7) ? l16 : OOB, 0);
    SB();
#pragma unroll 1
    while (kind == 0) {
      next();
      const float sv = scores<8>(ring, qb, lane, ldn);
      sc[cu * 32] = sv;
      tok = token(KBURST);
      mx = fmaxf(mx, sv);
      STAMP(sx); ++sx;
      if (svc && !(LD && leader)) service();
      SB();
    }
    STAMP(7);
    if (GB == 3) __hip_atomic_fetch_add(ctl, -lost, __ATOMIC_RELAXED, __HIP_MEMORY_SCOPE_WORKGROUP);
    if (LD == 1 && leader) { pv = echoes(seenm); SB(); }
    if (isf && w == 7) {
      const f32x2 ls = ln_stats(xa2, lane);
      v4u xn;
#pragma unroll
      for (int j = 0; j < 4; ++j) {
        const f32x2 v = unp(xa2[j]);
        xn[j] = __builtin_amdgcn_perm(asu(ln_apply(v.y, ls)), asu(ln_apply(v.x, ls)), 0x07060302u);
      }
      *reinterpret_cast<v4u*>(&s_xn[lane * 4]) = xn;
      LDS_FENCE();
      ctl_set(C_FC);
      SB();
    }
    st.m = STK == 2 ? -__builtin_inff() : red_max(mx); st.l = 0.f;
    st.a[0] = st.a[1] = (f32x4){0.f, 0.f, 0.f, 0.f};
#pragma unroll 1
    while (kind == 1) {
      float sv;
      for (;;) {
        sv = __hip_atomic_load(&sc[nu * 32], __ATOMIC_RELAXED, __HIP_MEMORY_SCOPE_WORKGROUP);
        if (__builtin_amdgcn_ballot_w64(asu(sv) == SENT) == 0) break;
        __builtin_amdgcn_s_sleep(1);
      }
      if (__builtin_amdgcn_ballot_w64(sv > st.m) != 0) {
        const float m2 = fmaxf(st.m, red_max(sv));
        const float al = __builtin_amdgcn_exp2f((st.m - m2) * EXP_C);
        st.m = m2; st.l *= al;
        st.a[0] *= al; st.a[1] *= al;
      }
      const float p = __builtin_amdgcn_exp2f((sv - st.m) * EXP_C);
      st.l += p;
      SB();
      next();
      if (kind == 3) {
        last();
        put_ml();
        phase_v<8>(ring, p, pm, st, [&](int) {});
        break;
      }
      phase_v<8>(ring, p, pm, st, ldn);
      tok = token(VBURST);
      STAMP(sx); ++sx;
      if (svc) service();
      SB();
    }
    STAMP(8);
    if (kind == 2) {
      next();
#pragma unroll 1
    while (kind != 3) {
      if (UF) { nba = __builtin_amdgcn_readfirstlane(nba); nbb = __builtin_amdgcn_readfirstlane(nbb); nsa = __builtin_amdgcn_readfirstlane(nsa); nsb = __builtin_amdgcn_readfirstlane(nsb); }
      const float p = phase_k4(ring, qb, st, lane, ldn);
      phase_v<4>(ring, p, pm, st, ldn);
      tok = token(VBURST);
      STAMP(sx); ++sx;
      SB();
      next();
    }
      SB();
      last();
      const float p = phase_k4(ring, qb, st, lane, [&](int) {});
      phase_v<4>(ring, p, pm, st, [&](int) {});
      put_ml();
    }
    pw[0] = st.a[0]; pw[64] = st.a[1];
    LDS_FENCE();
    SB();
    STAMP(12);
    if (idle) {
      LDS_FENCE(); bar(); LDS_FENCE();
      STAMP(2);
    }
    SB();
#pragma unroll 1
    for (int it = 0; it < SPIN_CAP && svc; ++it) {
      service();
      asm volatile("" :: "v"(pv));
    }
    LDS_FENCE();
    WAITV(0);
  }
  const lds4_t pr = (lds4_t)&s_part[0][lane * 4], pmr = (lds4_t)&s_ml[(lane >> 3) & 1][0];
  if (slot < PM) __hip_atomic_fetch_add(ctl, lane == C_DONE ? 1 : 0, __ATOMIC_RELAXED, __HIP_MEMORY_SCOPE_WORKGROUP);
  SB();
  const v4u rw4 = {(unsigned)(size_t)ws, (unsigned)((size_t)ws >> 32), WS_END, 0x00020000u};
  const unsigned fj = isf ? rank - FO : 4;
  auto clear = [&](int k, bool more) __attribute__((always_inline)) {
#pragma unroll 1
    for (int it = 0; it < SPIN_CAP; ++it) {
      const int x = __hip_atomic_load(ctl, __ATOMIC_RELAXED, __HIP_MEMORY_SCOPE_WORKGROUP);
      if (__builtin_amdgcn_readlane(x, C_TICK) >= nt + 8 - k && (!more || (__builtin_amdgcn_readlane(x, C_FC) && __builtin_amdgcn_readlane(x, C_OLD) == 8))) break;
      __builtin_amdgcn_s_sleep(1);
    }
    LDS_FENCE();
    SB();
  };
  const unsigned half = isy ? w : rank - SR;
  if (isy && w >= 4) {
    v4u wt[6][2];
    const rsrc_t rwo = mkrs(wa, XW ? 0 : 32 * D * 2);
    const rsrc_t rwd = mkrs(wb, (y < 112 && !XW) ? 28 * D * 2 : 0);
    unsigned o0 = (lane >> 3) * (D * 2) + (w - 4) * 256 + (lane & 7) * 16, o1 = w == 7 ? OOB : o0 + 128;
    unsigned o2 = o0 + (lane >> 5) * (4 * D * 2), o3 = o1 + (lane >> 5) * (4 * D * 2);
    asm volatile("" : "+v"(o0), "+v"(o1), "+v"(o2), "+v"(o3));
    SB();
    auto ldw = [&](int k) __attribute__((always_inline)) {
      const int i = (k + 4) % 6;
      wt[i][0] = i < 4 ? ld4<2>(rwo, o0, i * 8 * D * 2) : ld4<2>(rwd, o2, (i & 3) * 16 * D * 2);
      wt[i][1] = i < 4 ? ld4<2>(rwo, o1, i * 8 * D * 2) : ld4<2>(rwd, o3, (i & 3) * 16 * D * 2);
      SB();
    };
    if (YE) {
      clear(YK, false);
#pragma unroll
      for (int k = 0; k < YE; ++k) ldw(k);
    }
    LDS_FENCE(); bar(); LDS_FENCE();
    STAMP(2);
    if (YB) __builtin_amdgcn_s_barrier();
    if (YD) __builtin_amdgcn_s_sleep(YD);
    SB();
#pragma unroll
    for (int k = YE; k < 6; ++k) ldw(k);
    const unsigned spare = __builtin_amdgcn_raw_buffer_load_b32(rwo, (int)OOB, 0, 0);
    SB();
    const int p = (w - 4) * 32 + (lane >> 4) * 8 + (lane & 7);
    v4u dt = *reinterpret_cast<const v4u*>(&s_dst[2][p * 4]) & 1u;
    unsigned to = ((lane & 8) || p >= 112) ? OOB : my(p);
    f32x4 acc[2] = {{0.f, 0.f, 0.f, 0.f}, {0.f, 0.f, 0.f, 0.f}};
    auto bop = [&](float a) -> v4u { return pm & __builtin_bit_cast(unsigned, (bf16x2){(__bf16)a, (__bf16)a}); };
    auto step = [&](int i, const v4u& pb) { acc[0] = mfma(wt[i][0], pb, acc[0]); acc[1] = mfma(wt[i][1], pb, acc[1]); };
    lds4_t po = (lds4_t)&s_o[0][(lane >> 3) * 4];
    lds1_t ph = (lds1_t)&s_h[(lane & 4) * 4 + ((lane >> 4) & 1) * 8 + (lane & 3) * 2 + (lane >> 5)];
    asm volatile("" : "+v"(dt), "+v"(to), "+v"(po), "+v"(ph));
    SB();
    const v4u old = *reinterpret_cast<const v4u*>(&s_old[7][lane * 4]);
    v4u dr = old;
    if (w == 7) {
      if (G3) until(G3 + __builtin_amdgcn_readlane(tok, C_LEAD));
      dr = poll<PD3, PG3, 0>(rw4, pa, old);
      const v4u d = dr & ~1u;
      float g = tr16(tr32(asf(d[0]), asf(d[2])), tr32(asf(d[1]), asf(d[3])));
      g = rbf(g + dpp<ROR8>(g));
      const float u = dpp<ROR4>(g);
      *ph = rbf(g / (1.0f + __expf(-g))) * u;
      SB();
    }
    STAMP(14);
    LDS_FENCE(); bar(); LDS_FENCE();
    STAMP(3);
    const f32x2 hv = *reinterpret_cast<const f32x2*>(&s_h[(lane >> 3) * 2]);
    const f32x4 ma = *reinterpret_cast<const f32x4*>(&s_ml2[0]), mb = *reinterpret_cast<const f32x4*>(&s_ml2[4]);
    f32x4 pq[4];
#pragma unroll
    for (int k = 0; k < 4; ++k) pq[k] = po[k * 8];
    SB();
    const v4u h0 = bop(hv.x), h1 = bop(hv.y);
    {
      step(4, h0); step(5, h1);
      asm volatile("" : "+v"(acc[0]), "+v"(acc[1]));
      SB();
    }
    const float M = fmx(fmx(fmx(mb[0], mb[1]), mb[2]), mb[3]);
    f32x4 e = (mb - M) * EXP_C;
#pragma unroll
    for (int k = 0; k < 4; ++k) e[k] = __builtin_amdgcn_exp2f(e[k]);
    const float inv = __builtin_amdgcn_rcpf(__builtin_fmaf(ma[0], e[0], ma[1] * e[1]) + __builtin_fmaf(ma[2], e[2], ma[3] * e[3]));
    f32x2 av[2];
#pragma unroll
    for (int j = 0; j < 2; ++j) av[j] = (f32x2){pq[0][2 * j], pq[0][2 * j + 1]} * e[0];
#pragma unroll
    for (int k = 1; k < 4; ++k) {
#pragma unroll
      for (int j = 0; j < 2; ++j) av[j] = __builtin_elementwise_fma((f32x2){pq[k][2 * j], pq[k][2 * j + 1]}, (f32x2){e[k], e[k]}, av[j]);
    }
    av[0] *= inv; av[1] *= inv;
    {
      v4u pb[4];
#pragma unroll
      for (int i = 0; i < 4; ++i) pb[i] = bop(av[i >> 1][i & 1]);
      if (FB) { asm volatile("" : "+v"(pb[0]), "+v"(pb[1]), "+v"(pb[2]), "+v"(pb[3])); SB(); }
#pragma unroll
      for (int i = 0; i < 4; ++i) step(i, pb[i]);
    }
    f32x4 z;
#pragma unroll
    for (int v = 0; v < 4; ++v) {
      const float a0 = asf((unsigned)__builtin_amdgcn_update_dpp((int)asu(acc[0][v]), (int)asu(acc[0][v]), ROR8, 0xc, 0xf, false));
      const float a1 = asf((unsigned)__builtin_amdgcn_update_dpp((int)asu(acc[1][v]), (int)asu(acc[1][v]), ROR8, 0xc, 0xf, false));
      z[v] = tr32(a0, a1);
    }
    __builtin_amdgcn_raw_buffer_store_b128(tag4(z, dt), rws, (int)to, 0, S2);
    SB();
    STAMP(4);
    asm volatile("" :: "{v198}"(spare));
    poll_end(dr);
    STAMP_OUT();
    __builtin_amdgcn_endpgm();
  }
  if (half < 2) {
    ldsu_t tp = (ldsu_t)&s_dst[4 + half][lane * 4];
    unsigned to = mo_ + half * 4 * AREA;
    lds4_t pp = pr + half * 64;
    asm volatile("" : "+v"(tp), "+v"(to), "+v"(pp));
    SB();
    f32x4 ml[4], pt[8], o = {0.f, 0.f, 0.f, 0.f};
    float M = -3.0e38f, L = 0.f;
    if (PM) {
#pragma unroll 1
      for (int it = 0; it < SPIN_CAP && ctl_get(C_DONE) < PM; ++it) {}
      LDS_FENCE();
#pragma unroll
      for (int k = 0; k < 4; ++k) if ((k & 1) * 4 < PM) ml[k] = pmr[k];
#pragma unroll
      for (int k = 0; k < PM; ++k) pt[k] = pp[k * 128];
      M = ml[0][0];
#pragma unroll
      for (int k = 1; k < PM; ++k) M = fmx(M, ml[k >> 2][k & 3]);
#pragma unroll
      for (int k = 0; k < PM; ++k) {
        const float e = __builtin_amdgcn_exp2f((ml[k >> 2][k & 3] - M) * EXP_C);
        o += pt[k] * e;
        L += ml[2 + (k >> 2)][k & 3] * e;
      }
      asm volatile("" : "+v"(o), "+v"(M), "+v"(L));
      SB();
    }
    LDS_FENCE(); bar(); LDS_FENCE();
    STAMP(2);
#pragma unroll
    for (int k = 0; k < 4; ++k) if ((k & 1) * 4 + 4 > PM) ml[k] = pmr[k];
#pragma unroll
    for (int k = PM; k < 8; ++k) pt[k] = pp[k * 128];
    const v4u t = *tp;
    SB();
    {
      float m2 = M;
#pragma unroll
      for (int k = PM; k < 8; ++k) m2 = fmx(m2, ml[k >> 2][k & 3]);
      if (PM) {
        const float e = __builtin_amdgcn_exp2f((M - m2) * EXP_C);
        o *= e; L *= e;
      }
      M = m2;
#pragma unroll
      for (int k = PM; k < 8; ++k) {
        const float e = __builtin_amdgcn_exp2f((ml[k >> 2][k & 3] - M) * EXP_C);
        o += pt[k] * e;
        L += ml[2 + (k >> 2)][k & 3] * e;
      }
    }
    v4u msg;
#pragma unroll
    for (int j = 0; j < 3; ++j) msg[j] = tag<0x100>(__builtin_amdgcn_perm(asu(o[j]), asu(o[3]), 0x07060501u + j), t[j]);
    msg[3] = tag<1>(asu((lane & 1) ? L : M), t[3]);
    __builtin_amdgcn_raw_buffer_store_b128(msg, rws, (int)to, 0, S1);
    SB();
    if (!isy) STAMP(15);
  } else if (fj < 4) {
    unsigned fvo = ((lane >> 3) * (8 * F * 2) + (lane & 7) * 16) | lmask;
    const unsigned fb = ((f & 3) * 2 + (fj >> 1)) * (56 * F * 2) + (f >> 2) * 128;
    const rsrc_t rf = mkrs((fj & 1) ? wb : wa, FX ? 0 : D * F * 2);
    asm volatile("" : "+v"(fvo));
#pragma unroll
    for (int i = 0; i < FE; ++i) { ring[i] = ld4<2>(rf, fvo, fb + i * (F * 2)); SB(); }
    clear(FK, true);
    if (FD) __builtin_amdgcn_s_sleep(FD);
    SB();
    STAMP(46);
#pragma unroll
    for (int i = FE; i < 8; ++i) { ring[i] = ld4<2>(rf, fvo, fb + i * (F * 2)); SB(); }
    const v4u xn = *reinterpret_cast<const v4u*>(&s_xn[(((f & 3) * 2 + (fj >> 1)) * 7 + (lane >> 3)) * 4]);
    const int p = fj * 16 + ((lane >> 4) & 1) * 8 + (lane & 7);
    v4u t = *reinterpret_cast<const v4u*>(&s_dst[6][p * 4]);
    unsigned to = (lane & 40) ? OOB : mf(p);
    v4u pb[8];
#pragma unroll
    for (int i = 0; i < 8; ++i) {
      pb[i] = pm & __builtin_amdgcn_perm(xn[i >> 1], xn[i >> 1], (i & 1) ? 0x03020302u : 0x01000100u);
      asm volatile("" : "+v"(pb[i]));
    }
    asm volatile("" : "+v"(t), "+v"(to));
    SB();
    LDS_FENCE(); bar(); LDS_FENCE();
    SB();
    STAMP(2);
    f32x4 acc[2] = {{0.f, 0.f, 0.f, 0.f}, {0.f, 0.f, 0.f, 0.f}};
#pragma unroll
    for (int i = 0; i < 8; ++i) acc[i & 1] = mfma(ring[i], pb[i], acc[i & 1]);
    asm volatile("" :: "v"(pv));
    acc[0] += acc[1];
    f32x4 z;
#pragma unroll
    for (int v = 0; v < 4; ++v)
      z[v] = x32_add(asf((unsigned)__builtin_amdgcn_update_dpp((int)asu(acc[0][v]), (int)asu(acc[0][v]), ROR8, 0xc, 0xf, false)));
    __builtin_amdgcn_raw_buffer_store_b128(tag4(z, t), rws, (int)to, 0, S1);
    SB();
    STAMP(14);
  } else if (!idle) {
    LDS_FENCE(); bar(); LDS_FENCE();
    SB();
    STAMP(2);
  }
  const v4u old = *reinterpret_cast<const v4u*>(&s_old[w][lane * 4]);
  if (isy) {
    if (YB) { SB(); __builtin_amdgcn_s_barrier(); SB(); }
    lds1_t po = (lane & 8) ? (lds1_t)&s_ml2[w] : (lds1_t)&s_o[w][(lane & 7) * 4 + (lane >> 5) * 2 + ((lane >> 4) & 1)];
    lds1_t pq = (lds1_t)&s_ml2[4 + w];
    asm volatile("" : "+v"(po), "+v"(pq));
    if (G1) until(G1 + __builtin_amdgcn_readlane(tok, C_LEAD));
    STAMP(14);
    const v4u dr = poll<PD1, PG1, PS1>(rw4, pa, old);
    STAMP(15);
    const v4u d = dr & (v4u){~0x100u, ~0x100u, ~0x100u, ~1u};
    const float ot = dpp<XOR1>(asf(d[3]));
    const float m = (lane & 1) ? ot : asf(d[3]), l = (lane & 1) ? asf(d[3]) : ot;
    float M = fmx(m, dpp<ROR8>(m)); M = x16_mx(M); M = x32_mx(M);
    *pq = M;
    const float e = __builtin_amdgcn_exp2f((m - M) * EXP_C);
    const float v3 = asf(__builtin_amdgcn_perm(__builtin_amdgcn_perm(d[2], d[1], 0x04000c0cu), d[0], 0x0706000cu));
    float y0 = tr32(asf(d[0]) * e, asf(d[2]) * e), y1 = tr32(asf(d[1]) * e, v3 * e), L = x32_add(l * e);
    y0 = tr16(y0, y1); L = x16_add(L);
    const bool s_ = lane & 8;
    *po = (s_ ? L : y0) + dpp<ROR8>(s_ ? y0 : L);
    LDS_FENCE(); bar(); LDS_FENCE();
    STAMP(3);
    poll_end(dr);
    STAMP(4);
    STAMP_OUT();
    __builtin_amdgcn_endpgm();
  }
  if (isr) {
    const int col = (ro >> 1) * 32 + (ro & 1) * 4 + (lane & 3) * 8 + ((lane >> 2) & 3);
    float xo = asf((unsigned)xg[col] << 16);
    lds1_t po = (lds1_t)&s_r[(lane & 12) ? 8 : w][(lane & 3) * 4 + (lane >> 5) * 2 + ((lane >> 4) & 1)], pq = (lds1_t)&s_r[lane >> 4][lane & 15];
    const rsrc_t ro_ = mkrs(out, D * 2);
    unsigned oo = lane < 16 ? col * 2 : OOB;
    asm volatile("" : "+v"(xo), "+v"(po), "+v"(pq), "+v"(oo));
    if (G2) until(G2 + __builtin_amdgcn_readlane(tok, C_LEAD));
    const v4u dr = poll<PD2, PG2, PS2>(rw4, pa, old);
    const v4u d = dr & ~1u;
    float y0 = tr32(asf(d[0]), asf(d[2])), y1 = tr32(asf(d[1]), asf(d[3]));
    y0 = tr16(y0, y1);
    y0 += dpp<ROR8>(y0); y0 += dpp<ROR4>(y0);
    *po = y0;
    LDS_FENCE(); bar(); LDS_FENCE();
    if (w == 0) {
      float a = pq[0] + pq[64];
      a = x16_add(a); a = x32_add(a);
      __builtin_amdgcn_raw_buffer_store_b16((short)f2bf(xo + a), ro_, (int)oo, 0, SO);
    }
    STAMP(4);
    poll_end(dr);
  }
  if (!isr) STAMP(4);
  STAMP_OUT();
}

extern "C" void kernel_launch(void* const* d_in, const int* in_sizes, int n_in,
                              void* d_out, int out_size, void* d_ws, size_t ws_size,
                              hipStream_t stream) {
  fused_kernel<<<NWG, NT, 0, stream>>>((const u16*)d_in[0], (const u16*)d_in[1], (const u16*)d_in[2], (const u16*)d_in[3],
                                       (const u16*)d_in[4], (const u16*)d_in[6], (const u16*)d_in[7], (const u16*)d_in[8],
                                       (u16*)d_out, (char*)d_ws);
}
```
